```python
import torch

def _softmax(x, axis=-1):
    return torch.softmax(x, dim=axis)
def _gelu(x):
    return torch.nn.functional.gelu(x, approximate="tanh")
def _silu(x):
    return torch.nn.functional.silu(x)
def _sigmoid(x):
    return torch.sigmoid(x)
def _layernorm(x, axis=-1, eps=1e-5):
    m = x.mean(dim=axis, keepdim=True)
    v = x.var(dim=axis, keepdim=True, unbiased=False)
    return (x - m) / torch.sqrt(v + eps)
def _scatter_add(tgt, idx, src, axis=0):
    return tgt.clone().index_add_(axis, idx, src)
def _segment_sum(src, seg, n):
    out = torch.zeros((n,) + src.shape[1:], dtype=src.dtype)
    return out.index_add_(0, seg, src)
def _pad(x, pw):
    flat = tuple(v for pair in reversed(pw) for v in pair)
    return torch.nn.functional.pad(x, flat)

kernel_name = 'interleave_layers__ssm_conv_s4__sliding_local_dilated__prompt'

def setup_inputs():

    _w = (lambda a: (lambda t: t.to(torch.bfloat16) if t.is_floating_point() else t)(torch.as_tensor(a)))
    def _A_setup():
        B, L, D, N = 1, 2048, 512, 128
        A_re = -(lambda _x: torch.exp(torch.as_tensor(_x)))((lambda *s: torch.randn(*s, dtype=torch.bfloat16) * 1.6)(D, N))
        A_im = torch.pi * (lambda *s: torch.randn(*s, dtype=torch.bfloat16) * 0.1)(D, N)
        B_re = (lambda *s: torch.randn(*s, dtype=torch.bfloat16) * 0.1)(D, N)
        B_im = (lambda *s: torch.randn(*s, dtype=torch.bfloat16) * 0.1)(D, N)
        C_re = (lambda *s: torch.randn(*s, dtype=torch.bfloat16) * 0.1)(D, N)
        C_im = (lambda *s: torch.randn(*s, dtype=torch.bfloat16) * 0.1)(D, N)
        log_dt = (lambda *s: torch.randn(*s, dtype=torch.bfloat16) * 0.1)(D) * 0.5 - 4.0
        Dskip = (lambda *s: torch.randn(*s, dtype=torch.bfloat16) * 0.1)(D)
        return {
            "u": _w((lambda *s: torch.randn(*s, dtype=torch.bfloat16) * 0.05)(B, L, D)),
            "A_re": _w(A_re),
            "A_im": _w(A_im),
            "B_re": _w(B_re),
            "B_im": _w(B_im),
            "C_re": _w(C_re),
            "C_im": _w(C_im),
            "log_dt": _w(log_dt),
            "Dskip": _w(Dskip),
        }
    _A_in = _A_setup()

    def _B_setup():
        B, S, D = 1, 2048, 512
        return {
            "x": _w((lambda *s: torch.randn(*s, dtype=torch.bfloat16) * 0.05)(B, S, D)),
            "wqkv": _w((lambda *s: torch.randn(*s, dtype=torch.bfloat16) * 0.05)(D, 3 * D)),
            "wo": _w((lambda *s: torch.randn(*s, dtype=torch.bfloat16) * 0.05)(D, D)),
        }
    _B_in = _B_setup()

    return {
        "A_u": _A_in["u"],
        "A_A_re": _A_in["A_re"],
        "A_A_im": _A_in["A_im"],
        "A_B_re": _A_in["B_re"],
        "A_B_im": _A_in["B_im"],
        "A_C_re": _A_in["C_re"],
        "A_C_im": _A_in["C_im"],
        "A_log_dt": _A_in["log_dt"],
        "A_Dskip": _A_in["Dskip"],
        "B_x": _B_in["x"],
        "B_wqkv": _B_in["wqkv"],
        "B_wo": _B_in["wo"],
    }

def reference(A_u, A_A_re, A_A_im, A_B_re, A_B_im, A_C_re, A_C_im, A_log_dt, A_Dskip, B_x, B_wqkv, B_wo):
    def _A_ref(u, A_re, A_im, B_re, B_im, C_re, C_im, log_dt, Dskip):
        Bsz, L, D = u.shape
        dt = torch.exp(log_dt)[:, None]
        A = A_re + 1j * A_im
        Bc = B_re + 1j * B_im
        Cc = C_re + 1j * C_im
        logdA = dt * A
        dB = dt * Bc
        l = (lambda x, dt: x.to(dt))(torch.arange(L), A_re.dtype)
        P = torch.exp(logdA[:, :, None] * l[None, None, :])
        K = torch.einsum("dn,dnl,dn->dl", Cc, P, dB).real
        L2 = 2 * L
        Kp = _pad(K, ((0, 0), (0, L)))
        up = (lambda x, *d: x.permute(*d))(u, 0, 2, 1)
        up = _pad(up, ((0, 0), (0, 0), (0, L)))
        Kf = (lambda x, axes=(-2,-1): torch.fft.rfft2(x.float(), dim=axes))(Kp[:, None, :], axes=(-2, -1))
        Uf = (lambda x, axes=(-2,-1): torch.fft.rfft2(x.float(), dim=axes))(up[:, :, None, :], axes=(-2, -1))
        Yf = Uf * Kf[None, :, :, :]
        y = (lambda x, s, axes=(-2,-1): torch.fft.irfft2(x, s=s, dim=axes).to(torch.bfloat16))(Yf, (1, L2), axes=(-2, -1))[:, :, 0, :L]
        y = (lambda x, *d: x.permute(*d))(y, 0, 2, 1) + u * Dskip[None, None, :]
        return y

    def _B_ref(x, wqkv, wo):
        B, S, D = x.shape
        H = 32; Dh = D // H
        W, dil = 8, 2
        qkv = (lambda x, *d: x.permute(*d))((x @ wqkv).reshape(B, S, 3, H, Dh), 2, 0, 3, 1, 4)
        q, k, v = qkv[0], qkv[1], qkv[2]
        scale = Dh ** -0.5
        pos = torch.arange(S)
        off = torch.arange(W) * dil
        idx = pos[:, None] - off[None, :]
        mask = idx >= 0
        idx_c = torch.where(mask, idx, 0).reshape(-1)
        kg = k[:, :, idx_c, :].reshape(B, H, S, W, Dh)
        vg = v[:, :, idx_c, :].reshape(B, H, S, W, Dh)
        logits = torch.einsum("bhsd,bhswd->bhsw", q, kg) * scale
        logits = torch.where(mask[None, None, :, :], logits, -1e9)
        attn = _softmax(logits, axis=-1)
        o = torch.einsum("bhsw,bhswd->bhsd", attn, vg)
        return (lambda x, *d: x.permute(*d))(o, 0, 2, 1, 3).reshape(B, S, D) @ wo

    x = A_u
    x = x + _A_ref(_layernorm(x, axis=-1), A_A_re, A_A_im, A_B_re, A_B_im, A_C_re, A_C_im, A_log_dt, A_Dskip)
    x = x + _B_ref(_layernorm(x, axis=-1), B_wqkv, B_wo)
    x = x + _A_ref(_layernorm(x, axis=-1), A_A_re, A_A_im, A_B_re, A_B_im, A_C_re, A_C_im, A_log_dt, A_Dskip)
    x = x + _B_ref(_layernorm(x, axis=-1), B_wqkv, B_wo)
    return x
```

```cpp
#if defined(__HIP_DEVICE_COMPILE__)
#pragma clang attribute push (__attribute__((target("no-packed-fp32-ops"))), apply_to = function)
#endif
#include <hip/hip_runtime.h>
#include <hip/hip_bf16.h>
#include <stdint.h>
#define TIDX ((int)__builtin_amdgcn_workitem_id_x())
#define BIDX ((int)__builtin_amdgcn_workgroup_id_x())
#define ST(set, k)
#define ST_INIT()
#define ST_OUT()
#define KATTR
#define FAILC(k)

#define ST_FIX()
#define CST(k)
#define GST(k)
typedef unsigned short u16;
typedef unsigned int u32;
typedef unsigned long long u64;
using bf16x8 = __attribute__((ext_vector_type(8))) short;
using bf16x4 = __attribute__((ext_vector_type(4))) short;
using f16x8 = __attribute__((ext_vector_type(8))) _Float16;
using f16x4 = __attribute__((ext_vector_type(4))) _Float16;
using f32x2 = __attribute__((ext_vector_type(2))) float;
using f32x4 = __attribute__((ext_vector_type(4))) float;
using u32x2 = __attribute__((ext_vector_type(2))) unsigned;
using u32x4 = __attribute__((ext_vector_type(4))) unsigned;
#define LAS __attribute__((address_space(3)))
#define GAS __attribute__((address_space(1)))

#define D_ 512
#define NS_ 128
#define XS_ 2080

#define MB_ (1 << 20)
#define XTO(kp_) (OFF_O1 + (((kp_) >> 4) & 1) * MB_ + ((kp_) >> 5) * 131072 + ((kp_) & 15) * 8192)
#define XTS 8192
#define RB_OF(b_) (((b_) & 7) * 4 + ((b_) >> 6))
#define HB_OF(b_) (((b_) >> 3) & 7)
#define OFF_ST0 (5 * MB_)
#define OFF_TS (OFF_ST0 + 16384)
#define OFF_SG (OFF_ST0 + 32768)
#define OFF_ST2 (OFF_ST0 + 65536)
#define OFF_EP (OFF_ST0 + 131072)
#define OFF_WQ (9 * MB_ - 262144)
#define OFF_WO (10 * MB_ + 393216)
#define OFF_X1 (11 * MB_)
#define OFF_O1 (14 * MB_)
#define OFF_X2 (16 * MB_)
#define OFF_X3 (19 * MB_)
#define OFF_O2 (22 * MB_)
#define OFF_KV2 (26 * MB_)
#define KVO(L1_, rb_, h_) (((L1_) ? OFF_O2 : OFF_KV2) + (((h_) >> 3) & 1) * MB_ + (rb_) * 32768 + (((h_) >> 2) & 1) * 16384 + ((h_) >> 4) * 8192 + ((h_) & 3) * 2048)
#define WSB (32 * MB_)
#define XBS 32896
#define SPIN_CAP (1u << 14)
#define B6 0
#define OUTA 2
#define OUTB 0
#define WLD 8
#define SLP_FL 4
#define SLP_WE 0
#define R1F 3
#define D9 1750
#define WS5 5
#define SLP_B4 4
#define H1 9
#define DB2 450
#define D5 500
#define DQ2 0
#define DO2 900
#define SLP_U 2
#define SLP_F 2
#define SLP_G 0
#define R1D 1
#define WOE 4
#define SLP_H1 0
#define SLP_H2 2
#define WO0 0
#define SLP_XA2 6
#define CPF 5
#define SLP_XP 2
#define SLP_SP 4
#define SLP_S 6
#define SLP_XA1 4
#define SLP_XB1 6
#define SLP_OB1 6
#define SLP_OA1 7
#define SLP_C2 3
#define SLP_C3 15

__device__ __forceinline__ float bf2f(u16 v) { return __uint_as_float(((u32)v) << 16); }
typedef __attribute__((ext_vector_type(2))) __bf16 bf16x2v;
__device__ __forceinline__ u32 pk2(float a, float b) {
  f32x2 f = {a, b};
  bf16x2v v = __builtin_convertvector(f, bf16x2v);
  return __builtin_bit_cast(u32, v);
}
__device__ __forceinline__ float rbf(float f) { return __uint_as_float(pk2(f, 0.f) << 16); }
__device__ __forceinline__ float lo2f(u32 w) { return __uint_as_float(w << 16); }
__device__ __forceinline__ float hi2f(u32 w) { return __uint_as_float(w & 0xFFFF0000u); }
template <int CTRL>
__device__ __forceinline__ float dpp_mov(float v) {
  return __int_as_float(__builtin_amdgcn_update_dpp(0, __float_as_int(v), CTRL, 0xf, 0xf, false));
}
#define DPP_XOR1 0xB1
#define DPP_XOR2 0x4E
#define DPP_ROR8 0x128
template <int CTRL, int RMASK>
__device__ __forceinline__ float dpp_add(float v) {
  return v + __int_as_float(__builtin_amdgcn_update_dpp(0, __float_as_int(v), CTRL, RMASK, 0xf, false));
}
__device__ __forceinline__ void wave_sum3(float& a, float& b, float& c) {
#define WS3(CT_, RM_) a = dpp_add<CT_, RM_>(a); b = dpp_add<CT_, RM_>(b); c = dpp_add<CT_, RM_>(c); __builtin_amdgcn_sched_barrier(0);
  WS3(0x111, 0xf) WS3(0x112, 0xf) WS3(0x114, 0xf) WS3(0x118, 0xf) WS3(0x142, 0xa) WS3(0x143, 0xc)
  a = __int_as_float(__builtin_amdgcn_readlane(__float_as_int(a), 63));
  b = __int_as_float(__builtin_amdgcn_readlane(__float_as_int(b), 63));
  c = __int_as_float(__builtin_amdgcn_readlane(__float_as_int(c), 63));
}
__device__ __forceinline__ float rmax(float a, float b) {
  float r;
  asm("v_max_f32 %0, %1, %2" : "=v"(r) : "v"(a), "v"(b));
  return r;
}
__device__ __forceinline__ void lds_barrier() {
  asm volatile("s_waitcnt lgkmcnt(0)" ::: "memory");
  __builtin_amdgcn_s_barrier();
  asm volatile("" ::: "memory");
}
__device__ __forceinline__ void cexp2(float re2, float rev, float& orr, float& oi) {
  float e = __builtin_amdgcn_exp2f(re2);
  rev = __builtin_amdgcn_fractf(rev);
  float s = __builtin_amdgcn_sinf(rev);
  float c = __builtin_amdgcn_cosf(rev);
  orr = e * c;
  oi = e * s;
}
__device__ __forceinline__ void cexp3(float re2, float rev, float& e, float& c, float& s) {
  e = __builtin_amdgcn_exp2f(re2);
  rev = __builtin_amdgcn_fractf(rev);
  s = __builtin_amdgcn_sinf(rev); c = __builtin_amdgcn_cosf(rev);
}
__device__ __forceinline__ float dot2acc(u32 a, u32 b, float c) {
  return __builtin_amdgcn_fdot2_f32_bf16(__builtin_bit_cast(bf16x2v, a), __builtin_bit_cast(bf16x2v, b), c, false);
}
__device__ __forceinline__ u32 opaque(u32 v) { asm volatile("" : "+v"(v)); return v; }
__device__ __forceinline__ int lane_id() { return (int)__builtin_amdgcn_mbcnt_hi(~0u, __builtin_amdgcn_mbcnt_lo(~0u, opaque(0u))); }
__device__ __forceinline__ u32 pack_stats(float mean, float var) {
  float vb = rbf(var);
  float ve = rbf(vb + 1e-5f);
  float sd = __builtin_amdgcn_sqrtf(ve);
  return pk2(mean, sd);
}

#define SB() __builtin_amdgcn_sched_barrier(0)

#define KADD(id_, t_, v_) kadd_<(v_)>(t_)
#define KSLP(id_, v_) do { if ((v_) > 0) __builtin_amdgcn_s_sleep(v_); } while (0)
template <int V>
__device__ __forceinline__ u32 kadd_(u32 t) { u32 r; asm("s_add_i32 %0, %1, %2" : "=s"(r) : "s"(__builtin_amdgcn_readfirstlane(t)), "n"(V) : "scc"); return r; }
template <int V> struct IC { static constexpr int value = V; };
template <int B, int E, class F>
__device__ __forceinline__ void static_for(F& f) { if constexpr (B < E) { f(IC<B>{}); static_for<B + 1, E>(f); } }
typedef __amdgpu_buffer_rsrc_t rsrc_t;
__device__ __forceinline__ u32x4 ldw(rsrc_t r, int off) { return __builtin_amdgcn_raw_buffer_load_b128(r, off, 0, 16); }
__device__ __forceinline__ u32x4 ldws(rsrc_t r, int off, int soff) { return __builtin_amdgcn_raw_buffer_load_b128(r, off, soff, 16); }
__device__ __forceinline__ void stw(rsrc_t r, int off, u32x4 v) { __builtin_amdgcn_raw_buffer_store_b128(v, r, off, 0, 16); }
__device__ __forceinline__ u32 umax4(u32 m, u32x4 v) { return max(max(max(m, v[0]), v[1]), max(v[2], v[3])); }
template <int CTRL>
__device__ __forceinline__ u32 dpp_u(u32 v) { return (u32)__builtin_amdgcn_update_dpp(0, (int)v, CTRL, 0xf, 0xf, false); }

#define CSTR 4160
#define L_U 33280
#define L_TA 42496
#define L_TB (L_TA + 32768)
#define L_QB (L_TB + 32768)
#define L_TH (L_QB + 16384)
#define L_SP (L_QB + 32768)
#define L_FS (L_SP + 4096)
#define L_MI (L_FS + 3072)
#define L_XS (L_MI + 80)
#define L_WX (L_XS + 1024)
#define MEGA_LDS (L_WX + 12288)
#define L_REDA L_QB
#define L_REDB (L_QB + 16384)
#define L_XC L_REDB
#define L_X (L_REDB + 8192)
#define TBS 3136
#define L_WR (L_QB + 24576)
#define L_WS L_TA
#define L_KT L_TB
#define KTO 65024
#define FTS 528
#define L_P0 (L_FS + 2048)
#define STAT_SCALE 33554432.0f
#define C_OB 20
#define C_OT 24
#define M_FX 72
#define C_X2 28
#define C_G2 32
#define C_A 36
#define C_B 40
#define C_F 44
#define C_KH 48
#define C_P2 52
#define C_XC 8
#define C_P3 64
#define M_RD 68
#define M_DL 76
#define C_OV 28
#define C_U 56
#define C_W 60

typedef __attribute__((ext_vector_type(2))) _Float16 f16x2v;
typedef __attribute__((ext_vector_type(2))) short i16x2;
__device__ __forceinline__ u32 pkh(float a, float b) {
  u32 r;
  asm("v_cvt_pk_f16_f32 %0, %1, %2" : "=v"(r) : "v"(a), "v"(b));
  return r;
}
__device__ __forceinline__ u32 pkhn(float a, float b) {
  u32 r;
  asm("v_cvt_pk_f16_f32 %0, %1, -%2" : "=v"(r) : "v"(a), "v"(b));
  return r;
}
__device__ __forceinline__ u32 tb_addr(u32 bits2, u32 base2) {
  i16x2 b = __builtin_elementwise_max(__builtin_bit_cast(i16x2, bits2), (i16x2){0x3F00, 0x3F00});
  u32 r;
  asm("v_pk_mad_u16 %0, %1, 2, %2 op_sel_hi:[1,0,1]" : "=v"(r) : "v"(b), "s"(base2));
  return r;
}
__device__ __forceinline__ int bexp(float b) { return min(max((int)((__float_as_uint(b) >> 23) & 0xFF), 20), 240); }
__device__ __forceinline__ void cnt_add(char* smem, int off, int lane) {
  asm volatile("s_mov_b64 exec, 1\n\tds_add_u32 %0, %1\n\ts_mov_b64 exec, -1" :: "v"(L_MI + off), "v"(1u) : "memory");
}
__device__ __forceinline__ void cnt_wait(char* smem, int off, u32 target) {
#pragma unroll 1
  for (unsigned n = 0; n < (1u << 23) && (u32)__builtin_amdgcn_readfirstlane(__hip_atomic_load((LAS u32*)(smem + L_MI + off), __ATOMIC_RELAXED, __HIP_MEMORY_SCOPE_WORKGROUP)) < target; ++n)
    __builtin_amdgcn_s_sleep(2);
  asm volatile("" ::: "memory");
}
__device__ __forceinline__ void cnt_spin(char* smem, int off, u32 target) {
  for (unsigned n = 0; n < (1u << 24) && (u32)__builtin_amdgcn_readfirstlane(__hip_atomic_load((LAS u32*)(smem + L_MI + off), __ATOMIC_RELAXED, __HIP_MEMORY_SCOPE_WORKGROUP)) < target; ++n) {}
  asm volatile("" ::: "memory");
}
__device__ __forceinline__ void cnt_spin2(int o0, u32 t0, int o1, u32 t1) {
  for (unsigned n = 0; n < (1u << 24); ++n) {
    asm volatile("" ::: "memory");
    const u32 c0 = *(const volatile LAS u32*)(size_t)(u32)(L_MI + o0), c1 = *(const volatile LAS u32*)(size_t)(u32)(L_MI + o1);
    if (__builtin_amdgcn_readfirstlane(min((int)(c0 - t0), (int)(c1 - t1))) >= 0) break;
  }
  asm volatile("" ::: "memory");
}
__device__ __forceinline__ u32 now_() { return (u32)__builtin_amdgcn_s_memtime(); }
__device__ __forceinline__ u32 tmax(u32 a, u32 b) { return (int)(a - b) > 0 ? a : b; }
__device__ __forceinline__ void gate(u32 t) {
  u32 n = 256;
  asm volatile("1:\n\ts_memtime vcc\n\ts_sub_u32 %0, %0, 1\n\ts_waitcnt lgkmcnt(0)\n\ts_cbranch_scc1 2f\n\ts_sub_i32 vcc_lo, %1, vcc_lo\n\ts_cmp_lt_i32 vcc_lo, 1\n\ts_cbranch_scc1 2f\n\t"
               "s_cmp_lt_i32 vcc_lo, 64\n\ts_cbranch_scc1 1b\n\ts_sleep 1\n\ts_cmp_lt_i32 vcc_lo, 0x200\n\ts_cbranch_scc1 1b\n\ts_sleep 6\n\ts_branch 1b\n2:"
               : "+s"(n) : "s"(__builtin_amdgcn_readfirstlane(t)) : "memory", "vcc", "scc");
}
__device__ __forceinline__ u32 x0_mark(u32 tag) { return 0xFF807F80u + (((tag & 31u) + 1u) << 16) + ((tag >> 5) & 31u) + 1u; }
__device__ __forceinline__ bool tags_ok(const u32x4& f, u32 tag) {
  return __builtin_amdgcn_ballot_w64((opaque((f[0] ^ tag) | (f[1] ^ tag)) | opaque((f[2] ^ tag) | (f[3] ^ tag))) != 0u) == 0ull;
}
__device__ __forceinline__ void tags_wait(u32x4& f, rsrc_t wrs, int fo, u32 tag, int fid) {
  if (__builtin_expect(!tags_ok(f, tag), 0)) {
    for (unsigned spins = 0; spins <= SPIN_CAP; ++spins) {
      FAILC(fid);
      __builtin_amdgcn_s_sleep(SLP_F);
      asm volatile("" ::: "memory");
      if (tags_ok(ldw(wrs, fo), tag)) break;
    }
    __builtin_amdgcn_s_waitcnt(0x0F70);
  }
  asm volatile("" ::: "memory");
}
__device__ __forceinline__ void build_copies(char* smem, int cc, int ch, const u32 (&kw)[6]) {
  char* cop = smem + cc * 4 * CSTR + ch * 16;
  u32 sh[5];
#pragma unroll
  for (int k = 0; k < 5; ++k) sh[k] = __builtin_amdgcn_alignbit(kw[k + 1], kw[k], 16);
  u32x4 c0 = {kw[0], kw[1], kw[2], kw[3]}, c1 = {sh[0], sh[1], sh[2], sh[3]}, c2 = {kw[1], kw[2], kw[3], kw[4]}, c3 = {sh[1], sh[2], sh[3], sh[4]};
  asm volatile("" : "+v"(c0), "+v"(c1), "+v"(c2), "+v"(c3));
  *(u32x4*)(cop + 0 * CSTR) = c0; *(u32x4*)(cop + 1 * CSTR) = c1; *(u32x4*)(cop + 2 * CSTR) = c2; *(u32x4*)(cop + 3 * CSTR) = c3;
  asm volatile("s_nop 1" :: "v"(c0), "v"(c1), "v"(c2), "v"(c3) : "memory");
}
struct ConvPre { f32x4 x0, x1; };
__device__ __forceinline__ void conv_unp(ConvPre& P, u32x4 xw) {
#pragma unroll
  for (int e = 0; e < 4; ++e) { P.x0[e] = lo2f(xw[e]); P.x1[e] = hi2f(xw[e]); }
  asm volatile("" : "+v"(P.x0), "+v"(P.x1));
}
__device__ __forceinline__ u32x4 x2_ld(rsrc_t wrs, int kp, int l0) { return ldw(wrs, OFF_X2 + (kp * XS_ + l0) * 4); }
__device__ __forceinline__ bool x2_ok(const u32x4& xw) { return __all(umax4(0u, xw) != 0xFFFFFFFFu); }
__device__ __forceinline__ void x2_poll(rsrc_t wrs, int kp, int l0, u32x4& xw) {
  for (unsigned spins = 0; !x2_ok(xw) && spins <= SPIN_CAP; ++spins) {
    FAILC(17);
    __builtin_amdgcn_s_sleep(SLP_XP);
    asm volatile("" ::: "memory");
    xw = x2_ld(wrs, kp, l0);
  }
}
__device__ __forceinline__ void st2_ld(rsrc_t wrs, int l0, u32x4 (&g)[4]) {
#pragma unroll
  for (int e = 0; e < 4; ++e) g[e] = ldw(wrs, OFF_ST2 + ((l0 & ~255) + ((l0 >> 2) & 63)) * 16 + e * 1024);
}
__device__ __forceinline__ bool st2_ok(const u32x4 (&g)[4]) {
  u32 ok = 32u;
#pragma unroll
  for (int e = 0; e < 4; ++e) ok &= g[e][0] & g[e][2];
  return __all(ok != 0u);
}
__device__ __forceinline__ bool x2st_ok(const u32x4& xw, const u32x4 (&g)[4]) {
  u32 ok = umax4(0u, xw) != 0xFFFFFFFFu ? 32u : 0u;
#pragma unroll
  for (int e = 0; e < 4; ++e) ok &= g[e][0] & g[e][2];
  return __all(ok != 0u);
}
__device__ __forceinline__ void st2_poll(rsrc_t wrs, int l0, u32x4 (&g)[4]) {
  for (unsigned spins = 0; !st2_ok(g) && spins <= SPIN_CAP; ++spins) {
    FAILC(8);
    __builtin_amdgcn_s_sleep(SLP_SP);
    asm volatile("" ::: "memory");
    st2_ld(wrs, l0, g);
  }
}
template <bool UNP>
__device__ __forceinline__ void conv2_in(char* smem, u32 ua, ConvPre& P, u32x4 xw, u32x4 (&g)[4]) {
  f32x4 rs, nm;
#pragma unroll
  for (int e = 0; e < 4; ++e) {
    const float c1 = 4294967296.0f / (64.0f * 512.0f * STAT_SCALE), c2 = 1.0f / 4294967296.0f;
    const float t1 = fmaf((float)g[e][0], c2, (float)(int)g[e][1]), t2 = fmaf((float)g[e][2], c2, (float)(int)g[e][3]);
    const float m1 = -c1 * t1;
    rs[e] = __builtin_amdgcn_rsqf(fmaf(rmax(fmaf(m1, t1, t2), 0.f), c1, 1e-5f));
    nm[e] = m1 * rs[e];
  }
  if (UNP) conv_unp(P, xw);
  u32x2 u0 = {pkh(fmaf(P.x0[0], rs[0], nm[0]), fmaf(P.x0[1], rs[1], nm[1])), pkh(fmaf(P.x0[2], rs[2], nm[2]), fmaf(P.x0[3], rs[3], nm[3]))};
  u32x2 u1 = {pkh(fmaf(P.x1[0], rs[0], nm[0]), fmaf(P.x1[1], rs[1], nm[1])), pkh(fmaf(P.x1[2], rs[2], nm[2]), fmaf(P.x1[3], rs[3], nm[3]))};
  asm volatile("" : "+v"(u0), "+v"(u1));
  *(LAS u32x2*)(size_t)ua = u0;
  *(LAS u32x2*)(size_t)(ua + 4608) = u1;
  asm volatile("s_nop 1" :: "v"(u0), "v"(u1) : "memory");
}
__device__ __forceinline__ void pair_x(f32x4& lo, f32x4& hi, const f32x4& a, const f32x4& b) {
#define PXD " quad_perm:[1,0,3,2] row_mask:0xf bank_mask:0xf\n\t"
  asm volatile("s_mov_b64 vcc, %16\n\ts_nop 0\n\t"
               "v_cndmask_b32_dpp %0, %12, %8, vcc" PXD "v_cndmask_b32_dpp %1, %13, %9, vcc" PXD "v_cndmask_b32_dpp %2, %14, %10, vcc" PXD "v_cndmask_b32_dpp %3, %15, %11, vcc" PXD
               "s_not_b64 vcc, vcc\n\t"
               "v_cndmask_b32_dpp %4, %8, %12, vcc" PXD "v_cndmask_b32_dpp %5, %9, %13, vcc" PXD "v_cndmask_b32_dpp %6, %10, %14, vcc" PXD "v_cndmask_b32_dpp %7, %11, %15, vcc" PXD
               : "=&v"(lo[0]), "=&v"(lo[1]), "=&v"(lo[2]), "=&v"(lo[3]), "=&v"(hi[0]), "=&v"(hi[1]), "=&v"(hi[2]), "=&v"(hi[3])
               : "v"(a[0]), "v"(a[1]), "v"(a[2]), "v"(a[3]), "v"(b[0]), "v"(b[1]), "v"(b[2]), "v"(b[3]), "s"(0x5555555555555555ull) : "vcc", "scc");
}
__device__ __forceinline__ void conv_res(ConvPre& P) {
  f32x4 lo, hi;
  pair_x(lo, hi, P.x0, P.x1);
  P.x0 = lo; P.x1 = hi;
}
__device__ __forceinline__ u32x4 conv_epi(u32 rp0, u32 rp1, int off, float isc, const ConvPre& P) {
  const f32x4 a0 = *(const LAS f32x4*)(size_t)(rp0 + off), a1 = *(const LAS f32x4*)(size_t)(rp0 + off + 4096), b0 = *(const LAS f32x4*)(size_t)(rp1 + off), b1 = *(const LAS f32x4*)(size_t)(rp1 + off + 4096);
  __builtin_amdgcn_sched_barrier(0);
  u32x4 r;
#pragma unroll
  for (int e = 0; e < 2; ++e) {
    r[e] = pk2(fmaf(a1[2 * e], isc, fmaf(a0[2 * e], isc, P.x0[2 * e])), fmaf(a1[2 * e + 1], isc, fmaf(a0[2 * e + 1], isc, P.x0[2 * e + 1])));
    r[2 + e] = pk2(fmaf(b1[2 * e], isc, fmaf(b0[2 * e], isc, P.x1[2 * e])), fmaf(b1[2 * e + 1], isc, fmaf(b0[2 * e + 1], isc, P.x1[2 * e + 1])));
  }
  return r;
}
__device__ __forceinline__ void dma4k(rsrc_t r, int voff, int soff, int lds) {
  asm volatile("s_mov_b32 m0, %3\n\ts_nop 0\n\t"
               "buffer_load_dwordx4 %0, %1, %2 offen sc1 lds\n\t"
               "buffer_load_dwordx4 %0, %1, %2 offen offset:1024 sc1 lds\n\t"
               "buffer_load_dwordx4 %0, %1, %2 offen offset:2048 sc1 lds\n\t"
               "buffer_load_dwordx4 %0, %1, %2 offen offset:3072 sc1 lds"
               :: "v"(voff), "s"(r), "s"(soff), "s"(lds) : "memory");
}
__device__ __forceinline__ void dma1k(rsrc_t r, int voff, int soff, int lds) {
  asm volatile("s_mov_b32 m0, %3\n\ts_nop 0\n\tbuffer_load_dwordx4 %0, %1, %2 offen sc1 lds" :: "v"(voff), "s"(r), "s"(soff), "s"(lds) : "memory");
}
__device__ __forceinline__ int x_lane(int lane) { return ((lane & 31) >> 1) * 64 + (lane >> 5) * 32 + (lane & 1) * 16; }
__device__ __forceinline__ void x_dma(rsrc_t wrs, int xin, int rb, int tile, int a, int lane) {
  const int vo = x_lane(lane);
  dma4k(wrs, vo, xin + rb * XBS + a * 8192, tile + a * 8192);
  dma4k(wrs, vo, xin + rb * XBS + a * 8192 + 4096, tile + a * 8192 + 4096);
  if (tile == L_TB) {
    int so = xin + (rb - 1) * XBS + a * 8192;
    asm volatile("s_mov_b32 m0, %3\n\ts_nop 0\n\tbuffer_load_dwordx4 %1, %2, %0 offen sc1 lds\n\t"
                 "s_sub_u32 m0, m0, 1024\n\ts_nop 0\n\tbuffer_load_dwordx4 %1, %2, %0 offen offset:2048 sc1 lds\n\t"
                 "s_add_u32 m0, m0, 3072\n\ts_add_u32 %0, %0, 4096\n\tbuffer_load_dwordx4 %1, %2, %0 offen sc1 lds\n\t"
                 "s_sub_u32 m0, m0, 1024\n\ts_nop 0\n\tbuffer_load_dwordx4 %1, %2, %0 offen offset:2048 sc1 lds"
                 : "+s"(so) : "v"((lane >> 1) * 64 + 32 + (lane & 1) * 16), "s"(wrs), "s"(L_TH + a * 4096) : "memory", "scc");
  }
}
template <int S>
__device__ __forceinline__ bf16x8 ld_tr8(const LAS char* p) {
  const bf16x4 lo = __builtin_amdgcn_ds_read_tr16_b64_v4i16((LAS bf16x4*)p), hi = __builtin_amdgcn_ds_read_tr16_b64_v4i16((LAS bf16x4*)(p + S));
  return __builtin_shufflevector(lo, hi, 0, 1, 2, 3, 4, 5, 6, 7);
}

struct ConvK { u32 ah, ah2, bp, ra; f16x8 k[16]; };
__device__ __forceinline__ void a_conv_pre(ConvK& C, int a, int lane) {
  lane = (int)opaque((u32)lane);
  const int c = a >> 1, qq = a & 1, j16 = lane & 15, kg = lane >> 4;
  const int y00 = 2031 - j16 + 8 * kg - 128 * qq;
  const int sg = y00 & 3;
  C.ah = opaque((u32)(c * 4 * CSTR + sg * CSTR + 2 * (y00 - sg) - 3776));
  C.ah2 = opaque(C.ah + 8);
  C.bp = opaque((u32)(L_U + c * 4608 + (256 + 16 * j16 - 16 + 8 * kg - 128 * qq) * 2 - 192));
  C.ra = opaque((u32)(L_REDA + (a * 256 + 4 * j16 + (kg ^ ((j16 >> 1) & 3) ^ c)) * 16));
#pragma unroll
  for (int e = 0; e < 16; ++e) {
    const int ao = 3776 - (512 * (e & 3) + 64 * (e >> 2));
    const f16x4 h0 = *(const LAS f16x4*)(size_t)(C.ah + ao), h1 = *(const LAS f16x4*)(size_t)(C.ah2 + ao);
    C.k[e] = __builtin_shufflevector(h0, h1, 0, 1, 2, 3, 4, 5, 6, 7);
    __builtin_amdgcn_sched_barrier(0);
  }
#pragma unroll
  for (int e = 0; e < 16; ++e) asm volatile("" : "+v"(C.k[e]));
}
template <bool L1>
__device__ __forceinline__ void a_conv(char* smem, rsrc_t wrs, int rb, int a, const ConvK& C) {
  const LAS char* ah = (const LAS char*)(size_t)C.ah;
  const LAS char* ah2 = (const LAS char*)(size_t)C.ah2;
  const LAS char* bp = (const LAS char*)(size_t)C.bp;
  LAS f32x4* ra = (LAS f32x4*)(size_t)C.ra;
  f32x4 acc[8];
#pragma unroll
  for (int e = 0; e < 8; ++e) acc[e] = (f32x4){0.f, 0.f, 0.f, 0.f};
  f16x8 bf[4][8], fh[4][8];
#define S_RR(s_) ((s_) < 16 ? (s_) >> 2 : (s_) < 44 ? ((s_) - 16) / 7 : ((s_) - 44) >> 2)
#define S_MU(s_) ((s_) < 16 ? (s_) & 3 : (s_) < 44 ? ((s_) - 16) % 7 + 1 : ((s_) - 44) & 3)
#define S_NEW(s_) ((s_) < 16 || ((s_) < 44 && S_MU(s_) >= 4))
#define LDA(s_) do { const int ao_ = 3776 - (512 * S_MU(s_) + 64 * S_RR(s_));                                        \
      f16x4 h0_ = *(const LAS f16x4*)(ah + ao_), h1_ = *(const LAS f16x4*)(ah2 + ao_);                                \
      fh[S_RR(s_)][S_MU(s_)] = __builtin_shufflevector(h0_, h1_, 0, 1, 2, 3, 4, 5, 6, 7); } while (0)
#define LDB(r_, t_) bf[r_][t_] = *(const LAS f16x8*)(bp + 512 * (t_) + 192 - 64 * (r_))
#define CSTEPS(s0_, s1_)                                                                                               \
  _Pragma("unroll") for (int st = s0_; st < s1_; ++st) {                                                               \
    const int rr = S_RR(st), mu = S_MU(st);                                                                           \
    if (S_NEW(st + CPF) && (L1 || st + CPF >= 16)) LDA(st + CPF);                                                     \
    if (st < 4) { LDB(2 + (st >> 1), 2 * (st & 1)); LDB(2 + (st >> 1), 2 * (st & 1) + 1); }                           \
    if (st >= 44 && st < 48) { LDB(2 + ((st - 44) >> 1), 4 + 2 * (st & 1)); LDB(2 + ((st - 44) >> 1), 5 + 2 * (st & 1)); } \
    __builtin_amdgcn_sched_barrier(0);                                                                                \
    _Pragma("unroll") for (int cb = 0; cb < 8; ++cb) {                                                                \
      const int t = cb - mu;                                                                                          \
      const bool on = st < 16 ? (cb < 4 && t >= 0) : st < 44 ? (cb >= 4 && t >= 0 && t < 4) : (t >= 4);               \
      if (on) acc[cb] = __builtin_amdgcn_mfma_f32_16x16x32_f16(fh[rr][mu], bf[rr][t < 0 ? 0 : t], acc[cb], 0, 0, 0);  \
    }                                                                                                                 \
    __builtin_amdgcn_sched_barrier(0);                                                                                \
    if (st >= 13 && st < 16) ra[(st - 13) * 64] = acc[st - 13];                                                       \
    if (st >= 57) ra[1024 + (st - 57) * 64] = acc[4 + st - 57];                                                       \
  }
#pragma unroll
  for (int e = 0; e < 16; ++e) fh[e >> 2][e & 3] = C.k[e];
  __builtin_amdgcn_s_barrier();
  asm volatile("" ::: "memory");
  ST(!L1, 7);
#pragma unroll
  for (int e = 0; e < CPF; ++e) { if (L1) LDA(e); __builtin_amdgcn_sched_barrier(0); if (e < 2) { LDB(e, 0); __builtin_amdgcn_sched_barrier(0); LDB(e, 1); __builtin_amdgcn_sched_barrier(0); LDB(e, 2); __builtin_amdgcn_sched_barrier(0); LDB(e, 3); __builtin_amdgcn_sched_barrier(0); } }
  __builtin_amdgcn_sched_barrier(0);
  CSTEPS(0, 16)
  CSTEPS(16, 17)
  ra[3 * 64] = acc[3];
  __builtin_amdgcn_sched_barrier(0);
  asm volatile("" ::: "memory");
  CSTEPS(17, 18)
  if (L1) asm volatile("s_waitcnt vmcnt(0)" ::: "memory");
  asm volatile("s_waitcnt lgkmcnt(2)" ::: "memory");
  __builtin_amdgcn_s_barrier();
  asm volatile("" ::: "memory");
  ST(!L1, 8);
  CSTEPS(18, 44)
  if (!L1) ST(1, 5);
  if (!L1) __builtin_amdgcn_s_barrier();
  if (!L1) ST(1, 6);
  asm volatile("" ::: "memory");
#pragma unroll
  for (int e = 0; e < 8; ++e) { LDB(e >> 2, 4 + (e & 3)); __builtin_amdgcn_sched_barrier(0); }
  CSTEPS(44, 60)
  ra[1024 + 3 * 64] = acc[7];
  lds_barrier();
  ST(!L1, 9);
}

struct C1Pre { u32 ah, ah2, bp, xp, xc, cn; int so, fo0, fo1; };
#define C1_OPS "+{v16}"(C.ah), "+{v17}"(C.ah2), "+{v18}"(C.bp), "+{v19}"(C.xp), "+{v20}"(C.xc), "+{v21}"(C.cn), "+{v22}"(C.so), "+{v23}"(C.fo0), "+{v24}"(C.fo1)
__device__ __forceinline__ void c1_pre(C1Pre& C, int kp, int a, int lane) {
  lane = (int)opaque((u32)lane);
  const int c = a >> 1, j16 = lane & 15, kg = lane >> 4, P = a & 1;
  const int y0 = 2031 - j16 + 8 * kg - 128 * P, sg = y0 & 3;
  C.ah = (u32)(c * 4 * CSTR + sg * CSTR + 2 * (y0 - sg) - 3776);
  C.ah2 = C.ah + 8;
  C.bp = (u32)(L_U + c * 4608 + (240 + 16 * j16 + 8 * kg - 128 * P) * 2 - 192);
  C.xp = (u32)(L_X + j16 * 64 + kg * 16);
  C.xc = (u32)(L_TA + 24576 + c * 4096 + lane * 16);
  C.cn = (u32)(L_MI + 4 * c);
  C.so = OFF_X1 + (8 * ((kg & 1) ? 3 - P : P) + (j16 >> 1)) * XBS + kp * 128 + c * 64 + (j16 & 1) * 32 + (kg >> 1) * 16;
  C.fo0 = OFF_ST0 + (a * 64 + lane) * 16;
  C.fo1 = OFF_ST0 + 8192 + (HB_OF(BIDX) * 4 + a) * 128 + (lane & 3) * 16;
  asm volatile("" : C1_OPS);
}
static_assert(L_XC - L_TA - 24576 == 57344 && C_XC == 8 && C_KH == 48 && C_P2 == 52 && C_U == 56 && C_P3 == 64 && C_G2 == 32 && L_MI % 8 == 0, "generated offsets");
__device__ __forceinline__ void a_conv1(rsrc_t wrs, int a, C1Pre& C, u32x4& fl, u32x4& wfl, u32& g2) {
  asm volatile("s_bitcmp1_b32 %[a], 0\n\ts_cbranch_scc1 .Lc_odd\n\t"
  "ds_read_b64 v[36:37], v16 offset:3776\n\tds_read_b64 v[38:39], v17 offset:3776\n\tds_read_b64 v[40:41], v16 offset:3264\n\tds_read_b64 v[42:43], v17 offset:3264\n\t"
  "ds_read_b64 v[44:45], v16 offset:2752\n\tds_read_b64 v[46:47], v17 offset:2752\n\tds_read_b64 v[48:49], v16 offset:2240\n\tds_read_b64 v[50:51], v17 offset:2240\n\t"
  "ds_read_b64 v[52:53], v16 offset:3712\n\tds_read_b64 v[54:55], v17 offset:3712\n\tds_read_b64 v[56:57], v16 offset:3200\n\tds_read_b64 v[58:59], v17 offset:3200\n\t"
  "ds_read_b64 v[60:61], v16 offset:2688\n\tds_read_b64 v[62:63], v17 offset:2688\n\tds_read_b64 v[64:65], v16 offset:2176\n\tds_read_b64 v[66:67], v17 offset:2176\n\t"
  "ds_read_b64 v[68:69], v16 offset:3648\n\tds_read_b64 v[70:71], v17 offset:3648\n\tds_read_b64 v[72:73], v16 offset:3136\n\tds_read_b64 v[74:75], v17 offset:3136\n\t"
  "ds_read_b64 v[76:77], v16 offset:2624\n\tds_read_b64 v[78:79], v17 offset:2624\n\tds_read_b64 v[80:81], v16 offset:2112\n\tds_read_b64 v[82:83], v17 offset:2112\n\t"
  "ds_read_b64 v[84:85], v16 offset:3584\n\tds_read_b64 v[86:87], v17 offset:3584\n\tds_read_b64 v[88:89], v16 offset:3072\n\tds_read_b64 v[90:91], v17 offset:3072\n\t"
  "ds_read_b64 v[92:93], v16 offset:2560\n\tds_read_b64 v[94:95], v17 offset:2560\n\tds_read_b64 v[96:97], v16 offset:2048\n\tds_read_b64 v[98:99], v17 offset:2048\n\t"
  "ds_read_b32 v26, v21\n\tv_and_b32 v27, -8, v21\n\tv_mov_b32 v100, 1\n\t"
  CST(6)
  "s_mov_b32 vcc_hi, 0x1000000\n\t.Lc0_u:\n\tds_read_b32 v101, v27 offset:56\n\ts_sub_u32 vcc_hi, vcc_hi, 1\n\ts_cselect_b32 vcc_lo, 0, 4\n\ts_waitcnt lgkmcnt(0)\n\tv_readfirstlane_b32 s61, v101\n\ts_nop 1\n\ts_cmp_lt_u32 s61, vcc_lo\n\ts_cbranch_scc1 .Lc0_u\n\t"
  CST(7)
  "ds_read_b128 v[102:105], v18 offset:192\n\tds_read_b128 v[106:109], v18 offset:704\n\tds_read_b128 v[110:113], v18 offset:1216\n\tds_read_b128 v[116:119], v18 offset:128\n\t"
  "ds_read_b128 v[120:123], v18 offset:640\n\tds_read_b128 v[124:127], v18 offset:1152\n\tds_read_b128 v[128:131], v18 offset:64\n\ts_waitcnt lgkmcnt(6)\n\t"
  "v_mfma_f32_16x16x32_f16 v[132:135], v[40:43], v[102:105], 0\n\tds_read_b128 v[136:139], v18 offset:576\n\tds_read_b128 v[140:143], v18 offset:1728\n\t"
  "v_mfma_f32_16x16x32_f16 v[144:147], v[44:47], v[102:105], 0\n\tds_read_b128 v[148:151], v18 offset:1088\n\ts_waitcnt lgkmcnt(8)\n\t"
  "v_mfma_f32_16x16x32_f16 v[132:135], v[36:39], v[106:109], v[132:135]\n\tds_read_b128 v[152:155], v18 offset:1664\n\tds_read_b128 v[156:159], v18 offset:1600\n\t"
  "v_mfma_f32_16x16x32_f16 v[144:147], v[40:43], v[106:109], v[144:147]\n\tds_read_b128 v[160:163], v18 offset:0\n\tds_read_b32 v101, v27 offset:48\n\ts_waitcnt lgkmcnt(7)\n\t"
  "v_mfma_f32_16x16x32_f16 v[144:147], v[36:39], v[110:113], v[144:147]\n\tds_read_b128 v[164:167], v18 offset:512\n\t"
  "v_mfma_f32_16x16x32_f16 v[132:135], v[56:59], v[116:119], v[132:135]\n\tds_read_b128 v[168:171], v18 offset:1024\n\t"
  "v_mfma_f32_16x16x32_f16 v[144:147], v[60:63], v[116:119], v[144:147]\n\tds_read_b128 v[172:175], v18 offset:1536\n\t"
  "v_mfma_f32_16x16x32_f16 v[132:135], v[52:55], v[120:123], v[132:135]\n\t"
  "v_mfma_f32_16x16x32_f16 v[144:147], v[56:59], v[120:123], v[144:147]\n\t"
  "v_mfma_f32_16x16x32_f16 v[144:147], v[52:55], v[124:127], v[144:147]\n\t"
  "v_mfma_f32_16x16x32_f16 v[132:135], v[72:75], v[128:131], v[132:135]\n\tds_read_b128 v[176:179], v19 offset:0\n\tds_read_b128 v[180:183], v19 offset:3072\n\t"
  "v_mfma_f32_16x16x32_f16 v[144:147], v[76:79], v[128:131], v[144:147]\n\ts_waitcnt lgkmcnt(5)\n\tv_readfirstlane_b32 s61, v101\n\t"
  "v_mfma_f32_16x16x32_f16 v[132:135], v[68:71], v[136:139], v[132:135]\n\t"
  "v_mfma_f32_16x16x32_f16 v[144:147], v[72:75], v[136:139], v[144:147]\n\ts_cmp_lt_u32 s61, 4\n\ts_cbranch_scc1 .Lc0_kh\n.Lc0_kh_b:\n\t"
  "v_mfma_f32_16x16x32_f16 v[144:147], v[68:71], v[148:151], v[144:147]\n\tds_read_b64 v[184:185], v16 offset:1728\n\tds_read_b64 v[186:187], v17 offset:1728\n\t"
  "v_mfma_f32_16x16x32_f16 v[132:135], v[88:91], v[160:163], v[132:135]\n\tds_read_b64 v[188:189], v16 offset:1216\n\tds_read_b64 v[190:191], v17 offset:1216\n\t"
  "v_mfma_f32_16x16x32_f16 v[144:147], v[92:95], v[160:163], v[144:147]\n\tds_read_b64 v[192:193], v16 offset:704\n\ts_waitcnt lgkmcnt(7)\n\t"
  "v_mfma_f32_16x16x32_f16 v[132:135], v[84:87], v[164:167], v[132:135]\n\tds_read_b64 v[194:195], v17 offset:704\n\tds_read_b64 v[196:197], v16 offset:192\n\tds_read_b64 v[198:199], v17 offset:192\n\t"
  "v_mfma_f32_16x16x32_f16 v[144:147], v[88:91], v[164:167], v[144:147]\n\ts_waitcnt lgkmcnt(8)\n\tv_perm_b32 v176, v176, v176, s60\n\t"
  "v_mfma_f32_16x16x32_f16 v[144:147], v[84:87], v[168:171], v[144:147]\n\tds_write_b128 v20, v[132:135] offset:0\n\tv_perm_b32 v177, v177, v177, s60\n\t"
  "v_mfma_f32_16x16x32_f16 v[200:203], v[36:39], v[102:105], 0\n\tv_perm_b32 v178, v178, v178, s60\n\tv_perm_b32 v179, v179, v179, s60\n\t"
  "v_mfma_f32_16x16x32_f16 v[132:135], v[48:51], v[102:105], 0\n\tds_write_b128 v20, v[144:147] offset:1024\n\tv_perm_b32 v180, v180, v180, s60\n\t"
  "v_mfma_f32_16x16x32_f16 v[132:135], v[44:47], v[106:109], v[132:135]\n\ts_mov_b64 exec, 1\n\tds_add_u32 v21, v100 offset:8\n\ts_mov_b64 exec, -1\n\t"
  "v_mfma_f32_16x16x32_f16 v[132:135], v[40:43], v[110:113], v[132:135]\n\tv_perm_b32 v181, v181, v181, s60\n\tv_perm_b32 v182, v182, v182, s60\n\t"
  "v_mfma_f32_16x16x32_f16 v[132:135], v[36:39], v[140:143], v[132:135]\n\tv_perm_b32 v183, v183, v183, s60\n\t"
  "v_mfma_f32_16x16x32_f16 v[200:203], v[52:55], v[116:119], v[200:203]\n\t"
  "v_mfma_f32_16x16x32_f16 v[132:135], v[64:67], v[116:119], v[132:135]\n\t"
  "v_mfma_f32_16x16x32_f16 v[132:135], v[60:63], v[120:123], v[132:135]\n\t"
  "v_mfma_f32_16x16x32_f16 v[132:135], v[56:59], v[124:127], v[132:135]\n\t"
  "v_mfma_f32_16x16x32_f16 v[132:135], v[52:55], v[152:155], v[132:135]\n\t"
  "v_mfma_f32_16x16x32_f16 v[200:203], v[68:71], v[128:131], v[200:203]\n\t"
  "v_mfma_f32_16x16x32_f16 v[132:135], v[80:83], v[128:131], v[132:135]\n\tds_read_b32 v101, v21 offset:8\n\tds_read_b128 v[144:147], v20 offset:2048\n\tds_read_b128 v[204:207], v20 offset:3072\n\t"
  "v_mfma_f32_16x16x32_f16 v[132:135], v[76:79], v[136:139], v[132:135]\n\t"
  "v_mfma_f32_16x16x32_f16 v[132:135], v[72:75], v[148:151], v[132:135]\n\t"
  "v_mfma_f32_16x16x32_f16 v[132:135], v[68:71], v[156:159], v[132:135]\n\t"
  "v_mfma_f32_16x16x32_f16 v[200:203], v[84:87], v[160:163], v[200:203]\n\t"
  "v_mfma_f32_16x16x32_f16 v[132:135], v[96:99], v[160:163], v[132:135]\n\t"
  "v_mfma_f32_16x16x32_f16 v[132:135], v[92:95], v[164:167], v[132:135]\n\t"
  "v_mfma_f32_16x16x32_f16 v[132:135], v[88:91], v[168:171], v[132:135]\n\t"
  "v_mfma_f32_16x16x32_f16 v[132:135], v[84:87], v[172:175], v[132:135]\n\ts_waitcnt lgkmcnt(0)\n\tv_readfirstlane_b32 s61, v101\n\t"
  "v_mfma_f32_16x16x32_f16 v[208:211], v[40:43], v[140:143], 0\n\t"
  "v_mfma_f32_16x16x32_f16 v[208:211], v[44:47], v[110:113], v[208:211]\n\ts_cmp_lt_u32 s61, 2\n\ts_cbranch_scc1 .Lc0_x0\n.Lc0_x0_b:\n\tv_fmac_f32 v176, v144, v26\n\tv_fmac_f32 v177, v145, v26\n\t"
  "v_fmac_f32 v178, v146, v26\n\t"
  "v_mfma_f32_16x16x32_f16 v[212:215], v[44:47], v[140:143], 0\n\tv_fmac_f32 v179, v147, v26\n\tv_fmac_f32 v180, v204, v26\n\tv_fmac_f32 v181, v205, v26\n\t"
  "v_fmac_f32 v182, v206, v26\n\tv_fmac_f32 v183, v207, v26\n\t"
  "v_mfma_f32_16x16x32_f16 v[208:211], v[48:51], v[106:109], v[208:211]\n\tv_fmac_f32 v176, v200, v26\n\tv_fmac_f32 v177, v201, v26\n\t"
  "v_mfma_f32_16x16x32_f16 v[212:215], v[48:51], v[110:113], v[212:215]\n\tv_fmac_f32 v178, v202, v26\n\tv_fmac_f32 v179, v203, v26\n\tv_cvt_pk_bf16_f32 v144, v176, v177\n\t"
  "v_cvt_pk_bf16_f32 v145, v178, v179\n\tv_fmac_f32 v180, v132, v26\n\t"
  "v_mfma_f32_16x16x32_f16 v[176:179], v[48:51], v[140:143], 0\n\tv_fmac_f32 v181, v133, v26\n\tv_fmac_f32 v182, v134, v26\n\tv_fmac_f32 v183, v135, v26\n\t"
  "v_cvt_pk_bf16_f32 v146, v180, v181\n\tv_cvt_pk_bf16_f32 v147, v182, v183\n\t"
  "v_mfma_f32_16x16x32_f16 v[208:211], v[184:187], v[102:105], v[208:211]\n\tv_permlane16_swap_b32 v144, v146\n\tv_permlane16_swap_b32 v145, v147\n\ts_setprio 3\n\tbuffer_store_dwordx4 v[144:147], v22, s[56:59], 0 offen sc1\n\ts_setprio 0\n\t"
  CST(8)
  "v_mfma_f32_16x16x32_f16 v[212:215], v[184:187], v[106:109], v[212:215]\n\tds_read_b64 v[132:133], v16 offset:1664\n\tds_read_b64 v[134:135], v17 offset:1664\n\t"
  "v_mfma_f32_16x16x32_f16 v[176:179], v[184:187], v[110:113], v[176:179]\n\tds_read_b64 v[144:145], v16 offset:1152\n\tds_read_b64 v[146:147], v17 offset:1152\n\t"
  "v_mfma_f32_16x16x32_f16 v[180:183], v[184:187], v[140:143], 0\n\tds_read_b64 v[140:141], v16 offset:640\n\tds_read_b64 v[142:143], v17 offset:640\n\t"
  "v_mfma_f32_16x16x32_f16 v[212:215], v[188:191], v[102:105], v[212:215]\n\t"
  "v_mfma_f32_16x16x32_f16 v[176:179], v[188:191], v[106:109], v[176:179]\n\tds_read_b64 v[184:185], v16 offset:128\n\tds_read_b64 v[186:187], v17 offset:128\n\t"
  "v_mfma_f32_16x16x32_f16 v[180:183], v[188:191], v[110:113], v[180:183]\n\t"
  "v_mfma_f32_16x16x32_f16 v[176:179], v[192:195], v[102:105], v[176:179]\n\t"
  "v_mfma_f32_16x16x32_f16 v[180:183], v[192:195], v[106:109], v[180:183]\n\t"
  "v_mfma_f32_16x16x32_f16 v[180:183], v[196:199], v[102:105], v[180:183]\n\t"
  "v_mfma_f32_16x16x32_f16 v[208:211], v[56:59], v[152:155], v[208:211]\n\t"
  "v_mfma_f32_16x16x32_f16 v[208:211], v[60:63], v[124:127], v[208:211]\n\t"
  "v_mfma_f32_16x16x32_f16 v[212:215], v[60:63], v[152:155], v[212:215]\n\tds_read_b64 v[102:103], v16 offset:1600\n\tds_read_b64 v[104:105], v17 offset:1600\n\t"
  "v_mfma_f32_16x16x32_f16 v[208:211], v[64:67], v[120:123], v[208:211]\n\t"
  "v_mfma_f32_16x16x32_f16 v[212:215], v[64:67], v[124:127], v[212:215]\n\t"
  "v_mfma_f32_16x16x32_f16 v[176:179], v[64:67], v[152:155], v[176:179]\n\ts_waitcnt lgkmcnt(2)\n\t"
  "v_mfma_f32_16x16x32_f16 v[208:211], v[132:135], v[116:119], v[208:211]\n\tds_read_b64 v[108:109], v16 offset:1088\n\tds_read_b64 v[110:111], v17 offset:1088\n\t"
  "v_mfma_f32_16x16x32_f16 v[212:215], v[132:135], v[120:123], v[212:215]\n\t"
  "v_mfma_f32_16x16x32_f16 v[176:179], v[132:135], v[124:127], v[176:179]\n\t"
  "v_mfma_f32_16x16x32_f16 v[180:183], v[132:135], v[152:155], v[180:183]\n\tds_read_b64 v[112:113], v16 offset:576\n\tds_read_b64 v[114:115], v17 offset:576\n\t"
  "v_mfma_f32_16x16x32_f16 v[212:215], v[144:147], v[116:119], v[212:215]\n\t"
  "v_mfma_f32_16x16x32_f16 v[176:179], v[144:147], v[120:123], v[176:179]\n\tds_read_b64 v[132:133], v16 offset:64\n\tds_read_b64 v[134:135], v17 offset:64\n\t"
  "v_mfma_f32_16x16x32_f16 v[180:183], v[144:147], v[124:127], v[180:183]\n\t"
  "v_mfma_f32_16x16x32_f16 v[176:179], v[140:143], v[116:119], v[176:179]\n\t"
  "v_mfma_f32_16x16x32_f16 v[180:183], v[140:143], v[120:123], v[180:183]\n\tv_add_u32 v22, 0x101000, v22\n\t"
  "v_mfma_f32_16x16x32_f16 v[180:183], v[184:187], v[116:119], v[180:183]\n\t"
  "v_mfma_f32_16x16x32_f16 v[208:211], v[72:75], v[156:159], v[208:211]\n\t"
  "v_mfma_f32_16x16x32_f16 v[208:211], v[76:79], v[148:151], v[208:211]\n\t"
  "v_mfma_f32_16x16x32_f16 v[212:215], v[76:79], v[156:159], v[212:215]\n\tds_read_b64 v[116:117], v16 offset:1536\n\tds_read_b64 v[118:119], v17 offset:1536\n\t"
  "v_mfma_f32_16x16x32_f16 v[208:211], v[80:83], v[136:139], v[208:211]\n\t"
  "v_mfma_f32_16x16x32_f16 v[212:215], v[80:83], v[148:151], v[212:215]\n\t"
  "v_mfma_f32_16x16x32_f16 v[176:179], v[80:83], v[156:159], v[176:179]\n\ts_waitcnt lgkmcnt(2)\n\t"
  "v_mfma_f32_16x16x32_f16 v[208:211], v[102:105], v[128:131], v[208:211]\n\tds_read_b32 v101, v27 offset:52\n\tds_read_b64 v[120:121], v16 offset:1024\n\t"
  "v_mfma_f32_16x16x32_f16 v[212:215], v[102:105], v[136:139], v[212:215]\n\tds_read_b64 v[122:123], v17 offset:1024\n\t"
  "v_mfma_f32_16x16x32_f16 v[176:179], v[102:105], v[148:151], v[176:179]\n\t"
  "v_mfma_f32_16x16x32_f16 v[180:183], v[102:105], v[156:159], v[180:183]\n\tds_read_b64 v[102:103], v16 offset:512\n\tds_read_b64 v[104:105], v17 offset:512\n\t"
  "v_mfma_f32_16x16x32_f16 v[212:215], v[108:111], v[128:131], v[212:215]\n\t"
  "v_mfma_f32_16x16x32_f16 v[176:179], v[108:111], v[136:139], v[176:179]\n\tds_read_b64 v[124:125], v16 offset:0\n\tds_read_b64 v[126:127], v17 offset:0\n\t"
  "v_mfma_f32_16x16x32_f16 v[180:183], v[108:111], v[148:151], v[180:183]\n\t"
  "v_mfma_f32_16x16x32_f16 v[176:179], v[112:115], v[128:131], v[176:179]\n\t"
  "v_mfma_f32_16x16x32_f16 v[180:183], v[112:115], v[136:139], v[180:183]\n\ts_waitcnt lgkmcnt(5)\n\tv_readfirstlane_b32 s61, v101\n\t"
  "v_mfma_f32_16x16x32_f16 v[180:183], v[132:135], v[128:131], v[180:183]\n\t"
  "v_mfma_f32_16x16x32_f16 v[208:211], v[88:91], v[172:175], v[208:211]\n\ts_cmp_lt_u32 s61, 4\n\ts_cbranch_scc1 .Lc0_ub\n.Lc0_ub_b:\n\t"
  "v_mfma_f32_16x16x32_f16 v[208:211], v[92:95], v[168:171], v[208:211]\n\tds_read_b128 v[108:111], v18 offset:2240\n\tds_read_b128 v[112:115], v18 offset:2752\n\t"
  "v_mfma_f32_16x16x32_f16 v[212:215], v[92:95], v[172:175], v[212:215]\n\tds_read_b128 v[128:131], v18 offset:3264\n\tds_read_b128 v[132:135], v18 offset:3776\n\t"
  "v_mfma_f32_16x16x32_f16 v[208:211], v[96:99], v[164:167], v[208:211]\n\t"
  "v_mfma_f32_16x16x32_f16 v[212:215], v[96:99], v[168:171], v[212:215]\n\t"
  "v_mfma_f32_16x16x32_f16 v[176:179], v[96:99], v[172:175], v[176:179]\n\tds_read_b128 v[136:139], v18 offset:2176\n\tds_read_b128 v[140:143], v18 offset:2688\n\t"
  "v_mfma_f32_16x16x32_f16 v[208:211], v[116:119], v[160:163], v[208:211]\n\tds_read_b128 v[144:147], v18 offset:3200\n\tds_read_b128 v[148:151], v18 offset:3712\n\t"
  "v_mfma_f32_16x16x32_f16 v[212:215], v[116:119], v[164:167], v[212:215]\n\t"
  "v_mfma_f32_16x16x32_f16 v[176:179], v[116:119], v[168:171], v[176:179]\n\t"
  "v_mfma_f32_16x16x32_f16 v[180:183], v[116:119], v[172:175], v[180:183]\n\ts_waitcnt lgkmcnt(6)\n\t"
  "v_mfma_f32_16x16x32_f16 v[212:215], v[120:123], v[160:163], v[212:215]\n\tds_read_b128 v[116:119], v18 offset:2112\n\tds_read_b128 v[152:155], v18 offset:2624\n\t"
  "v_mfma_f32_16x16x32_f16 v[176:179], v[120:123], v[164:167], v[176:179]\n\tds_read_b128 v[156:159], v18 offset:3136\n\tds_read_b128 v[172:175], v18 offset:3648\n\t"
  "v_mfma_f32_16x16x32_f16 v[180:183], v[120:123], v[168:171], v[180:183]\n\t"
  "v_mfma_f32_16x16x32_f16 v[176:179], v[102:105], v[160:163], v[176:179]\n\t"
  "v_mfma_f32_16x16x32_f16 v[180:183], v[102:105], v[164:167], v[180:183]\n\t"
  "v_mfma_f32_16x16x32_f16 v[180:183], v[124:127], v[160:163], v[180:183]\n\tds_read_b128 v[104:107], v18 offset:2048\n\tds_read_b128 v[120:123], v18 offset:2560\n\t"
  "v_mfma_f32_16x16x32_f16 v[212:215], v[40:43], v[108:111], v[212:215]\n\tds_read_b128 v[124:127], v18 offset:3072\n\tds_read_b128 v[160:163], v18 offset:3584\n\t"
  "v_mfma_f32_16x16x32_f16 v[176:179], v[44:47], v[108:111], v[176:179]\n\t"
  "v_mfma_f32_16x16x32_f16 v[212:215], v[36:39], v[112:115], v[212:215]\n\t"
  "v_mfma_f32_16x16x32_f16 v[176:179], v[40:43], v[112:115], v[176:179]\n\ts_waitcnt lgkmcnt(4)\n\t"
  "v_mfma_f32_16x16x32_f16 v[176:179], v[36:39], v[128:131], v[176:179]\n\t"
  "v_mfma_f32_16x16x32_f16 v[212:215], v[56:59], v[136:139], v[212:215]\n\t"
  "v_mfma_f32_16x16x32_f16 v[176:179], v[60:63], v[136:139], v[176:179]\n\t"
  "v_mfma_f32_16x16x32_f16 v[212:215], v[52:55], v[140:143], v[212:215]\n\t"
  "v_mfma_f32_16x16x32_f16 v[176:179], v[56:59], v[140:143], v[176:179]\n\t"
  "v_mfma_f32_16x16x32_f16 v[176:179], v[52:55], v[144:147], v[176:179]\n\t"
  "v_mfma_f32_16x16x32_f16 v[212:215], v[72:75], v[116:119], v[212:215]\n\tds_read_b128 v[164:167], v19 offset:4096\n\tds_read_b128 v[168:171], v19 offset:7168\n\t"
  "v_mfma_f32_16x16x32_f16 v[176:179], v[76:79], v[116:119], v[176:179]\n\t"
  "v_mfma_f32_16x16x32_f16 v[212:215], v[68:71], v[152:155], v[212:215]\n\t"
  "v_mfma_f32_16x16x32_f16 v[176:179], v[72:75], v[152:155], v[176:179]\n\t"
  "v_mfma_f32_16x16x32_f16 v[176:179], v[68:71], v[156:159], v[176:179]\n\ts_waitcnt lgkmcnt(2)\n\t"
  "v_mfma_f32_16x16x32_f16 v[212:215], v[88:91], v[104:107], v[212:215]\n\t"
  "v_mfma_f32_16x16x32_f16 v[176:179], v[92:95], v[104:107], v[176:179]\n\t"
  "v_mfma_f32_16x16x32_f16 v[212:215], v[84:87], v[120:123], v[212:215]\n\t"
  "v_mfma_f32_16x16x32_f16 v[176:179], v[88:91], v[120:123], v[176:179]\n\tds_read_b32 v101, v27 offset:64\n\ts_waitcnt lgkmcnt(1)\n\t"
  "v_mfma_f32_16x16x32_f16 v[176:179], v[84:87], v[124:127], v[176:179]\n\tds_write_b128 v20, v[212:215] offset:57344\n\tv_perm_b32 v164, v164, v164, s60\n\t"
  "v_mfma_f32_16x16x32_f16 v[208:211], v[36:39], v[108:111], v[208:211]\n\ts_setprio 3\n\tbuffer_load_dwordx4 v[28:31], v23, s[56:59], 0 offen sc1\n\tbuffer_load_dwordx4 v[32:35], v24, s[56:59], 0 offen sc1\n\ts_setprio 0\n\t"
  "v_mfma_f32_16x16x32_f16 v[180:183], v[48:51], v[108:111], v[180:183]\n\tds_write_b128 v20, v[176:179] offset:58368\n\tv_perm_b32 v165, v165, v165, s60\n\t"
  "v_mfma_f32_16x16x32_f16 v[180:183], v[44:47], v[112:115], v[180:183]\n\ts_mov_b64 exec, 1\n\tds_add_u32 v27, v100 offset:16\n\ts_mov_b64 exec, -1\n\t"
  "v_mfma_f32_16x16x32_f16 v[180:183], v[40:43], v[128:131], v[180:183]\n\tv_perm_b32 v166, v166, v166, s60\n\tv_perm_b32 v167, v167, v167, s60\n\t"
  "v_mfma_f32_16x16x32_f16 v[180:183], v[36:39], v[132:135], v[180:183]\n\tv_perm_b32 v168, v168, v168, s60\n\tv_perm_b32 v169, v169, v169, s60\n\t"
  "v_mfma_f32_16x16x32_f16 v[208:211], v[52:55], v[136:139], v[208:211]\n\tv_perm_b32 v170, v170, v170, s60\n\tv_perm_b32 v171, v171, v171, s60\n\t"
  "v_mfma_f32_16x16x32_f16 v[180:183], v[64:67], v[136:139], v[180:183]\n\ts_waitcnt lgkmcnt(3)\n\tv_readfirstlane_b32 s61, v101\n\t"
  "v_mfma_f32_16x16x32_f16 v[180:183], v[60:63], v[140:143], v[180:183]\n\t"
  "v_mfma_f32_16x16x32_f16 v[180:183], v[56:59], v[144:147], v[180:183]\n\ts_cmp_lt_u32 s61, 4\n\ts_cbranch_scc1 .Lc0_p3\n.Lc0_p3_b:\n\t"
  "v_mfma_f32_16x16x32_f16 v[180:183], v[52:55], v[148:151], v[180:183]\n\tds_read_b32 v36, v27 offset:16\n\tds_read_b128 v[40:43], v20 offset:59392\n\tds_read_b128 v[44:47], v20 offset:60416\n\t"
  "v_mfma_f32_16x16x32_f16 v[208:211], v[68:71], v[116:119], v[208:211]\n\t"
  "v_mfma_f32_16x16x32_f16 v[180:183], v[80:83], v[116:119], v[180:183]\n\t"
  "v_mfma_f32_16x16x32_f16 v[180:183], v[76:79], v[152:155], v[180:183]\n\t"
  "v_mfma_f32_16x16x32_f16 v[180:183], v[72:75], v[156:159], v[180:183]\n\t"
  "v_mfma_f32_16x16x32_f16 v[180:183], v[68:71], v[172:175], v[180:183]\n\t"
  "v_mfma_f32_16x16x32_f16 v[208:211], v[84:87], v[104:107], v[208:211]\n\t"
  "v_mfma_f32_16x16x32_f16 v[180:183], v[96:99], v[104:107], v[180:183]\n\t"
  "v_mfma_f32_16x16x32_f16 v[180:183], v[92:95], v[120:123], v[180:183]\n\ts_waitcnt lgkmcnt(0)\n\tv_readfirstlane_b32 s61, v36\n\tds_read_b32 v25, v27 offset:32\n\t"
  "v_mfma_f32_16x16x32_f16 v[180:183], v[88:91], v[124:127], v[180:183]\n\ts_cmp_lt_u32 s61, 4\n\ts_cbranch_scc1 .Lc0_x4\n.Lc0_x4_b:\n\t"
  "v_mfma_f32_16x16x32_f16 v[180:183], v[84:87], v[160:163], v[180:183]\n\tv_fmac_f32 v164, v40, v26\n\tv_fmac_f32 v165, v41, v26\n\tv_fmac_f32 v166, v42, v26\n\t"
  "v_fmac_f32 v167, v43, v26\n\tv_fmac_f32 v168, v44, v26\n\tv_fmac_f32 v169, v45, v26\n\tv_fmac_f32 v170, v46, v26\n\t"
  "v_fmac_f32 v171, v47, v26\n\tv_fmac_f32 v164, v208, v26\n\tv_fmac_f32 v165, v209, v26\n\tv_fmac_f32 v166, v210, v26\n\t"
  "v_fmac_f32 v167, v211, v26\n\tv_cvt_pk_bf16_f32 v36, v164, v165\n\tv_cvt_pk_bf16_f32 v37, v166, v167\n\tv_fmac_f32 v168, v180, v26\n\t"
  "v_fmac_f32 v169, v181, v26\n\tv_fmac_f32 v170, v182, v26\n\tv_fmac_f32 v171, v183, v26\n\tv_cvt_pk_bf16_f32 v38, v168, v169\n\t"
  "v_cvt_pk_bf16_f32 v39, v170, v171\n\ts_nop 0\n\tv_permlane16_swap_b32 v36, v38\n\tv_permlane16_swap_b32 v37, v39\n\t"
  "s_setprio 3\n\tbuffer_store_dwordx4 v[36:39], v22, s[56:59], 0 offen sc1\n\ts_setprio 0\n\ts_waitcnt lgkmcnt(0)\n\ts_nop 0\n\t"
  "s_branch .Lc_end\n.Lc_odd:\n\t"
  "ds_read_b64 v[36:37], v16 offset:3776\n\tds_read_b64 v[38:39], v17 offset:3776\n\tds_read_b64 v[40:41], v16 offset:3264\n\tds_read_b64 v[42:43], v17 offset:3264\n\t"
  "ds_read_b64 v[44:45], v16 offset:2752\n\tds_read_b64 v[46:47], v17 offset:2752\n\tds_read_b64 v[48:49], v16 offset:2240\n\tds_read_b64 v[50:51], v17 offset:2240\n\t"
  "ds_read_b64 v[52:53], v16 offset:3712\n\tds_read_b64 v[54:55], v17 offset:3712\n\tds_read_b64 v[56:57], v16 offset:3200\n\tds_read_b64 v[58:59], v17 offset:3200\n\t"
  "ds_read_b64 v[60:61], v16 offset:2688\n\tds_read_b64 v[62:63], v17 offset:2688\n\tds_read_b64 v[64:65], v16 offset:2176\n\tds_read_b64 v[66:67], v17 offset:2176\n\t"
  "ds_read_b64 v[68:69], v16 offset:3648\n\tds_read_b64 v[70:71], v17 offset:3648\n\tds_read_b64 v[72:73], v16 offset:3136\n\tds_read_b64 v[74:75], v17 offset:3136\n\t"
  "ds_read_b64 v[76:77], v16 offset:2624\n\tds_read_b64 v[78:79], v17 offset:2624\n\tds_read_b64 v[80:81], v16 offset:2112\n\tds_read_b64 v[82:83], v17 offset:2112\n\t"
  "ds_read_b64 v[84:85], v16 offset:3584\n\tds_read_b64 v[86:87], v17 offset:3584\n\tds_read_b64 v[88:89], v16 offset:3072\n\tds_read_b64 v[90:91], v17 offset:3072\n\t"
  "ds_read_b64 v[92:93], v16 offset:2560\n\tds_read_b64 v[94:95], v17 offset:2560\n\tds_read_b64 v[96:97], v16 offset:2048\n\tds_read_b64 v[98:99], v17 offset:2048\n\t"
  "ds_read_b32 v26, v21\n\tv_and_b32 v27, -8, v21\n\tv_mov_b32 v100, 1\n\t"
  CST(6)
  "s_mov_b32 vcc_hi, 0x1000000\n\t.Lc1_u:\n\tds_read_b32 v101, v27 offset:56\n\ts_sub_u32 vcc_hi, vcc_hi, 1\n\ts_cselect_b32 vcc_lo, 0, 4\n\ts_waitcnt lgkmcnt(0)\n\tv_readfirstlane_b32 s61, v101\n\ts_nop 1\n\ts_cmp_lt_u32 s61, vcc_lo\n\ts_cbranch_scc1 .Lc1_u\n\t"
  CST(7)
  "ds_read_b128 v[102:105], v18 offset:192\n\tds_read_b128 v[106:109], v18 offset:704\n\tds_read_b128 v[110:113], v18 offset:1216\n\tds_read_b128 v[114:117], v18 offset:1728\n\t"
  "ds_read_b128 v[120:123], v18 offset:128\n\tds_read_b128 v[124:127], v18 offset:640\n\tds_read_b128 v[128:131], v18 offset:1152\n\tds_read_b128 v[132:135], v18 offset:1664\n\t"
  "ds_read_b128 v[136:139], v18 offset:64\n\ts_waitcnt lgkmcnt(8)\n\t"
  "v_mfma_f32_16x16x32_f16 v[140:143], v[36:39], v[102:105], 0\n\tds_read_b128 v[144:147], v18 offset:576\n\t"
  "v_mfma_f32_16x16x32_f16 v[148:151], v[48:51], v[102:105], 0\n\tds_read_b128 v[152:155], v18 offset:1088\n\ts_waitcnt lgkmcnt(9)\n\t"
  "v_mfma_f32_16x16x32_f16 v[148:151], v[44:47], v[106:109], v[148:151]\n\tds_read_b128 v[156:159], v18 offset:1600\n\ts_waitcnt lgkmcnt(3)\n\t"
  "v_mfma_f32_16x16x32_f16 v[148:151], v[40:43], v[110:113], v[148:151]\n\tds_read_b128 v[160:163], v18 offset:0\n\tds_read_b32 v101, v27 offset:48\n\t"
  "v_mfma_f32_16x16x32_f16 v[148:151], v[36:39], v[114:117], v[148:151]\n\tds_read_b128 v[164:167], v18 offset:512\n\t"
  "v_mfma_f32_16x16x32_f16 v[140:143], v[52:55], v[120:123], v[140:143]\n\tds_read_b128 v[168:171], v18 offset:1024\n\t"
  "v_mfma_f32_16x16x32_f16 v[148:151], v[64:67], v[120:123], v[148:151]\n\tds_read_b128 v[172:175], v18 offset:1536\n\t"
  "v_mfma_f32_16x16x32_f16 v[148:151], v[60:63], v[124:127], v[148:151]\n\t"
  "v_mfma_f32_16x16x32_f16 v[148:151], v[56:59], v[128:131], v[148:151]\n\t"
  "v_mfma_f32_16x16x32_f16 v[148:151], v[52:55], v[132:135], v[148:151]\n\t"
  "v_mfma_f32_16x16x32_f16 v[140:143], v[68:71], v[136:139], v[140:143]\n\tds_read_b128 v[176:179], v19 offset:1024\n\tds_read_b128 v[180:183], v19 offset:2048\n\t"
  "v_mfma_f32_16x16x32_f16 v[148:151], v[80:83], v[136:139], v[148:151]\n\ts_waitcnt lgkmcnt(5)\n\tv_readfirstlane_b32 s61, v101\n\t"
  "v_mfma_f32_16x16x32_f16 v[148:151], v[76:79], v[144:147], v[148:151]\n\t"
  "v_mfma_f32_16x16x32_f16 v[148:151], v[72:75], v[152:155], v[148:151]\n\ts_cmp_lt_u32 s61, 4\n\ts_cbranch_scc1 .Lc1_kh\n.Lc1_kh_b:\n\t"
  "v_mfma_f32_16x16x32_f16 v[148:151], v[68:71], v[156:159], v[148:151]\n\tds_read_b64 v[184:185], v16 offset:1728\n\tds_read_b64 v[186:187], v17 offset:1728\n\t"
  "v_mfma_f32_16x16x32_f16 v[140:143], v[84:87], v[160:163], v[140:143]\n\tds_read_b64 v[188:189], v16 offset:1216\n\tds_read_b64 v[190:191], v17 offset:1216\n\t"
  "v_mfma_f32_16x16x32_f16 v[148:151], v[96:99], v[160:163], v[148:151]\n\tds_read_b64 v[192:193], v16 offset:704\n\ts_waitcnt lgkmcnt(7)\n\t"
  "v_mfma_f32_16x16x32_f16 v[148:151], v[92:95], v[164:167], v[148:151]\n\tds_read_b64 v[194:195], v17 offset:704\n\tds_read_b64 v[196:197], v16 offset:192\n\tds_read_b64 v[198:199], v17 offset:192\n\t"
  "v_mfma_f32_16x16x32_f16 v[148:151], v[88:91], v[168:171], v[148:151]\n\ts_waitcnt lgkmcnt(8)\n\tv_perm_b32 v176, v176, v176, s60\n\t"
  "v_mfma_f32_16x16x32_f16 v[148:151], v[84:87], v[172:175], v[148:151]\n\tds_write_b128 v20, v[140:143] offset:2048\n\tv_perm_b32 v177, v177, v177, s60\n\t"
  "v_mfma_f32_16x16x32_f16 v[200:203], v[40:43], v[102:105], 0\n\tv_perm_b32 v178, v178, v178, s60\n\tv_perm_b32 v179, v179, v179, s60\n\t"
  "v_mfma_f32_16x16x32_f16 v[140:143], v[44:47], v[102:105], 0\n\tds_write_b128 v20, v[148:151] offset:3072\n\tv_perm_b32 v180, v180, v180, s60\n\t"
  "v_mfma_f32_16x16x32_f16 v[200:203], v[36:39], v[106:109], v[200:203]\n\ts_mov_b64 exec, 1\n\tds_add_u32 v21, v100 offset:8\n\ts_mov_b64 exec, -1\n\t"
  "v_mfma_f32_16x16x32_f16 v[140:143], v[40:43], v[106:109], v[140:143]\n\tv_perm_b32 v181, v181, v181, s60\n\tv_perm_b32 v182, v182, v182, s60\n\t"
  "v_mfma_f32_16x16x32_f16 v[140:143], v[36:39], v[110:113], v[140:143]\n\tv_perm_b32 v183, v183, v183, s60\n\t"
  "v_mfma_f32_16x16x32_f16 v[200:203], v[56:59], v[120:123], v[200:203]\n\t"
  "v_mfma_f32_16x16x32_f16 v[140:143], v[60:63], v[120:123], v[140:143]\n\t"
  "v_mfma_f32_16x16x32_f16 v[200:203], v[52:55], v[124:127], v[200:203]\n\t"
  "v_mfma_f32_16x16x32_f16 v[140:143], v[56:59], v[124:127], v[140:143]\n\t"
  "v_mfma_f32_16x16x32_f16 v[140:143], v[52:55], v[128:131], v[140:143]\n\t"
  "v_mfma_f32_16x16x32_f16 v[200:203], v[72:75], v[136:139], v[200:203]\n\t"
  "v_mfma_f32_16x16x32_f16 v[140:143], v[76:79], v[136:139], v[140:143]\n\tds_read_b32 v101, v21 offset:8\n\tds_read_b128 v[148:151], v20 offset:0\n\tds_read_b128 v[204:207], v20 offset:1024\n\t"
  "v_mfma_f32_16x16x32_f16 v[200:203], v[68:71], v[144:147], v[200:203]\n\t"
  "v_mfma_f32_16x16x32_f16 v[140:143], v[72:75], v[144:147], v[140:143]\n\t"
  "v_mfma_f32_16x16x32_f16 v[140:143], v[68:71], v[152:155], v[140:143]\n\t"
  "v_mfma_f32_16x16x32_f16 v[200:203], v[88:91], v[160:163], v[200:203]\n\t"
  "v_mfma_f32_16x16x32_f16 v[140:143], v[92:95], v[160:163], v[140:143]\n\t"
  "v_mfma_f32_16x16x32_f16 v[200:203], v[84:87], v[164:167], v[200:203]\n\t"
  "v_mfma_f32_16x16x32_f16 v[140:143], v[88:91], v[164:167], v[140:143]\n\t"
  "v_mfma_f32_16x16x32_f16 v[140:143], v[84:87], v[168:171], v[140:143]\n\ts_waitcnt lgkmcnt(0)\n\tv_readfirstlane_b32 s61, v101\n\t"
  "v_mfma_f32_16x16x32_f16 v[208:211], v[40:43], v[114:117], 0\n\t"
  "v_mfma_f32_16x16x32_f16 v[208:211], v[44:47], v[110:113], v[208:211]\n\ts_cmp_lt_u32 s61, 2\n\ts_cbranch_scc1 .Lc1_x0\n.Lc1_x0_b:\n\tv_fmac_f32 v176, v148, v26\n\tv_fmac_f32 v177, v149, v26\n\t"
  "v_fmac_f32 v178, v150, v26\n\t"
  "v_mfma_f32_16x16x32_f16 v[212:215], v[44:47], v[114:117], 0\n\tv_fmac_f32 v179, v151, v26\n\tv_fmac_f32 v180, v204, v26\n\tv_fmac_f32 v181, v205, v26\n\t"
  "v_fmac_f32 v182, v206, v26\n\tv_fmac_f32 v183, v207, v26\n\t"
  "v_mfma_f32_16x16x32_f16 v[208:211], v[48:51], v[106:109], v[208:211]\n\tv_fmac_f32 v176, v200, v26\n\tv_fmac_f32 v177, v201, v26\n\t"
  "v_mfma_f32_16x16x32_f16 v[212:215], v[48:51], v[110:113], v[212:215]\n\tv_fmac_f32 v178, v202, v26\n\tv_fmac_f32 v179, v203, v26\n\tv_cvt_pk_bf16_f32 v148, v176, v177\n\t"
  "v_cvt_pk_bf16_f32 v149, v178, v179\n\tv_fmac_f32 v180, v140, v26\n\t"
  "v_mfma_f32_16x16x32_f16 v[176:179], v[48:51], v[114:117], 0\n\tv_fmac_f32 v181, v141, v26\n\tv_fmac_f32 v182, v142, v26\n\tv_fmac_f32 v183, v143, v26\n\t"
  "v_cvt_pk_bf16_f32 v150, v180, v181\n\tv_cvt_pk_bf16_f32 v151, v182, v183\n\t"
  "v_mfma_f32_16x16x32_f16 v[208:211], v[184:187], v[102:105], v[208:211]\n\tv_permlane16_swap_b32 v148, v150\n\tv_permlane16_swap_b32 v149, v151\n\ts_setprio 3\n\tbuffer_store_dwordx4 v[148:151], v22, s[56:59], 0 offen sc1\n\ts_setprio 0\n\t"
  CST(8)
  "v_mfma_f32_16x16x32_f16 v[212:215], v[184:187], v[106:109], v[212:215]\n\tds_read_b64 v[140:141], v16 offset:1664\n\tds_read_b64 v[142:143], v17 offset:1664\n\t"
  "v_mfma_f32_16x16x32_f16 v[176:179], v[184:187], v[110:113], v[176:179]\n\tds_read_b64 v[148:149], v16 offset:1152\n\tds_read_b64 v[150:151], v17 offset:1152\n\t"
  "v_mfma_f32_16x16x32_f16 v[180:183], v[184:187], v[114:117], 0\n\tds_read_b64 v[116:117], v16 offset:640\n\tds_read_b64 v[118:119], v17 offset:640\n\t"
  "v_mfma_f32_16x16x32_f16 v[212:215], v[188:191], v[102:105], v[212:215]\n\t"
  "v_mfma_f32_16x16x32_f16 v[176:179], v[188:191], v[106:109], v[176:179]\n\tds_read_b64 v[184:185], v16 offset:128\n\tds_read_b64 v[186:187], v17 offset:128\n\t"
  "v_mfma_f32_16x16x32_f16 v[180:183], v[188:191], v[110:113], v[180:183]\n\t"
  "v_mfma_f32_16x16x32_f16 v[176:179], v[192:195], v[102:105], v[176:179]\n\t"
  "v_mfma_f32_16x16x32_f16 v[180:183], v[192:195], v[106:109], v[180:183]\n\t"
  "v_mfma_f32_16x16x32_f16 v[180:183], v[196:199], v[102:105], v[180:183]\n\t"
  "v_mfma_f32_16x16x32_f16 v[208:211], v[56:59], v[132:135], v[208:211]\n\t"
  "v_mfma_f32_16x16x32_f16 v[208:211], v[60:63], v[128:131], v[208:211]\n\t"
  "v_mfma_f32_16x16x32_f16 v[212:215], v[60:63], v[132:135], v[212:215]\n\tds_read_b64 v[102:103], v16 offset:1600\n\tds_read_b64 v[104:105], v17 offset:1600\n\t"
  "v_mfma_f32_16x16x32_f16 v[208:211], v[64:67], v[124:127], v[208:211]\n\t"
  "v_mfma_f32_16x16x32_f16 v[212:215], v[64:67], v[128:131], v[212:215]\n\t"
  "v_mfma_f32_16x16x32_f16 v[176:179], v[64:67], v[132:135], v[176:179]\n\ts_waitcnt lgkmcnt(2)\n\t"
  "v_mfma_f32_16x16x32_f16 v[208:211], v[140:143], v[120:123], v[208:211]\n\tds_read_b64 v[108:109], v16 offset:1088\n\tds_read_b64 v[110:111], v17 offset:1088\n\t"
  "v_mfma_f32_16x16x32_f16 v[212:215], v[140:143], v[124:127], v[212:215]\n\t"
  "v_mfma_f32_16x16x32_f16 v[176:179], v[140:143], v[128:131], v[176:179]\n\t"
  "v_mfma_f32_16x16x32_f16 v[180:183], v[140:143], v[132:135], v[180:183]\n\tds_read_b64 v[112:113], v16 offset:576\n\tds_read_b64 v[114:115], v17 offset:576\n\t"
  "v_mfma_f32_16x16x32_f16 v[212:215], v[148:151], v[120:123], v[212:215]\n\t"
  "v_mfma_f32_16x16x32_f16 v[176:179], v[148:151], v[124:127], v[176:179]\n\tds_read_b64 v[132:133], v16 offset:64\n\tds_read_b64 v[134:135], v17 offset:64\n\t"
  "v_mfma_f32_16x16x32_f16 v[180:183], v[148:151], v[128:131], v[180:183]\n\t"
  "v_mfma_f32_16x16x32_f16 v[176:179], v[116:119], v[120:123], v[176:179]\n\t"
  "v_mfma_f32_16x16x32_f16 v[180:183], v[116:119], v[124:127], v[180:183]\n\tv_add_u32 v22, 0x101000, v22\n\t"
  "v_mfma_f32_16x16x32_f16 v[180:183], v[184:187], v[120:123], v[180:183]\n\t"
  "v_mfma_f32_16x16x32_f16 v[208:211], v[72:75], v[156:159], v[208:211]\n\t"
  "v_mfma_f32_16x16x32_f16 v[208:211], v[76:79], v[152:155], v[208:211]\n\t"
  "v_mfma_f32_16x16x32_f16 v[212:215], v[76:79], v[156:159], v[212:215]\n\tds_read_b64 v[116:117], v16 offset:1536\n\tds_read_b64 v[118:119], v17 offset:1536\n\t"
  "v_mfma_f32_16x16x32_f16 v[208:211], v[80:83], v[144:147], v[208:211]\n\t"
  "v_mfma_f32_16x16x32_f16 v[212:215], v[80:83], v[152:155], v[212:215]\n\t"
  "v_mfma_f32_16x16x32_f16 v[176:179], v[80:83], v[156:159], v[176:179]\n\ts_waitcnt lgkmcnt(2)\n\t"
  "v_mfma_f32_16x16x32_f16 v[208:211], v[102:105], v[136:139], v[208:211]\n\tds_read_b32 v101, v27 offset:52\n\tds_read_b64 v[120:121], v16 offset:1024\n\t"
  "v_mfma_f32_16x16x32_f16 v[212:215], v[102:105], v[144:147], v[212:215]\n\tds_read_b64 v[122:123], v17 offset:1024\n\t"
  "v_mfma_f32_16x16x32_f16 v[176:179], v[102:105], v[152:155], v[176:179]\n\t"
  "v_mfma_f32_16x16x32_f16 v[180:183], v[102:105], v[156:159], v[180:183]\n\tds_read_b64 v[102:103], v16 offset:512\n\tds_read_b64 v[104:105], v17 offset:512\n\t"
  "v_mfma_f32_16x16x32_f16 v[212:215], v[108:111], v[136:139], v[212:215]\n\t"
  "v_mfma_f32_16x16x32_f16 v[176:179], v[108:111], v[144:147], v[176:179]\n\tds_read_b64 v[124:125], v16 offset:0\n\tds_read_b64 v[126:127], v17 offset:0\n\t"
  "v_mfma_f32_16x16x32_f16 v[180:183], v[108:111], v[152:155], v[180:183]\n\t"
  "v_mfma_f32_16x16x32_f16 v[176:179], v[112:115], v[136:139], v[176:179]\n\t"
  "v_mfma_f32_16x16x32_f16 v[180:183], v[112:115], v[144:147], v[180:183]\n\ts_waitcnt lgkmcnt(5)\n\tv_readfirstlane_b32 s61, v101\n\t"
  "v_mfma_f32_16x16x32_f16 v[180:183], v[132:135], v[136:139], v[180:183]\n\t"
  "v_mfma_f32_16x16x32_f16 v[208:211], v[88:91], v[172:175], v[208:211]\n\ts_cmp_lt_u32 s61, 4\n\ts_cbranch_scc1 .Lc1_ub\n.Lc1_ub_b:\n\t"
  "v_mfma_f32_16x16x32_f16 v[208:211], v[92:95], v[168:171], v[208:211]\n\tds_read_b128 v[108:111], v18 offset:2240\n\tds_read_b128 v[112:115], v18 offset:2752\n\t"
  "v_mfma_f32_16x16x32_f16 v[212:215], v[92:95], v[172:175], v[212:215]\n\tds_read_b128 v[128:131], v18 offset:3264\n\tds_read_b128 v[132:135], v18 offset:3776\n\t"
  "v_mfma_f32_16x16x32_f16 v[208:211], v[96:99], v[164:167], v[208:211]\n\t"
  "v_mfma_f32_16x16x32_f16 v[212:215], v[96:99], v[168:171], v[212:215]\n\t"
  "v_mfma_f32_16x16x32_f16 v[176:179], v[96:99], v[172:175], v[176:179]\n\tds_read_b128 v[136:139], v18 offset:2176\n\tds_read_b128 v[140:143], v18 offset:2688\n\t"
  "v_mfma_f32_16x16x32_f16 v[208:211], v[116:119], v[160:163], v[208:211]\n\tds_read_b128 v[144:147], v18 offset:3200\n\tds_read_b128 v[148:151], v18 offset:3712\n\t"
  "v_mfma_f32_16x16x32_f16 v[212:215], v[116:119], v[164:167], v[212:215]\n\t"
  "v_mfma_f32_16x16x32_f16 v[176:179], v[116:119], v[168:171], v[176:179]\n\t"
  "v_mfma_f32_16x16x32_f16 v[180:183], v[116:119], v[172:175], v[180:183]\n\ts_waitcnt lgkmcnt(6)\n\t"
  "v_mfma_f32_16x16x32_f16 v[212:215], v[120:123], v[160:163], v[212:215]\n\tds_read_b128 v[116:119], v18 offset:2112\n\tds_read_b128 v[152:155], v18 offset:2624\n\t"
  "v_mfma_f32_16x16x32_f16 v[176:179], v[120:123], v[164:167], v[176:179]\n\tds_read_b128 v[156:159], v18 offset:3136\n\tds_read_b128 v[172:175], v18 offset:3648\n\t"
  "v_mfma_f32_16x16x32_f16 v[180:183], v[120:123], v[168:171], v[180:183]\n\t"
  "v_mfma_f32_16x16x32_f16 v[176:179], v[102:105], v[160:163], v[176:179]\n\t"
  "v_mfma_f32_16x16x32_f16 v[180:183], v[102:105], v[164:167], v[180:183]\n\t"
  "v_mfma_f32_16x16x32_f16 v[180:183], v[124:127], v[160:163], v[180:183]\n\tds_read_b128 v[104:107], v18 offset:2048\n\tds_read_b128 v[120:123], v18 offset:2560\n\t"
  "v_mfma_f32_16x16x32_f16 v[208:211], v[36:39], v[108:111], v[208:211]\n\tds_read_b128 v[124:127], v18 offset:3072\n\tds_read_b128 v[160:163], v18 offset:3584\n\t"
  "v_mfma_f32_16x16x32_f16 v[180:183], v[48:51], v[108:111], v[180:183]\n\t"
  "v_mfma_f32_16x16x32_f16 v[180:183], v[44:47], v[112:115], v[180:183]\n\ts_waitcnt lgkmcnt(6)\n\t"
  "v_mfma_f32_16x16x32_f16 v[180:183], v[40:43], v[128:131], v[180:183]\n\t"
  "v_mfma_f32_16x16x32_f16 v[180:183], v[36:39], v[132:135], v[180:183]\n\t"
  "v_mfma_f32_16x16x32_f16 v[208:211], v[52:55], v[136:139], v[208:211]\n\t"
  "v_mfma_f32_16x16x32_f16 v[180:183], v[64:67], v[136:139], v[180:183]\n\t"
  "v_mfma_f32_16x16x32_f16 v[180:183], v[60:63], v[140:143], v[180:183]\n\t"
  "v_mfma_f32_16x16x32_f16 v[180:183], v[56:59], v[144:147], v[180:183]\n\t"
  "v_mfma_f32_16x16x32_f16 v[180:183], v[52:55], v[148:151], v[180:183]\n\t"
  "v_mfma_f32_16x16x32_f16 v[208:211], v[68:71], v[116:119], v[208:211]\n\tds_read_b128 v[48:51], v19 offset:5120\n\tds_read_b128 v[64:67], v19 offset:6144\n\t"
  "v_mfma_f32_16x16x32_f16 v[180:183], v[80:83], v[116:119], v[180:183]\n\t"
  "v_mfma_f32_16x16x32_f16 v[180:183], v[76:79], v[152:155], v[180:183]\n\ts_waitcnt lgkmcnt(2)\n\t"
  "v_mfma_f32_16x16x32_f16 v[180:183], v[72:75], v[156:159], v[180:183]\n\t"
  "v_mfma_f32_16x16x32_f16 v[180:183], v[68:71], v[172:175], v[180:183]\n\t"
  "v_mfma_f32_16x16x32_f16 v[208:211], v[84:87], v[104:107], v[208:211]\n\t"
  "v_mfma_f32_16x16x32_f16 v[180:183], v[96:99], v[104:107], v[180:183]\n\t"
  "v_mfma_f32_16x16x32_f16 v[180:183], v[92:95], v[120:123], v[180:183]\n\t"
  "v_mfma_f32_16x16x32_f16 v[180:183], v[88:91], v[124:127], v[180:183]\n\tds_read_b32 v101, v27 offset:64\n\ts_waitcnt lgkmcnt(1)\n\t"
  "v_mfma_f32_16x16x32_f16 v[180:183], v[84:87], v[160:163], v[180:183]\n\tds_write_b128 v20, v[208:211] offset:59392\n\tv_perm_b32 v48, v48, v48, s60\n\t"
  "v_mfma_f32_16x16x32_f16 v[212:215], v[40:43], v[108:111], v[212:215]\n\ts_setprio 3\n\tbuffer_load_dwordx4 v[28:31], v23, s[56:59], 0 offen sc1\n\tbuffer_load_dwordx4 v[32:35], v24, s[56:59], 0 offen sc1\n\ts_setprio 0\n\t"
  "v_mfma_f32_16x16x32_f16 v[176:179], v[44:47], v[108:111], v[176:179]\n\tds_write_b128 v20, v[180:183] offset:60416\n\tv_perm_b32 v49, v49, v49, s60\n\t"
  "v_mfma_f32_16x16x32_f16 v[212:215], v[36:39], v[112:115], v[212:215]\n\ts_mov_b64 exec, 1\n\tds_add_u32 v27, v100 offset:16\n\ts_mov_b64 exec, -1\n\t"
  "v_mfma_f32_16x16x32_f16 v[176:179], v[40:43], v[112:115], v[176:179]\n\tv_perm_b32 v50, v50, v50, s60\n\tv_perm_b32 v51, v51, v51, s60\n\t"
  "v_mfma_f32_16x16x32_f16 v[176:179], v[36:39], v[128:131], v[176:179]\n\tv_perm_b32 v64, v64, v64, s60\n\tv_perm_b32 v65, v65, v65, s60\n\t"
  "v_mfma_f32_16x16x32_f16 v[212:215], v[56:59], v[136:139], v[212:215]\n\tv_perm_b32 v66, v66, v66, s60\n\tv_perm_b32 v67, v67, v67, s60\n\t"
  "v_mfma_f32_16x16x32_f16 v[176:179], v[60:63], v[136:139], v[176:179]\n\ts_waitcnt lgkmcnt(3)\n\tv_readfirstlane_b32 s61, v101\n\t"
  "v_mfma_f32_16x16x32_f16 v[212:215], v[52:55], v[140:143], v[212:215]\n\t"
  "v_mfma_f32_16x16x32_f16 v[176:179], v[56:59], v[140:143], v[176:179]\n\ts_cmp_lt_u32 s61, 4\n\ts_cbranch_scc1 .Lc1_p3\n.Lc1_p3_b:\n\t"
  "v_mfma_f32_16x16x32_f16 v[176:179], v[52:55], v[144:147], v[176:179]\n\tds_read_b32 v36, v27 offset:16\n\tds_read_b128 v[40:43], v20 offset:57344\n\tds_read_b128 v[44:47], v20 offset:58368\n\t"
  "v_mfma_f32_16x16x32_f16 v[212:215], v[72:75], v[116:119], v[212:215]\n\t"
  "v_mfma_f32_16x16x32_f16 v[176:179], v[76:79], v[116:119], v[176:179]\n\t"
  "v_mfma_f32_16x16x32_f16 v[212:215], v[68:71], v[152:155], v[212:215]\n\t"
  "v_mfma_f32_16x16x32_f16 v[176:179], v[72:75], v[152:155], v[176:179]\n\t"
  "v_mfma_f32_16x16x32_f16 v[176:179], v[68:71], v[156:159], v[176:179]\n\t"
  "v_mfma_f32_16x16x32_f16 v[212:215], v[88:91], v[104:107], v[212:215]\n\t"
  "v_mfma_f32_16x16x32_f16 v[176:179], v[92:95], v[104:107], v[176:179]\n\t"
  "v_mfma_f32_16x16x32_f16 v[212:215], v[84:87], v[120:123], v[212:215]\n\ts_waitcnt lgkmcnt(0)\n\tv_readfirstlane_b32 s61, v36\n\tds_read_b32 v25, v27 offset:32\n\t"
  "v_mfma_f32_16x16x32_f16 v[176:179], v[88:91], v[120:123], v[176:179]\n\ts_cmp_lt_u32 s61, 4\n\ts_cbranch_scc1 .Lc1_x4\n.Lc1_x4_b:\n\t"
  "v_mfma_f32_16x16x32_f16 v[176:179], v[84:87], v[124:127], v[176:179]\n\tv_fmac_f32 v48, v40, v26\n\tv_fmac_f32 v49, v41, v26\n\tv_fmac_f32 v50, v42, v26\n\t"
  "v_fmac_f32 v51, v43, v26\n\tv_fmac_f32 v64, v44, v26\n\tv_fmac_f32 v65, v45, v26\n\tv_fmac_f32 v66, v46, v26\n\t"
  "v_fmac_f32 v67, v47, v26\n\tv_fmac_f32 v48, v212, v26\n\tv_fmac_f32 v49, v213, v26\n\tv_fmac_f32 v50, v214, v26\n\t"
  "v_fmac_f32 v51, v215, v26\n\tv_cvt_pk_bf16_f32 v36, v48, v49\n\tv_cvt_pk_bf16_f32 v37, v50, v51\n\tv_fmac_f32 v64, v176, v26\n\t"
  "v_fmac_f32 v65, v177, v26\n\tv_fmac_f32 v66, v178, v26\n\tv_fmac_f32 v67, v179, v26\n\tv_cvt_pk_bf16_f32 v38, v64, v65\n\t"
  "v_cvt_pk_bf16_f32 v39, v66, v67\n\ts_nop 0\n\tv_permlane16_swap_b32 v36, v38\n\tv_permlane16_swap_b32 v37, v39\n\t"
  "s_setprio 3\n\tbuffer_store_dwordx4 v[36:39], v22, s[56:59], 0 offen sc1\n\ts_setprio 0\n\ts_waitcnt lgkmcnt(0)\n\ts_nop 0\n\t"
  ".Lc_end:\n\ts_waitcnt vmcnt(1) lgkmcnt(0)"
  : C1_OPS, "=&{v[28:31]}"(fl), "=&{v[32:35]}"(wfl), "=&{v25}"(g2)
  : "{s[56:59]}"(wrs), "{s60}"((a >> 1) ? 0x03020c0cu : 0x01000c0cu), [a] "s"(a)
  : "memory", "scc", "vcc", "s61", "v26", "v27", "v36", "v37", "v38", "v39", "v40", "v41", "v42", "v43", "v44", "v45", "v46", "v47", "v48", "v49", "v50", "v51", "v52", "v53", "v54", "v55", "v56", "v57", "v58", "v59", "v60", "v61", "v62", "v63", "v64", "v65", "v66", "v67", "v68", "v69", "v70", "v71", "v72", "v73", "v74", "v75", "v76", "v77", "v78", "v79", "v80", "v81", "v82", "v83", "v84", "v85", "v86", "v87", "v88", "v89", "v90", "v91", "v92", "v93", "v94", "v95", "v96", "v97", "v98", "v99", "v100", "v101", "v102", "v103", "v104", "v105", "v106", "v107", "v108", "v109", "v110", "v111", "v112", "v113", "v114", "v115", "v116", "v117", "v118", "v119", "v120", "v121", "v122", "v123", "v124", "v125", "v126", "v127", "v128", "v129", "v130", "v131", "v132", "v133", "v134", "v135", "v136", "v137", "v138", "v139", "v140", "v141", "v142", "v143", "v144", "v145", "v146", "v147", "v148", "v149", "v150", "v151", "v152", "v153", "v154", "v155", "v156", "v157", "v158", "v159", "v160", "v161", "v162", "v163", "v164", "v165", "v166", "v167", "v168", "v169", "v170", "v171", "v172", "v173", "v174", "v175", "v176", "v177", "v178", "v179", "v180", "v181", "v182", "v183", "v184", "v185", "v186", "v187", "v188", "v189", "v190", "v191", "v192", "v193", "v194", "v195", "v196", "v197", "v198", "v199", "v200", "v201", "v202", "v203", "v204", "v205", "v206", "v207", "v208", "v209", "v210", "v211", "v212", "v213", "v214", "v215", "v216", "v217", "v218", "v219", "v220", "v221", "v222", "v223");
  ST(0, 9);
}

struct XTake { u32 p, sw, idx; bf16x8 one; u64 m0, m1, m2; };
typedef __attribute__((ext_vector_type(16))) short bf16x16;
__device__ __forceinline__ void x_pre(XTake& X, int a, int lane) {
  X.p = (u32)(L_TA + a * 8192 + lane * 8);
  X.sw = (u32)(L_SP + ((lane & 15) * 4 + a) * 4 + ((lane >> 4) == ((lane >> 2) & 3) ? 768 : 0));
  X.idx = (lane & 1) ? 0xEEEEu : 0x4444u;
  X.one = __builtin_bit_cast(bf16x8, (u32x4){0x3F803F80u, 0x3F803F80u, 0x3F803F80u, 0x3F803F80u});
  X.m0 = 0xAAAAAAAAAAAAAAAAull; X.m1 = 0xCCCCCCCCCCCCCCCCull; X.m2 = 0xF0000F0000F0000Full;
  asm volatile("" : "+v"(X.p), "+v"(X.sw), "+v"(X.idx), "+v"(X.one));
  asm volatile("" : "+s"(X.m0), "+s"(X.m1), "+s"(X.m2));
}
template <int H, int NY = 0, bool ALL = false>
__device__ __forceinline__ void x_take(const XTake& X, char* smem, rsrc_t wrs, int xin, int rb, int a, int lane, u32 sent = 0xFFFFFFFFu) {
  constexpr int tile = H ? L_TB : L_TA, NT = H ? 3 : 2;
  const LAS char* p = (const LAS char*)(size_t)(H ? opaque(X.p + (L_TB - L_TA)) : X.p);
  const LAS char* ph = (const LAS char*)(size_t)(H ? opaque(X.p + (L_TH - L_TA) - a * 4096) : 0u);
  LAS float* spw = (LAS float*)(size_t)X.sw + H * 384;
  float s = 0.f, dv[NT];
  bf16x16 xf[NT][2];
  f32x4 ss[NT], sg[NT];
  auto ld_ = [&](int o, int j) __attribute__((always_inline)) {
    if (o < 2) xf[o][j] = __builtin_shufflevector(ld_tr8<1024>(p + o * 512 + j * 4096), ld_tr8<1024>(p + o * 512 + j * 4096 + 2048), 0, 1, 2, 3, 4, 5, 6, 7, 8, 9, 10, 11, 12, 13, 14, 15);
    else xf[o][j] = __builtin_shufflevector(ld_tr8<512>(ph + j * 2048), ld_tr8<512>(ph + j * 2048 + 1024), 0, 1, 2, 3, 4, 5, 6, 7, 8, 9, 10, 11, 12, 13, 14, 15);
    SB();
  };
  auto gr_ = [&](int o, int j) __attribute__((always_inline)) {
#define XLO(v_) __builtin_shufflevector(v_, v_, 0, 1, 2, 3, 4, 5, 6, 7)
#define XHI(v_) __builtin_shufflevector(v_, v_, 8, 9, 10, 11, 12, 13, 14, 15)
    if (j == 0) asm volatile("v_mfma_f32_16x16x32_bf16 %0, %1, %1, 0" : "=&v"(sg[o]) : "v"(XLO(xf[o][0])));
    else asm volatile("v_mfma_f32_16x16x32_bf16 %0, %1, %1, %0" : "+v"(sg[o]) : "v"(XLO(xf[o][1])));
    asm volatile("v_mfma_f32_16x16x32_bf16 %0, %1, %1, %0" : "+v"(sg[o]) : "v"(XHI(xf[o][j])));
    SB();
  };
  auto sm_ = [&](int o, int j) __attribute__((always_inline)) {
    asm volatile("v_smfmac_f32_16x16x64_bf16 %0, %1, %2, %3" : "+v"(ss[o]) : "v"(X.one), "v"(xf[o][j]), "v"(X.idx));
    SB();
  };
  auto fin_ = [&](int o, auto V_) __attribute__((always_inline)) {
    float t;
    float& d = dv[o];
    asm volatile("v_cndmask_b32_e64 %0, %3, %4, %9\n\tv_cndmask_b32_e64 %1, %5, %6, %9\n\tv_cndmask_b32_e64 %0, %0, %1, %10\n\t.if %12 >= 0\n\ts_nop %12\n\t.endif\n\t"
                 "v_add_f32 %1, %7, %8\n\tv_cndmask_b32_e64 %0, %1, %0, %11\n\tv_add_f32 %2, %2, %1"
                 : "=&v"(d), "=&v"(t), "+v"(s) : "v"(sg[o][0]), "v"(sg[o][1]), "v"(sg[o][2]), "v"(sg[o][3]), "v"(ss[o][0]), "v"(ss[o][1]),
                   "s"(X.m0), "s"(X.m1), "s"(X.m2), "n"(decltype(V_)::value - 1), "v"(ss[o]));
    SB();
  };
  auto zero_ = [&]() __attribute__((always_inline)) {
#pragma unroll
    for (int o = 0; o < NT; ++o) { ss[o] = (f32x4){0.f, 0.f, 0.f, 0.f}; asm volatile("" : "+v"(ss[o])); }
    asm volatile("" : "+v"(s));
  };
  auto first_ = [&]() __attribute__((always_inline)) { gr_(0, 0); sm_(0, 0); gr_(1, 0); sm_(1, 0); };
  auto second_ = [&]() __attribute__((always_inline)) {
    gr_(0, 1); sm_(0, 1); gr_(1, 1); fin_(0, IC<0>{}); sm_(1, 1);
    if (NT == 2) { fin_(1, IC<5>{}); spw[0] = dv[0]; spw[64] = dv[1]; }
  };
  auto third_ = [&]() __attribute__((always_inline)) {
    gr_(2, 0); fin_(1, IC<0>{}); gr_(2, 1);
    spw[0] = dv[0]; spw[64] = dv[1];
    SB();
    sm_(2, 0); sm_(2, 1); fin_(NT - 1, IC<5>{});
    spw[128] = dv[NT - 1];
  };
  zero_();
  SB();
  if (H) {
    const u32 sa = opaque((u32)(L_TB + a * 8192 + (lane >> 4) * 1024 + (lane & 15) * 32 + ((lane >> 3) & 1) * 512 + ((lane >> 4) & 3) * 8 + 4));
#pragma unroll 1
    for (unsigned spins = 0; spins <= SPIN_CAP; ++spins) {
      x_dma(wrs, xin, rb, tile, a, lane);
      asm volatile("s_waitcnt vmcnt(8)" ::: "memory");
      if (__builtin_amdgcn_ballot_w64(*(const LAS u32*)(size_t)sa == sent) == 0ull) break;
      FAILC(20);
    }
  } else asm volatile("s_waitcnt vmcnt(%0)" :: "n"(4 + NY) : "memory");
  if (H && B6 == 0) { __builtin_amdgcn_s_barrier(); ST(xin != OFF_X1, 14); asm volatile("" ::: "memory"); }
  ld_(0, 0); ld_(1, 0);
  if (!ALL && !(H && B6 == 1)) first_();
  asm volatile("s_waitcnt vmcnt(%0)" :: "n"(4 * (NT - 2) + NY) : "memory");
  if (H && B6) { __builtin_amdgcn_s_barrier(); ST(xin != OFF_X1, 14); asm volatile("" ::: "memory"); }
  ld_(0, 1); ld_(1, 1);
  if (ALL || (H && B6 == 1)) first_();
  second_();
  if (NT == 3) {
    asm volatile("s_waitcnt vmcnt(%0)" :: "n"(NY) : "memory");
    ld_(2, 0); ld_(2, 1);
    third_();
  }
  if (__builtin_expect(__builtin_amdgcn_ballot_w64(s != s) != 0ull, 0)) {
    for (unsigned spins = 0;; ++spins) {
      FAILC((xin == OFF_X1 ? 1 : 9) + H);
      if (H) *(u32*)(smem + L_MI + M_RD) = xin == OFF_X1 ? 1 : 2;
      x_dma(wrs, xin, rb, tile, a, lane);
      asm volatile("s_waitcnt vmcnt(0)" ::: "memory");
      s = 0.f;
#pragma unroll
      for (int o = 0; o < 2 * NT; ++o) ld_(o >> 1, o & 1);
      zero_();
      first_(); second_();
      if (NT == 3) third_();
      if (!__any(s != s)) break;
      bool st = false;
#pragma unroll 1
      for (int e = 0; e < 4 * NT; ++e) { const u32x4 v = *(const u32x4*)(smem + (e < 8 ? tile + a * 8192 : L_TH + a * 4096 - 8192) + lane * 16 + e * 1024); st |= v[1] == sent || v[3] == sent; }
      if (!__any(st) || spins > SPIN_CAP) break;
    }
  }
}
#define PIN(x_) asm volatile("" : "+v"(x_))
struct HaloMsg { u32x4 k, v; };
__device__ __forceinline__ void halo_ld(HaloMsg& h, rsrc_t wrs, int ho) { h.k = ldw(wrs, ho); h.v = ldw(wrs, ho + 1024); }
__device__ __forceinline__ bool halo_ok(const HaloMsg& h, u32 tag) {
  return __builtin_amdgcn_ballot_w64((opaque((h.k[1] ^ tag) | (h.k[3] ^ tag)) | opaque((h.v[1] ^ tag) | (h.v[3] ^ tag))) != 0u) == 0ull;
}
struct AttC { f32x4 mp, mo, mf; u32x2 one; u32 sl, su; };
__device__ __forceinline__ void att_pre(AttC& C, int lane, bool first) {
  const float NINF = -__builtin_inff();
  const u64 pm[4] = {0x0fff00ff000f0000ull, 0x3fff03ff003f0003ull, 0xf000ff00fff0ffffull, 0xc000fc00ffc0fffcull};
#pragma unroll
  for (int e = 0; e < 4; ++e) {
    const u64 par = (e & 1) ? 0xAAAAAAAAAAAAAAAAull : 0x5555555555555555ull;
    C.mp[e] = __builtin_amdgcn_inverse_ballot_w64(pm[e >> 1] & par) ? 0.f : NINF;
    C.mo[e] = __builtin_amdgcn_inverse_ballot_w64(pm[2 + (e >> 1)] & par) ? 0.f : NINF;
    C.mf[e] = first ? NINF : C.mp[e];
  }
  C.one = (u32x2){0x3F803F80u, 0x3F803F80u};
  C.sl = (lane & 1) ? 0x01000c0cu : 0x0c0c0100u; C.su = (lane & 1) ? 0x03020c0cu : 0x0c0c0302u;
  asm volatile("" : "+v"(C.mp), "+v"(C.mo), "+v"(C.mf), "+v"(C.one), "+v"(C.sl), "+v"(C.su));
}
template <class F>
__device__ __forceinline__ void attention(const AttC& C, u32 qp, HaloMsg& hm, int ho, u32 tag, rsrc_t wrs, int oo, int fid, F mid) {
  u32x4 vv, ow;
  u32x2 q0;
  f32x4 l0, o1;
  float inv;
#define SOFT(a_, b_, c_, d_, F1_, F2_, W_, V_, O_)                                                                                                      \
  F1_ "v_permlane16_swap_b32 v232, v233\n\tv_max_f32 v234, v232, v233\n\tv_max_f32 v235, v232, v233\n\t"                                                \
  F2_ "v_permlane32_swap_b32 v234, v235\n\tv_max_f32 v232, v234, v235\n\tv_mul_f32 v232, 0xbeb8aa3b, v232\n\t"                                          \
  "v_fmamk_f32 " a_ ", " a_ ", 0x3eb8aa3b, v232\n\tv_fmamk_f32 " b_ ", " b_ ", 0x3eb8aa3b, v232\n\t"                                                    \
  "v_fmamk_f32 " c_ ", " c_ ", 0x3eb8aa3b, v232\n\tv_fmamk_f32 " d_ ", " d_ ", 0x3eb8aa3b, v232\n\t"                                                    \
  "v_exp_f32 " a_ ", " a_ "\n\tv_exp_f32 " b_ ", " b_ "\n\tv_exp_f32 " c_ ", " c_ "\n\tv_exp_f32 " d_ ", " d_ "\n\t"                                    \
  "v_cvt_pk_bf16_f32 v232, " a_ ", " b_ "\n\tv_cvt_pk_bf16_f32 v233, " c_ ", " d_ "\n\t"                                                                \
  "v_perm_b32 v226, v232, v232, %[sl]\n\tv_perm_b32 v227, v232, v232, %[su]\n\t"                                                                        \
  "v_mfma_f32_16x16x16_bf16 v[236:239], %[one], v[232:233], 0\n\t"                                                                                      \
  "v_perm_b32 v224, v233, v233, %[sl]\n\tv_perm_b32 v225, v233, v233, %[su]\n\t" W_ "\n\ts_nop 0\n\t"                                                   \
  "v_mfma_f32_16x16x32_bf16 " O_ ", " V_ ", v[224:227], 0\n\t"
  asm volatile(
      "ds_read2st64_b32 v[214:215], %[qp] offset0:4 offset1:5\n\tds_read2st64_b32 v[216:217], %[qp] offset0:6 offset1:7\n\t"
      "ds_read2st64_b32 v[212:213], %[qp] offset0:0 offset1:1\n\tds_read2st64_b32 v[218:219], %[qp] offset0:10 offset1:11\n\t"
      "ds_read2st64_b32 v[200:201], %[qp] offset0:2 offset1:3\n\tds_read2st64_b32 v[202:203], %[qp] offset0:8 offset1:9\n\t"
      "s_waitcnt lgkmcnt(4)\n\tv_mfma_f32_16x16x16_bf16 v[220:223], v[216:217], v[214:215], %[mp]\n\t"
      "s_waitcnt lgkmcnt(3)\n\tv_mfma_f32_16x16x16_bf16 v[224:227], v[212:213], v[214:215], %[mo]\n\t"
      "s_waitcnt lgkmcnt(2)\n\tv_mfma_f32_16x16x16_bf16 v[228:231], v[216:217], v[218:219], %[mo]\n\t"
      "v_max_f32 v220, v220, v221\n\tv_max_f32 v221, v222, v223\n\tv_max_f32 v232, v220, v221\n\t"
      "v_max_f32 v222, v224, v225\n\tv_max_f32 v223, v226, v227\n\t"
      "v_max3_f32 v233, v232, v222, v223\n\tv_max3_f32 v232, v232, v222, v223\n\t"
      SOFT("v220", "v221", "v222", "v223", "s_nop 0\n\tv_max_f32 v228, v228, v229\n\t", "v_max_f32 v229, v230, v231\n\tv_max_f32 v230, v228, v229\n\t", "s_waitcnt lgkmcnt(0)", "v[200:203]", "v[244:247]")
      "v_rcp_f32 v248, v236"
      : "=&{v[200:203]}"(vv), "=&{v[218:219]}"(q0), "=&{v[228:231]}"(l0), "=&{v[244:247]}"(o1), "=&{v248}"(inv)
      : [qp] "v"(qp), [mp] "v"(C.mp), [mo] "v"(C.mo), [one] "v"(C.one), [sl] "v"(C.sl), [su] "v"(C.su)
      : "memory", "v212", "v213", "v214", "v215", "v216", "v217", "v220", "v221", "v222", "v223", "v224", "v225", "v226", "v227", "v232", "v233", "v234", "v235", "v236", "v237", "v238", "v239");
  mid();
  {
    u32 n = SPIN_CAP;
    asm volatile("1:\n\tv_bitop3_b32 v232, v209, v211, %[tg] bitop3:0x7e\n\tv_bitop3_b32 v233, v205, v207, %[tg] bitop3:0x7e\n\tv_or_b32 v232, v232, v233\n\tv_cmp_ne_u32 vcc, 0, v232\n\t"
                 "s_cbranch_vccz 2f\n\ts_sleep 2\n\tbuffer_load_dwordx4 v[208:211], %[ho], %[rs], 0 offen sc1\n\tbuffer_load_dwordx4 v[204:207], %[ho], %[rs], 0 offen offset:1024 sc1\n\t"
                 "s_sub_u32 %[n], %[n], 1\n\ts_waitcnt vmcnt(0)\n\ts_cbranch_scc0 1b\n2:"
                 : "+{v[208:211]}"(hm.k), "+{v[204:207]}"(hm.v), [n] "+s"(n) : [tg] "s"(tag), [ho] "v"(ho), [rs] "s"(wrs) : "memory", "vcc", "scc", "v232", "v233");
  }
  asm volatile(
      "v_mov_b32 v209, v210\n\tv_mov_b32 v205, v206\n\ts_nop 0\n\tv_mfma_f32_16x16x16_bf16 v[220:223], v[208:209], v[218:219], %[mf]\n\t"
      "v_mul_f32 v244, v244, v248\n\tv_mul_f32 v245, v245, v248\n\tv_mul_f32 v246, v246, v248\n\tv_mul_f32 v247, v247, v248\n\t"
      "v_cvt_pk_bf16_f32 v242, v244, v245\n\tv_cvt_pk_bf16_f32 v243, v246, v247\n\ts_nop 1\n\t"
      "v_max_f32 v220, v220, v221\n\tv_max_f32 v221, v222, v223\n\t"
      "v_max3_f32 v233, v230, v220, v221\n\tv_max3_f32 v232, v230, v220, v221\n\t"
      SOFT("v220", "v221", "v228", "v229", "s_nop 1\n\t", "s_nop 1\n\t", "s_nop 0", "v[202:205]", "v[244:247]")
      "v_rcp_f32 v248, v236\n\ts_nop 6\n\t"
      "v_mul_f32 v244, v244, v248\n\tv_mul_f32 v245, v245, v248\n\tv_mul_f32 v246, v246, v248\n\tv_mul_f32 v247, v247, v248\n\t"
      "v_cvt_pk_bf16_f32 v240, v244, v245\n\tv_cvt_pk_bf16_f32 v241, v246, v247\n\ts_nop 0\n\t"
      "v_permlane16_swap_b32 v240, v242\n\tv_permlane16_swap_b32 v241, v243"
      : "=&{v[240:243]}"(ow), "+{v[208:211]}"(hm.k), "+{v[204:207]}"(hm.v), "+{v[228:231]}"(l0), "+{v[244:247]}"(o1), "+{v248}"(inv)
      : "{v[200:203]}"(vv), "{v[218:219]}"(q0), [mf] "v"(C.mf), [one] "v"(C.one), [sl] "v"(C.sl), [su] "v"(C.su)
      : "v220", "v221", "v222", "v223", "v224", "v225", "v226", "v227", "v232", "v233", "v234", "v235", "v236", "v237", "v238", "v239");
  stw(wrs, oo, ow);
  asm volatile("s_nop 1" :: "v"(ow) : "memory");
}
__device__ __forceinline__ void o_dma(rsrc_t wrs, int og, int ot, int a, int lane) {
  dma4k(wrs, lane * 16, og + a * 4096, ot + a * 4096);
  dma4k(wrs, lane * 16, og + 16384 + a * 4096, ot + 16384 + a * 4096);
}
__device__ __forceinline__ void o_get(rsrc_t wrs, int vo, int g0, int g1, int ot, u32 sa, u32 mark, int fid) {
#pragma unroll 1
  for (unsigned spins = 0; spins <= SPIN_CAP; ++spins) {
    dma4k(wrs, vo, g0, ot);
    dma4k(wrs, vo, g1, ot + 16384);
    asm volatile("s_waitcnt vmcnt(4)" ::: "memory");
    if (__builtin_amdgcn_ballot_w64(*(const LAS u32*)(size_t)sa == mark) == 0ull) break;
    FAILC(fid);
  }
}
template <bool L1, class F>
__device__ __forceinline__ void wo_mma2(f32x4 (&acc)[2], char* smem, int ot, rsrc_t wrs, int og, int a, int lane, const bf16x8 (&wof)[16], u32 cnt, const f32x4 (&r0)[2], const u32 (&qa)[4], u32 sent, F late) {
  u32x4 of[32];
#define LDO2(s_) of[s_] = *(const LAS u32x4*)((const LAS char*)(size_t)qa[(s_) >> 3] + (ot - L_TA) + ((s_) & 1) * 16384 + (((s_) >> 1) & 3) * 1024)
#define WMM(s_) do { SB(); if ((s_) < 2) asm volatile("v_mfma_f32_16x16x32_bf16 %0, %1, %2, %3" : "=&v"(acc[(s_) & 1]) \
        : "v"(L1 ? __builtin_bit_cast(bf16x8, of[s_]) : wof[0]), "v"(L1 ? wof[0] : __builtin_bit_cast(bf16x8, of[s_])), "v"(r0[(s_) & 1])); \
      else if (L1) asm volatile("v_mfma_f32_16x16x32_bf16 %0, %1, %2, %0" : "+v"(acc[(s_) & 1]) : "v"(of[s_]), "v"(wof[(s_) >> 1])); \
      else asm volatile("v_mfma_f32_16x16x32_bf16 %0, %1, %2, %0" : "+v"(acc[(s_) & 1]) : "v"(wof[(s_) >> 1]), "v"(of[s_])); SB(); } while (0)
  u32 mx = 0, cv;
#pragma unroll
  for (int s = 0; s < 8; ++s) { LDO2(s); SB(); }
#pragma unroll
  for (int s = 0; s < 3; ++s) {
    WMM(s);
#pragma unroll
    for (int e = 3 * s; e < 3 * s + 3 && e < 8; ++e) mx = max(max(mx, of[e][1]), of[e][3]);
    asm volatile("" : "+v"(mx));
  }
  if (__builtin_expect(!__all(mx < 0xFF810000u), 0)) {
    for (unsigned spins = 0; spins <= SPIN_CAP; ++spins) {
      FAILC((L1 ? 5 : 13) + (ot == L_TB));
      __builtin_amdgcn_s_sleep(2);
      o_dma(wrs, og, ot, a, lane);
      asm volatile("s_waitcnt vmcnt(0)" ::: "memory");
      bool st = false;
#pragma unroll
      for (int s = 0; s < 8; ++s) { LDO2(s); st |= of[s][1] == sent || of[s][3] == sent; }
      if (!__any(st)) break;
    }
    WMM(0); WMM(1); WMM(2);
    asm volatile("s_nop 7" : "+v"(acc[0]), "+v"(acc[1]));
  }
  cnt_add(smem, C_OT, lane);
  cv = *(const volatile LAS u32*)(size_t)(u32)(L_MI + C_OT);
  SB();
#pragma unroll
  for (int s = 3; s < WS5; ++s) WMM(s);
  {
    u32 n = 1u << 24;
    asm volatile("1:\n\tv_readfirstlane_b32 vcc_lo, %0\n\ts_sub_u32 %1, %1, 1\n\ts_cbranch_scc1 2f\n\ts_cmp_ge_u32 vcc_lo, %3\n\ts_cbranch_scc1 2f\n\tds_read_b32 %0, %2\n\ts_waitcnt lgkmcnt(0)\n\ts_branch 1b\n2:"
                 : "+v"(cv), "+s"(n) : "v"(L_MI + C_OT), "s"(cnt) : "memory", "vcc", "scc");
  }
#pragma unroll
  for (int s = 8; s < WS5 + 8; ++s) { LDO2(s); SB(); }
#pragma unroll
  for (int s = WS5; s < 32; ++s) {
    WMM(s);
    if (s + 8 < 32) LDO2(s + 8);
    if (s == 24) late();
  }
  asm volatile("s_nop 7" : "+v"(acc[0]), "+v"(acc[1]));
}
template <class F>
__device__ __forceinline__ void wo_last(f32x4 (&acc)[2], char* smem, rsrc_t wrs, int og, int a, int lane, const bf16x8 (&wof)[16], const f32x4 (&r0)[2], const u32 (&qa)[4], u32 sent, F put) {
  u32x4 of[32];
#define LDO3(q_, o_, s_) of[s_] = *(const LAS u32x4*)((const LAS char*)(size_t)q_[((s_) >> 2) & 3] + (o_) + ((s_) >> 4) * 16384 + ((s_) & 3) * 1024)
#define WM3(s_) do { SB(); if (((s_) & 15) == 0) asm volatile("v_mfma_f32_16x16x32_bf16 %0, %1, %2, %3" : "=&v"(acc[(s_) >> 4]) : "v"(wof[0]), "v"(of[s_]), "v"(r0[(s_) >> 4])); \
      else asm volatile("v_mfma_f32_16x16x32_bf16 %0, %1, %2, %0" : "+v"(acc[(s_) >> 4]) : "v"(wof[(s_) & 15]), "v"(of[s_])); SB(); } while (0)
  u32 qb[4];
#pragma unroll
  for (int e = 0; e < 4; ++e) qb[e] = qa[e];
  SB();
  __builtin_amdgcn_s_barrier();
  ST(1, 19);
#pragma unroll 1
  for (u32 pass = 0;; ++pass) {
    asm volatile("" : "+v"(qb[0]), "+v"(qb[1]), "+v"(qb[2]), "+v"(qb[3]) :: "memory");
#pragma unroll
    for (int s = 0; s < WLD; ++s) { LDO3(qb, L_TB - L_TA, s); SB(); }
#pragma unroll
    for (int s = 0; s < 32; ++s) {
      WM3(s);
      if (s + WLD == 16) asm volatile("s_cmp_lg_u32 %0, 0\n\ts_cbranch_scc1 1f\n\ts_barrier\n1:" :: "s"(__builtin_amdgcn_readfirstlane(pass)) : "memory", "scc");
      if (s + WLD < 32) LDO3(qb, L_TB - L_TA, s + WLD);
    }
    asm volatile("s_nop 7" : "+v"(acc[0]), "+v"(acc[1]));
    put();
    SB();
    if (__builtin_expect(__builtin_amdgcn_ballot_w64(__builtin_isunordered(acc[0][0], acc[1][0])) == 0ull, 1)) break;
    asm volatile("s_waitcnt vmcnt(0)" ::: "memory");
    FAILC(14);
    if (pass == 0) {
      lds_barrier();
      const u32 fx = *(const u32*)(smem + L_MI + M_FX);
#pragma unroll
      for (int e = 0; e < 4; ++e) qb[e] -= ((fx >> (8 * ((a + e) & 3))) & 1u) * (L_TB - L_TA);
    } else {
      u32 m = 1;
#pragma unroll 1
      for (int s = 0; s < 32; ++s) {
        const u32x4 v = *(const LAS u32x4*)(size_t)((s & 8 ? (s & 4 ? qb[3] : qb[2]) : (s & 4 ? qb[1] : qb[0])) + (L_TB - L_TA) + (s >> 4) * 16384 + (s & 3) * 1024);
        m = min(min(m, v[1] ^ sent), v[3] ^ sent);
      }
      if (__builtin_amdgcn_ballot_w64(m == 0u) == 0ull || pass > SPIN_CAP) break;
      lds_barrier();
      o_dma(wrs, og, __builtin_amdgcn_readfirstlane(qb[0]) + (L_TB - L_TA) - a * 4096, a, lane);
      asm volatile("s_waitcnt vmcnt(0)" ::: "memory");
      lds_barrier();
    }
  }
}
struct WoPre { f32x4 r[2]; int so; u32 xs, ao; };
template <bool L1>
__device__ __forceinline__ void wo_pre(WoPre& P, const u32x2 (&res)[2], char* ws, int H, int rb, int hb, int a, int lane) {
  const int i16 = lane & 15, kg = lane >> 4, R0 = 1024 * H + 32 * rb;
#pragma unroll
  for (int t = 0; t < 2; ++t) P.r[t] = (f32x4){lo2f(res[t][0]), hi2f(res[t][0]), lo2f(res[t][1]), hi2f(res[t][1])};
  if (L1) {
    P.so = OFF_X2 + ((32 * hb + 8 * a + (i16 >> 1)) * XS_ + R0 + 16 * (lane & 1) + 4 * kg) * 4;
    P.xs = (u32)(L_XS + H * 512 + lane * 8);
    P.ao = (u32)(OFF_ST2 + ((R0 & ~255) + ((lane >> 1) & 3) * 64 + ((R0 >> 2) & 63) + 4 * (lane & 1) + kg) * 16 + (lane & 8));
    asm volatile("" : "+v"(P.r[0]), "+v"(P.r[1]), "+v"(P.so), "+v"(P.xs), "+v"(P.ao));
  } else {
    P.so = ((R0 + 16 * (kg & 1) + i16) * 256 + 32 * hb + 8 * a + 4 * (kg >> 1)) * 4;
    asm volatile("" : "+v"(P.r[0]), "+v"(P.r[1]), "+v"(P.so));
  }
}
template <bool L1>
__device__ __forceinline__ void wo_epi(const f32x4 (&acc)[2], const WoPre& P, char* smem, rsrc_t wrs, char* ws, u32* out, int H, int lane, u32 ONES) {
  if (L1) {
    f32x4 lo_, hi_;
    pair_x(lo_, hi_, acc[0], acc[1]);
    u32x4 v;
#pragma unroll
    for (int e = 0; e < 4; ++e) v[e] = pk2(lo_[e], hi_[e]);
    asm volatile("" : "+v"(v));
    stw(wrs, P.so, v);
    SB();
    float sv, q0, q1, q2, q3;
#define RD_(d_, a_, c_, m_) "v_add_f32_dpp " d_ ", " a_ ", " a_ " " c_ " row_mask:0xf bank_mask:" m_ "\n\t"
    asm volatile("v_dot2_f32_bf16 %5, %13, %17, 0\n\tv_dot2_f32_bf16 %6, %14, %17, 0\n\tv_dot2_f32_bf16 %7, %15, %17, 0\n\tv_dot2_f32_bf16 %8, %16, %17, 0\n\t"
                 "v_dot2_f32_bf16 %9, %13, %13, 0\n\tv_dot2_f32_bf16 %10, %14, %14, 0\n\tv_dot2_f32_bf16 %11, %15, %15, 0\n\tv_dot2_f32_bf16 %12, %16, %16, 0\n\t"
                 RD_("%1", "%5", "row_ror:8", "0x3") RD_("%2", "%6", "row_ror:8", "0x3") RD_("%3", "%7", "row_ror:8", "0x3") RD_("%4", "%8", "row_ror:8", "0x3")
                 RD_("%1", "%9", "row_ror:8", "0xc") RD_("%2", "%10", "row_ror:8", "0xc") RD_("%3", "%11", "row_ror:8", "0xc") RD_("%4", "%12", "row_ror:8", "0xc")
                 RD_("%5", "%1", "row_shl:4", "0x5") RD_("%6", "%2", "row_shl:4", "0x5") RD_("%5", "%3", "row_shr:4", "0xa") RD_("%6", "%4", "row_shr:4", "0xa")
                 "s_nop 0\n\t"
                 RD_("%1", "%5", "quad_perm:[2,3,0,1]", "0xf") RD_("%2", "%6", "quad_perm:[2,3,0,1]", "0xf")
                 "v_cndmask_b32_e64 %0, %1, %2, %18"
                 : "=&v"(sv), "=&v"(q0), "=&v"(q1), "=&v"(q2), "=&v"(q3), "=&v"(lo_[0]), "=&v"(lo_[1]), "=&v"(lo_[2]), "=&v"(lo_[3]), "=&v"(hi_[0]), "=&v"(hi_[1]), "=&v"(hi_[2]), "=&v"(hi_[3])
                 : "v"(v[0]), "v"(v[1]), "v"(v[2]), "v"(v[3]), "v"(ONES), "s"(0xCCCCCCCCCCCCCCCCull));
    const float hf = __builtin_floorf(sv * 0.5f);
    const u32 lo = (u32)(fmaf(sv, 0.5f, -hf) * 4294967296.0f);
    const u64 mine = ((u64)(u32)(int)hf << 32) | (u64)((lo & ~63u) | 1u);
    const u64 old = __hip_atomic_fetch_add((LAS u64*)(size_t)P.xs, mine, __ATOMIC_RELAXED, __HIP_MEMORY_SCOPE_WORKGROUP);
    if (((u32)old & 63u) == 3u) __hip_atomic_fetch_add((GAS u64*)((GAS char*)ws + P.ao), old + mine, __ATOMIC_RELAXED, __HIP_MEMORY_SCOPE_AGENT);
  } else {
    auto r0s = __builtin_amdgcn_permlane16_swap(pk2(acc[0][0], acc[0][1]), pk2(acc[1][0], acc[1][1]), false, false);
    auto r1s = __builtin_amdgcn_permlane16_swap(pk2(acc[0][2], acc[0][3]), pk2(acc[1][2], acc[1][3]), false, false);
    const rsrc_t ors = __builtin_amdgcn_make_buffer_rsrc((void*)out, 0, 2 * MB_, 0x00020000);
    if (H == 0) __builtin_amdgcn_raw_buffer_store_b128((u32x4){r0s[0], r1s[0], r0s[1], r1s[1]}, ors, P.so, 0, OUTA);
    else __builtin_amdgcn_raw_buffer_store_b128((u32x4){r0s[0], r1s[0], r0s[1], r1s[1]}, ors, P.so, 0, OUTB);
  }
}
template <bool L1>
__device__ __forceinline__ void res_ld(u32x2 (&res)[2], const char* smem, int tile, int hb, int a, int lane) {
#pragma unroll
  for (int t = 0; t < 2; ++t) {
    const int o = tile + t * 512 + (4 * hb + a) * 1024;
    if (L1) res[t] = *(const u32x2*)(smem + o + (lane & 15) * 32 + (lane >> 4) * 8);
    else res[t] = __builtin_bit_cast(u32x2, __builtin_amdgcn_ds_read_tr16_b64_v4i16((LAS bf16x4*)(size_t)(u32)(o + lane * 8)));
  }
}
template <bool L1>
__device__ __forceinline__ void a_blayer(char* smem, rsrc_t wrs, char* ws, u32* out, int t, int a, bf16x8 (&wof)[16], u32 tag, u32x4 wfl, ConvK& ck, XTake& xt, AttC& ac) {
  const u32 t9 = L1 ? 0u : KADD(2, now_(), D9);
  if (!L1) { KSLP(13, SLP_B4); SB(); }
  t = (int)opaque((u32)t);
  const int lane = t & 63, i16 = lane & 15, kg = lane >> 4;
  const int XIN = L1 ? OFF_X1 : OFF_X3, OBUF = L1 ? OFF_O1 : OFF_O2;
  const int bid = BIDX;
  const int rb = RB_OF(bid), hb = HB_OF(bid);
  if (!L1) { x_pre(xt, a, lane); att_pre(ac, lane, rb == 0); }
  const int og = OBUF + rb * 32768;
  u32x2 resa[2], resb[2];
  const int wfo = OFF_ST0 + 8192 + (hb * 4 + a) * 128 + (lane & 3) * 16;
#define LDWO(e_) wof[e_] = __builtin_bit_cast(bf16x8, ldws(wrs, ((hb * 64 + a) * 64 + lane) * 16, OFF_WO + ((4 * a + (e_)) & 15) * 4096))
  auto ld_wo = [&](int e0, int e1) __attribute__((always_inline)) {
#pragma unroll
    for (int e = e0; e < e1; ++e) LDWO(e);
    __builtin_amdgcn_sched_barrier(0);
  };
  const u32 mark = x0_mark(tag), sent = L1 ? mark : 0xFFFFFFFFu;
  if (L1) {
    tags_wait(wfl, wrs, wfo, tag, 6); ld_wo(0, WOE);
    SB();
    x_pre(xt, a, lane); SB();
    x_take<0, WOE, true>(xt, smem, wrs, XIN, rb, a, lane, sent);
  }
  ST(!L1, 10);
  SB();
  lds_barrier();
  ST(!L1, 11);
  const u32 t5 = L1 ? 0u : KADD(20, now_(), D5);
  res_ld<L1>(resa, smem, L_TA, hb, a, lane);
  auto pf_x3 = [&]() __attribute__((always_inline)) {
    const u32x4 SENT = {0xFFFFFFFFu, 0xFFFFFFFFu, 0xFFFFFFFFu, 0xFFFFFFFFu};
    const int xo = OFF_X3 + (8 * a + (lane >> 3)) * XBS + ((bid & 7) * 32 + (bid >> 3)) * 128 + (lane & 7) * 16;
    stw(wrs, xo, SENT); stw(wrs, xo + 32 * XBS, SENT);
  };
  const int oo = (int)opaque((u32)(OBUF + (((rb * 2 + (kg & 1)) * 16 + hb * 2 + (a >> 1)) * 64 + (2 * (a & 1) + (kg >> 1)) * 16 + i16) * 16));
  const u32 ONES = opaque(0x3F803F80u);
  if (L1) att_pre(ac, lane, rb == 0);
  if (L1) pf_x3();
  if (L1) SB();
  const int ho = (int)opaque((u32)((rb == 0 ? WSB : KVO(L1, rb, hb * 4 + a)) + lane * 16));
  const u32 qp = opaque((u32)(L_QB + a * 3072 + lane * 4));
  const int tso = (int)opaque((u32)(OFF_TS + rb * 128 + (lane & 7) * 8)), tsw = (int)opaque((u32)(t == hb ? tso : WSB));
  HaloMsg hm;
  SB();
  if (L1) KSLP(1, SLP_XB1); else gate(tmax(t9, t5));
  ST(!L1, 12);
  x_take<1, 0, false>(xt, smem, wrs, XIN, 32 + rb, a, lane, sent);
  ST(!L1, 13);
  if (L1) SB();
  lds_barrier();
  ST(!L1, 15);
  if (L1) KSLP(3, SLP_H1); else KSLP(4, SLP_H2);
  halo_ld(hm, wrs, ho);
  u32 t7 = 0, tq = 0;
  if (!L1) {
    t7 = (u32)__builtin_amdgcn_s_memrealtime();
    tq = KADD(6, now_(), DQ2);
    __builtin_amdgcn_raw_buffer_store_b64((u32x2){t7, tag}, wrs, tsw, 0, 16);
  }
  SB();
  attention(ac, qp, hm, ho, rb == 0 ? 0u : L1 ? mark : tag, wrs, oo, L1 ? 3 : 11, [&]() __attribute__((always_inline)) { if (L1) SB(); });
  const u32 to = L1 ? 0u : KADD(21, now_(), DO2);
  ST(!L1, 16);
  if (L1) __builtin_amdgcn_raw_buffer_store_b32(tag, wrs, (bid | t) == 0 ? OFF_EP : WSB, 0, 16);
  if (L1) ld_wo(WOE, 16);
  res_ld<L1>(resb, smem, L_TB, hb, a, lane);
  cnt_add(smem, C_OB, lane);
  WoPre wp, wq;
  u32 qa[4];
#pragma unroll
  for (int e = 0; e < 4; ++e) qa[e] = opaque((u32)(L_TA + ((a + e) & 3) * 4096 + lane * 16));
  wo_pre<L1>(wp, resa, ws, 0, rb, hb, a, lane);
  wo_pre<L1>(wq, resb, ws, 1, rb, hb, a, lane);
  const u32 sa = opaque((u32)(L_TA + a * 4096 + lane * 64 + (lane & 14) * 4 + 4));
  SB();
  if (L1) KSLP(5, SLP_OA1); else gate(tmax(tq, to));
  ST(!L1, 17);
  u32x2 ts;
  if (!L1) asm volatile("buffer_load_dwordx2 %0, %1, %2, 0 offen sc1" : "=&{v[200:201]}"(ts) : "v"(tso), "s"(wrs) : "memory");
  o_get(wrs, lane * 16, og + a * 4096, og + 16384 + a * 4096, L_TA + a * 4096, sa, mark, L1 ? 21 : 23);
  if (!L1) {
    int d;
    asm volatile("v_subrev_u32 %0, %2, v200\n\tv_cmp_eq_u32 vcc, %3, v201\n\tv_min_i32 %0, 0x96, %0\n\tv_cndmask_b32 %0, 64, %0, vcc\n\t"
                 "s_nop 1\n\tv_max_i32_dpp %0, %0, %0 quad_perm:[1,0,3,2] row_mask:0xf bank_mask:0xf\n\ts_nop 1\n\tv_max_i32_dpp %0, %0, %0 quad_perm:[2,3,0,1] row_mask:0xf bank_mask:0xf\n\t"
                 "s_nop 1\n\tv_max_i32_dpp %0, %0, %0 row_half_mirror row_mask:0xf bank_mask:0xf\n\tv_max_i32 %0, 0, %0\n\tv_mul_u32_u24 %0, 20, %0"
                 : "=&v"(d), "+{v[200:201]}"(ts) : "s"(t7), "s"(tag) : "vcc", "memory");
    *(int*)(smem + L_MI + M_DL) = d;
    cnt_add(smem, C_OV, lane);
  }
  u32 ob = 0;
  int ob0 = og + MB_ + a * 4096, ob1 = ob0 + 16384;
  if (L1) asm volatile("" : "+s"(ob0), "+s"(ob1));
  asm volatile("s_waitcnt vmcnt(0)" ::: "memory");
  f32x4 acc[2];
  wo_mma2<L1>(acc, smem, L_TA, wrs, og, a, lane, wof, L1 ? 4 : 12, wp.r, qa, mark, [&]() __attribute__((always_inline)) { if (L1) ob = *(const volatile LAS u32*)(size_t)(u32)(L_MI + C_OB); });
  ST(!L1, 18);
  wo_epi<L1>(acc, wp, smem, wrs, ws, out, 0, lane, ONES);
  if (L1) {
    cnt_add(smem, C_X2, lane);
    if (__builtin_expect((u32)__builtin_amdgcn_readfirstlane(ob) < 8u, 0)) cnt_spin(smem, C_OB, 8);
    dma4k(wrs, lane * 16, ob0, L_TB + a * 4096);
    dma4k(wrs, lane * 16, ob1, L_TB + 16384 + a * 4096);
    a_conv_pre(ck, a, lane);
    ST(0, 19);
    asm volatile("s_waitcnt vmcnt(0)" ::: "memory");
    wo_mma2<L1>(acc, smem, L_TB, wrs, og + MB_, a, lane, wof, 8, wq.r, qa, mark, []() {});
    wo_epi<L1>(acc, wq, smem, wrs, ws, out, 1, lane, ONES);
  } else wo_last(acc, smem, wrs, og + MB_, a, lane, wof, wq.r, qa, mark, [&]() __attribute__((always_inline)) { wo_epi<L1>(acc, wq, smem, wrs, ws, out, 1, lane, ONES); });
  ST(!L1, 20); if (L1) ST(1, 1);
}

#define WREGS(X) \
  X(0, 76, 77, 78, 79) X(1, 80, 81, 82, 83) X(2, 84, 85, 86, 87) X(3, 88, 89, 90, 91) X(4, 92, 93, 94, 95) X(5, 96, 97, 98, 99) \
  X(6, 100, 101, 102, 103) X(7, 104, 105, 106, 107) X(8, 108, 109, 110, 111) X(9, 112, 113, 114, 115) X(10, 116, 117, 118, 119) X(11, 120, 121, 122, 123) \
  X(12, 124, 125, 126, 127) X(13, 128, 129, 130, 131) X(14, 132, 133, 134, 135) X(15, 136, 137, 138, 139) X(16, 140, 141, 142, 143) X(17, 144, 145, 146, 147) \
  X(18, 148, 149, 150, 151) X(19, 152, 153, 154, 155) X(20, 156, 157, 158, 159) X(21, 160, 161, 162, 163) X(22, 164, 165, 166, 167) X(23, 168, 169, 170, 171) \
  X(24, 172, 173, 174, 175) X(25, 176, 177, 178, 179) X(26, 180, 181, 182, 183) X(27, 184, 185, 186, 187) X(28, 188, 189, 190, 191) X(29, 192, 193, 194, 195) \
  X(30, 196, 197, 198, 199) X(31, 200, 201, 202, 203) X(32, 204, 205, 206, 207) X(33, 208, 209, 210, 211) X(34, 212, 213, 214, 215) X(35, 216, 217, 218, 219) \
  X(36, 220, 221, 222, 223) X(37, 224, 225, 226, 227) X(38, 228, 229, 230, 231) X(39, 232, 233, 234, 235) X(40, 236, 237, 238, 239) X(41, 240, 241, 242, 243) \
  X(42, 244, 245, 246, 247) X(43, 248, 249, 250, 251) X(44, 252, 253, 254, 255) X(45, 64, 65, 66, 67) X(46, 68, 69, 70, 71) X(47, 72, 73, 74, 75)
__device__ __forceinline__ void w_dma(rsrc_t r, int voff, int soff, int lds) {
  asm volatile("s_mov_b32 m0, %3\n\ts_nop 0\n\tbuffer_load_dwordx4 %0, %1, %2 offen lds\n\t"
               "s_add_u32 m0, m0, 3072\n\ts_nop 0\n\tbuffer_load_dwordx4 %0, %1, %2 offen offset:1024 lds\n\t"
               "s_add_u32 m0, m0, 3072\n\ts_nop 0\n\tbuffer_load_dwordx4 %0, %1, %2 offen offset:2048 lds"
               :: "v"(voff), "s"(r), "s"(soff), "s"(lds) : "memory", "scc");
}
template <int I>
__device__ __forceinline__ void w_ld(bf16x8& r, int vo, rsrc_t rs) {
#define X(n_, a_, b_, c_, d_) if constexpr (I == n_) asm volatile("buffer_load_dwordx4 %0, %1, %2, %3 offen offset:%4 sc1" : "={v[" #a_ ":" #d_ "]}"(r) : "v"(vo), "s"(rs), "s"(OFF_WQ + (I / 3) * 98304), "n"((I % 3) * 1024) : "memory");
  WREGS(X)
#undef X
}
template <int I, int O>
__device__ __forceinline__ void w_get(bf16x8& r, u32 a) {
#define X(n_, a_, b_, c_, d_) if constexpr (I == n_) asm volatile("ds_read_b64_tr_b16 v[" #a_ ":" #b_ "], %1 offset:%2\n\tds_read_b64_tr_b16 v[" #c_ ":" #d_ "], %1 offset:%3" : "=&{v[" #a_ ":" #d_ "]}"(r) : "v"(a), "n"(O), "n"(O + 2048) : "memory");
  WREGS(X)
#undef X
}
template <int I>
__device__ __forceinline__ void w_pin(const bf16x8& r) {
#define X(n_, a_, b_, c_, d_) if constexpr (I == n_) asm volatile("" :: "{v[" #a_ ":" #d_ "]}"(r));
  WREGS(X)
#undef X
}
template <int I>
__device__ __forceinline__ void w_sum(f32x4& d, const bf16x8& one, u32 idx, const bf16x8 (&wf)[48]) {
  w_pin<I>(wf[I]); w_pin<(I + 1) % 48>(wf[(I + 1) % 48]);
  asm volatile("v_smfmac_f32_16x16x64_bf16 %0, %1, v[%3:%3+7], %2" : "+v"(d) : "v"(one), "v"(idx), "n"(I < 45 ? 76 + 4 * I : 64 + 4 * (I - 45)));
  w_pin<I>(wf[I]); w_pin<(I + 1) % 48>(wf[(I + 1) % 48]);
}
struct GPre { u32 ap, l4, fr, fq, sp, wx, mo, oo, tg, ly; };
template <bool L1>
__device__ __forceinline__ void g_pre(GPre& P, int g, u32 tag) {
  const int lane = lane_id(), bid = BIDX;
  const int rb = RB_OF(bid), hb = HB_OF(bid);
  P.mo = (u32)KVO(L1, (rb + 1) & 31, hb * 4 + g);
  P.oo = (u32)((L1 ? OFF_O1 : OFF_O2) + MB_ + ((rb * 32 + hb * 2 + (g >> 1)) * 64 + 32 * (g & 1)) * 16);
  P.tg = L1 ? x0_mark(tag) : tag;
  P.ly = L1 ? 1 : 2;
  asm volatile("" : "+{s60}"(P.mo), "+{s61}"(P.oo), "+{s62}"(P.tg), "+{s63}"(P.ly));
  P.ap = (u32)(L_TA + lane * 8);
  P.l4 = (u32)(lane * 4);
  P.fr = (u32)(L_FS + g * 768 + (lane & 15) * 4);
  P.fq = (u32)(L_FS + g * 768 + (lane >> 4) * 16);
  P.sp = (u32)(L_SP + (lane & 31) * 16);
  P.wx = (u32)(L_WX + g * 3072 + lane * 16);
  asm volatile("" : "+{v0}"(P.ap), "+{v1}"(P.l4), "+{v2}"(P.fr), "+{v3}"(P.fq), "+{v4}"(P.sp), "+{v5}"(P.wx));
}
template <bool L1, class F>
__device__ __forceinline__ void g_blayer(char* smem, rsrc_t wrs, int g, u32 tag, bf16x8 (&wf)[48], GPre& P, F mid) {
  if (!L1) ST(1, 10);
  auto pin = [&](auto I_) __attribute__((always_inline)) { w_pin<decltype(I_)::value>(wf[decltype(I_)::value]); };
  static_for<0, 45>(pin);
#define G_OPS : "+{v0}"(P.ap), "+{v1}"(P.l4), "+{v2}"(P.fr), "+{v3}"(P.fq), "+{v4}"(P.sp), "+{v5}"(P.wx) \
  : "{s[56:59]}"(wrs), "{s60}"(P.mo), "{s61}"(P.oo), "{s62}"(P.tg), "{s63}"(P.ly), "{s64}"(L_QB + g * 3072), "{s65}"(L_FS + g * 768), "{s66}"(L_WX + g * 3072) \
  : "memory", "scc", "vcc", "v6", "v7", "v8", "v9", "v10", "v11", "v12", "v13", "v14", "v15", "v16", "v17", "v18", "v19", "v20", "v21", "v22", "v23", "v24", "v25", "v26", "v27", "v28", "v29", "v30", "v31", "v32", "v33", "v34", "v35", "v36", "v37", "v38", "v39", "v40", "v41", "v42", "v43", "v44", "v45", "v46", "v47", "v48", "v49", "v50", "v51", "v52", "v53", "v54", "v55", "v56", "v57", "v58", "v59", "v60", "v61", "v62", "v63", "v64", "v65", "v66", "v67", "v68", "v69", "v70", "v71", "v72", "v73", "v74", "v75", "s54", "s55", "s67", "s68", "s69", "s70", "s71", "s72", "s73", "s74", "s75", "s76", "s77"
  if constexpr (L1) asm volatile(".Lg_body:\n\t"
  "ds_read_b128 v[56:59], v5 offset:1024\n\tds_read_b128 v[40:43], v5 offset:2048\n\tds_read_b128 v[16:19], v5 offset:0\n\t"
  "s_waitcnt lgkmcnt(3)\n\ts_barrier\n\t" GST(11)
  "ds_read_b64_tr_b16 v[28:29], v0 offset:31232\n\tds_read_b64_tr_b16 v[30:31], v0 offset:32256\n\tds_read_b64_tr_b16 v[48:49], v0 offset:512\n\tds_read_b64_tr_b16 v[50:51], v0 offset:1536\n\t"
  "ds_read_b128 v[64:67], v4\n\tds_read_b128 v[72:75], v4 offset:768\n\tds_read_b64_tr_b16 v[36:37], v0 offset:2560\n\tds_read_b64_tr_b16 v[38:39], v0 offset:3584\n\t"
  "ds_read_b64_tr_b16 v[24:25], v0 offset:4608\n\tds_read_b64_tr_b16 v[26:27], v0 offset:5632\n\ts_mov_b32 s68, 0xaaaaaaaa\n\ts_mov_b32 s69, 0xaaaaaaaa\n\t"
  "s_mov_b32 s70, 0xf0000\n\ts_mov_b32 s71, 0xfff00ff\n\ts_mov_b32 s72, 0x3f0003\n\ts_mov_b32 s73, 0x3fff03ff\n\t"
  "s_mov_b32 s74, 0xfff0ffff\n\ts_mov_b32 s75, 0xf000ff00\n\ts_mov_b32 s76, 0xffc0fffc\n\ts_mov_b32 s77, 0xc000fc00\n\t"
  "s_mov_b32 s67, 0x0c0c0100\n\ts_mov_b32 s55, 0x0c0c0302\n\ts_waitcnt lgkmcnt(8)\n\t"
  "v_mfma_f32_16x16x32_bf16 v[12:15], v[56:59], v[28:31], 0\n\tds_read_b64_tr_b16 v[4:5], v0 offset:6656\n\t"
  "v_mfma_f32_16x16x32_bf16 v[56:59], v[28:31], v[40:43], 0\n\tds_read_b64_tr_b16 v[6:7], v0 offset:7680\n\t"
  "v_mfma_f32_16x16x32_bf16 v[68:71], v[16:19], v[28:31], 0\n\ts_waitcnt lgkmcnt(2)\n\t"
  "v_mfma_f32_16x16x32_bf16 v[12:15], v[80:83], v[48:51], v[12:15]\n\tds_read_b64_tr_b16 v[44:45], v0 offset:8704\n\tv_add_f32 v64, v64, v65\n\t"
  "v_mfma_f32_16x16x32_bf16 v[56:59], v[48:51], v[84:87], v[56:59]\n\tds_read_b64_tr_b16 v[46:47], v0 offset:9728\n\tv_add_f32 v66, v66, v67\n\t"
  "v_mfma_f32_16x16x32_bf16 v[68:71], v[76:79], v[48:51], v[68:71]\n\tv_add_f32 v72, v72, v73\n\tv_add_f32 v64, v64, v66\n\t"
  "v_mfma_f32_16x16x32_bf16 v[12:15], v[92:95], v[36:39], v[12:15]\n\tds_read_b64_tr_b16 v[40:41], v0 offset:10752\n\tv_add_f32 v74, v74, v75\n\t"
  "v_mfma_f32_16x16x32_bf16 v[56:59], v[36:39], v[96:99], v[56:59]\n\tds_read_b64_tr_b16 v[42:43], v0 offset:11776\n\tv_mul_f32 v65, 0x3b000000, v64\n\t"
  "v_mfma_f32_16x16x32_bf16 v[68:71], v[88:91], v[36:39], v[68:71]\n\tv_add_f32 v72, v72, v74\n\tv_mul_f32_e64 v66, -v65, v65\n\t"
  "v_mfma_f32_16x16x32_bf16 v[12:15], v[104:107], v[24:27], v[12:15]\n\tds_read_b64_tr_b16 v[8:9], v0 offset:12800\n\tv_fmac_f32 v66, 0x3b000000, v72\n\t"
  "v_mfma_f32_16x16x32_bf16 v[56:59], v[24:27], v[108:111], v[56:59]\n\tds_read_b64_tr_b16 v[10:11], v0 offset:13824\n\tv_max_f32 v66, 0, v66\n\t"
  "v_mfma_f32_16x16x32_bf16 v[68:71], v[100:103], v[24:27], v[68:71]\n\tv_add_f32 v66, 0x3727c5ac, v66\n\ts_waitcnt lgkmcnt(4)\n\t"
  "v_mfma_f32_16x16x32_bf16 v[12:15], v[116:119], v[4:7], v[12:15]\n\tds_read_b64_tr_b16 v[28:29], v0 offset:14848\n\tv_add_u32 v67, s65, v1\n\t"
  "v_mfma_f32_16x16x32_bf16 v[56:59], v[4:7], v[120:123], v[56:59]\n\tds_read_b64_tr_b16 v[30:31], v0 offset:15872\n\t"
  "v_mfma_f32_16x16x32_bf16 v[68:71], v[112:115], v[4:7], v[68:71]\n\tv_rsq_f32 v66, v66\n\t"
  "v_mfma_f32_16x16x32_bf16 v[12:15], v[128:131], v[44:47], v[12:15]\n\tds_read_b64_tr_b16 v[4:5], v0 offset:16896\n\tds_write2st64_b32 v67, v66, v65 offset1:1\n\t"
  "v_mfma_f32_16x16x32_bf16 v[56:59], v[44:47], v[132:135], v[56:59]\n\tds_read_b64_tr_b16 v[6:7], v0 offset:17920\n\tds_read_b128 v[24:27], v3 offset:576\n\t"
  "v_mfma_f32_16x16x32_bf16 v[68:71], v[124:127], v[44:47], v[68:71]\n\tds_read2_b32 v[44:45], v2 offset0:64 offset1:80\n\ts_waitcnt lgkmcnt(7)\n\t"
  "v_mfma_f32_16x16x32_bf16 v[12:15], v[140:143], v[40:43], v[12:15]\n\tds_read_b64_tr_b16 v[16:17], v0 offset:18944\n\tds_read_b128 v[36:39], v3 offset:256\n\t"
  "v_mfma_f32_16x16x32_bf16 v[56:59], v[40:43], v[144:147], v[56:59]\n\tds_read_b64_tr_b16 v[18:19], v0 offset:19968\n\tds_read_b128 v[32:35], v3 offset:512\n\t"
  "v_mfma_f32_16x16x32_bf16 v[68:71], v[136:139], v[40:43], v[68:71]\n\tds_read_b32 v47, v2 offset:640\n\t"
  "v_mfma_f32_16x16x32_bf16 v[12:15], v[152:155], v[8:11], v[12:15]\n\tds_read_b64_tr_b16 v[20:21], v0 offset:20992\n\t"
  "v_mfma_f32_16x16x32_bf16 v[56:59], v[8:11], v[156:159], v[56:59]\n\tds_read_b64_tr_b16 v[22:23], v0 offset:22016\n\t"
  "v_mfma_f32_16x16x32_bf16 v[68:71], v[148:151], v[8:11], v[68:71]\n\ts_waitcnt lgkmcnt(9)\n\t"
  "v_mfma_f32_16x16x32_bf16 v[12:15], v[164:167], v[28:31], v[12:15]\n\tds_read_b64_tr_b16 v[64:65], v0 offset:23040\n\ts_waitcnt lgkmcnt(8)\n\t"
  "v_mfma_f32_16x16x32_bf16 v[56:59], v[28:31], v[168:171], v[56:59]\n\tds_read_b64_tr_b16 v[66:67], v0 offset:24064\n\tv_mul_f32_e64 v52, -v44, v24\n\t"
  "v_mfma_f32_16x16x32_bf16 v[68:71], v[160:163], v[28:31], v[68:71]\n\tv_mul_f32_e64 v53, -v44, v25\n\tv_mul_f32_e64 v54, -v44, v26\n\t"
  "v_mfma_f32_16x16x32_bf16 v[12:15], v[176:179], v[4:7], v[12:15]\n\tds_read_b64_tr_b16 v[48:49], v0 offset:25088\n\tv_mul_f32_e64 v55, -v44, v27\n\t"
  "v_mfma_f32_16x16x32_bf16 v[56:59], v[4:7], v[180:183], v[56:59]\n\tds_read_b64_tr_b16 v[50:51], v0 offset:26112\n\ts_waitcnt lgkmcnt(5)\n\t"
  "v_mfma_f32_16x16x32_bf16 v[68:71], v[172:175], v[4:7], v[68:71]\n\tv_mul_f32_e64 v72, -v36, v47\n\tv_mul_f32_e64 v73, -v37, v47\n\t"
  "v_mfma_f32_16x16x32_bf16 v[12:15], v[188:191], v[16:19], v[12:15]\n\t"
  GST(20)
  "ds_read_b64_tr_b16 v[8:9], v0 offset:27136\n\tds_read2_b32 v[40:41], v2 offset0:0 offset1:16\n\t"
  "v_mfma_f32_16x16x32_bf16 v[56:59], v[16:19], v[192:195], v[56:59]\n\tds_read_b64_tr_b16 v[10:11], v0 offset:28160\n\tv_mul_f32_e64 v74, -v38, v47\n\t"
  "v_mfma_f32_16x16x32_bf16 v[68:71], v[184:187], v[16:19], v[68:71]\n\tv_mul_f32_e64 v75, -v39, v47\n\ts_waitcnt lgkmcnt(5)\n\t"
  "v_mfma_f32_16x16x32_bf16 v[12:15], v[200:203], v[20:23], v[12:15]\n\tds_read_b64_tr_b16 v[16:17], v0 offset:29184\n\tv_mul_f32_e64 v60, -v44, v32\n\t"
  "v_mfma_f32_16x16x32_bf16 v[56:59], v[20:23], v[204:207], v[56:59]\n\tds_read_b64_tr_b16 v[18:19], v0 offset:30208\n\tv_mul_f32_e64 v61, -v44, v33\n\t"
  "v_mfma_f32_16x16x32_bf16 v[68:71], v[196:199], v[20:23], v[68:71]\n\tv_mul_f32_e64 v62, -v44, v34\n\tv_mul_f32_e64 v63, -v44, v35\n\t"
  "v_mfma_f32_16x16x32_bf16 v[12:15], v[212:215], v[64:67], v[12:15]\n\tds_read_b64_tr_b16 v[36:37], v0 offset:0\n\t"
  "v_mfma_f32_16x16x32_bf16 v[56:59], v[64:67], v[216:219], v[56:59]\n\tds_read_b64_tr_b16 v[38:39], v0 offset:1024\n\t"
  "v_mfma_f32_16x16x32_bf16 v[68:71], v[208:211], v[64:67], v[68:71]\n\ts_waitcnt lgkmcnt(4)\n\t"
  "v_mfma_f32_16x16x32_bf16 v[12:15], v[224:227], v[48:51], v[12:15]\n\tds_read_b64_tr_b16 v[4:5], v0 offset:2048\n\t"
  "v_mfma_f32_16x16x32_bf16 v[56:59], v[48:51], v[228:231], v[56:59]\n\tds_read_b64_tr_b16 v[6:7], v0 offset:3072\n\tv_lshl_add_u32 v43, v1, 2, s60\n\t"
  "v_mfma_f32_16x16x32_bf16 v[68:71], v[220:223], v[48:51], v[68:71]\n\t"
  "v_mfma_f32_16x16x32_bf16 v[12:15], v[236:239], v[8:11], v[12:15]\n\tds_read_b64_tr_b16 v[20:21], v0 offset:4096\n\tds_read_b128 v[48:51], v3 offset:320\n\t"
  "v_mfma_f32_16x16x32_bf16 v[56:59], v[8:11], v[240:243], v[56:59]\n\tds_read_b64_tr_b16 v[22:23], v0 offset:5120\n\t"
  "v_mfma_f32_16x16x32_bf16 v[68:71], v[232:235], v[8:11], v[68:71]\n\tds_read_b128 v[8:11], v3 offset:64\n\ts_waitcnt lgkmcnt(6)\n\t"
  "v_mfma_f32_16x16x32_bf16 v[12:15], v[248:251], v[16:19], v[12:15]\n\tds_read_b64_tr_b16 v[28:29], v0 offset:6144\n\t"
  "v_mfma_f32_16x16x32_bf16 v[56:59], v[16:19], v[252:255], v[56:59]\n\tds_read_b64_tr_b16 v[30:31], v0 offset:7168\n\t"
  "v_mfma_f32_16x16x32_bf16 v[68:71], v[244:247], v[16:19], v[68:71]\n\tv_fma_f32 v12, -v45, v24, v12\n\tv_fma_f32 v13, -v45, v25, v13\n\t"
  "v_mfma_f32_16x16x32_bf16 v[52:55], v[80:83], v[36:39], v[52:55]\n\tds_read_b64_tr_b16 v[16:17], v0 offset:8192\n\tv_fma_f32 v14, -v45, v26, v14\n\t"
  "v_mfma_f32_16x16x32_bf16 v[72:75], v[36:39], v[84:87], v[72:75]\n\tds_read_b64_tr_b16 v[18:19], v0 offset:9216\n\tv_fma_f32 v15, -v45, v27, v15\n\t"
  "v_mfma_f32_16x16x32_bf16 v[60:63], v[76:79], v[36:39], v[60:63]\n\tv_mul_f32 v12, v12, v41\n\ts_waitcnt lgkmcnt(5)\n\t"
  "v_mfma_f32_16x16x32_bf16 v[52:55], v[92:95], v[4:7], v[52:55]\n\tds_read_b64_tr_b16 v[64:65], v0 offset:10240\n\tv_mul_f32 v13, v13, v41\n\t"
  "v_mfma_f32_16x16x32_bf16 v[72:75], v[4:7], v[96:99], v[72:75]\n\tds_read_b64_tr_b16 v[66:67], v0 offset:11264\n\tv_mul_f32 v14, v14, v41\n\t"
  "v_mfma_f32_16x16x32_bf16 v[60:63], v[88:91], v[4:7], v[60:63]\n\t"
  GST(17)
  "v_mul_f32 v15, v15, v41\n\tv_cvt_pk_bf16_f32 v12, v12, v13\n\t"
  "v_mfma_f32_16x16x32_bf16 v[52:55], v[104:107], v[20:23], v[52:55]\n\tds_read_b64_tr_b16 v[4:5], v0 offset:12288\n\tv_cvt_pk_bf16_f32 v14, v14, v15\n\t"
  "v_mfma_f32_16x16x32_bf16 v[72:75], v[20:23], v[108:111], v[72:75]\n\tds_read_b64_tr_b16 v[6:7], v0 offset:13312\n\tv_mov_b32 v13, s62\n\t"
  "v_mfma_f32_16x16x32_bf16 v[60:63], v[100:103], v[20:23], v[60:63]\n\tv_mov_b32 v15, s62\n\ts_waitcnt lgkmcnt(4)\n\t"
  "v_mfma_f32_16x16x32_bf16 v[52:55], v[116:119], v[28:31], v[52:55]\n\tds_read_b64_tr_b16 v[20:21], v0 offset:14336\n\tv_fma_f32 v56, -v48, v47, v56\n\t"
  "v_mfma_f32_16x16x32_bf16 v[72:75], v[28:31], v[120:123], v[72:75]\n\tds_read_b64_tr_b16 v[22:23], v0 offset:15360\n\tv_fma_f32 v57, -v49, v47, v57\n\t"
  "v_mfma_f32_16x16x32_bf16 v[60:63], v[112:115], v[28:31], v[60:63]\n\tv_fma_f32 v58, -v50, v47, v58\n\tv_fma_f32 v59, -v51, v47, v59\n\t"
  "v_mfma_f32_16x16x32_bf16 v[52:55], v[128:131], v[16:19], v[52:55]\n\tds_read_b64_tr_b16 v[36:37], v0 offset:16384\n\tv_mul_f32 v56, v56, v8\n\t"
  "v_mfma_f32_16x16x32_bf16 v[72:75], v[16:19], v[132:135], v[72:75]\n\tds_read_b64_tr_b16 v[38:39], v0 offset:17408\n\tv_mul_f32 v57, v57, v9\n\t"
  "v_mfma_f32_16x16x32_bf16 v[60:63], v[124:127], v[16:19], v[60:63]\n\tv_mul_f32 v58, v58, v10\n\ts_waitcnt lgkmcnt(4)\n\t"
  "v_mfma_f32_16x16x32_bf16 v[52:55], v[140:143], v[64:67], v[52:55]\n\tds_read_b64_tr_b16 v[16:17], v0 offset:18432\n\tv_mul_f32 v59, v59, v11\n\t"
  "v_mfma_f32_16x16x32_bf16 v[72:75], v[64:67], v[144:147], v[72:75]\n\tds_read_b64_tr_b16 v[18:19], v0 offset:19456\n\tv_cvt_pk_bf16_f32 v56, v56, v57\n\t"
  "v_mfma_f32_16x16x32_bf16 v[60:63], v[136:139], v[64:67], v[60:63]\n\tv_cvt_pk_bf16_f32 v58, v58, v59\n\tv_mov_b32 v57, s62\n\t"
  "v_mfma_f32_16x16x32_bf16 v[52:55], v[152:155], v[4:7], v[52:55]\n\tds_read_b64_tr_b16 v[8:9], v0 offset:20480\n\tv_mov_b32 v59, s62\n\t"
  "v_mfma_f32_16x16x32_bf16 v[72:75], v[4:7], v[156:159], v[72:75]\n\tds_read_b64_tr_b16 v[10:11], v0 offset:21504\n\tv_fma_f32 v68, -v45, v32, v68\n\t"
  "v_mfma_f32_16x16x32_bf16 v[60:63], v[148:151], v[4:7], v[60:63]\n\tv_add_u32 v46, s64, v1\n\ts_waitcnt lgkmcnt(4)\n\t"
  "v_mfma_f32_16x16x32_bf16 v[52:55], v[164:167], v[20:23], v[52:55]\n\tds_read_b64_tr_b16 v[48:49], v0 offset:22528\n\tv_fma_f32 v69, -v45, v33, v69\n\t"
  "v_mfma_f32_16x16x32_bf16 v[72:75], v[20:23], v[168:171], v[72:75]\n\tds_read_b64_tr_b16 v[50:51], v0 offset:23552\n\tv_fma_f32 v70, -v45, v34, v70\n\t"
  "v_mfma_f32_16x16x32_bf16 v[60:63], v[160:163], v[20:23], v[60:63]\n\tv_fma_f32 v71, -v45, v35, v71\n\tv_mul_f32 v68, v68, v41\n\t"
  "v_mfma_f32_16x16x32_bf16 v[52:55], v[176:179], v[36:39], v[52:55]\n\tds_read_b64_tr_b16 v[28:29], v0 offset:24576\n\tv_mul_f32 v69, v69, v41\n\t"
  "v_mfma_f32_16x16x32_bf16 v[72:75], v[36:39], v[180:183], v[72:75]\n\tds_read_b64_tr_b16 v[30:31], v0 offset:25600\n\tv_mul_f32 v70, v70, v41\n\t"
  "v_mfma_f32_16x16x32_bf16 v[60:63], v[172:175], v[36:39], v[60:63]\n\tv_mul_f32 v71, v71, v41\n\ts_waitcnt lgkmcnt(4)\n\t"
  "v_mfma_f32_16x16x32_bf16 v[52:55], v[188:191], v[16:19], v[52:55]\n\tds_read_b64_tr_b16 v[20:21], v0 offset:26624\n\tv_cvt_pk_bf16_f32 v68, v68, v69\n\t"
  "v_mfma_f32_16x16x32_bf16 v[72:75], v[16:19], v[192:195], v[72:75]\n\tds_read_b64_tr_b16 v[22:23], v0 offset:27648\n\tv_cvt_pk_bf16_f32 v70, v70, v71\n\t"
  "v_mfma_f32_16x16x32_bf16 v[60:63], v[184:187], v[16:19], v[60:63]\n\t"
  "v_mfma_f32_16x16x32_bf16 v[52:55], v[200:203], v[8:11], v[52:55]\n\t"
  GST(13)
  "ds_read_b64_tr_b16 v[36:37], v0 offset:28672\n\t"
  "v_mfma_f32_16x16x32_bf16 v[72:75], v[8:11], v[204:207], v[72:75]\n\ts_barrier\n\tds_read_b64_tr_b16 v[38:39], v0 offset:29696\n\t"
  "v_mfma_f32_16x16x32_bf16 v[60:63], v[196:199], v[8:11], v[60:63]\n\tbuffer_store_dwordx4 v[12:15], v43, s[56:59], 0 offen offset:0 sc1\n\ts_waitcnt lgkmcnt(4)\n\t"
  "v_mfma_f32_16x16x32_bf16 v[52:55], v[212:215], v[48:51], v[52:55]\n\tds_read_b64_tr_b16 v[64:65], v0 offset:30720\n\tbuffer_store_dwordx4 v[56:59], v43, s[56:59], 0 offen offset:1024 sc1\n\t"
  "v_mfma_f32_16x16x32_bf16 v[72:75], v[48:51], v[216:219], v[72:75]\n\tds_read_b64_tr_b16 v[66:67], v0 offset:31744\n\tv_lshl_add_u32 v4, v1, 2, s66\n\tv_add_u32 v0, 0x8000, v0\n\t"
  "v_mfma_f32_16x16x32_bf16 v[60:63], v[208:211], v[48:51], v[60:63]\n\tds_read_b128 v[42:45], v4 offset:1024\n\tds_read_b128 v[8:11], v4 offset:2048\n\t"
  "v_mfma_f32_16x16x32_bf16 v[52:55], v[224:227], v[28:31], v[52:55]\n\tds_read_b64_tr_b16 v[16:17], v0 offset:0\n\tds_read_b128 v[48:51], v4 offset:0\n\t"
  "v_mfma_f32_16x16x32_bf16 v[72:75], v[28:31], v[228:231], v[72:75]\n\tds_read_b64_tr_b16 v[18:19], v0 offset:1024\n\tv_min_u32 v41, 188, v1\n\t"
  "v_mfma_f32_16x16x32_bf16 v[60:63], v[220:223], v[28:31], v[60:63]\n\tds_read_b128 v[4:7], v3\n\ts_waitcnt lgkmcnt(8)\n\t"
  "v_mfma_f32_16x16x32_bf16 v[52:55], v[236:239], v[20:23], v[52:55]\n\tds_read_b64_tr_b16 v[28:29], v0 offset:2048\n\tv_lshlrev_b32 v41, 2, v41\n\t"
  "v_mfma_f32_16x16x32_bf16 v[72:75], v[20:23], v[240:243], v[72:75]\n\tds_read_b64_tr_b16 v[30:31], v0 offset:3072\n\tv_add_u32 v41, 0x22c00, v41\n\t"
  "v_mfma_f32_16x16x32_bf16 v[60:63], v[232:235], v[20:23], v[60:63]\n\t"
  "v_mfma_f32_16x16x32_bf16 v[52:55], v[248:251], v[36:39], v[52:55]\n\tds_read_b64_tr_b16 v[20:21], v0 offset:8192\n\t"
  "v_mfma_f32_16x16x32_bf16 v[72:75], v[36:39], v[252:255], v[72:75]\n\tds_read_b64_tr_b16 v[22:23], v0 offset:9216\n\t"
  "v_mfma_f32_16x16x32_bf16 v[60:63], v[244:247], v[36:39], v[60:63]\n\ts_waitcnt lgkmcnt(5)\n\t"
  "v_mfma_f32_16x16x32_bf16 v[52:55], v[42:45], v[64:67], v[52:55]\n\tds_read_b64_tr_b16 v[42:43], v0 offset:10240\n\t"
  "v_mfma_f32_16x16x32_bf16 v[72:75], v[64:67], v[8:11], v[72:75]\n\tds_read_b64_tr_b16 v[44:45], v0 offset:11264\n\t"
  "v_mfma_f32_16x16x32_bf16 v[60:63], v[48:51], v[64:67], v[60:63]\n\t"
  GST(12)
  "v_mul_f32 v52, v52, v40\n\tv_mul_f32 v53, v53, v40\n\t"
  "v_mfma_f32_16x16x32_bf16 v[8:11], v[80:83], v[16:19], 0\n\tds_read_b64_tr_b16 v[36:37], v0 offset:16384\n\tv_mul_f32 v54, v54, v40\n\t"
  "v_mfma_f32_16x16x32_bf16 v[48:51], v[16:19], v[84:87], 0\n\tds_read_b64_tr_b16 v[38:39], v0 offset:17408\n\tv_mul_f32 v55, v55, v40\n\t"
  "v_mfma_f32_16x16x32_bf16 v[64:67], v[76:79], v[16:19], 0\n\tv_cvt_pk_bf16_f32 v52, v52, v53\n\ts_waitcnt lgkmcnt(4)\n\t"
  "v_mfma_f32_16x16x32_bf16 v[8:11], v[92:95], v[28:31], v[8:11]\n\tds_read_b64_tr_b16 v[16:17], v0 offset:18432\n\tv_cvt_pk_bf16_f32 v54, v54, v55\n\t"
  "v_mfma_f32_16x16x32_bf16 v[48:51], v[28:31], v[96:99], v[48:51]\n\tds_read_b64_tr_b16 v[18:19], v0 offset:19456\n\tv_mul_f32 v60, v60, v40\n\t"
  "v_mfma_f32_16x16x32_bf16 v[64:67], v[88:91], v[28:31], v[64:67]\n\tv_mul_f32 v61, v61, v40\n\tv_mul_f32 v62, v62, v40\n\t"
  "v_mfma_f32_16x16x32_bf16 v[8:11], v[128:131], v[20:23], v[8:11]\n\tds_read_b64_tr_b16 v[28:29], v0 offset:24576\n\tds_write2st64_b32 v46, v12, v14 offset0:0 offset1:1\n\tds_write2st64_b32 v46, v52, v54 offset0:6 offset1:7\n\t"
  "v_mfma_f32_16x16x32_bf16 v[48:51], v[20:23], v[132:135], v[48:51]\n\tds_read_b64_tr_b16 v[30:31], v0 offset:25600\n\tds_write2st64_b32 v46, v56, v58 offset0:2 offset1:3\n\t"
  "v_mfma_f32_16x16x32_bf16 v[64:67], v[124:127], v[20:23], v[64:67]\n\tds_write2st64_b32 v46, v68, v70 offset0:4 offset1:5\n\tv_mul_f32 v63, v63, v40\n\tv_cvt_pk_bf16_f32 v60, v60, v61\n\t"
  "v_cvt_pk_bf16_f32 v62, v62, v63\n\ts_waitcnt lgkmcnt(8)\n\t"
  "v_mfma_f32_16x16x32_bf16 v[8:11], v[140:143], v[42:45], v[8:11]\n\tds_read_b64_tr_b16 v[12:13], v0 offset:26624\n\tds_write2st64_b32 v46, v60, v62 offset0:10 offset1:11\n\t"
  "v_mfma_f32_16x16x32_bf16 v[48:51], v[42:45], v[144:147], v[48:51]\n\tds_read_b64_tr_b16 v[14:15], v0 offset:27648\n\tv_mul_f32 v72, v72, v4\n\t"
  "v_mfma_f32_16x16x32_bf16 v[64:67], v[136:139], v[42:45], v[64:67]\n\tv_mul_f32 v73, v73, v5\n\tv_mul_f32 v74, v74, v6\n\tv_mul_f32 v75, v75, v7\n\t"
  "v_cvt_pk_bf16_f32 v72, v72, v73\n\tv_cvt_pk_bf16_f32 v74, v74, v75\n\t"
  "v_mfma_f32_16x16x32_bf16 v[8:11], v[176:179], v[36:39], v[8:11]\n\tds_write2st64_b32 v46, v72, v74 offset0:8 offset1:9\n\t"
  "v_mfma_f32_16x16x32_bf16 v[48:51], v[36:39], v[180:183], v[48:51]\n\t"
  "v_mfma_f32_16x16x32_bf16 v[64:67], v[172:175], v[36:39], v[64:67]\n\tv_and_b32 v44, 0x40, v1\n\ts_waitcnt lgkmcnt(4)\n\t"
  "v_mfma_f32_16x16x32_bf16 v[8:11], v[188:191], v[16:19], v[8:11]\n\tv_lshl_add_u32 v44, v44, 8, s61\n\tv_and_b32 v45, 0x3c, v1\n\t"
  "v_mfma_f32_16x16x32_bf16 v[48:51], v[16:19], v[192:195], v[48:51]\n\ts_waitcnt lgkmcnt(0)\n\ts_barrier\n\tds_read_b32 v62, v41 offset:5700\n\t"
  "ds_read_b128 v[72:75], v41\n\tds_read_b128 v[52:55], v41 offset:768\n\t"
  "v_mfma_f32_16x16x32_bf16 v[64:67], v[184:187], v[16:19], v[64:67]\n\tds_read_b64_tr_b16 v[40:41], v0 offset:4096\n\tds_read_b64_tr_b16 v[42:43], v0 offset:5120\n\t"
  "v_mfma_f32_16x16x32_bf16 v[8:11], v[224:227], v[28:31], v[8:11]\n\tds_read_b64_tr_b16 v[20:21], v0 offset:6144\n\tds_read_b64_tr_b16 v[22:23], v0 offset:7168\n\t"
  "v_mfma_f32_16x16x32_bf16 v[48:51], v[28:31], v[228:231], v[48:51]\n\tds_read_b64_tr_b16 v[36:37], v0 offset:12288\n\tv_lshl_add_u32 v44, v45, 2, v44\n\t"
  "v_mfma_f32_16x16x32_bf16 v[64:67], v[220:223], v[28:31], v[64:67]\n\tds_read_b64_tr_b16 v[38:39], v0 offset:13312\n\tv_and_b32 v45, 0x80, v1\n\t"
  "v_mfma_f32_16x16x32_bf16 v[8:11], v[236:239], v[12:15], v[8:11]\n\tds_read_b64_tr_b16 v[28:29], v0 offset:14336\n\tv_lshl_add_u32 v44, v45, 1, v44\n\t"
  "v_mfma_f32_16x16x32_bf16 v[48:51], v[12:15], v[240:243], v[48:51]\n\tds_read_b64_tr_b16 v[30:31], v0 offset:15360\n\t"
  "v_mfma_f32_16x16x32_bf16 v[64:67], v[232:235], v[12:15], v[64:67]\n\ts_waitcnt lgkmcnt(9)\n\ts_waitcnt lgkmcnt(6)\n\t"
  "v_mfma_f32_16x16x32_bf16 v[8:11], v[104:107], v[40:43], v[8:11]\n\tds_read_b64_tr_b16 v[68:69], v0 offset:20480\n\tv_readfirstlane_b32 s54, v62\n\t"
  "v_mfma_f32_16x16x32_bf16 v[48:51], v[40:43], v[108:111], v[48:51]\n\tds_read_b64_tr_b16 v[70:71], v0 offset:21504\n\tv_add_f32 v72, v72, v73\n\t"
  "v_mfma_f32_16x16x32_bf16 v[64:67], v[100:103], v[40:43], v[64:67]\n\ts_cmp_eq_u32 s54, s63\n\ts_cbranch_scc1 .Lg_redo\n.Lg_back:\n\ts_waitcnt lgkmcnt(5)\n\t"
  "v_mfma_f32_16x16x32_bf16 v[8:11], v[116:119], v[20:23], v[8:11]\n\tds_read_b64_tr_b16 v[56:57], v0 offset:22528\n\tv_add_f32 v74, v74, v75\n\t"
  "v_mfma_f32_16x16x32_bf16 v[48:51], v[20:23], v[120:123], v[48:51]\n\tds_read_b64_tr_b16 v[58:59], v0 offset:23552\n\tv_add_f32 v52, v52, v53\n\t"
  "v_mfma_f32_16x16x32_bf16 v[64:67], v[112:115], v[20:23], v[64:67]\n\tv_lshl_add_u32 v45, v1, 2, s66\n\ts_waitcnt lgkmcnt(4)\n\t"
  "v_mfma_f32_16x16x32_bf16 v[8:11], v[152:155], v[36:39], v[8:11]\n\tds_read_b64_tr_b16 v[16:17], v0 offset:30720\n\tv_add_f32 v72, v72, v74\n\t"
  "v_mfma_f32_16x16x32_bf16 v[48:51], v[36:39], v[156:159], v[48:51]\n\tds_read_b64_tr_b16 v[18:19], v0 offset:31744\n\tds_read_b128 v[20:23], v45 offset:1024\n\t"
  "v_mfma_f32_16x16x32_bf16 v[64:67], v[148:151], v[36:39], v[64:67]\n\tv_add_f32 v54, v54, v55\n\tv_mul_f32 v73, 0x3b000000, v72\n\t"
  "v_mfma_f32_16x16x32_bf16 v[8:11], v[164:167], v[28:31], v[8:11]\n\tds_read_b64_tr_b16 v[60:61], v0 offset:28672\n\tv_add_f32 v52, v52, v54\n\t"
  "v_mfma_f32_16x16x32_bf16 v[48:51], v[28:31], v[168:171], v[48:51]\n\tds_read_b64_tr_b16 v[62:63], v0 offset:29696\n\tv_mul_f32_e64 v74, -v73, v73\n\tv_fmac_f32 v74, 0x3b000000, v52\n\t"
  "v_max_f32 v74, 0, v74\n\tv_add_f32 v74, 0x3727c5ac, v74\n\t"
  "v_mfma_f32_16x16x32_bf16 v[64:67], v[160:163], v[28:31], v[64:67]\n\tv_rsq_f32 v74, v74\n\tv_add_u32 v75, s65, v1\n\tds_write2st64_b32 v75, v74, v73 offset1:1\n\t"
  "ds_read_b128 v[4:7], v45 offset:2048\n\ts_waitcnt lgkmcnt(6)\n\t"
  "v_mfma_f32_16x16x32_bf16 v[8:11], v[200:203], v[68:71], v[8:11]\n\tds_read_b64_tr_b16 v[12:13], v0 offset:49152\n\tds_read2_b32 v[72:73], v2 offset0:64 offset1:80\n\tds_read_b128 v[28:31], v45 offset:0\n\t"
  "v_mfma_f32_16x16x32_bf16 v[48:51], v[68:71], v[204:207], v[48:51]\n\tds_read_b64_tr_b16 v[14:15], v0 offset:49664\n\t"
  "v_mfma_f32_16x16x32_bf16 v[64:67], v[196:199], v[68:71], v[64:67]\n\tds_read_b64_tr_b16 v[52:53], v0 offset:50176\n\t"
  "v_mfma_f32_16x16x32_bf16 v[8:11], v[212:215], v[56:59], v[8:11]\n\tds_read_b64_tr_b16 v[54:55], v0 offset:50688\n\t"
  "v_mfma_f32_16x16x32_bf16 v[48:51], v[56:59], v[216:219], v[48:51]\n\tds_read_b64_tr_b16 v[68:69], v0 offset:51200\n\t"
  "v_mfma_f32_16x16x32_bf16 v[64:67], v[208:211], v[56:59], v[64:67]\n\tds_read_b64_tr_b16 v[70:71], v0 offset:51712\n\ts_waitcnt lgkmcnt(10)\n\t"
  "v_mfma_f32_16x16x32_bf16 v[8:11], v[20:23], v[16:19], v[8:11]\n\tds_read_b64_tr_b16 v[40:41], v0 offset:52224\n\ts_waitcnt lgkmcnt(9)\n\t"
  "v_mfma_f32_16x16x32_bf16 v[48:51], v[16:19], v[4:7], v[48:51]\n\tds_read_b64_tr_b16 v[42:43], v0 offset:52736\n\ts_waitcnt lgkmcnt(7)\n\t"
  "v_mfma_f32_16x16x32_bf16 v[64:67], v[28:31], v[16:19], v[64:67]\n\tds_read_b64_tr_b16 v[36:37], v0 offset:53248\n\tds_read_b128 v[16:19], v3 offset:256\n\t"
  "v_mfma_f32_16x16x32_bf16 v[8:11], v[248:251], v[60:63], v[8:11]\n\tds_read_b64_tr_b16 v[38:39], v0 offset:53760\n\t"
  "v_mfma_f32_16x16x32_bf16 v[48:51], v[60:63], v[252:255], v[48:51]\n\tds_read_b64_tr_b16 v[56:57], v0 offset:54272\n\t"
  "v_mfma_f32_16x16x32_bf16 v[64:67], v[244:247], v[60:63], v[64:67]\n\tds_read_b64_tr_b16 v[58:59], v0 offset:54784\n\ts_waitcnt lgkmcnt(8)\n\t"
  "v_mfma_f32_16x16x32_bf16 v[60:63], v[80:83], v[12:15], 0\n\t"
  GST(18)
  "ds_read_b64_tr_b16 v[28:29], v0 offset:55296\n\tv_fma_f32 v8, -v72, v24, v8\n\t"
  "v_mfma_f32_16x16x32_bf16 v[20:23], v[12:15], v[84:87], 0\n\tds_read_b64_tr_b16 v[30:31], v0 offset:55808\n\tv_fma_f32 v9, -v72, v25, v9\n\t"
  "v_mfma_f32_16x16x32_bf16 v[60:63], v[92:95], v[52:55], v[60:63]\n\tds_read_b64_tr_b16 v[4:5], v0 offset:56320\n\tv_fma_f32 v10, -v72, v26, v10\n\t"
  "v_mfma_f32_16x16x32_bf16 v[20:23], v[52:55], v[96:99], v[20:23]\n\tds_read_b64_tr_b16 v[6:7], v0 offset:56832\n\ts_waitcnt lgkmcnt(7)\n\t"
  "v_mfma_f32_16x16x32_bf16 v[60:63], v[104:107], v[68:71], v[60:63]\n\tds_read_b64_tr_b16 v[52:53], v0 offset:57344\n\tv_fma_f32 v11, -v72, v27, v11\n\t"
  "v_mfma_f32_16x16x32_bf16 v[20:23], v[68:71], v[108:111], v[20:23]\n\tds_read_b64_tr_b16 v[54:55], v0 offset:57856\n\tv_cvt_pk_bf16_f32 v74, v8, v9\n\t"
  "v_mfma_f32_16x16x32_bf16 v[60:63], v[116:119], v[40:43], v[60:63]\n\tds_read_b64_tr_b16 v[8:9], v0 offset:58368\n\tv_cvt_pk_bf16_f32 v75, v10, v11\n\t"
  "v_mfma_f32_16x16x32_bf16 v[20:23], v[40:43], v[120:123], v[20:23]\n\tds_read_b64_tr_b16 v[10:11], v0 offset:58880\n\ts_waitcnt lgkmcnt(7)\n\t"
  "v_mfma_f32_16x16x32_bf16 v[60:63], v[128:131], v[36:39], v[60:63]\n\tds_read_b64_tr_b16 v[68:69], v0 offset:59392\n\tv_fma_f32 v64, -v72, v32, v64\n\t"
  "v_mfma_f32_16x16x32_bf16 v[20:23], v[36:39], v[132:135], v[20:23]\n\tds_read_b64_tr_b16 v[70:71], v0 offset:59904\n\tv_fma_f32 v65, -v72, v33, v65\n\t"
  "v_mfma_f32_16x16x32_bf16 v[60:63], v[140:143], v[56:59], v[60:63]\n\tds_read_b64_tr_b16 v[40:41], v0 offset:60416\n\tv_fma_f32 v66, -v72, v34, v66\n\t"
  "v_mfma_f32_16x16x32_bf16 v[20:23], v[56:59], v[144:147], v[20:23]\n\tds_read_b64_tr_b16 v[42:43], v0 offset:60928\n\ts_waitcnt lgkmcnt(7)\n\t"
  "v_mfma_f32_16x16x32_bf16 v[60:63], v[152:155], v[28:31], v[60:63]\n\tds_read_b64_tr_b16 v[36:37], v0 offset:61440\n\tv_fma_f32 v67, -v72, v35, v67\n\t"
  "v_mfma_f32_16x16x32_bf16 v[20:23], v[28:31], v[156:159], v[20:23]\n\tds_read_b64_tr_b16 v[38:39], v0 offset:61952\n\tv_cvt_pk_bf16_f32 v28, v64, v65\n\t"
  "v_mfma_f32_16x16x32_bf16 v[60:63], v[164:167], v[4:7], v[60:63]\n\tds_read_b64_tr_b16 v[64:65], v0 offset:62464\n\tv_cvt_pk_bf16_f32 v29, v66, v67\n\t"
  "v_mfma_f32_16x16x32_bf16 v[20:23], v[4:7], v[168:171], v[20:23]\n\tds_read_b64_tr_b16 v[66:67], v0 offset:62976\n\ts_waitcnt lgkmcnt(7)\n\t"
  "v_mfma_f32_16x16x32_bf16 v[60:63], v[176:179], v[52:55], v[60:63]\n\tds_read_b64_tr_b16 v[56:57], v0 offset:63488\n\tv_fma_f32 v48, -v16, v47, v48\n\t"
  "v_mfma_f32_16x16x32_bf16 v[20:23], v[52:55], v[180:183], v[20:23]\n\tds_read_b64_tr_b16 v[58:59], v0 offset:64000\n\tv_fma_f32 v49, -v17, v47, v49\n\t"
  "v_mfma_f32_16x16x32_bf16 v[60:63], v[188:191], v[8:11], v[60:63]\n\tds_read_b64_tr_b16 v[52:53], v0 offset:64512\n\tv_lshl_add_u32 v31, v1, 2, s66\n\t"
  "v_mfma_f32_16x16x32_bf16 v[20:23], v[8:11], v[192:195], v[20:23]\n\tds_read_b64_tr_b16 v[54:55], v0 offset:65024\n\ts_waitcnt lgkmcnt(7)\n\t"
  "v_mfma_f32_16x16x32_bf16 v[60:63], v[200:203], v[68:71], v[60:63]\n\tds_read_b64_tr_b16 v[8:9], v0 offset:31232\n\tv_fma_f32 v50, -v18, v47, v50\n\t"
  "v_mfma_f32_16x16x32_bf16 v[20:23], v[68:71], v[204:207], v[20:23]\n\tds_read_b64_tr_b16 v[10:11], v0 offset:32256\n\tv_fma_f32 v51, -v19, v47, v51\n\t"
  "v_mfma_f32_16x16x32_bf16 v[60:63], v[212:215], v[40:43], v[60:63]\n\tv_cvt_pk_bf16_f32 v70, v48, v49\n\tv_cvt_pk_bf16_f32 v71, v50, v51\n\tds_read_b128 v[48:51], v31 offset:1024\n\t"
  "v_mfma_f32_16x16x32_bf16 v[20:23], v[40:43], v[216:219], v[20:23]\n\tds_read_b64_tr_b16 v[12:13], v0 offset:512\n\tds_read_b128 v[16:19], v31 offset:2048\n\ts_waitcnt lgkmcnt(8)\n\t"
  "v_mfma_f32_16x16x32_bf16 v[60:63], v[224:227], v[36:39], v[60:63]\n\tds_read_b64_tr_b16 v[14:15], v0 offset:1536\n\tds_read_b32 v68, v2 offset:384\n\tv_mul_f32_e64 v4, -v73, v24\n\t"
  "v_mul_f32_e64 v5, -v73, v25\n\tv_mul_f32_e64 v6, -v73, v26\n\t"
  "v_mfma_f32_16x16x32_bf16 v[20:23], v[36:39], v[228:231], v[20:23]\n\tv_mul_f32_e64 v7, -v73, v27\n\tds_read_b128 v[40:43], v31 offset:0\n\t"
  "v_mfma_f32_16x16x32_bf16 v[60:63], v[236:239], v[64:67], v[60:63]\n\tds_read_b64_tr_b16 v[36:37], v0 offset:2560\n\tv_mul_f32_e64 v30, -v73, v32\n\t"
  "v_mfma_f32_16x16x32_bf16 v[20:23], v[64:67], v[240:243], v[20:23]\n\tds_read_b64_tr_b16 v[38:39], v0 offset:3584\n\ts_waitcnt lgkmcnt(8)\n\t"
  "v_mfma_f32_16x16x32_bf16 v[60:63], v[248:251], v[56:59], v[60:63]\n\tv_mul_f32_e64 v31, -v73, v33\n\tv_mul_f32_e64 v32, -v73, v34\n\tv_mul_f32_e64 v33, -v73, v35\n\t"
  "v_mfma_f32_16x16x32_bf16 v[20:23], v[56:59], v[252:255], v[20:23]\n\tds_read_b64_tr_b16 v[64:65], v0 offset:4608\n\ts_waitcnt lgkmcnt(6)\n\t"
  "v_mfma_f32_16x16x32_bf16 v[60:63], v[48:51], v[52:55], v[60:63]\n\tds_read_b64_tr_b16 v[66:67], v0 offset:5632\n\tds_read2_b32 v[34:35], v2 offset0:0 offset1:16\n\t"
  "v_mfma_f32_16x16x32_bf16 v[20:23], v[52:55], v[16:19], v[20:23]\n\t"
  "v_mfma_f32_16x16x32_bf16 v[4:7], v[48:51], v[8:11], v[4:7]\n\t"
  GST(19)
  "ds_read_b64_tr_b16 v[56:57], v0 offset:6656\n\tv_and_or_b32 v46, v1, 4, v3\n\tds_read2_b32 v[72:73], v46 offset0:32 offset1:34\n\tds_read2_b32 v[52:53], v46 offset0:0 offset1:2\n\t"
  "v_mov_b32 v55, 0xff800000\n\ts_waitcnt lgkmcnt(7)\n\t"
  "v_mfma_f32_16x16x32_bf16 v[30:33], v[40:43], v[8:11], v[30:33]\n\tds_read_b64_tr_b16 v[58:59], v0 offset:7680\n\tv_cndmask_b32_e64 v54, v55, 0, s[70:71]\n\tv_cndmask_b32_e64 v45, v55, 0, s[72:73]\n\t"
  "v_cndmask_b32_e64 v2, v55, 0, s[74:75]\n\tv_cndmask_b32_e64 v55, v55, 0, s[76:77]\n\t"
  "v_mfma_f32_16x16x32_bf16 v[40:43], v[8:11], v[16:19], 0\n\tv_fma_f32 v60, -v68, v24, v60\n\tv_fma_f32 v61, -v68, v25, v61\n\t"
  "v_mfma_f32_16x16x32_bf16 v[4:7], v[80:83], v[12:15], v[4:7]\n\tds_read_b64_tr_b16 v[8:9], v0 offset:8704\n\tv_fma_f32 v62, -v68, v26, v62\n\t"
  "v_mfma_f32_16x16x32_bf16 v[30:33], v[76:79], v[12:15], v[30:33]\n\tds_read_b64_tr_b16 v[10:11], v0 offset:9728\n\tv_mfma_f32_16x16x16_bf16 v[48:51], v[74:75], v[28:29], 0\n\tv_fma_f32 v63, -v68, v27, v63\n\t"
  "v_cvt_pk_bf16_f32 v60, v60, v61\n\t"
  "v_mfma_f32_16x16x32_bf16 v[40:43], v[12:15], v[84:87], v[40:43]\n\tv_cvt_pk_bf16_f32 v61, v62, v63\n\ts_waitcnt lgkmcnt(3)\n\t"
  "v_mfma_f32_16x16x32_bf16 v[4:7], v[92:95], v[36:39], v[4:7]\n\tds_read_b64_tr_b16 v[12:13], v0 offset:10752\n\tv_mfma_f32_16x16x16_bf16 v[24:27], v[60:61], v[28:29], 0\n\tv_cndmask_b32_e64 v29, v48, v49, s[68:69]\n\t"
  "v_fma_f32 v29, v29, v52, v2\n\t"
  "v_mfma_f32_16x16x32_bf16 v[30:33], v[88:91], v[36:39], v[30:33]\n\tds_read_b64_tr_b16 v[14:15], v0 offset:11776\n\tv_cndmask_b32_e64 v28, v50, v51, s[68:69]\n\t"
  "v_mfma_f32_16x16x32_bf16 v[40:43], v[36:39], v[96:99], v[40:43]\n\tds_read_b128 v[16:19], v3 offset:384\n\tv_fma_f32 v28, v28, v53, v55\n\t"
  "v_mfma_f32_16x16x32_bf16 v[4:7], v[104:107], v[64:67], v[4:7]\n\tds_read_b64_tr_b16 v[36:37], v0 offset:12800\n\tv_max_f32 v60, v29, v28\n\t"
  "v_mfma_f32_16x16x32_bf16 v[30:33], v[100:103], v[64:67], v[30:33]\n\tds_read_b64_tr_b16 v[38:39], v0 offset:13824\n\tv_cndmask_b32_e64 v50, v24, v25, s[68:69]\n\t"
  "v_mfma_f32_16x16x32_bf16 v[40:43], v[64:67], v[108:111], v[40:43]\n\tv_fma_f32 v50, v50, v72, v54\n\ts_waitcnt lgkmcnt(5)\n\t"
  "v_mfma_f32_16x16x32_bf16 v[4:7], v[116:119], v[56:59], v[4:7]\n\tds_read_b64_tr_b16 v[64:65], v0 offset:14848\n\tv_cndmask_b32_e64 v48, v26, v27, s[68:69]\n\t"
  "v_mfma_f32_16x16x32_bf16 v[30:33], v[112:115], v[56:59], v[30:33]\n\tds_read_b64_tr_b16 v[66:67], v0 offset:15872\n\tv_fma_f32 v48, v48, v73, v45\n\t"
  "v_mfma_f32_16x16x32_bf16 v[40:43], v[56:59], v[120:123], v[40:43]\n\tv_lshlrev_b32 v51, 2, v1\n\tv_alignbit_b32 v49, s67, s67, v51\n\t"
  "v_mfma_f32_16x16x32_bf16 v[4:7], v[128:131], v[8:11], v[4:7]\n\tds_read_b64_tr_b16 v[24:25], v0 offset:16896\n\tv_alignbit_b32 v51, s55, s55, v51\n\t"
  "v_mfma_f32_16x16x32_bf16 v[30:33], v[124:127], v[8:11], v[30:33]\n\tds_read_b64_tr_b16 v[26:27], v0 offset:17920\n\tv_mov_b32 v58, 0x3f803f80\n\t"
  "v_mfma_f32_16x16x32_bf16 v[40:43], v[8:11], v[132:135], v[40:43]\n\tv_mov_b32 v59, 0x3f803f80\n\ts_waitcnt lgkmcnt(4)\n\t"
  "v_mfma_f32_16x16x32_bf16 v[4:7], v[140:143], v[12:15], v[4:7]\n\tds_read_b64_tr_b16 v[8:9], v0 offset:18944\n\tv_mul_f32 v34, 0x3eb8aa3b, v34\n\t"
  "v_mfma_f32_16x16x32_bf16 v[30:33], v[136:139], v[12:15], v[30:33]\n\tds_read_b64_tr_b16 v[10:11], v0 offset:19968\n\tv_max3_f32 v61, v60, v50, v48\n\t"
  "v_mfma_f32_16x16x32_bf16 v[40:43], v[12:15], v[144:147], v[40:43]\n\tv_mul_f32 v35, 0x3eb8aa3b, v35\n\tv_max3_f32 v60, v60, v50, v48\n\t"
  "v_mfma_f32_16x16x32_bf16 v[4:7], v[152:155], v[36:39], v[4:7]\n\tds_read_b64_tr_b16 v[12:13], v0 offset:20992\n\tv_fma_f32 v20, -v16, v47, v20\n\t"
  "v_mfma_f32_16x16x32_bf16 v[30:33], v[148:151], v[36:39], v[30:33]\n\tds_read_b64_tr_b16 v[14:15], v0 offset:22016\n\tv_fma_f32 v21, -v17, v47, v21\n\t"
  "v_mfma_f32_16x16x32_bf16 v[40:43], v[36:39], v[156:159], v[40:43]\n\tv_fma_f32 v22, -v18, v47, v22\n\ts_waitcnt lgkmcnt(4)\n\t"
  "v_mfma_f32_16x16x32_bf16 v[4:7], v[164:167], v[64:67], v[4:7]\n\tds_read_b64_tr_b16 v[16:17], v0 offset:23040\n\tv_fma_f32 v23, -v19, v47, v23\n\t"
  "v_mfma_f32_16x16x32_bf16 v[30:33], v[160:163], v[64:67], v[30:33]\n\tds_read_b64_tr_b16 v[18:19], v0 offset:24064\n\tv_cvt_pk_bf16_f32 v68, v20, v21\n\t"
  "v_mfma_f32_16x16x32_bf16 v[40:43], v[64:67], v[168:171], v[40:43]\n\tv_permlane16_swap_b32 v60, v61\n\t"
  "v_mfma_f32_16x16x32_bf16 v[4:7], v[176:179], v[24:27], v[4:7]\n\tds_read_b64_tr_b16 v[36:37], v0 offset:25088\n\tv_max_f32 v62, v60, v61\n\t"
  "v_mfma_f32_16x16x32_bf16 v[30:33], v[172:175], v[24:27], v[30:33]\n\tds_read_b64_tr_b16 v[38:39], v0 offset:26112\n\tv_max_f32 v63, v60, v61\n\t"
  "v_mfma_f32_16x16x32_bf16 v[40:43], v[24:27], v[180:183], v[40:43]\n\tds_read_b64_tr_b16 v[24:25], v0 offset:27136\n\ts_waitcnt lgkmcnt(5)\n\t"
  "v_mfma_f32_16x16x32_bf16 v[4:7], v[188:191], v[8:11], v[4:7]\n\tds_read_b64_tr_b16 v[26:27], v0 offset:28160\n\tv_cvt_pk_bf16_f32 v69, v22, v23\n\t"
  "v_mfma_f32_16x16x32_bf16 v[30:33], v[184:187], v[8:11], v[30:33]\n\tds_read_b64_tr_b16 v[20:21], v0 offset:29184\n\tds_read2_b32 v[56:57], v46 offset0:16 offset1:18\n\t"
  "v_mfma_f32_16x16x32_bf16 v[40:43], v[8:11], v[192:195], v[40:43]\n\tds_read_b64_tr_b16 v[22:23], v0 offset:30208\n\tv_permlane32_swap_b32 v62, v63\n\t"
  "v_mfma_f32_16x16x32_bf16 v[4:7], v[200:203], v[12:15], v[4:7]\n\tv_max_f32 v60, v62, v63\n\tv_mul_f32_e64 v60, -v60, v34\n\t"
  "v_mfma_f32_16x16x32_bf16 v[30:33], v[196:199], v[12:15], v[30:33]\n\tv_fma_f32 v50, v50, v34, v60\n\tv_fma_f32 v48, v48, v34, v60\n\t"
  "v_mfma_f32_16x16x32_bf16 v[40:43], v[12:15], v[204:207], v[40:43]\n\tv_fma_f32 v29, v29, v34, v60\n\ts_waitcnt lgkmcnt(5)\n\t"
  "v_mfma_f32_16x16x32_bf16 v[4:7], v[212:215], v[16:19], v[4:7]\n\tv_fma_f32 v28, v28, v34, v60\n\tds_read_b128 v[64:67], v3 offset:320\n\t"
  "v_mfma_f32_16x16x32_bf16 v[30:33], v[208:211], v[16:19], v[30:33]\n\tv_exp_f32 v50, v50\n\t"
  "v_mfma_f32_16x16x32_bf16 v[40:43], v[16:19], v[216:219], v[40:43]\n\tv_exp_f32 v48, v48\n\t"
  "v_mfma_f32_16x16x32_bf16 v[4:7], v[224:227], v[36:39], v[4:7]\n\tv_exp_f32 v29, v29\n\t"
  "v_mfma_f32_16x16x32_bf16 v[30:33], v[220:223], v[36:39], v[30:33]\n\tv_cvt_pk_bf16_f32 v60, v50, v48\n\ts_waitcnt lgkmcnt(1)\n\t"
  "v_mfma_f32_16x16x32_bf16 v[4:7], v[236:239], v[24:27], v[4:7]\n\tv_exp_f32 v28, v28\n\t"
  "v_mfma_f32_16x16x32_bf16 v[30:33], v[232:235], v[24:27], v[30:33]\n\tv_cvt_pk_bf16_f32 v61, v29, v28\n\tv_mul_f32 v50, v50, v72\n\t"
  "v_mfma_f32_16x16x32_bf16 v[4:7], v[248:251], v[20:23], v[4:7]\n\tv_mul_f32 v48, v48, v73\n\tv_mfma_f32_16x16x16_bf16 v[8:11], v[58:59], v[60:61], 0\n\tv_mul_f32 v29, v29, v52\n\t"
  "v_mul_f32 v28, v28, v53\n\t"
  "v_mfma_f32_16x16x32_bf16 v[30:33], v[244:247], v[20:23], v[30:33]\n\tv_cvt_pk_bf16_f32 v4, v4, v5\n\tv_cvt_pk_bf16_f32 v5, v6, v7\n\t"
  "v_mfma_f32_16x16x32_bf16 v[40:43], v[36:39], v[228:231], v[40:43]\n\tv_cvt_pk_bf16_f32 v50, v50, v48\n\tv_cvt_pk_bf16_f32 v29, v29, v28\n\t"
  "v_mfma_f32_16x16x32_bf16 v[40:43], v[24:27], v[240:243], v[40:43]\n\tv_cvt_pk_bf16_f32 v30, v30, v31\n\tv_cvt_pk_bf16_f32 v31, v32, v33\n\t"
  "v_mfma_f32_16x16x32_bf16 v[40:43], v[20:23], v[252:255], v[40:43]\n\t"
  GST(14)
  "v_perm_b32 v32, v50, v50, v49\n\tv_mfma_f32_16x16x16_bf16 v[12:15], v[4:5], v[30:31], 0\n\tv_mfma_f32_16x16x16_bf16 v[4:7], v[74:75], v[30:31], 0\n\ts_waitcnt lgkmcnt(0)\n\t"
  "v_fma_f32 v40, -v64, v47, v40\n\tv_fma_f32 v41, -v65, v47, v41\n\tv_fma_f32 v42, -v66, v47, v42\n\tv_cndmask_b32_e64 v28, v12, v13, s[68:69]\n\t"
  "v_fma_f32 v28, v28, v56, v2\n\tv_cndmask_b32_e64 v31, v14, v15, s[68:69]\n\tv_fma_f32 v31, v31, v57, v55\n\tv_max_f32 v64, v28, v31\n\t"
  "v_cndmask_b32_e64 v46, v4, v5, s[68:69]\n\tv_fma_f32 v46, v46, v52, v54\n\tv_cndmask_b32_e64 v34, v6, v7, s[68:69]\n\tv_fma_f32 v34, v34, v53, v45\n\t"
  "v_max3_f32 v65, v64, v46, v34\n\tv_max3_f32 v64, v64, v46, v34\n\tv_fma_f32 v43, -v67, v47, v43\n\tv_cvt_pk_bf16_f32 v72, v40, v41\n\t"
  "v_permlane16_swap_b32 v64, v65\n\tv_max_f32 v66, v64, v65\n\tv_max_f32 v67, v64, v65\n\tv_cvt_pk_bf16_f32 v73, v42, v43\n\t"
  "v_perm_b32 v33, v50, v50, v51\n\tv_permlane32_swap_b32 v66, v67\n\tv_max_f32 v64, v66, v67\n\tv_mul_f32_e64 v64, -v64, v35\n\t"
  "v_fma_f32 v46, v46, v35, v64\n\tv_fma_f32 v34, v34, v35, v64\n\tv_fma_f32 v28, v28, v35, v64\n\tv_fma_f32 v31, v31, v35, v64\n\t"
  "v_exp_f32 v46, v46\n\tv_exp_f32 v34, v34\n\tv_exp_f32 v28, v28\n\tv_exp_f32 v31, v31\n\t"
  "v_cvt_pk_bf16_f32 v64, v46, v34\n\tv_cvt_pk_bf16_f32 v65, v28, v31\n\tv_mul_f32 v46, v46, v52\n\tv_mul_f32 v34, v34, v53\n\t"
  "v_mfma_f32_16x16x16_bf16 v[40:43], v[58:59], v[64:65], 0\n\tv_mul_f32 v28, v28, v56\n\tv_mul_f32 v31, v31, v57\n\tv_cvt_pk_bf16_f32 v46, v46, v34\n\t"
  "v_cvt_pk_bf16_f32 v28, v28, v31\n\tv_perm_b32 v20, v46, v46, v49\n\tv_perm_b32 v34, v29, v29, v49\n\tv_perm_b32 v21, v46, v46, v51\n\t"
  "v_perm_b32 v35, v29, v29, v51\n\tv_perm_b32 v22, v28, v28, v49\n\tv_perm_b32 v23, v28, v28, v51\n\t"
  "v_mfma_f32_16x16x32_bf16 v[4:7], v[68:71], v[32:35], 0\n\tv_rcp_f32 v8, v8\n\t"
  "v_mfma_f32_16x16x32_bf16 v[24:27], v[70:73], v[20:23], 0\n\tv_rcp_f32 v40, v40\n\ts_nop 1\n\tv_mul_f32 v4, v4, v8\n\t"
  "v_mul_f32 v5, v5, v8\n\tv_mul_f32 v6, v6, v8\n\tv_mul_f32 v7, v7, v8\n\tv_cvt_pk_bf16_f32 v28, v4, v5\n\t"
  "v_mul_f32 v24, v24, v40\n\tv_mul_f32 v25, v25, v40\n\tv_mul_f32 v26, v26, v40\n\tv_mul_f32 v27, v27, v40\n\t"
  "v_cvt_pk_bf16_f32 v30, v24, v25\n\tv_cvt_pk_bf16_f32 v31, v26, v27\n\tv_cvt_pk_bf16_f32 v29, v6, v7\n\tv_permlane16_swap_b32 v28, v30\n\t"
  "s_nop 0\n\tv_permlane16_swap_b32 v29, v31\n\tbuffer_store_dwordx4 v[28:31], v44, s[56:59], 0 offen sc1\n\t"
  "s_cmp_eq_u32 s63, 2\n\ts_cbranch_scc1 .Lg_ret\n\ts_nop 0"
  G_OPS);
  else asm volatile("s_branch .Lg_body\n.Lg_ret:\n\ts_nop 0" G_OPS);
  static_for<0, 45>(pin);
  ST(!L1, 16);
  if (L1) {
    KSLP(7, SLP_OB1);
    cnt_add(smem, C_OB, 0);
    mid();
    cnt_wait(smem, C_X2, 4);
    return;
  }
  u32 t16 = KADD(8, now_(), DB2);
  cnt_add(smem, C_OB, 0);
  const int bid = BIDX, lane = lane_id();
  int og0 = OFF_O2 + MB_ + RB_OF(bid) * 32768 + g * 4096, og1 = og0 + 16384, vo = lane * 16;
  u32 sa = (u32)(L_TB + g * 4096 + lane * 64 + (lane & 14) * 4 + 4);
  asm volatile("" : "+s"(og0), "+s"(og1), "+v"(vo), "+v"(sa));
  u32 dl;
  for (unsigned n = 0; n < (1u << 24); ++n) {
    asm volatile("" ::: "memory");
    const u32 c0 = *(const volatile LAS u32*)(size_t)(u32)(L_MI + C_OV), c1 = *(const volatile LAS u32*)(size_t)(u32)(L_MI + C_OB);
    dl = *(const volatile LAS u32*)(size_t)(u32)(L_MI + M_DL);
    if ((u32)__builtin_amdgcn_readfirstlane(min(c0 + 11u, c1)) >= 16u) break;
  }
  gate(t16 + dl);
  ST(1, 15);
  const u32 mark = x0_mark(tag);
  o_get(wrs, vo, og0, og1, L_TB + g * 4096, sa, mark, 25);
  asm volatile("s_barrier\n\ts_waitcnt vmcnt(0)\n\ts_barrier" ::: "memory");
  if (__any(*(const LAS u32*)(size_t)(sa + 16384) == mark)) {
    FAILC(12);
    for (unsigned spins = 0; spins <= SPIN_CAP; ++spins) {
      dma4k(wrs, vo, og0, L_TA + g * 4096);
      dma4k(wrs, vo, og1, L_TA + 16384 + g * 4096);
      asm volatile("s_waitcnt vmcnt(0)" ::: "memory");
      if (!__any(*(const LAS u32*)(size_t)(sa - (L_TB - L_TA)) == mark || *(const LAS u32*)(size_t)(sa - (L_TB - L_TA) + 16384) == mark)) break;
    }
    smem[L_MI + M_FX + g] = 1;
    asm volatile("s_waitcnt lgkmcnt(0)" ::: "memory");
  }
}

using u32x8 = __attribute__((ext_vector_type(8))) unsigned;
using u32x16 = __attribute__((ext_vector_type(16))) unsigned;
__global__ __launch_bounds__(512, 2) KATTR void k_mega(const u16*, const u16*, const u16*, const u16*, const u16*, const u16*, const u16*, const u16*,
                                                       const u16*, const u16*, const u16*, char*, u32*) {
  extern __shared__ __attribute__((aligned(16))) char smem[];
  u32x16 ka;
  u32x8 kb;
  u32x2 kc;
  asm volatile("s_load_dwordx16 %0, %3, 0x0\n\ts_load_dwordx8 %1, %3, 0x40\n\ts_load_dwordx2 %2, %3, 0x60"
               : "=&s"(ka), "=&s"(kb), "=&s"(kc) : "s"(__builtin_amdgcn_kernarg_segment_ptr()) : "memory");
  const int tid = TIDX, lane = tid & 63;
  const int w = __builtin_amdgcn_readfirstlane(tid >> 6);
  const int bid = BIDX;
  ST_INIT(); ST(0, 0); ST(1, 0);
  if (w == 0) {
    u64 pc;
    int clen;
    asm volatile("s_getpc_b64 %0\n.Lcw_base:\n\ts_mov_b32 %1, .Lcw_end-.Lcw_base-4" : "=s"(pc), "=s"(clen));
    asm volatile("s_mov_b32 m0, %2\n\ts_nop 0\n\tglobal_load_lds_dword %0, %1" :: "v"(min(min(lane, 15) * 128 + (bid >> 3) * 2048, clen)), "s"(pc), "s"(L_FS) : "memory");
  }
  const int kp = (bid & 7) * 32 + (bid >> 3), hb = HB_OF(bid);
  const int c = (w >> 1) & 1, g = w & 3;
  u32 po = (u32)(kp * 512 + (tid & 255) * 2);
  const int pb = (bid & 7) + 8 * (bid >> 6), ps = (bid >> 3) & 7, gr = pb * 8 + ps;
  u32 xo = (u32)((w < 4 ? (pb * 64 + 16 * w) * 1024 + ps * 128 : gr * 8192 + g * 2048) + (lane & 7) * 16 + (lane >> 3) * (w < 4 ? 2048 : 128));
  asm volatile("" : "+v"(po), "+v"(xo) :: "memory");
  asm volatile("s_waitcnt lgkmcnt(0)" : "+s"(ka), "+s"(kb), "+s"(kc) :: "memory");
  ST_FIX();
#define KP(T_, v_, i_) ((T_)(((u64)v_[2 * (i_) + 1] << 32) | v_[2 * (i_)]))
  const u16 *x0 = KP(const u16*, ka, 0), *A_re = KP(const u16*, ka, 1), *A_im = KP(const u16*, ka, 2), *B_re = KP(const u16*, ka, 3), *B_im = KP(const u16*, ka, 4);
  const u16 *C_re = KP(const u16*, ka, 5), *C_im = KP(const u16*, ka, 6), *log_dt = KP(const u16*, ka, 7), *Dskip = KP(const u16*, kb, 0), *wqkv = KP(const u16*, kb, 1), *wo = KP(const u16*, kb, 2);
  char* ws = KP(char*, kb, 3);
  u32* out = KP(u32*, kc, 0);
#define LD_A() asm volatile("global_load_ushort v248, %0, %1\n\tglobal_load_ushort v249, %0, %2" :: "v"(po), "s"(A_re), "s"(A_im) : "memory", "v248", "v249")
  u32 ldtw = 0, dskw = 0;
  if (w < 4) {
    asm volatile("s_load_dword %0, %2, %4\n\ts_load_dword %1, %3, %4" : "=&s"(ldtw), "=&s"(dskw) : "s"(log_dt), "s"(Dskip), "s"(4 * kp) : "memory");
    LD_A();
  }
  asm volatile("global_load_dwordx4 v[240:243], %0, %1\n\tglobal_load_dwordx4 v[244:247], %0, %1 offset:1024\n\tglobal_load_dword v254, %2, %3 sc1"
               :: "v"(xo), "s"(x0), "v"(OFF_EP), "s"(ws) : "memory", "v240", "v254");
  if (w >= 4) {
    asm volatile("global_load_ushort v250, %0, %1\n\tglobal_load_ushort v251, %0, %2\n\tglobal_load_ushort v252, %0, %3\n\tglobal_load_ushort v253, %0, %4"
                 :: "v"(po), "s"(B_re), "s"(B_im), "s"(C_re), "s"(C_im) : "memory", "v250", "v253");
    LD_A();
  }
  __builtin_amdgcn_sched_barrier(0);
  const rsrc_t wrs = __builtin_amdgcn_make_buffer_rsrc((void*)ws, 0, WSB, 0x00020000);
  if (w >= 4) {
    asm volatile("s_mov_b32 m0, %2\n\ts_nop 0\n\tglobal_load_lds_dword %0, %1" :: "v"((bid * 16 + g * 4 + min(lane, 3)) * 128), "s"(wqkv), "s"(L_FS + w * 256) : "memory");
    u32x4 zz = {0, 0, 0, 0};
    asm volatile("" : "+v"(zz));
    const int z0 = (int)opaque((u32)(L_U + (lane >> 5) * 4608 + (lane & 31) * 16)), z1 = (int)opaque((u32)(L_MI + min((tid & 255) * 16 - 1024, 1088)));
    *(u32x4*)(smem + (g == 0 ? z0 : z1)) = zz;
    asm volatile("s_nop 1\n\ts_waitcnt lgkmcnt(0)" :: "v"(zz) : "memory");
  }
  if (w >= 4) asm volatile("s_load_dword %0, %1, %2" : "=s"(ldtw) : "s"(log_dt), "s"(4 * kp) : "memory");
  __builtin_amdgcn_sched_barrier(0);
#define XT_GET(x_) asm volatile("v_mov_b64 %0, v[240:241]\n\tv_mov_b64 %1, v[242:243]\n\tv_mov_b64 %2, v[244:245]\n\tv_mov_b64 %3, v[246:247]" \
                   : "=v"(x_[0].xy), "=v"(x_[0].zw), "=v"(x_[1].xy), "=v"(x_[1].zw) :: "memory")
  const int ftb = c ? L_TA : 0;
#define PUT(e_, r_, i_) *(LAS u32*)(wa + (e_) * FTS) = pkh(r_, i_)
#define CMR(a_, b_) fmaf(a_[0], b_[0], -(a_[1] * b_[1]))
#define CMI(a_, b_) fmaf(a_[0], b_[1], a_[1] * b_[0])
#define CMUL(a_, b_) (f32x2){CMR(a_, b_), CMI(a_, b_)}
  struct P0A { u32 gad[12], sad[8], ua, ur; };
  auto p0_addr = [&](P0A& A, int lane, int ut) __attribute__((always_inline)) {
    const int i = lane & 15, kk = lane >> 4;
    const u32 sb = (u32)((L_KT - KTO + c * TBS - 0x7E00) & 0xFFFF) * 0x10001u;
    const float f0 = __uint_as_float(opaque(__float_as_uint((float)((ut < 2 ? 1023 : 2047) - 512 * (w & 1) - 8 * lane))));
#pragma unroll
    for (int e = 0; e < 6; ++e) { const u32 ga = tb_addr(pk2(f0 - (float)(2 * e), f0 - (float)(2 * e + 1)), sb); A.gad[2 * e] = ga & 0xFFFFu; A.gad[2 * e + 1] = ga >> 16; }
    if (ut < 2) {
      A.ua = (u32)(ftb + i * FTS + kk * 16); A.ur = A.ua + ut * 16 * FTS;
      const int a0 = 128 * (i >> 3) + (i & 7), a1 = i < 8 ? 256 + 128 * (i >> 2) + 2 * (i & 3) : 128 * (i >> 1) + 4 * (i & 1), a2 = min(1024 + 128 * i, 2064);
#pragma unroll
      for (int t = 0; t < 2; ++t) {
        const float st = (t && i >= 8) ? 16.f : 8.f;
        const float fb = (float)(t ? a2 : ut == 0 ? a0 : a1) + st * (float)(4 * kk);
        const u32 s0 = tb_addr(pk2(fb, fb + st), sb), s1 = tb_addr(pk2(fb + 2.f * st, fb + 3.f * st), sb);
        A.sad[4 * t] = s0 & 0xFFFFu; A.sad[4 * t + 1] = s0 >> 16; A.sad[4 * t + 2] = s1 & 0xFFFFu; A.sad[4 * t + 3] = s1 >> 16;
      }
      asm volatile("" : "+v"(A.ua), "+v"(A.ur), "+v"(A.sad[0]), "+v"(A.sad[1]), "+v"(A.sad[2]), "+v"(A.sad[3]), "+v"(A.sad[4]), "+v"(A.sad[5]), "+v"(A.sad[6]), "+v"(A.sad[7]));
    }
    asm volatile("" : "+v"(A.gad[0]), "+v"(A.gad[1]), "+v"(A.gad[2]), "+v"(A.gad[3]), "+v"(A.gad[4]), "+v"(A.gad[5]), "+v"(A.gad[6]), "+v"(A.gad[7]), "+v"(A.gad[8]), "+v"(A.gad[9]), "+v"(A.gad[10]));
    __builtin_amdgcn_sched_barrier(0);
  };
  auto k_unit = [&](const P0A& A, float dk, bool lag0) __attribute__((always_inline)) {
    f16x8 fr[8], fc[8];
    const f32x2 bd = *(const f32x2*)(smem + L_P0 + 512 + c * 8);
    __builtin_amdgcn_sched_barrier(0);
#pragma unroll
    for (int e = 0; e < 8; ++e) {
      fc[e] = *(const LAS f16x8*)(size_t)(A.ua + e * 64 + 41 * FTS);
      fr[e] = *(const LAS f16x8*)(size_t)(A.ur + e * 64);
      __builtin_amdgcn_sched_barrier(0);
    }
    f16x8 f2[8];
    if (!(w & 1)) {
#pragma unroll
      for (int e = 0; e < 8; ++e) { f2[e] = *(const LAS f16x8*)(size_t)(A.ua + e * 64 + 32 * FTS); __builtin_amdgcn_sched_barrier(0); }
    }
    const int ec = bexp(bd[0] + bd[1] + fabsf(dk));
    const float f0 = __uint_as_float((u32)max(127 + bexp(bd[0]) - ec, 0) << 23), f1 = __uint_as_float((u32)max(127 + bexp(bd[1]) - ec, 0) << 23);
    const float d0s = lag0 ? dk * __uint_as_float((u32)(268 - ec) << 23) : 0.f;
    auto unit = [&](const f16x8 (&fr)[8], u32x4 sa, float add) __attribute__((always_inline)) {
      f32x4 k0 = {0.f, 0.f, 0.f, 0.f}, k1 = k0;
#pragma unroll
      for (int e = 0; e < 4; ++e) k0 = __builtin_amdgcn_mfma_f32_16x16x32_f16(fc[e], fr[e], k0, 0, 0, 0);
#pragma unroll
      for (int e = 4; e < 8; ++e) k1 = __builtin_amdgcn_mfma_f32_16x16x32_f16(fc[e], fr[e], k1, 0, 0, 0);
      k0 *= f0; k0[0] += add;
      const u32 h01 = pkh(fmaf(k1[0], f1, k0[0]), fmaf(k1[1], f1, k0[1])), h23 = pkh(fmaf(k1[2], f1, k0[2]), fmaf(k1[3], f1, k0[3]));
      *(LAS u16*)((LAS char*)(size_t)sa[0] + KTO) = (u16)h01; *(LAS u16*)((LAS char*)(size_t)sa[1] + KTO) = (u16)(h01 >> 16);
      *(LAS u16*)((LAS char*)(size_t)sa[2] + KTO) = (u16)h23; *(LAS u16*)((LAS char*)(size_t)sa[3] + KTO) = (u16)(h23 >> 16);
    };
    if (!(w & 1)) {
      unit(fr, (u32x4){A.sad[0], A.sad[1], A.sad[2], A.sad[3]}, d0s);
      unit(f2, (u32x4){A.sad[4], A.sad[5], A.sad[6], A.sad[7]}, 0.f);
    } else {
      unit(fr, (u32x4){A.sad[0], A.sad[1], A.sad[2], A.sad[3]}, 0.f);
      *(float*)(smem + L_MI + 4 * c) = __uint_as_float((u32)(ec - 14) << 23);
    }
  };
  auto k_get = [&](u32 (&kh)[11], const P0A& A) __attribute__((always_inline)) {
#pragma unroll
    for (int e = 0; e < 11; ++e) kh[e] = *(const LAS u16*)((const LAS char*)(size_t)A.gad[e] + KTO);
    __builtin_amdgcn_sched_barrier(0);
  };
  auto k_copies = [&](const u32 (&kh)[11], int lane, bool low) __attribute__((always_inline)) {
    u32 kw[6];
#pragma unroll
    for (int e = 0; e < 5; ++e) kw[e] = __builtin_amdgcn_perm(kh[2 * e + 1], kh[2 * e], 0x05040100u);
    kw[5] = kh[10];
    if (low && (w & 1) && lane == 63) { kw[4] = 0; kw[5] = 0; }
    build_copies(smem, c, (w & 1) * 64 + lane + (low ? 128 : 0), kw);
    __builtin_amdgcn_sched_barrier(0);
  };
  __builtin_amdgcn_sched_barrier(0);
  int w2 = w;
  asm volatile("" : "+s"(w2));
  if (w < 4) {
  P0A pa;
  p0_addr(pa, lane, w & 1);
  LAS char* wa = (LAS char*)(size_t)opaque((u32)(ftb + (tid & 127) * 4));
  u32 tg;
  const bool lo = pb < 16;
  const int xc8 = lane >> 3, xrp = lane & 7;
  const int xts = lo ? 128 : XTS, xt2 = 2 * xts;
  const int xto = (int)opaque((u32)(lo ? OFF_X1 + (pb * 4 + w) * XBS + (ps * 32 + 4 * xc8) * 128 + xrp * 16
                                       : XTO(ps * 32 + 4 * xc8) + ((pb & 15) * 64 + 16 * w + 2 * xrp) * 8));
  auto xpub = [&]() __attribute__((always_inline)) {
    u32x4 q[4];
    u32x4 xt[2];
    __builtin_amdgcn_sched_barrier(0);
    asm volatile("s_waitcnt vmcnt(0)\n\tv_add_u32 %0, 1, v254" : "=v"(tg) :: "memory");
    XT_GET(xt);
    const int sl = ((lane & 7) * 8 + (lane >> 3)) * 4;
#pragma unroll
    for (int e = 0; e < 8; ++e) xt[e & 1][e >> 1] = (u32)__builtin_amdgcn_ds_bpermute(sl, (int)xt[e & 1][e >> 1]);
#pragma unroll
    for (int e = 0; e < 4; ++e) q[e] = (u32x4){xt[0][e], x0_mark(tg), xt[1][e], x0_mark(tg)};
    asm volatile("" : "+v"(q[0]), "+v"(q[1]), "+v"(q[2]), "+v"(q[3]));
#pragma unroll
    for (int e = 0; e < 4; ++e) stw(wrs, xto + (e & 1) * xts + (e >> 1) * xt2, q[e]);
    asm volatile("s_nop 1" :: "v"(q[0]), "v"(q[1]), "v"(q[2]), "v"(q[3]) : "memory");
    ST(0, 3);
    __builtin_amdgcn_sched_barrier(0);
  };
  u32 ha, hz, hs, ht;
  C1Pre cp;
  int xs0, xs1, xl0, xl1, xv;
  u32 za;
  auto setup1 = [&]() __attribute__((always_inline)) {
    SB();
    const int t = (w & 1) * 64 + lane;
    ha = (u32)(L_KT + c * TBS - 0x7E00 + 2 * (0x44FF - t)); hz = ha - (t == 127 ? 2 : 0);
    hs = (lane & 1) ? 0x01000302u : 0x03020302u; ht = (lane & 1) ? 0x01000100u : 0x01000302u;
    asm volatile("" : "+v"(ha), "+v"(hz), "+v"(hs), "+v"(ht));
    SB();
  };
  auto setup2 = [&]() __attribute__((always_inline)) {
    SB();
    c1_pre(cp, kp, w, lane);
    xs0 = OFF_X1 + RB_OF(bid) * XBS + w * 8192; xs1 = xs0 + 4096; xl0 = L_TA + w * 8192; xl1 = xl0 + 4096; xv = x_lane(lane);
    za = (u32)(((lane >> 1) & 7) * CSTR + 4096 + (lane & 1) * 16);
    asm volatile("" : "+s"(xs0), "+s"(xs1), "+s"(xl0), "+s"(xl1), "+v"(xv), "+v"(za));
    SB();
  };
  {
    float ar, ai;
#define B0() do { SB(); __builtin_amdgcn_s_barrier(); SB(); } while (0)
    B0();
    asm volatile("s_waitcnt vmcnt(3)\n\tv_lshlrev_b32 %0, 16, v248\n\tv_lshlrev_b32 %1, 16, v249" : "=v"(ar), "=v"(ai) :: "memory");
    asm volatile("s_waitcnt lgkmcnt(0)" : "+s"(ldtw), "+s"(dskw) :: "memory");
    ST(0, 1);
    const float dt = rbf(__expf(c ? hi2f(ldtw) : lo2f(ldtw)));
    const float zr = dt * ar * 1.4426950408889634f, zi = dt * ai * 0.15915494309189535f;
    float cs = __builtin_amdgcn_exp2f(fmaxf(zr, 0.f) * -1928.f);
    f32x2 P[8], Q1, R[17];
    { float a_, b_; cexp2(zr, zi, a_, b_); P[1] = (f32x2){a_, b_}; cexp2(zr * 128.f, zi * 128.f, a_, b_); Q1 = (f32x2){a_, b_}; }
    asm volatile("" : "+v"(cs), "+v"(P[1]), "+v"(Q1));
    xpub();
    P[2] = CMUL(P[1], P[1]);
    const f32x2 Q2 = CMUL(Q1, Q1);
    R[1] = Q1 * cs; R[2] = Q2 * cs;
    P[3] = CMUL(P[2], P[1]); P[4] = CMUL(P[2], P[2]);
    const f32x2 Q4 = CMUL(Q2, Q2);
    R[3] = CMUL(R[1], Q2); R[4] = Q4 * cs;
    P[5] = CMUL(P[4], P[1]); P[6] = CMUL(P[3], P[3]); P[7] = CMUL(P[4], P[3]);
#pragma unroll
    for (int j = 5; j <= 16; ++j) R[j] = CMUL(R[j - 4], Q4);
    asm volatile("" : "+v"(R[13]), "+v"(R[14]), "+v"(R[15]), "+v"(R[16]), "+v"(P[7]), "+v"(P[6]), "+v"(P[5]));
    PUT(0, cs, 0.f); PUT(8, R[1][0], R[1][1]);
#pragma unroll
    for (int r = 1; r < 8; ++r) { PUT(r, P[r][0] * cs, P[r][1] * cs); PUT(8 + r, CMR(R[1], P[r]), CMI(R[1], P[r])); }
    asm volatile("" ::: "memory");
#pragma unroll
    for (int j = 2; j < 4; ++j) {
      PUT(16 + 4 * (j - 2), R[j][0], R[j][1]);
#pragma unroll
      for (int r = 1; r < 4; ++r) PUT(16 + 4 * (j - 2) + r, CMR(R[j], P[2 * r]), CMI(R[j], P[2 * r]));
    }
#pragma unroll
    for (int j = 4; j < 8; ++j) { PUT(24 + 2 * (j - 4), R[j][0], R[j][1]); PUT(25 + 2 * (j - 4), CMR(R[j], P[4]), CMI(R[j], P[4])); }
#pragma unroll
    for (int j = 8; j <= 16; ++j) PUT(24 + j, R[j][0], R[j][1]);
    __builtin_amdgcn_sched_barrier(0);
  }
  ST(0, 2);
  cnt_add(smem, C_F, lane);
  u32 stag = __builtin_amdgcn_readfirstlane(tg), g2;
  asm volatile("" : "+s"(stag));
  cnt_spin(smem, C_F, 8);
  k_unit(pa, c ? hi2f(dskw) : lo2f(dskw), (tid & 127) == 0);
  cnt_add(smem, C_A, lane);
  setup1();
  setup2();
  cnt_spin(smem, C_A, 4);
  ST(0, 4);
  {
    u32 kl[11];
    k_get(kl, pa);
    const u32 h0 = *(const LAS u16*)(size_t)ha, h1 = *(const LAS u16*)(size_t)(ha + 2), h2 = *(const LAS u16*)(size_t)hz;
    SB();
    u32x4 zz = {0, 0, 0, 0};
    asm volatile("" : "+v"(zz));
    *(LAS u32x4*)(size_t)za = zz;
    k_copies(kl, lane, true);
    asm volatile("" :: "v"(zz));
    cnt_add(smem, C_A, lane);
    ST(0, 5);
    {
      const u32 p = h0 | (h1 << 16), hh = __builtin_amdgcn_perm(p, p, 0x03020302u), ll = __builtin_amdgcn_perm(p, p, 0x01000100u);
      const u32 hx = __builtin_amdgcn_perm(p, p, hs), xl = __builtin_amdgcn_perm(p, p, ht);
      u32x4 c0 = {hh, hx, ll, ll}, c1 = {hh, xl, ll, ll}, c2 = {hx, ll, ll, ll}, c3 = {xl, ll, ll, h0 | (h2 << 16)};
      asm volatile("" : "+v"(c0), "+v"(c1), "+v"(c2), "+v"(c3));
      char* cop = smem + c * 4 * CSTR + ((w & 1) * 64 + lane) * 16;
      *(u32x4*)(cop + 0 * CSTR) = c0; *(u32x4*)(cop + 1 * CSTR) = c1; *(u32x4*)(cop + 2 * CSTR) = c2; *(u32x4*)(cop + 3 * CSTR) = c3;
      asm volatile("s_nop 1" :: "v"(c0), "v"(c1), "v"(c2), "v"(c3) : "memory");
      cnt_add(smem, C_KH, lane);
    }
  }
  cnt_spin(smem, C_A, 8);
  bf16x8 wof[16];
  u32x4 pfl, wfl;
  XTake xt;
  AttC ac;
  a_conv1(wrs, w, cp, pfl, wfl, g2);
  asm volatile("" : "+v"(pfl), "+v"(wfl));
  SB();
  KSLP(15, SLP_XA1);
  tags_wait(pfl, wrs, OFF_ST0 + tid * 16, stag, 19);
  dma4k(wrs, xv, xs0, xl0);
  dma4k(wrs, xv, xs1, xl1);
  KSLP(14, SLP_WE);
  SB();
  ConvK ck;
  a_blayer<true>(smem, wrs, ws, out, tid, w, wof, stag, wfl, ck, xt, ac);
  ST(1, 2);
  a_conv<false>(smem, wrs, RB_OF(bid), w, ck);
  a_blayer<false>(smem, wrs, ws, out, tid, w, wof, stag, wfl, ck, xt, ac);
  ST_OUT();
  }
  if (w2 >= 4) {
    const int lane = lane_id();
    LAS char* wa = (LAS char*)(size_t)opaque((u32)(ftb + (w & 1) * 256 + lane * 4));
    u32 tag;
    u32 st0, st1;
    int pbo;
    auto cols = [&]() __attribute__((always_inline)) {
    {
      float ar, ai, br, bi, cr, ci;
      asm volatile("v_lshlrev_b32 %0, 16, v248\n\tv_lshlrev_b32 %1, 16, v249\n\tv_lshlrev_b32 %2, 16, v250\n\t"
                   "v_lshlrev_b32 %3, 16, v251\n\tv_lshlrev_b32 %4, 16, v252\n\tv_lshlrev_b32 %5, 16, v253"
                   : "=v"(ar), "=v"(ai), "=v"(br), "=v"(bi), "=v"(cr), "=v"(ci) :: "memory");
      asm volatile("s_waitcnt lgkmcnt(0)" : "+s"(ldtw) :: "memory");
      const float dt = rbf(__expf(c ? hi2f(ldtw) : lo2f(ldtw)));
      const float dbr = dt * br, dbi = dt * bi;
      const float zr = dt * ar * 1.4426950408889634f, zi = dt * ai * 0.15915494309189535f;
      const float zp = fmaxf(zr, 0.f), g1 = __builtin_amdgcn_exp2f(zp * 1928.f);
      const float wr = (cr * dbr - ci * dbi) * g1, wi = (cr * dbi + ci * dbr) * g1;
      float bd = (fabsf(wr) + fabsf(wi)) * __builtin_amdgcn_exp2f(zp * 120.f);
      f32x2 S1, C[16];
      { float a_, b_; cexp2(zr * 8.f, zi * 8.f, a_, b_); S1 = (f32x2){a_, b_}; }
      bd = dpp_add<0x111, 0xf>(bd); bd = dpp_add<0x112, 0xf>(bd);
      const f32x2 S2 = CMUL(S1, S1);
      bd = dpp_add<0x114, 0xf>(bd); bd = dpp_add<0x118, 0xf>(bd);
      const f32x2 S4 = CMUL(S2, S2);
      bd = dpp_add<0x142, 0xa>(bd); bd = dpp_add<0x143, 0xc>(bd);
      bd = __int_as_float(__builtin_amdgcn_readlane(__float_as_int(bd), 63));
      *(float*)(smem + L_P0 + 512 + g * 4) = bd;
      const float sc = __uint_as_float((u32)(268 - bexp(bd)) << 23);
      C[0] = (f32x2){wr * sc, wi * sc};
      C[1] = CMUL(C[0], S1); C[2] = CMUL(C[0], S2); C[3] = CMUL(C[1], S2);
#pragma unroll
      for (int e = 4; e < 16; ++e) C[e] = CMUL(C[e - 4], S4);
#pragma unroll
      for (int e = 0; e < 16; ++e) *(LAS u32*)(wa + (41 + e) * FTS) = pkhn(C[e][0], C[e][1]);
    }
      cnt_add(smem, C_F, lane);
      ST(0, 2);
    };
    auto stats = [&]() __attribute__((always_inline)) {
    {
      const u32 ONES = opaque(0x3F803F80u);
      u32x4 xt[2];
      XT_GET(xt);
      float sm[2] = {0.f, 0.f}, sq[2] = {0.f, 0.f};
#pragma unroll
      for (int e = 0; e < 8; ++e) { sm[e & 1] = dot2acc(xt[e & 1][e >> 1], ONES, sm[e & 1]); sq[e & 1] = dot2acc(xt[e & 1][e >> 1], xt[e & 1][e >> 1], sq[e & 1]); }
      const bool b0 = lane & 1, b1 = lane & 2;
      const float v0 = (b0 ? sm[1] : sm[0]) + dpp_mov<DPP_XOR1>(b0 ? sm[0] : sm[1]), v1 = (b0 ? sq[1] : sq[0]) + dpp_mov<DPP_XOR1>(b0 ? sq[0] : sq[1]);
      float v = (b1 ? v1 : v0) + dpp_mov<DPP_XOR2>(b1 ? v0 : v1);
      v += dpp_mov<0x124>(v); v += dpp_mov<DPP_ROR8>(v);
      auto r = __builtin_amdgcn_permlane16_swap(__float_as_uint(v), __float_as_uint(v), false, false);
      v = __uint_as_float(r[0]) + __uint_as_float(r[1]);
      auto q = __builtin_amdgcn_permlane32_swap(__float_as_uint(v), __float_as_uint(v), false, false);
      v = (__uint_as_float(q[0]) + __uint_as_float(q[1])) * (1.f / 512.f);
      st0 = pack_stats(v, fmaxf(dpp_mov<DPP_XOR2>(v) - v * v, 0.f));
      st1 = dpp_u<DPP_XOR1>(st0);
    }
    };
    auto publish = [&]() __attribute__((always_inline)) {
      asm volatile("v_add_u32 %0, 1, v254" : "=v"(tag) :: "memory");
      stw(wrs, pbo, (u32x4){st0, tag, st1, tag});
      __builtin_amdgcn_sched_barrier(0);
    };
#define VMW(n_) asm volatile("s_waitcnt vmcnt(%0)" :: "n"(n_) : "memory")
    constexpr int PTN = 2;
    const int ut = 16 + (bid >> 3), us = ut / 3, um = ut % 3;
    const rsrc_t qrs = __builtin_amdgcn_make_buffer_rsrc((void*)wqkv, 0, 1572864, 0x00020000), ors = __builtin_amdgcn_make_buffer_rsrc((void*)wo, 0, 524288, 0x00020000);
    const int wv0 = (8 * g + (lane >> 3)) * 3072 + (((lane & 7) ^ ((lane >> 3) & 6)) << 4);
#define W_DMA1(r_, v_, s_, l_) asm volatile("s_mov_b32 m0, %3\n\ts_nop 0\n\tbuffer_load_dwordx4 %0, %1, %2 offen lds" :: "v"(v_), "s"(r_), "s"(s_), "s"(l_) : "memory")
    W_DMA1(qrs, wv0, us * 98304 + um * 1024 + (bid & 7) * 128, L_WR + g * 1024);
    W_DMA1(ors, ((bid >> 3) * 32 + 8 * g + (lane >> 3)) * 1024 + (((lane & 7) ^ (((lane >> 3) & 2) | ((g & 1) << 2))) << 4), (bid & 7) * 128, L_WR + 4096 + g * 1024);
    B0();
    const int ln = lane;
    pbo = (int)opaque((u32)(lane == 0 ? OFF_SG + gr * 64 + g * 16 : WSB));
    const int wvo = (int)opaque((u32)(wv0 + hb * 128));
#define WSO(s_) ((s_) < 4 ? 40960 + (s_) * 12288 : L_WX - L_WS)
    auto wraw = [&](auto S_) __attribute__((always_inline)) { constexpr int s = decltype(S_)::value; w_dma(qrs, wvo, s * 98304, L_WS + WSO(s) + g * 1024); };
    __builtin_amdgcn_s_setprio(1);
    VMW(8 + PTN);
    ST(0, 1);
    stats();
    VMW(7 + PTN);
    publish();
    ST(0, 10);
    VMW(2 + PTN);
    cols();
    SB();
    __builtin_amdgcn_s_setprio(0);
    static_for<0, R1D>(wraw);
    VMW(3 * R1D + 1); cnt_add(smem, C_B, lane);
    SB();
    const u32 wga = opaque((u32)(L_WS + (ln >> 2) * 128 + (((2 * g + ((ln & 3) >> 1)) ^ ((ln >> 2) & 6)) << 4) + (ln & 1) * 8)), wgb = opaque(wga + 53248u);
    const u32 to = opaque((u32)(L_WR + 4096 + (8 * (lane >> 4) + ((lane >> 2) & 3)) * 128 + (((2 * g + ((lane & 3) >> 1)) ^ (((lane >> 2) & 2) | ((lane >> 2) & 4))) << 4) + (lane & 1) * 8));
    const int us0 = (int)opaque((u32)(OFF_WQ + us * 98304 + ((bid & 7) * 4 + g) * 3072 + um * 1024 + lane * 16));
    const int us1 = (int)opaque((u32)((bid < 128 ? OFF_WO + (((bid & 7) * 16 + (bid >> 3)) * 4 + g) * 1024 : WSB) + lane * 16));
    cnt_spin(smem, C_B, 4);
    {
#define TR4(a_) __builtin_amdgcn_ds_read_tr16_b64_v4i16((LAS bf16x4*)(size_t)(a_))
      const bf16x4 q0 = TR4(wgb + (L_WR - L_WS - 53248)), q1 = TR4(wgb + (L_WR - L_WS - 53248 + 2048)), q2 = TR4(to), q3 = TR4(to + 512);
      cnt_add(smem, C_B, lane);
      stw(wrs, us0, __builtin_bit_cast(u32x4, __builtin_shufflevector(q0, q1, 0, 1, 2, 3, 4, 5, 6, 7)));
      stw(wrs, us1, __builtin_bit_cast(u32x4, __builtin_shufflevector(q2, q3, 0, 1, 2, 3, 4, 5, 6, 7)));
      SB();
    }
    static_for<R1D, R1F>(wraw);
    SB();
    const u32 stag = __builtin_amdgcn_readfirstlane(tag);
    const int fso = (int)opaque((u32)(lane < (bid < 128 ? 2 : 1) ? OFF_ST0 + 4096 + lane * 4096 + ((bid & 7) * 4 + g) * 128 + (bid >> 3) * 4 : WSB));
    const int wbo = (int)opaque((u32)((hb * 4 + g) * 3072 + ln * 16));
    const int fo = (int)opaque((u32)(OFF_ST0 + 4096 + (hb * 4 + g) * 128 + (ln & 7) * 16));
    const int r0 = 256 * g + 2 * ln;
    const int sto = (int)opaque((u32)(OFF_SG + r0 * 8)), xto = (int)opaque((u32)(XTO(kp) + r0 * 8));
    const int xlo = (int)opaque((u32)(OFF_X1 + (16 * g + (ln >> 3)) * XBS + kp * 128 + (ln & 7) * 16));
    const u32 uw = opaque((u32)(L_U + (256 + r0) * 2)), xw = opaque((u32)(L_X + r0 * 4));
    const int mso = (int)opaque((u32)(ln == 0 ? OFF_ST0 + kp * 16 + g * 4 : WSB));
    const int p2o = (int)opaque((u32)(OFF_X2 + (kp * XS_ + 256 * g + 4 * ln) * 4)), pso = (int)opaque((u32)(ln < 2 ? OFF_ST2 + (512 * g + 2 * bid + ln) * 16 : WSB));
    SB();
    VMW(3 * (R1F - R1D));
    __builtin_amdgcn_raw_buffer_store_b32(tag, wrs, fso, 0, 16);
    ST(0, 6);
    SB();
    static_for<R1F, 5>(wraw);
    W_DMA1(qrs, wvo, 5 * 98304, L_SP + g * 1024);
    bf16x8 wf[48];
    GPre gp;
    {
      auto wget = [&](auto S_) __attribute__((always_inline)) {
        constexpr int s = decltype(S_)::value, o = WSO(s) >= 53248 ? WSO(s) - 53248 : WSO(s);
        w_get<3 * s, o>(wf[3 * s], WSO(s) >= 53248 ? wgb : wga);
        w_get<3 * s + 1, o + 4096>(wf[3 * s + 1], WSO(s) >= 53248 ? wgb : wga);
        w_get<3 * s + 2, o + 8192>(wf[3 * s + 2], WSO(s) >= 53248 ? wgb : wga);
      };
      const u32 mk = x0_mark(stag);
      auto wld = [&](auto I_) __attribute__((always_inline)) { w_ld<decltype(I_)::value>(wf[decltype(I_)::value], wbo, wrs); };
      u32x4 gs[4], gx[4];
#define G_LD(e_) do { gs[e_] = ldws(wrs, sto + ((e_) & 1) * 1024, ((e_) >> 1) * 8192); gx[e_] = (e_) < 2 ? ldws(wrs, xlo, (e_) * 8 * XBS) : ldw(wrs, xto + ((e_) - 2) * 1024); } while (0)
#define G_B2(v_, t_) opaque((v_[1] ^ (t_)) | (v_[3] ^ (t_)))
#define G_OK(h_) (__builtin_amdgcn_ballot_w64(((G_B2(gs[2 * (h_)], stag) | G_B2(gs[2 * (h_) + 1], stag)) | (G_B2(gx[2 * (h_)], mk) | G_B2(gx[2 * (h_) + 1], mk))) != 0u) == 0ull)
      auto take = [&](auto H_) __attribute__((always_inline)) {
        constexpr int H = decltype(H_)::value;
        if (__builtin_expect(!G_OK(H), 0)) {
          for (unsigned spins = 0;; ++spins) {
            FAILC(H ? 4 : 0);
            __builtin_amdgcn_s_sleep(SLP_U);
            asm volatile("" ::: "memory");
            G_LD(2 * H); G_LD(2 * H + 1);
            if (G_OK(H) || spins > SPIN_CAP) break;
          }
        }
#pragma unroll
        for (int e = 2 * H; e < 2 * H + 2; ++e) {
          const float rs0 = __builtin_amdgcn_rcpf(hi2f(gs[e][0])), rs1 = __builtin_amdgcn_rcpf(hi2f(gs[e][2])), n0 = -lo2f(gs[e][0]) * rs0, n1 = -lo2f(gs[e][2]) * rs1;
          const int r = (e & 1) * 128 + (e >> 1) * 1024;
          *(LAS u32*)(size_t)(uw + r * 2) = pkh(fmaf(lo2f(gx[e][0]), rs0, n0), fmaf(lo2f(gx[e][2]), rs1, n1));
          *(LAS u32*)(size_t)(uw + r * 2 + 4608) = pkh(fmaf(hi2f(gx[e][0]), rs0, n0), fmaf(hi2f(gx[e][2]), rs1, n1));
          *(LAS u32x2*)(size_t)(xw + r * 4) = (u32x2){gx[e][0], gx[e][2]};
        }
        __builtin_amdgcn_sched_barrier(0);
      };
      KSLP(16, SLP_G);
      asm volatile("" ::: "memory");
#pragma unroll
      for (int e = 0; e < 4; ++e) G_LD(e);
      __builtin_amdgcn_sched_barrier(0);
      ST(0, 5);
      f32x4 cs[3];
      u32x4 SENT = {0xFFFFFFFFu, 0xFFFFFFFFu, 0xFFFFFFFFu, 0xFFFFFFFFu};
      const u32 o1 = ((ln >> 1) & 1) == (ln >> 5) ? 0x3F803F80u : 0u;
      bf16x8 one8 = __builtin_bit_cast(bf16x8, (u32x4){o1, o1, o1, o1});
      u32 sidx = (ln & 1) ? 0xEEEEu : 0x4444u;
      cs[0] = cs[1] = cs[2] = (f32x4){0.f, 0.f, 0.f, 0.f};
      asm volatile("" : "+v"(one8), "+v"(sidx), "+v"(cs[0]), "+v"(cs[1]), "+v"(cs[2]), "+v"(SENT));
      auto wsum = [&](auto I_) __attribute__((always_inline)) { constexpr int i = 2 * decltype(I_)::value + 1; w_sum<i>(cs[i % 3], one8, sidx, wf); };
      cnt_spin(smem, C_B, 8);
      __builtin_amdgcn_s_setprio(1);
      VMW(8);
      cnt_add(smem, C_W, ln);
      u32x4 fl;
      asm volatile("" : "+v"(gs[0]), "+v"(gs[1]), "+v"(gx[0]), "+v"(gx[1]) :: "memory");
      take(IC<0>{});
      cnt_add(smem, C_U, ln);
      __builtin_amdgcn_raw_buffer_store_b32(tag, wrs, mso, 0, 16);
      ST(0, 3);
      stw(wrs, p2o, SENT); __builtin_amdgcn_raw_buffer_store_b128(SENT, wrs, p2o, 4096, 16);
      stw(wrs, pso, __builtin_bit_cast(u32x4, cs[0]));
      asm volatile("s_nop 1" :: "v"(SENT) : "memory");
      SB();
      auto rest = [&]() __attribute__((always_inline)) {
        cnt_spin(smem, C_W, 4);
        static_for<0, 5>(wget);
        w_get<15, L_SP - L_WS - 53248>(wf[15], wgb);
        cnt_add(smem, C_B, ln);
        take(IC<1>{});
        cnt_add(smem, C_P2, ln);
        ST(0, 4);
      };
      rest();
      { KSLP(17, SLP_FL); fl = ldw(wrs, fo); SB(); asm volatile("s_waitcnt lgkmcnt(0)" ::: "memory"); static_for<0, 7>(wsum); g_pre<true>(gp, g, stag); }
      asm volatile("" : "+v"(gs[2]), "+v"(gs[3]), "+v"(gx[2]), "+v"(gx[3]), "+v"(fl) :: "memory");
      tags_wait(fl, wrs, fo, stag, 15);
      ST(0, 7);
      static_for<45, 48>(wld);
      static_for<16, 16 + H1>(wld);
      VMW(3 + H1);
      cnt_add(smem, C_P3, ln);
      static_for<16 + H1, 45>(wld);
      __builtin_amdgcn_s_setprio(0);
      ST(0, 9);
      cnt_spin(smem, C_B, 12);
      VMW(29);
#pragma unroll
      for (int e = 0; e < 3; ++e) *(LAS bf16x8*)(size_t)(gp.wx + e * 1024) = wf[45 + e];
      static_for<22, 24>(wsum);
      VMW(12); static_for<7, 16>(wsum);
      VMW(6); static_for<16, 19>(wsum);
      VMW(2); static_for<19, 21>(wsum);
      VMW(0); static_for<21, 22>(wsum);
      asm volatile("s_nop 7" : "+v"(cs[0]), "+v"(cs[1]), "+v"(cs[2]));
#pragma unroll
      for (int e = 0; e < 3; ++e) *(LAS float*)(size_t)(gp.fr + 512 + e * 64) = (cs[e][0] + cs[e][1]) + (cs[(e + 2) % 3][2] + cs[(e + 2) % 3][3]);
      asm volatile("" ::: "memory");
      ST(0, 8);
    }
    {
      ConvPre pa2, pb2;
      u32x4 xa, xb;
      int ln2, l2, xo2, xo, so;
      u32 ua, rp0, rp1;
      float isc;
      g_blayer<true>(smem, wrs, g, stag, wf, gp, [&]() __attribute__((always_inline)) {
        ln2 = lane_id();
        l2 = 256 * g + 4 * ln2;
        xo2 = (int)opaque((u32)(OFF_X3 + (8 * g + (ln2 >> 3)) * XBS + kp * 128 + (ln2 & 1) * 64 + (ln2 & 6) * 8));
        xo = (int)opaque((u32)(OFF_X2 + (kp * XS_ + l2) * 4)); so = (int)opaque((u32)(OFF_ST2 + (256 * g + ln2) * 16));
        ua = opaque((u32)(L_U + (256 + l2) * 2));
        rp0 = opaque((u32)(L_REDA + (ln2 & 1) * 8192 + (g * 64 + (ln2 ^ ((ln2 >> 3) & 3))) * 16)); rp1 = opaque(rp0 ^ 16u);
        isc = *(const float*)(smem + L_MI + 4 * (ln2 & 1));
        asm volatile("" : "+v"(isc));
      });
#define VM0() __builtin_amdgcn_s_waitcnt(0x0F70)
      KSLP(9, SLP_C2);
      u32x4 gg[4];
      ST(1, 2);
      xa = ldw(wrs, xo);
      asm volatile("" : "+v"(xa));
      if (__builtin_expect(!x2_ok(xa), 0)) { FAILC(16); __builtin_amdgcn_s_sleep(SLP_XP); xa = x2_ld(wrs, kp, l2); x2_poll(wrs, kp, l2, xa); VM0(); }
      KSLP(10, SLP_S);
      asm volatile("" ::: "memory");
#pragma unroll
      for (int e = 0; e < 4; ++e) gg[e] = ldw(wrs, so + e * 1024);
      SB();
      conv_unp(pa2, xa);
      asm volatile("" : "+v"(gg[0]), "+v"(gg[1]), "+v"(gg[2]), "+v"(gg[3]));
      if (__builtin_expect(!st2_ok(gg), 0)) { FAILC(7); __builtin_amdgcn_s_sleep(SLP_SP); st2_ld(wrs, l2, gg); st2_poll(wrs, l2, gg); VM0(); }
      conv2_in<false>(smem, ua, pa2, xa, gg);
      ST(1, 3);
      lds_barrier();
      ST(1, 7);
      conv_res(pa2);
      const int so2 = (int)opaque((u32)(so + 16384));
      auto rq_b = [&]() __attribute__((always_inline)) {
        KSLP(11, SLP_C3);
        asm volatile("" ::: "memory");
        xb = ldws(wrs, xo, 4096);
        SB();
#pragma unroll
        for (int e = 0; e < 4; ++e) gg[e] = ldw(wrs, so2 + e * 1024);
        SB();
      };
      rq_b();
      __builtin_amdgcn_s_barrier();
      asm volatile("" ::: "memory");
      ST(1, 8);
      stw(wrs, xo2, conv_epi(rp0, rp1, 0, isc, pa2));
      SB();
      asm volatile("" : "+v"(xb), "+v"(gg[0]), "+v"(gg[1]), "+v"(gg[2]), "+v"(gg[3]) :: "memory");
      ST(1, 4);
      if (__builtin_expect(!x2st_ok(xb, gg), 0)) {
        FAILC(18);
        xb = x2_ld(wrs, kp, l2 + 1024);
        st2_ld(wrs, l2 + 1024, gg);
        if (!x2_ok(xb)) {
          x2_poll(wrs, kp, l2 + 1024, xb);
          st2_ld(wrs, l2 + 1024, gg);
        }
        st2_poll(wrs, l2 + 1024, gg);
        VM0();
      }
      ST(1, 1);
      conv2_in<true>(smem, ua + 2048, pb2, xb, gg);
      ST(1, 5);
      lds_barrier();
      ST(1, 6);
      const int rb = RB_OF(bid);
      conv_res(pb2);
      XTake xt;
      x_pre(xt, g, ln2);
      g_pre<false>(gp, g, stag);
      const int xo3 = (int)opaque((u32)(xo2 + 32 * XBS));
      KSLP(12, SLP_XA2);
      x_dma(wrs, OFF_X3, rb, L_TA, g, ln2);
      SB();
      __builtin_amdgcn_s_barrier();
      asm volatile("" ::: "memory");
      ST(1, 9);
      u32x4 ov = conv_epi(rp0, rp1, 16384, isc, pb2);
      stw(wrs, xo3, ov);
      SB();
      x_take<0, 1, true>(xt, smem, wrs, OFF_X3, rb, g, ln2);
      asm volatile("" :: "v"(ov));
    }
    g_blayer<false>(smem, wrs, g, stag, wf, gp, []() {});

    ST_OUT();
  }
}

__device__ __attribute__((used, noinline)) void k_text_end() {
  asm volatile(
  ".Lg_redo:\n\ts_waitcnt lgkmcnt(0)\n\t"
  "ds_read_b64_tr_b16 v[20:21], v0 offset:0\n\tds_read_b64_tr_b16 v[22:23], v0 offset:1024\n\ts_waitcnt lgkmcnt(0)\n\t"
  "v_mfma_f32_16x16x32_bf16 v[8:11], v[80:83], v[20:23], 0\n\tv_mfma_f32_16x16x32_bf16 v[48:51], v[20:23], v[84:87], 0\n\tv_mfma_f32_16x16x32_bf16 v[64:67], v[76:79], v[20:23], 0\n\t"
  "ds_read_b64_tr_b16 v[20:21], v0 offset:2048\n\tds_read_b64_tr_b16 v[22:23], v0 offset:3072\n\ts_waitcnt lgkmcnt(0)\n\t"
  "v_mfma_f32_16x16x32_bf16 v[8:11], v[92:95], v[20:23], v[8:11]\n\tv_mfma_f32_16x16x32_bf16 v[48:51], v[20:23], v[96:99], v[48:51]\n\tv_mfma_f32_16x16x32_bf16 v[64:67], v[88:91], v[20:23], v[64:67]\n\t"
  "ds_read_b64_tr_b16 v[20:21], v0 offset:8192\n\tds_read_b64_tr_b16 v[22:23], v0 offset:9216\n\ts_waitcnt lgkmcnt(0)\n\t"
  "v_mfma_f32_16x16x32_bf16 v[8:11], v[128:131], v[20:23], v[8:11]\n\tv_mfma_f32_16x16x32_bf16 v[48:51], v[20:23], v[132:135], v[48:51]\n\tv_mfma_f32_16x16x32_bf16 v[64:67], v[124:127], v[20:23], v[64:67]\n\t"
  "ds_read_b64_tr_b16 v[20:21], v0 offset:10240\n\tds_read_b64_tr_b16 v[22:23], v0 offset:11264\n\ts_waitcnt lgkmcnt(0)\n\t"
  "v_mfma_f32_16x16x32_bf16 v[8:11], v[140:143], v[20:23], v[8:11]\n\tv_mfma_f32_16x16x32_bf16 v[48:51], v[20:23], v[144:147], v[48:51]\n\tv_mfma_f32_16x16x32_bf16 v[64:67], v[136:139], v[20:23], v[64:67]\n\t"
  "ds_read_b64_tr_b16 v[20:21], v0 offset:16384\n\tds_read_b64_tr_b16 v[22:23], v0 offset:17408\n\ts_waitcnt lgkmcnt(0)\n\t"
  "v_mfma_f32_16x16x32_bf16 v[8:11], v[176:179], v[20:23], v[8:11]\n\tv_mfma_f32_16x16x32_bf16 v[48:51], v[20:23], v[180:183], v[48:51]\n\tv_mfma_f32_16x16x32_bf16 v[64:67], v[172:175], v[20:23], v[64:67]\n\t"
  "ds_read_b64_tr_b16 v[20:21], v0 offset:18432\n\tds_read_b64_tr_b16 v[22:23], v0 offset:19456\n\ts_waitcnt lgkmcnt(0)\n\t"
  "v_mfma_f32_16x16x32_bf16 v[8:11], v[188:191], v[20:23], v[8:11]\n\tv_mfma_f32_16x16x32_bf16 v[48:51], v[20:23], v[192:195], v[48:51]\n\tv_mfma_f32_16x16x32_bf16 v[64:67], v[184:187], v[20:23], v[64:67]\n\t"
  "ds_read_b64_tr_b16 v[20:21], v0 offset:24576\n\tds_read_b64_tr_b16 v[22:23], v0 offset:25600\n\ts_waitcnt lgkmcnt(0)\n\t"
  "v_mfma_f32_16x16x32_bf16 v[8:11], v[224:227], v[20:23], v[8:11]\n\tv_mfma_f32_16x16x32_bf16 v[48:51], v[20:23], v[228:231], v[48:51]\n\tv_mfma_f32_16x16x32_bf16 v[64:67], v[220:223], v[20:23], v[64:67]\n\t"
  "ds_read_b64_tr_b16 v[20:21], v0 offset:26624\n\tds_read_b64_tr_b16 v[22:23], v0 offset:27648\n\ts_waitcnt lgkmcnt(0)\n\t"
  "v_mfma_f32_16x16x32_bf16 v[8:11], v[236:239], v[20:23], v[8:11]\n\tv_mfma_f32_16x16x32_bf16 v[48:51], v[20:23], v[240:243], v[48:51]\n\tv_mfma_f32_16x16x32_bf16 v[64:67], v[232:235], v[20:23], v[64:67]\n\t"
  "ds_read_b64_tr_b16 v[20:21], v0 offset:4096\n\tds_read_b64_tr_b16 v[22:23], v0 offset:5120\n\ts_waitcnt lgkmcnt(0)\n\t"
  "v_mfma_f32_16x16x32_bf16 v[8:11], v[104:107], v[20:23], v[8:11]\n\tv_mfma_f32_16x16x32_bf16 v[48:51], v[20:23], v[108:111], v[48:51]\n\tv_mfma_f32_16x16x32_bf16 v[64:67], v[100:103], v[20:23], v[64:67]\n\t"
  "ds_read_b64_tr_b16 v[20:21], v0 offset:6144\n\tds_read_b64_tr_b16 v[22:23], v0 offset:7168\n\tds_read_b64_tr_b16 v[36:37], v0 offset:12288\n\tds_read_b64_tr_b16 v[38:39], v0 offset:13312\n\tds_read_b64_tr_b16 v[28:29], v0 offset:14336\n\tds_read_b64_tr_b16 v[30:31], v0 offset:15360\n\tds_read_b64_tr_b16 v[68:69], v0 offset:20480\n\tds_read_b64_tr_b16 v[70:71], v0 offset:21504\n\t"
  "s_waitcnt lgkmcnt(0)\n\ts_branch .Lg_back\n\t"
  ".Lc0_kh:\n\ts_mov_b32 vcc_hi, 0x1000000\n\t.Lc0_kh_s:\n\tds_read_b32 v101, v27 offset:48\n\ts_sub_u32 vcc_hi, vcc_hi, 1\n\ts_cselect_b32 vcc_lo, 0, 4\n\ts_waitcnt lgkmcnt(0)\n\tv_readfirstlane_b32 s61, v101\n\ts_nop 1\n\ts_cmp_lt_u32 s61, vcc_lo\n\ts_cbranch_scc1 .Lc0_kh_s\n\t"
  "s_waitcnt lgkmcnt(0)\n\ts_branch .Lc0_kh_b\n\t"
  ".Lc0_x0:\n\ts_mov_b32 vcc_hi, 0x1000000\n\t.Lc0_x0_s:\n\tds_read_b32 v101, v21 offset:8\n\ts_sub_u32 vcc_hi, vcc_hi, 1\n\ts_cselect_b32 vcc_lo, 0, 2\n\ts_waitcnt lgkmcnt(0)\n\tv_readfirstlane_b32 s61, v101\n\ts_nop 1\n\ts_cmp_lt_u32 s61, vcc_lo\n\ts_cbranch_scc1 .Lc0_x0_s\n\t"
  "ds_read_b128 v[144:147], v20 offset:2048\n\tds_read_b128 v[204:207], v20 offset:3072\n\ts_waitcnt lgkmcnt(0)\n\ts_branch .Lc0_x0_b\n\t"
  ".Lc0_ub:\n\ts_mov_b32 vcc_hi, 0x1000000\n\t.Lc0_ub_s:\n\tds_read_b32 v101, v27 offset:52\n\ts_sub_u32 vcc_hi, vcc_hi, 1\n\ts_cselect_b32 vcc_lo, 0, 4\n\ts_waitcnt lgkmcnt(0)\n\tv_readfirstlane_b32 s61, v101\n\ts_nop 1\n\ts_cmp_lt_u32 s61, vcc_lo\n\ts_cbranch_scc1 .Lc0_ub_s\n\t"
  "s_waitcnt lgkmcnt(0)\n\ts_branch .Lc0_ub_b\n\t"
  ".Lc0_p3:\n\ts_mov_b32 vcc_hi, 0x1000000\n\t.Lc0_p3_s:\n\tds_read_b32 v101, v27 offset:64\n\ts_sub_u32 vcc_hi, vcc_hi, 1\n\ts_cselect_b32 vcc_lo, 0, 4\n\ts_waitcnt lgkmcnt(0)\n\tv_readfirstlane_b32 s61, v101\n\ts_nop 1\n\ts_cmp_lt_u32 s61, vcc_lo\n\ts_cbranch_scc1 .Lc0_p3_s\n\t"
  "s_waitcnt lgkmcnt(0)\n\ts_branch .Lc0_p3_b\n\t"
  ".Lc0_x4:\n\ts_mov_b32 vcc_hi, 0x1000000\n\t.Lc0_x4_s:\n\tds_read_b32 v36, v27 offset:16\n\ts_sub_u32 vcc_hi, vcc_hi, 1\n\ts_cselect_b32 vcc_lo, 0, 4\n\ts_waitcnt lgkmcnt(0)\n\tv_readfirstlane_b32 s61, v36\n\ts_nop 1\n\ts_cmp_lt_u32 s61, vcc_lo\n\ts_cbranch_scc1 .Lc0_x4_s\n\t"
  "ds_read_b128 v[40:43], v20 offset:59392\n\tds_read_b128 v[44:47], v20 offset:60416\n\ts_waitcnt lgkmcnt(0)\n\ts_branch .Lc0_x4_b\n\t"
  ".Lc1_kh:\n\ts_mov_b32 vcc_hi, 0x1000000\n\t.Lc1_kh_s:\n\tds_read_b32 v101, v27 offset:48\n\ts_sub_u32 vcc_hi, vcc_hi, 1\n\ts_cselect_b32 vcc_lo, 0, 4\n\ts_waitcnt lgkmcnt(0)\n\tv_readfirstlane_b32 s61, v101\n\ts_nop 1\n\ts_cmp_lt_u32 s61, vcc_lo\n\ts_cbranch_scc1 .Lc1_kh_s\n\t"
  "s_waitcnt lgkmcnt(0)\n\ts_branch .Lc1_kh_b\n\t"
  ".Lc1_x0:\n\ts_mov_b32 vcc_hi, 0x1000000\n\t.Lc1_x0_s:\n\tds_read_b32 v101, v21 offset:8\n\ts_sub_u32 vcc_hi, vcc_hi, 1\n\ts_cselect_b32 vcc_lo, 0, 2\n\ts_waitcnt lgkmcnt(0)\n\tv_readfirstlane_b32 s61, v101\n\ts_nop 1\n\ts_cmp_lt_u32 s61, vcc_lo\n\ts_cbranch_scc1 .Lc1_x0_s\n\t"
  "ds_read_b128 v[148:151], v20 offset:0\n\tds_read_b128 v[204:207], v20 offset:1024\n\ts_waitcnt lgkmcnt(0)\n\ts_branch .Lc1_x0_b\n\t"
  ".Lc1_ub:\n\ts_mov_b32 vcc_hi, 0x1000000\n\t.Lc1_ub_s:\n\tds_read_b32 v101, v27 offset:52\n\ts_sub_u32 vcc_hi, vcc_hi, 1\n\ts_cselect_b32 vcc_lo, 0, 4\n\ts_waitcnt lgkmcnt(0)\n\tv_readfirstlane_b32 s61, v101\n\ts_nop 1\n\ts_cmp_lt_u32 s61, vcc_lo\n\ts_cbranch_scc1 .Lc1_ub_s\n\t"
  "s_waitcnt lgkmcnt(0)\n\ts_branch .Lc1_ub_b\n\t"
  ".Lc1_p3:\n\ts_mov_b32 vcc_hi, 0x1000000\n\t.Lc1_p3_s:\n\tds_read_b32 v101, v27 offset:64\n\ts_sub_u32 vcc_hi, vcc_hi, 1\n\ts_cselect_b32 vcc_lo, 0, 4\n\ts_waitcnt lgkmcnt(0)\n\tv_readfirstlane_b32 s61, v101\n\ts_nop 1\n\ts_cmp_lt_u32 s61, vcc_lo\n\ts_cbranch_scc1 .Lc1_p3_s\n\t"
  "s_waitcnt lgkmcnt(0)\n\ts_branch .Lc1_p3_b\n\t"
  ".Lc1_x4:\n\ts_mov_b32 vcc_hi, 0x1000000\n\t.Lc1_x4_s:\n\tds_read_b32 v36, v27 offset:16\n\ts_sub_u32 vcc_hi, vcc_hi, 1\n\ts_cselect_b32 vcc_lo, 0, 4\n\ts_waitcnt lgkmcnt(0)\n\tv_readfirstlane_b32 s61, v36\n\ts_nop 1\n\ts_cmp_lt_u32 s61, vcc_lo\n\ts_cbranch_scc1 .Lc1_x4_s\n\t"
  "ds_read_b128 v[40:43], v20 offset:57344\n\tds_read_b128 v[44:47], v20 offset:58368\n\ts_waitcnt lgkmcnt(0)\n\ts_branch .Lc1_x4_b\n\t"
  ".Lcw_end:");
}

extern "C" void kernel_launch(void* const* d_in, const int* in_sizes, int n_in, void* d_out, int out_size,
                              void* d_ws, size_t ws_size, hipStream_t stream) {
  (void)hipFuncSetAttribute((const void*)k_mega, hipFuncAttributeMaxDynamicSharedMemorySize, MEGA_LDS);
  k_mega<<<256, 512, MEGA_LDS, stream>>>((const u16*)d_in[0], (const u16*)d_in[1], (const u16*)d_in[2], (const u16*)d_in[3],
                                         (const u16*)d_in[4], (const u16*)d_in[5], (const u16*)d_in[6], (const u16*)d_in[7],
                                         (const u16*)d_in[8], (const u16*)d_in[10], (const u16*)d_in[11], (char*)d_ws,
                                         (u32*)d_out);
}
#if defined(__HIP_DEVICE_COMPILE__)
#pragma clang attribute pop
#endif
```

```cpp
#if defined(__HIP_DEVICE_COMPILE__)
#pragma clang attribute push (__attribute__((target("no-packed-fp32-ops"))), apply_to = function)
#endif
#include <hip/hip_runtime.h>
#include <hip/hip_bf16.h>
#include <stdint.h>
#define TIDX ((int)__builtin_amdgcn_workitem_id_x())
#define BIDX ((int)__builtin_amdgcn_workgroup_id_x())
#define ST(set, k)
#define ST_INIT()
#define ST_OUT()
#define KATTR
#define FAILC(k)

#define ST_FIX()
#define CST(k)
#define GST(k)
typedef unsigned short u16;
typedef unsigned int u32;
typedef unsigned long long u64;
using bf16x8 = __attribute__((ext_vector_type(8))) short;
using bf16x4 = __attribute__((ext_vector_type(4))) short;
using f16x8 = __attribute__((ext_vector_type(8))) _Float16;
using f16x4 = __attribute__((ext_vector_type(4))) _Float16;
using f32x2 = __attribute__((ext_vector_type(2))) float;
using f32x4 = __attribute__((ext_vector_type(4))) float;
using u32x2 = __attribute__((ext_vector_type(2))) unsigned;
using u32x4 = __attribute__((ext_vector_type(4))) unsigned;
#define LAS __attribute__((address_space(3)))
#define GAS __attribute__((address_space(1)))

#define D_ 512
#define NS_ 128
#define XS_ 2080

#define MB_ (1 << 20)
#define XTO(kp_) (OFF_O1 + (((kp_) >> 4) & 1) * MB_ + ((kp_) >> 5) * 131072 + ((kp_) & 15) * 8192)
#define XTS 8192
#define RB_OF(b_) (((b_) & 7) * 4 + ((b_) >> 6))
#define HB_OF(b_) (((b_) >> 3) & 7)
#define OFF_ST0 (5 * MB_)
#define OFF_TS (OFF_ST0 + 16384)
#define OFF_SG (OFF_ST0 + 32768)
#define OFF_ST2 (OFF_ST0 + 65536)
#define OFF_EP (OFF_ST0 + 131072)
#define OFF_WQ (9 * MB_ - 262144)
#define OFF_WO (10 * MB_ + 393216)
#define OFF_X1 (11 * MB_)
#define OFF_O1 (14 * MB_)
#define OFF_X2 (16 * MB_)
#define OFF_X3 (19 * MB_)
#define OFF_O2 (22 * MB_)
#define OFF_KV2 (26 * MB_)
#define KVO(L1_, rb_, h_) (((L1_) ? OFF_O2 : OFF_KV2) + (((h_) >> 3) & 1) * MB_ + (rb_) * 32768 + (((h_) >> 2) & 1) * 16384 + ((h_) >> 4) * 8192 + ((h_) & 3) * 2048)
#define WSB (32 * MB_)
#define XBS 32896
#define SPIN_CAP (1u << 14)
#define XPB 1
#define GPE 1
#define B6 0
#define OUTA 2
#define OUTB 0
#define WLD 8
#define SLP_FL 4
#define SLP_WE 0
#define R1F 3
#define D9 1300
#define WS5 5
#define SLP_B4 4
#define H1 9
#define DB2 450
#define D5 200
#define DQ2 0
#define DO2 900
#define SLP_U 2
#define SLP_F 2
#define SLP_G 0
#define R1D 1
#define WOE 4
#define SLP_H1 0
#define SLP_H2 2
#define WO0 0
#define SLP_XA2 2
#define CPF 5
#define SLP_XP 2
#define SLP_SP 4
#define SLP_S 6
#define SLP_XA1 4
#define SLP_XB1 6
#define SLP_OB1 6
#define SLP_OA1 7
#define SLP_C2 3
#define SLP_C3 15

__device__ __forceinline__ float bf2f(u16 v) { return __uint_as_float(((u32)v) << 16); }
typedef __attribute__((ext_vector_type(2))) __bf16 bf16x2v;
__device__ __forceinline__ u32 pk2(float a, float b) {
  f32x2 f = {a, b};
  bf16x2v v = __builtin_convertvector(f, bf16x2v);
  return __builtin_bit_cast(u32, v);
}
__device__ __forceinline__ float rbf(float f) { return __uint_as_float(pk2(f, 0.f) << 16); }
__device__ __forceinline__ float lo2f(u32 w) { return __uint_as_float(w << 16); }
__device__ __forceinline__ float hi2f(u32 w) { return __uint_as_float(w & 0xFFFF0000u); }
template <int CTRL>
__device__ __forceinline__ float dpp_mov(float v) {
  return __int_as_float(__builtin_amdgcn_update_dpp(0, __float_as_int(v), CTRL, 0xf, 0xf, false));
}
#define DPP_XOR1 0xB1
#define DPP_XOR2 0x4E
#define DPP_ROR8 0x128
template <int CTRL, int RMASK>
__device__ __forceinline__ float dpp_add(float v) {
  return v + __int_as_float(__builtin_amdgcn_update_dpp(0, __float_as_int(v), CTRL, RMASK, 0xf, false));
}
__device__ __forceinline__ void wave_sum3(float& a, float& b, float& c) {
#define WS3(CT_, RM_) a = dpp_add<CT_, RM_>(a); b = dpp_add<CT_, RM_>(b); c = dpp_add<CT_, RM_>(c); __builtin_amdgcn_sched_barrier(0);
  WS3(0x111, 0xf) WS3(0x112, 0xf) WS3(0x114, 0xf) WS3(0x118, 0xf) WS3(0x142, 0xa) WS3(0x143, 0xc)
  a = __int_as_float(__builtin_amdgcn_readlane(__float_as_int(a), 63));
  b = __int_as_float(__builtin_amdgcn_readlane(__float_as_int(b), 63));
  c = __int_as_float(__builtin_amdgcn_readlane(__float_as_int(c), 63));
}
__device__ __forceinline__ float rmax(float a, float b) {
  float r;
  asm("v_max_f32 %0, %1, %2" : "=v"(r) : "v"(a), "v"(b));
  return r;
}
__device__ __forceinline__ void lds_barrier() {
  asm volatile("s_waitcnt lgkmcnt(0)" ::: "memory");
  __builtin_amdgcn_s_barrier();
  asm volatile("" ::: "memory");
}
__device__ __forceinline__ void cexp2(float re2, float rev, float& orr, float& oi) {
  float e = __builtin_amdgcn_exp2f(re2);
  rev = __builtin_amdgcn_fractf(rev);
  float s = __builtin_amdgcn_sinf(rev);
  float c = __builtin_amdgcn_cosf(rev);
  orr = e * c;
  oi = e * s;
}
__device__ __forceinline__ void cexp3(float re2, float rev, float& e, float& c, float& s) {
  e = __builtin_amdgcn_exp2f(re2);
  rev = __builtin_amdgcn_fractf(rev);
  s = __builtin_amdgcn_sinf(rev); c = __builtin_amdgcn_cosf(rev);
}
__device__ __forceinline__ float dot2acc(u32 a, u32 b, float c) {
  return __builtin_amdgcn_fdot2_f32_bf16(__builtin_bit_cast(bf16x2v, a), __builtin_bit_cast(bf16x2v, b), c, false);
}
__device__ __forceinline__ u32 opaque(u32 v) { asm volatile("" : "+v"(v)); return v; }
__device__ __forceinline__ int lane_id() { return (int)__builtin_amdgcn_mbcnt_hi(~0u, __builtin_amdgcn_mbcnt_lo(~0u, opaque(0u))); }
__device__ __forceinline__ u32 pack_stats(float mean, float var) {
  float vb = rbf(var);
  float ve = rbf(vb + 1e-5f);
  float sd = __builtin_amdgcn_sqrtf(ve);
  return pk2(mean, sd);
}

#define SB() __builtin_amdgcn_sched_barrier(0)

#define KADD(id_, t_, v_) kadd_<(v_)>(t_)
#define KSLP(id_, v_) do { if ((v_) > 0) __builtin_amdgcn_s_sleep(v_); } while (0)
template <int V>
__device__ __forceinline__ u32 kadd_(u32 t) { u32 r; asm("s_add_i32 %0, %1, %2" : "=s"(r) : "s"(__builtin_amdgcn_readfirstlane(t)), "n"(V) : "scc"); return r; }
template <int V> struct IC { static constexpr int value = V; };
template <int B, int E, class F>
__device__ __forceinline__ void static_for(F& f) { if constexpr (B < E) { f(IC<B>{}); static_for<B + 1, E>(f); } }
typedef __amdgpu_buffer_rsrc_t rsrc_t;
__device__ __forceinline__ u32x4 ldw(rsrc_t r, int off) { return __builtin_amdgcn_raw_buffer_load_b128(r, off, 0, 16); }
__device__ __forceinline__ u32x4 ldws(rsrc_t r, int off, int soff) { return __builtin_amdgcn_raw_buffer_load_b128(r, off, soff, 16); }
__device__ __forceinline__ void stw(rsrc_t r, int off, u32x4 v) { __builtin_amdgcn_raw_buffer_store_b128(v, r, off, 0, 16); }
__device__ __forceinline__ u32 umax4(u32 m, u32x4 v) { return max(max(max(m, v[0]), v[1]), max(v[2], v[3])); }
template <int CTRL>
__device__ __forceinline__ u32 dpp_u(u32 v) { return (u32)__builtin_amdgcn_update_dpp(0, (int)v, CTRL, 0xf, 0xf, false); }

#define CSTR 4160
#define L_U 33280
#define L_TA 42496
#define L_TB (L_TA + 32768)
#define L_QB (L_TB + 32768)
#define L_TH (L_QB + 16384)
#define L_SP (L_QB + 32768)
#define L_FS (L_SP + 4096)
#define L_MI (L_FS + 3072)
#define L_XS (L_MI + 80)
#define L_WX (L_XS + 1024)
#define MEGA_LDS (L_WX + 12288)
#define L_REDA L_QB
#define L_REDB (L_QB + 16384)
#define L_XC L_REDB
#define L_X (L_REDB + 8192)
#define TBS 3136
#define L_WR (L_QB + 24576)
#define L_WS L_TA
#define L_KT L_TB
#define KTO 65024
#define FTS 528
#define L_P0 (L_FS + 2048)
#define STAT_SCALE 33554432.0f
#define C_OB 20
#define C_OT 24
#define M_FX 72
#define C_X2 28
#define C_G2 32
#define C_A 36
#define C_B 40
#define C_F 44
#define C_KH 48
#define C_P2 52
#define C_XC 8
#define C_P3 64
#define M_RD 68
#define M_DL 76
#define C_OV 28
#define C_U 56
#define C_W 60

typedef __attribute__((ext_vector_type(2))) _Float16 f16x2v;
typedef __attribute__((ext_vector_type(2))) short i16x2;
__device__ __forceinline__ u32 pkh(float a, float b) {
  u32 r;
  asm("v_cvt_pk_f16_f32 %0, %1, %2" : "=v"(r) : "v"(a), "v"(b));
  return r;
}
__device__ __forceinline__ u32 pkhn(float a, float b) {
  u32 r;
  asm("v_cvt_pk_f16_f32 %0, %1, -%2" : "=v"(r) : "v"(a), "v"(b));
  return r;
}
__device__ __forceinline__ u32 tb_addr(u32 bits2, u32 base2) {
  i16x2 b = __builtin_elementwise_max(__builtin_bit_cast(i16x2, bits2), (i16x2){0x3F00, 0x3F00});
  u32 r;
  asm("v_pk_mad_u16 %0, %1, 2, %2 op_sel_hi:[1,0,1]" : "=v"(r) : "v"(b), "s"(base2));
  return r;
}
__device__ __forceinline__ int bexp(float b) { return min(max((int)((__float_as_uint(b) >> 23) & 0xFF), 20), 240); }
__device__ __forceinline__ void cnt_add(char* smem, int off, int lane) {
  asm volatile("s_mov_b64 exec, 1\n\tds_add_u32 %0, %1\n\ts_mov_b64 exec, -1" :: "v"(L_MI + off), "v"(1u) : "memory");
}
__device__ __forceinline__ void cnt_wait(char* smem, int off, u32 target) {
#pragma unroll 1
  for (unsigned n = 0; n < (1u << 23) && (u32)__builtin_amdgcn_readfirstlane(__hip_atomic_load((LAS u32*)(smem + L_MI + off), __ATOMIC_RELAXED, __HIP_MEMORY_SCOPE_WORKGROUP)) < target; ++n)
    __builtin_amdgcn_s_sleep(2);
  asm volatile("" ::: "memory");
}
__device__ __forceinline__ void cnt_spin(char* smem, int off, u32 target) {
  for (unsigned n = 0; n < (1u << 24) && (u32)__builtin_amdgcn_readfirstlane(__hip_atomic_load((LAS u32*)(smem + L_MI + off), __ATOMIC_RELAXED, __HIP_MEMORY_SCOPE_WORKGROUP)) < target; ++n) {}
  asm volatile("" ::: "memory");
}
__device__ __forceinline__ void cnt_spin2(int o0, u32 t0, int o1, u32 t1) {
  for (unsigned n = 0; n < (1u << 24); ++n) {
    asm volatile("" ::: "memory");
    const u32 c0 = *(const volatile LAS u32*)(size_t)(u32)(L_MI + o0), c1 = *(const volatile LAS u32*)(size_t)(u32)(L_MI + o1);
    if (__builtin_amdgcn_readfirstlane(min((int)(c0 - t0), (int)(c1 - t1))) >= 0) break;
  }
  asm volatile("" ::: "memory");
}
__device__ __forceinline__ u32 now_() { return (u32)__builtin_amdgcn_s_memtime(); }
__device__ __forceinline__ u32 tmax(u32 a, u32 b) { return (int)(a - b) > 0 ? a : b; }
__device__ __forceinline__ void gate(u32 t) {
  u32 n = 256;
  asm volatile("1:\n\ts_memtime vcc\n\ts_sub_u32 %0, %0, 1\n\ts_waitcnt lgkmcnt(0)\n\ts_cbranch_scc1 2f\n\ts_sub_i32 vcc_lo, %1, vcc_lo\n\ts_cmp_lt_i32 vcc_lo, 1\n\ts_cbranch_scc1 2f\n\t"
               "s_cmp_lt_i32 vcc_lo, 64\n\ts_cbranch_scc1 1b\n\ts_sleep 1\n\ts_cmp_lt_i32 vcc_lo, 0x200\n\ts_cbranch_scc1 1b\n\ts_sleep 6\n\ts_branch 1b\n2:"
               : "+s"(n) : "s"(__builtin_amdgcn_readfirstlane(t)) : "memory", "vcc", "scc");
}
__device__ __forceinline__ u32 x0_mark(u32 tag) { return 0xFF807F80u + (((tag & 31u) + 1u) << 16) + ((tag >> 5) & 31u) + 1u; }
__device__ __forceinline__ bool tags_ok(const u32x4& f, u32 tag) {
  return __builtin_amdgcn_ballot_w64((opaque((f[0] ^ tag) | (f[1] ^ tag)) | opaque((f[2] ^ tag) | (f[3] ^ tag))) != 0u) == 0ull;
}
__device__ __forceinline__ void tags_wait(u32x4& f, rsrc_t wrs, int fo, u32 tag, int fid) {
  if (__builtin_expect(!tags_ok(f, tag), 0)) {
    for (unsigned spins = 0; spins <= SPIN_CAP; ++spins) {
      FAILC(fid);
      __builtin_amdgcn_s_sleep(SLP_F);
      asm volatile("" ::: "memory");
      if (tags_ok(ldw(wrs, fo), tag)) break;
    }
    __builtin_amdgcn_s_waitcnt(0x0F70);
  }
  asm volatile("" ::: "memory");
}
__device__ __forceinline__ void build_copies(char* smem, int cc, int ch, const u32 (&kw)[6]) {
  char* cop = smem + cc * 4 * CSTR + ch * 16;
  u32 sh[5];
#pragma unroll
  for (int k = 0; k < 5; ++k) sh[k] = __builtin_amdgcn_alignbit(kw[k + 1], kw[k], 16);
  u32x4 c0 = {kw[0], kw[1], kw[2], kw[3]}, c1 = {sh[0], sh[1], sh[2], sh[3]}, c2 = {kw[1], kw[2], kw[3], kw[4]}, c3 = {sh[1], sh[2], sh[3], sh[4]};
  asm volatile("" : "+v"(c0), "+v"(c1), "+v"(c2), "+v"(c3));
  *(u32x4*)(cop + 0 * CSTR) = c0; *(u32x4*)(cop + 1 * CSTR) = c1; *(u32x4*)(cop + 2 * CSTR) = c2; *(u32x4*)(cop + 3 * CSTR) = c3;
  asm volatile("s_nop 1" :: "v"(c0), "v"(c1), "v"(c2), "v"(c3) : "memory");
}
struct ConvPre { f32x4 x0, x1; };
__device__ __forceinline__ void conv_unp(ConvPre& P, u32x4 xw) {
#pragma unroll
  for (int e = 0; e < 4; ++e) { P.x0[e] = lo2f(xw[e]); P.x1[e] = hi2f(xw[e]); }
  asm volatile("" : "+v"(P.x0), "+v"(P.x1));
}
__device__ __forceinline__ u32x4 x2_ld(rsrc_t wrs, int kp, int l0) { return ldw(wrs, OFF_X2 + (kp * XS_ + l0) * 4); }
__device__ __forceinline__ bool x2_ok(const u32x4& xw) { return __all(umax4(0u, xw) != 0xFFFFFFFFu); }
__device__ __forceinline__ void x2_poll(rsrc_t wrs, int kp, int l0, u32x4& xw) {
  for (unsigned spins = 0; !x2_ok(xw) && spins <= SPIN_CAP; ++spins) {
    FAILC(17);
    __builtin_amdgcn_s_sleep(SLP_XP);
    asm volatile("" ::: "memory");
    xw = x2_ld(wrs, kp, l0);
  }
}
__device__ __forceinline__ void st2_ld(rsrc_t wrs, int l0, u32x4 (&g)[4]) {
#pragma unroll
  for (int e = 0; e < 4; ++e) g[e] = ldw(wrs, OFF_ST2 + ((l0 & ~255) + ((l0 >> 2) & 63)) * 16 + e * 1024);
}
__device__ __forceinline__ bool st2_ok(const u32x4 (&g)[4]) {
  u32 ok = 32u;
#pragma unroll
  for (int e = 0; e < 4; ++e) ok &= g[e][0] & g[e][2];
  return __all(ok != 0u);
}
__device__ __forceinline__ bool x2st_ok(const u32x4& xw, const u32x4 (&g)[4]) {
  u32 ok = umax4(0u, xw) != 0xFFFFFFFFu ? 32u : 0u;
#pragma unroll
  for (int e = 0; e < 4; ++e) ok &= g[e][0] & g[e][2];
  return __all(ok != 0u);
}
__device__ __forceinline__ void st2_poll(rsrc_t wrs, int l0, u32x4 (&g)[4]) {
  for (unsigned spins = 0; !st2_ok(g) && spins <= SPIN_CAP; ++spins) {
    FAILC(8);
    __builtin_amdgcn_s_sleep(SLP_SP);
    asm volatile("" ::: "memory");
    st2_ld(wrs, l0, g);
  }
}
template <bool UNP>
__device__ __forceinline__ void conv2_in(char* smem, u32 ua, ConvPre& P, u32x4 xw, u32x4 (&g)[4]) {
  f32x4 rs, nm;
#pragma unroll
  for (int e = 0; e < 4; ++e) {
    const float c1 = 4294967296.0f / (64.0f * 512.0f * STAT_SCALE), c2 = 1.0f / 4294967296.0f;
    const float t1 = fmaf((float)g[e][0], c2, (float)(int)g[e][1]), t2 = fmaf((float)g[e][2], c2, (float)(int)g[e][3]);
    const float m1 = -c1 * t1;
    rs[e] = __builtin_amdgcn_rsqf(fmaf(rmax(fmaf(m1, t1, t2), 0.f), c1, 1e-5f));
    nm[e] = m1 * rs[e];
  }
  if (UNP) conv_unp(P, xw);
  u32x2 u0 = {pkh(fmaf(P.x0[0], rs[0], nm[0]), fmaf(P.x0[1], rs[1], nm[1])), pkh(fmaf(P.x0[2], rs[2], nm[2]), fmaf(P.x0[3], rs[3], nm[3]))};
  u32x2 u1 = {pkh(fmaf(P.x1[0], rs[0], nm[0]), fmaf(P.x1[1], rs[1], nm[1])), pkh(fmaf(P.x1[2], rs[2], nm[2]), fmaf(P.x1[3], rs[3], nm[3]))};
  asm volatile("" : "+v"(u0), "+v"(u1));
  *(LAS u32x2*)(size_t)ua = u0;
  *(LAS u32x2*)(size_t)(ua + 4608) = u1;
  asm volatile("s_nop 1" :: "v"(u0), "v"(u1) : "memory");
}
__device__ __forceinline__ void pair_x(f32x4& lo, f32x4& hi, const f32x4& a, const f32x4& b) {
#define PXD " quad_perm:[1,0,3,2] row_mask:0xf bank_mask:0xf\n\t"
  asm volatile("s_mov_b64 vcc, %16\n\ts_nop 0\n\t"
               "v_cndmask_b32_dpp %0, %12, %8, vcc" PXD "v_cndmask_b32_dpp %1, %13, %9, vcc" PXD "v_cndmask_b32_dpp %2, %14, %10, vcc" PXD "v_cndmask_b32_dpp %3, %15, %11, vcc" PXD
               "s_not_b64 vcc, vcc\n\t"
               "v_cndmask_b32_dpp %4, %8, %12, vcc" PXD "v_cndmask_b32_dpp %5, %9, %13, vcc" PXD "v_cndmask_b32_dpp %6, %10, %14, vcc" PXD "v_cndmask_b32_dpp %7, %11, %15, vcc" PXD
               : "=&v"(lo[0]), "=&v"(lo[1]), "=&v"(lo[2]), "=&v"(lo[3]), "=&v"(hi[0]), "=&v"(hi[1]), "=&v"(hi[2]), "=&v"(hi[3])
               : "v"(a[0]), "v"(a[1]), "v"(a[2]), "v"(a[3]), "v"(b[0]), "v"(b[1]), "v"(b[2]), "v"(b[3]), "s"(0x5555555555555555ull) : "vcc", "scc");
}
__device__ __forceinline__ void conv_res(ConvPre& P) {
  f32x4 lo, hi;
  pair_x(lo, hi, P.x0, P.x1);
  P.x0 = lo; P.x1 = hi;
}
__device__ __forceinline__ u32x4 conv_epi(u32 rp0, u32 rp1, int off, float isc, const ConvPre& P) {
  const f32x4 a0 = *(const LAS f32x4*)(size_t)(rp0 + off), a1 = *(const LAS f32x4*)(size_t)(rp0 + off + 4096), b0 = *(const LAS f32x4*)(size_t)(rp1 + off), b1 = *(const LAS f32x4*)(size_t)(rp1 + off + 4096);
  __builtin_amdgcn_sched_barrier(0);
  u32x4 r;
#pragma unroll
  for (int e = 0; e < 2; ++e) {
    r[e] = pk2(fmaf(a1[2 * e], isc, fmaf(a0[2 * e], isc, P.x0[2 * e])), fmaf(a1[2 * e + 1], isc, fmaf(a0[2 * e + 1], isc, P.x0[2 * e + 1])));
    r[2 + e] = pk2(fmaf(b1[2 * e], isc, fmaf(b0[2 * e], isc, P.x1[2 * e])), fmaf(b1[2 * e + 1], isc, fmaf(b0[2 * e + 1], isc, P.x1[2 * e + 1])));
  }
  return r;
}
__device__ __forceinline__ void dma4k(rsrc_t r, int voff, int soff, int lds) {
  asm volatile("s_mov_b32 m0, %3\n\ts_nop 0\n\t"
               "buffer_load_dwordx4 %0, %1, %2 offen sc1 lds\n\t"
               "buffer_load_dwordx4 %0, %1, %2 offen offset:1024 sc1 lds\n\t"
               "buffer_load_dwordx4 %0, %1, %2 offen offset:2048 sc1 lds\n\t"
               "buffer_load_dwordx4 %0, %1, %2 offen offset:3072 sc1 lds"
               :: "v"(voff), "s"(r), "s"(soff), "s"(lds) : "memory");
}
__device__ __forceinline__ void dma1k(rsrc_t r, int voff, int soff, int lds) {
  asm volatile("s_mov_b32 m0, %3\n\ts_nop 0\n\tbuffer_load_dwordx4 %0, %1, %2 offen sc1 lds" :: "v"(voff), "s"(r), "s"(soff), "s"(lds) : "memory");
}
__device__ __forceinline__ int x_lane(int lane) { return ((lane & 31) >> 1) * 64 + (lane >> 5) * 32 + (lane & 1) * 16; }
__device__ __forceinline__ void x_dma(rsrc_t wrs, int xin, int rb, int tile, int a, int lane) {
  const int vo = x_lane(lane);
  dma4k(wrs, vo, xin + rb * XBS + a * 8192, tile + a * 8192);
  dma4k(wrs, vo, xin + rb * XBS + a * 8192 + 4096, tile + a * 8192 + 4096);
  if (tile == L_TB) {
    int so = xin + (rb - 1) * XBS + a * 8192;
    asm volatile("s_mov_b32 m0, %3\n\ts_nop 0\n\tbuffer_load_dwordx4 %1, %2, %0 offen sc1 lds\n\t"
                 "s_sub_u32 m0, m0, 1024\n\ts_nop 0\n\tbuffer_load_dwordx4 %1, %2, %0 offen offset:2048 sc1 lds\n\t"
                 "s_add_u32 m0, m0, 3072\n\ts_add_u32 %0, %0, 4096\n\tbuffer_load_dwordx4 %1, %2, %0 offen sc1 lds\n\t"
                 "s_sub_u32 m0, m0, 1024\n\ts_nop 0\n\tbuffer_load_dwordx4 %1, %2, %0 offen offset:2048 sc1 lds"
                 : "+s"(so) : "v"((lane >> 1) * 64 + 32 + (lane & 1) * 16), "s"(wrs), "s"(L_TH + a * 4096) : "memory", "scc");
  }
}
template <int S>
__device__ __forceinline__ bf16x8 ld_tr8(const LAS char* p) {
  const bf16x4 lo = __builtin_amdgcn_ds_read_tr16_b64_v4i16((LAS bf16x4*)p), hi = __builtin_amdgcn_ds_read_tr16_b64_v4i16((LAS bf16x4*)(p + S));
  return __builtin_shufflevector(lo, hi, 0, 1, 2, 3, 4, 5, 6, 7);
}

struct ConvK { u32 ah, ah2, bp, ra; f16x8 k[16]; };
__device__ __forceinline__ void a_conv_pre(ConvK& C, int a, int lane) {
  lane = (int)opaque((u32)lane);
  const int c = a >> 1, qq = a & 1, j16 = lane & 15, kg = lane >> 4;
  const int y00 = 2031 - j16 + 8 * kg - 128 * qq;
  const int sg = y00 & 3;
  C.ah = opaque((u32)(c * 4 * CSTR + sg * CSTR + 2 * (y00 - sg) - 3776));
  C.ah2 = opaque(C.ah + 8);
  C.bp = opaque((u32)(L_U + c * 4608 + (256 + 16 * j16 - 16 + 8 * kg - 128 * qq) * 2 - 192));
  C.ra = opaque((u32)(L_REDA + (a * 256 + 4 * j16 + (kg ^ ((j16 >> 1) & 3) ^ c)) * 16));
#pragma unroll
  for (int e = 0; e < 16; ++e) {
    const int ao = 3776 - (512 * (e & 3) + 64 * (e >> 2));
    const f16x4 h0 = *(const LAS f16x4*)(size_t)(C.ah + ao), h1 = *(const LAS f16x4*)(size_t)(C.ah2 + ao);
    C.k[e] = __builtin_shufflevector(h0, h1, 0, 1, 2, 3, 4, 5, 6, 7);
    __builtin_amdgcn_sched_barrier(0);
  }
#pragma unroll
  for (int e = 0; e < 16; ++e) asm volatile("" : "+v"(C.k[e]));
}
template <bool L1>
__device__ __forceinline__ void a_conv(char* smem, rsrc_t wrs, int rb, int a, const ConvK& C) {
  const LAS char* ah = (const LAS char*)(size_t)C.ah;
  const LAS char* ah2 = (const LAS char*)(size_t)C.ah2;
  const LAS char* bp = (const LAS char*)(size_t)C.bp;
  LAS f32x4* ra = (LAS f32x4*)(size_t)C.ra;
  f32x4 acc[8];
#pragma unroll
  for (int e = 0; e < 8; ++e) acc[e] = (f32x4){0.f, 0.f, 0.f, 0.f};
  f16x8 bf[4][8], fh[4][8];
#define S_RR(s_) ((s_) < 16 ? (s_) >> 2 : (s_) < 44 ? ((s_) - 16) / 7 : ((s_) - 44) >> 2)
#define S_MU(s_) ((s_) < 16 ? (s_) & 3 : (s_) < 44 ? ((s_) - 16) % 7 + 1 : ((s_) - 44) & 3)
#define S_NEW(s_) ((s_) < 16 || ((s_) < 44 && S_MU(s_) >= 4))
#define LDA(s_) do { const int ao_ = 3776 - (512 * S_MU(s_) + 64 * S_RR(s_));                                        \
      f16x4 h0_ = *(const LAS f16x4*)(ah + ao_), h1_ = *(const LAS f16x4*)(ah2 + ao_);                                \
      fh[S_RR(s_)][S_MU(s_)] = __builtin_shufflevector(h0_, h1_, 0, 1, 2, 3, 4, 5, 6, 7); } while (0)
#define LDB(r_, t_) bf[r_][t_] = *(const LAS f16x8*)(bp + 512 * (t_) + 192 - 64 * (r_))
#define CSTEPS(s0_, s1_)                                                                                               \
  _Pragma("unroll") for (int st = s0_; st < s1_; ++st) {                                                               \
    const int rr = S_RR(st), mu = S_MU(st);                                                                           \
    if (S_NEW(st + CPF) && (L1 || st + CPF >= 16)) LDA(st + CPF);                                                     \
    if (st < 4) { LDB(2 + (st >> 1), 2 * (st & 1)); LDB(2 + (st >> 1), 2 * (st & 1) + 1); }                           \
    if (st >= 44 && st < 48) { LDB(2 + ((st - 44) >> 1), 4 + 2 * (st & 1)); LDB(2 + ((st - 44) >> 1), 5 + 2 * (st & 1)); } \
    __builtin_amdgcn_sched_barrier(0);                                                                                \
    _Pragma("unroll") for (int cb = 0; cb < 8; ++cb) {                                                                \
      const int t = cb - mu;                                                                                          \
      const bool on = st < 16 ? (cb < 4 && t >= 0) : st < 44 ? (cb >= 4 && t >= 0 && t < 4) : (t >= 4);               \
      if (on) acc[cb] = __builtin_amdgcn_mfma_f32_16x16x32_f16(fh[rr][mu], bf[rr][t < 0 ? 0 : t], acc[cb], 0, 0, 0);  \
    }                                                                                                                 \
    __builtin_amdgcn_sched_barrier(0);                                                                                \
    if (st >= 13 && st < 16) ra[(st - 13) * 64] = acc[st - 13];                                                       \
    if (st >= 57) ra[1024 + (st - 57) * 64] = acc[4 + st - 57];                                                       \
  }
#pragma unroll
  for (int e = 0; e < 16; ++e) fh[e >> 2][e & 3] = C.k[e];
  __builtin_amdgcn_s_barrier();
  asm volatile("" ::: "memory");
  ST(!L1, 7);
#pragma unroll
  for (int e = 0; e < CPF; ++e) { if (L1) LDA(e); __builtin_amdgcn_sched_barrier(0); if (e < 2) { LDB(e, 0); __builtin_amdgcn_sched_barrier(0); LDB(e, 1); __builtin_amdgcn_sched_barrier(0); LDB(e, 2); __builtin_amdgcn_sched_barrier(0); LDB(e, 3); __builtin_amdgcn_sched_barrier(0); } }
  __builtin_amdgcn_sched_barrier(0);
  CSTEPS(0, 16)
  CSTEPS(16, 17)
  ra[3 * 64] = acc[3];
  __builtin_amdgcn_sched_barrier(0);
  asm volatile("" ::: "memory");
  CSTEPS(17, 18)
  if (L1) asm volatile("s_waitcnt vmcnt(0)" ::: "memory");
  asm volatile("s_waitcnt lgkmcnt(2)" ::: "memory");
  __builtin_amdgcn_s_barrier();
  asm volatile("" ::: "memory");
  ST(!L1, 8);
  CSTEPS(18, 44)
  if (!L1) ST(1, 5);
  if (!L1) __builtin_amdgcn_s_barrier();
  if (!L1) ST(1, 6);
  asm volatile("" ::: "memory");
#pragma unroll
  for (int e = 0; e < 8; ++e) { LDB(e >> 2, 4 + (e & 3)); __builtin_amdgcn_sched_barrier(0); }
  CSTEPS(44, 60)
  ra[1024 + 3 * 64] = acc[7];
  lds_barrier();
  ST(!L1, 9);
}

struct C1Pre { u32 ah, ah2, bp, xp, xc, cn; int so, fo0, fo1; };
#define C1_OPS "+{v16}"(C.ah), "+{v17}"(C.ah2), "+{v18}"(C.bp), "+{v19}"(C.xp), "+{v20}"(C.xc), "+{v21}"(C.cn), "+{v22}"(C.so), "+{v23}"(C.fo0), "+{v24}"(C.fo1)
__device__ __forceinline__ void c1_pre(C1Pre& C, int kp, int a, int lane) {
  lane = (int)opaque((u32)lane);
  const int c = a >> 1, j16 = lane & 15, kg = lane >> 4, P = a & 1;
  const int y0 = 2031 - j16 + 8 * kg - 128 * P, sg = y0 & 3;
  C.ah = (u32)(c * 4 * CSTR + sg * CSTR + 2 * (y0 - sg) - 3776);
  C.ah2 = C.ah + 8;
  C.bp = (u32)(L_U + c * 4608 + (240 + 16 * j16 + 8 * kg - 128 * P) * 2 - 192);
  C.xp = (u32)(L_X + j16 * 64 + kg * 16);
  C.xc = (u32)(L_TA + 24576 + c * 4096 + lane * 16);
  C.cn = (u32)(L_MI + 4 * c);
  C.so = OFF_X1 + (8 * ((kg & 1) ? 3 - P : P) + (j16 >> 1)) * XBS + kp * 128 + c * 64 + (j16 & 1) * 32 + (kg >> 1) * 16;
  C.fo0 = OFF_ST0 + (a * 64 + lane) * 16;
  C.fo1 = OFF_ST0 + 8192 + (HB_OF(BIDX) * 4 + a) * 128 + (lane & 3) * 16;
  asm volatile("" : C1_OPS);
}
static_assert(L_XC - L_TA - 24576 == 57344 && C_XC == 8 && C_KH == 48 && C_P2 == 52 && C_U == 56 && C_P3 == 64 && C_G2 == 32 && L_MI % 8 == 0, "generated offsets");
__device__ __forceinline__ void a_conv1(rsrc_t wrs, int a, C1Pre& C, u32x4& fl, u32x4& wfl, u32& g2) {
  asm volatile("s_bitcmp1_b32 %[a], 0\n\ts_cbranch_scc1 .Lc_odd\n\t"
  "ds_read_b64 v[36:37], v16 offset:3776\n\tds_read_b64 v[38:39], v17 offset:3776\n\tds_read_b64 v[40:41], v16 offset:3264\n\tds_read_b64 v[42:43], v17 offset:3264\n\t"
  "ds_read_b64 v[44:45], v16 offset:2752\n\tds_read_b64 v[46:47], v17 offset:2752\n\tds_read_b64 v[48:49], v16 offset:2240\n\tds_read_b64 v[50:51], v17 offset:2240\n\t"
  "ds_read_b64 v[52:53], v16 offset:3712\n\tds_read_b64 v[54:55], v17 offset:3712\n\tds_read_b64 v[56:57], v16 offset:3200\n\tds_read_b64 v[58:59], v17 offset:3200\n\t"
  "ds_read_b64 v[60:61], v16 offset:2688\n\tds_read_b64 v[62:63], v17 offset:2688\n\tds_read_b64 v[64:65], v16 offset:2176\n\tds_read_b64 v[66:67], v17 offset:2176\n\t"
  "ds_read_b64 v[68:69], v16 offset:3648\n\tds_read_b64 v[70:71], v17 offset:3648\n\tds_read_b64 v[72:73], v16 offset:3136\n\tds_read_b64 v[74:75], v17 offset:3136\n\t"
  "ds_read_b64 v[76:77], v16 offset:2624\n\tds_read_b64 v[78:79], v17 offset:2624\n\tds_read_b64 v[80:81], v16 offset:2112\n\tds_read_b64 v[82:83], v17 offset:2112\n\t"
  "ds_read_b64 v[84:85], v16 offset:3584\n\tds_read_b64 v[86:87], v17 offset:3584\n\tds_read_b64 v[88:89], v16 offset:3072\n\tds_read_b64 v[90:91], v17 offset:3072\n\t"
  "ds_read_b64 v[92:93], v16 offset:2560\n\tds_read_b64 v[94:95], v17 offset:2560\n\tds_read_b64 v[96:97], v16 offset:2048\n\tds_read_b64 v[98:99], v17 offset:2048\n\t"
  "ds_read_b32 v26, v21\n\tv_and_b32 v27, -8, v21\n\tv_mov_b32 v100, 1\n\t"
  CST(6)
  "s_mov_b32 vcc_hi, 0x1000000\n\t.Lc0_u:\n\tds_read_b32 v101, v27 offset:56\n\ts_sub_u32 vcc_hi, vcc_hi, 1\n\ts_cselect_b32 vcc_lo, 0, 4\n\ts_waitcnt lgkmcnt(0)\n\tv_readfirstlane_b32 s61, v101\n\ts_nop 1\n\ts_cmp_lt_u32 s61, vcc_lo\n\ts_cbranch_scc1 .Lc0_u\n\t"
  CST(7)
  "ds_read_b128 v[102:105], v18 offset:192\n\tds_read_b128 v[106:109], v18 offset:704\n\tds_read_b128 v[110:113], v18 offset:1216\n\tds_read_b128 v[116:119], v18 offset:128\n\t"
  "ds_read_b128 v[120:123], v18 offset:640\n\tds_read_b128 v[124:127], v18 offset:1152\n\tds_read_b128 v[128:131], v18 offset:64\n\ts_waitcnt lgkmcnt(6)\n\t"
  "v_mfma_f32_16x16x32_f16 v[132:135], v[40:43], v[102:105], 0\n\tds_read_b128 v[136:139], v18 offset:576\n\tds_read_b128 v[140:143], v18 offset:1728\n\t"
  "v_mfma_f32_16x16x32_f16 v[144:147], v[44:47], v[102:105], 0\n\tds_read_b128 v[148:151], v18 offset:1088\n\ts_waitcnt lgkmcnt(8)\n\t"
  "v_mfma_f32_16x16x32_f16 v[132:135], v[36:39], v[106:109], v[132:135]\n\tds_read_b128 v[152:155], v18 offset:1664\n\tds_read_b128 v[156:159], v18 offset:1600\n\t"
  "v_mfma_f32_16x16x32_f16 v[144:147], v[40:43], v[106:109], v[144:147]\n\tds_read_b128 v[160:163], v18 offset:0\n\tds_read_b32 v101, v27 offset:48\n\ts_waitcnt lgkmcnt(7)\n\t"
  "v_mfma_f32_16x16x32_f16 v[144:147], v[36:39], v[110:113], v[144:147]\n\tds_read_b128 v[164:167], v18 offset:512\n\t"
  "v_mfma_f32_16x16x32_f16 v[132:135], v[56:59], v[116:119], v[132:135]\n\tds_read_b128 v[168:171], v18 offset:1024\n\t"
  "v_mfma_f32_16x16x32_f16 v[144:147], v[60:63], v[116:119], v[144:147]\n\tds_read_b128 v[172:175], v18 offset:1536\n\t"
  "v_mfma_f32_16x16x32_f16 v[132:135], v[52:55], v[120:123], v[132:135]\n\t"
  "v_mfma_f32_16x16x32_f16 v[144:147], v[56:59], v[120:123], v[144:147]\n\t"
  "v_mfma_f32_16x16x32_f16 v[144:147], v[52:55], v[124:127], v[144:147]\n\t"
  "v_mfma_f32_16x16x32_f16 v[132:135], v[72:75], v[128:131], v[132:135]\n\tds_read_b128 v[176:179], v19 offset:0\n\tds_read_b128 v[180:183], v19 offset:3072\n\t"
  "v_mfma_f32_16x16x32_f16 v[144:147], v[76:79], v[128:131], v[144:147]\n\ts_waitcnt lgkmcnt(5)\n\tv_readfirstlane_b32 s61, v101\n\t"
  "v_mfma_f32_16x16x32_f16 v[132:135], v[68:71], v[136:139], v[132:135]\n\t"
  "v_mfma_f32_16x16x32_f16 v[144:147], v[72:75], v[136:139], v[144:147]\n\ts_cmp_lt_u32 s61, 4\n\ts_cbranch_scc1 .Lc0_kh\n.Lc0_kh_b:\n\t"
  "v_mfma_f32_16x16x32_f16 v[144:147], v[68:71], v[148:151], v[144:147]\n\tds_read_b64 v[184:185], v16 offset:1728\n\tds_read_b64 v[186:187], v17 offset:1728\n\t"
  "v_mfma_f32_16x16x32_f16 v[132:135], v[88:91], v[160:163], v[132:135]\n\tds_read_b64 v[188:189], v16 offset:1216\n\tds_read_b64 v[190:191], v17 offset:1216\n\t"
  "v_mfma_f32_16x16x32_f16 v[144:147], v[92:95], v[160:163], v[144:147]\n\tds_read_b64 v[192:193], v16 offset:704\n\ts_waitcnt lgkmcnt(7)\n\t"
  "v_mfma_f32_16x16x32_f16 v[132:135], v[84:87], v[164:167], v[132:135]\n\tds_read_b64 v[194:195], v17 offset:704\n\tds_read_b64 v[196:197], v16 offset:192\n\tds_read_b64 v[198:199], v17 offset:192\n\t"
  "v_mfma_f32_16x16x32_f16 v[144:147], v[88:91], v[164:167], v[144:147]\n\ts_waitcnt lgkmcnt(8)\n\tv_perm_b32 v176, v176, v176, s60\n\t"
  "v_mfma_f32_16x16x32_f16 v[144:147], v[84:87], v[168:171], v[144:147]\n\tds_write_b128 v20, v[132:135] offset:0\n\tv_perm_b32 v177, v177, v177, s60\n\t"
  "v_mfma_f32_16x16x32_f16 v[200:203], v[36:39], v[102:105], 0\n\tv_perm_b32 v178, v178, v178, s60\n\tv_perm_b32 v179, v179, v179, s60\n\t"
  "v_mfma_f32_16x16x32_f16 v[132:135], v[48:51], v[102:105], 0\n\tds_write_b128 v20, v[144:147] offset:1024\n\tv_perm_b32 v180, v180, v180, s60\n\t"
  "v_mfma_f32_16x16x32_f16 v[132:135], v[44:47], v[106:109], v[132:135]\n\ts_mov_b64 exec, 1\n\tds_add_u32 v21, v100 offset:8\n\ts_mov_b64 exec, -1\n\t"
  "v_mfma_f32_16x16x32_f16 v[132:135], v[40:43], v[110:113], v[132:135]\n\tv_perm_b32 v181, v181, v181, s60\n\tv_perm_b32 v182, v182, v182, s60\n\t"
  "v_mfma_f32_16x16x32_f16 v[132:135], v[36:39], v[140:143], v[132:135]\n\tv_perm_b32 v183, v183, v183, s60\n\t"
  "v_mfma_f32_16x16x32_f16 v[200:203], v[52:55], v[116:119], v[200:203]\n\t"
  "v_mfma_f32_16x16x32_f16 v[132:135], v[64:67], v[116:119], v[132:135]\n\t"
  "v_mfma_f32_16x16x32_f16 v[132:135], v[60:63], v[120:123], v[132:135]\n\t"
  "v_mfma_f32_16x16x32_f16 v[132:135], v[56:59], v[124:127], v[132:135]\n\t"
  "v_mfma_f32_16x16x32_f16 v[132:135], v[52:55], v[152:155], v[132:135]\n\t"
  "v_mfma_f32_16x16x32_f16 v[200:203], v[68:71], v[128:131], v[200:203]\n\t"
  "v_mfma_f32_16x16x32_f16 v[132:135], v[80:83], v[128:131], v[132:135]\n\tds_read_b32 v101, v21 offset:8\n\tds_read_b128 v[144:147], v20 offset:2048\n\tds_read_b128 v[204:207], v20 offset:3072\n\t"
  "v_mfma_f32_16x16x32_f16 v[132:135], v[76:79], v[136:139], v[132:135]\n\t"
  "v_mfma_f32_16x16x32_f16 v[132:135], v[72:75], v[148:151], v[132:135]\n\t"
  "v_mfma_f32_16x16x32_f16 v[132:135], v[68:71], v[156:159], v[132:135]\n\t"
  "v_mfma_f32_16x16x32_f16 v[200:203], v[84:87], v[160:163], v[200:203]\n\t"
  "v_mfma_f32_16x16x32_f16 v[132:135], v[96:99], v[160:163], v[132:135]\n\t"
  "v_mfma_f32_16x16x32_f16 v[132:135], v[92:95], v[164:167], v[132:135]\n\t"
  "v_mfma_f32_16x16x32_f16 v[132:135], v[88:91], v[168:171], v[132:135]\n\t"
  "v_mfma_f32_16x16x32_f16 v[132:135], v[84:87], v[172:175], v[132:135]\n\ts_waitcnt lgkmcnt(0)\n\tv_readfirstlane_b32 s61, v101\n\t"
  "v_mfma_f32_16x16x32_f16 v[208:211], v[40:43], v[140:143], 0\n\t"
  "v_mfma_f32_16x16x32_f16 v[208:211], v[44:47], v[110:113], v[208:211]\n\ts_cmp_lt_u32 s61, 2\n\ts_cbranch_scc1 .Lc0_x0\n.Lc0_x0_b:\n\tv_fmac_f32 v176, v144, v26\n\tv_fmac_f32 v177, v145, v26\n\t"
  "v_fmac_f32 v178, v146, v26\n\t"
  "v_mfma_f32_16x16x32_f16 v[212:215], v[44:47], v[140:143], 0\n\tv_fmac_f32 v179, v147, v26\n\tv_fmac_f32 v180, v204, v26\n\tv_fmac_f32 v181, v205, v26\n\t"
  "v_fmac_f32 v182, v206, v26\n\tv_fmac_f32 v183, v207, v26\n\t"
  "v_mfma_f32_16x16x32_f16 v[208:211], v[48:51], v[106:109], v[208:211]\n\tv_fmac_f32 v176, v200, v26\n\tv_fmac_f32 v177, v201, v26\n\t"
  "v_mfma_f32_16x16x32_f16 v[212:215], v[48:51], v[110:113], v[212:215]\n\tv_fmac_f32 v178, v202, v26\n\tv_fmac_f32 v179, v203, v26\n\tv_cvt_pk_bf16_f32 v144, v176, v177\n\t"
  "v_cvt_pk_bf16_f32 v145, v178, v179\n\tv_fmac_f32 v180, v132, v26\n\t"
  "v_mfma_f32_16x16x32_f16 v[176:179], v[48:51], v[140:143], 0\n\tv_fmac_f32 v181, v133, v26\n\tv_fmac_f32 v182, v134, v26\n\tv_fmac_f32 v183, v135, v26\n\t"
  "v_cvt_pk_bf16_f32 v146, v180, v181\n\tv_cvt_pk_bf16_f32 v147, v182, v183\n\t"
  "v_mfma_f32_16x16x32_f16 v[208:211], v[184:187], v[102:105], v[208:211]\n\tv_permlane16_swap_b32 v144, v146\n\tv_permlane16_swap_b32 v145, v147\n\ts_setprio 3\n\tbuffer_store_dwordx4 v[144:147], v22, s[56:59], 0 offen sc1\n\ts_setprio 0\n\t"
  CST(8)
  "v_mfma_f32_16x16x32_f16 v[212:215], v[184:187], v[106:109], v[212:215]\n\tds_read_b64 v[132:133], v16 offset:1664\n\tds_read_b64 v[134:135], v17 offset:1664\n\t"
  "v_mfma_f32_16x16x32_f16 v[176:179], v[184:187], v[110:113], v[176:179]\n\tds_read_b64 v[144:145], v16 offset:1152\n\tds_read_b64 v[146:147], v17 offset:1152\n\t"
  "v_mfma_f32_16x16x32_f16 v[180:183], v[184:187], v[140:143], 0\n\tds_read_b64 v[140:141], v16 offset:640\n\tds_read_b64 v[142:143], v17 offset:640\n\t"
  "v_mfma_f32_16x16x32_f16 v[212:215], v[188:191], v[102:105], v[212:215]\n\t"
  "v_mfma_f32_16x16x32_f16 v[176:179], v[188:191], v[106:109], v[176:179]\n\tds_read_b64 v[184:185], v16 offset:128\n\tds_read_b64 v[186:187], v17 offset:128\n\t"
  "v_mfma_f32_16x16x32_f16 v[180:183], v[188:191], v[110:113], v[180:183]\n\t"
  "v_mfma_f32_16x16x32_f16 v[176:179], v[192:195], v[102:105], v[176:179]\n\t"
  "v_mfma_f32_16x16x32_f16 v[180:183], v[192:195], v[106:109], v[180:183]\n\t"
  "v_mfma_f32_16x16x32_f16 v[180:183], v[196:199], v[102:105], v[180:183]\n\t"
  "v_mfma_f32_16x16x32_f16 v[208:211], v[56:59], v[152:155], v[208:211]\n\t"
  "v_mfma_f32_16x16x32_f16 v[208:211], v[60:63], v[124:127], v[208:211]\n\t"
  "v_mfma_f32_16x16x32_f16 v[212:215], v[60:63], v[152:155], v[212:215]\n\tds_read_b64 v[102:103], v16 offset:1600\n\tds_read_b64 v[104:105], v17 offset:1600\n\t"
  "v_mfma_f32_16x16x32_f16 v[208:211], v[64:67], v[120:123], v[208:211]\n\t"
  "v_mfma_f32_16x16x32_f16 v[212:215], v[64:67], v[124:127], v[212:215]\n\t"
  "v_mfma_f32_16x16x32_f16 v[176:179], v[64:67], v[152:155], v[176:179]\n\ts_waitcnt lgkmcnt(2)\n\t"
  "v_mfma_f32_16x16x32_f16 v[208:211], v[132:135], v[116:119], v[208:211]\n\tds_read_b64 v[108:109], v16 offset:1088\n\tds_read_b64 v[110:111], v17 offset:1088\n\t"
  "v_mfma_f32_16x16x32_f16 v[212:215], v[132:135], v[120:123], v[212:215]\n\t"
  "v_mfma_f32_16x16x32_f16 v[176:179], v[132:135], v[124:127], v[176:179]\n\t"
  "v_mfma_f32_16x16x32_f16 v[180:183], v[132:135], v[152:155], v[180:183]\n\tds_read_b64 v[112:113], v16 offset:576\n\tds_read_b64 v[114:115], v17 offset:576\n\t"
  "v_mfma_f32_16x16x32_f16 v[212:215], v[144:147], v[116:119], v[212:215]\n\t"
  "v_mfma_f32_16x16x32_f16 v[176:179], v[144:147], v[120:123], v[176:179]\n\tds_read_b64 v[132:133], v16 offset:64\n\tds_read_b64 v[134:135], v17 offset:64\n\t"
  "v_mfma_f32_16x16x32_f16 v[180:183], v[144:147], v[124:127], v[180:183]\n\t"
  "v_mfma_f32_16x16x32_f16 v[176:179], v[140:143], v[116:119], v[176:179]\n\t"
  "v_mfma_f32_16x16x32_f16 v[180:183], v[140:143], v[120:123], v[180:183]\n\tv_add_u32 v22, 0x101000, v22\n\t"
  "v_mfma_f32_16x16x32_f16 v[180:183], v[184:187], v[116:119], v[180:183]\n\t"
  "v_mfma_f32_16x16x32_f16 v[208:211], v[72:75], v[156:159], v[208:211]\n\t"
  "v_mfma_f32_16x16x32_f16 v[208:211], v[76:79], v[148:151], v[208:211]\n\t"
  "v_mfma_f32_16x16x32_f16 v[212:215], v[76:79], v[156:159], v[212:215]\n\tds_read_b64 v[116:117], v16 offset:1536\n\tds_read_b64 v[118:119], v17 offset:1536\n\t"
  "v_mfma_f32_16x16x32_f16 v[208:211], v[80:83], v[136:139], v[208:211]\n\t"
  "v_mfma_f32_16x16x32_f16 v[212:215], v[80:83], v[148:151], v[212:215]\n\t"
  "v_mfma_f32_16x16x32_f16 v[176:179], v[80:83], v[156:159], v[176:179]\n\ts_waitcnt lgkmcnt(2)\n\t"
  "v_mfma_f32_16x16x32_f16 v[208:211], v[102:105], v[128:131], v[208:211]\n\tds_read_b32 v101, v27 offset:52\n\tds_read_b64 v[120:121], v16 offset:1024\n\t"
  "v_mfma_f32_16x16x32_f16 v[212:215], v[102:105], v[136:139], v[212:215]\n\tds_read_b64 v[122:123], v17 offset:1024\n\t"
  "v_mfma_f32_16x16x32_f16 v[176:179], v[102:105], v[148:151], v[176:179]\n\t"
  "v_mfma_f32_16x16x32_f16 v[180:183], v[102:105], v[156:159], v[180:183]\n\tds_read_b64 v[102:103], v16 offset:512\n\tds_read_b64 v[104:105], v17 offset:512\n\t"
  "v_mfma_f32_16x16x32_f16 v[212:215], v[108:111], v[128:131], v[212:215]\n\t"
  "v_mfma_f32_16x16x32_f16 v[176:179], v[108:111], v[136:139], v[176:179]\n\tds_read_b64 v[124:125], v16 offset:0\n\tds_read_b64 v[126:127], v17 offset:0\n\t"
  "v_mfma_f32_16x16x32_f16 v[180:183], v[108:111], v[148:151], v[180:183]\n\t"
  "v_mfma_f32_16x16x32_f16 v[176:179], v[112:115], v[128:131], v[176:179]\n\t"
  "v_mfma_f32_16x16x32_f16 v[180:183], v[112:115], v[136:139], v[180:183]\n\ts_waitcnt lgkmcnt(5)\n\tv_readfirstlane_b32 s61, v101\n\t"
  "v_mfma_f32_16x16x32_f16 v[180:183], v[132:135], v[128:131], v[180:183]\n\t"
  "v_mfma_f32_16x16x32_f16 v[208:211], v[88:91], v[172:175], v[208:211]\n\ts_cmp_lt_u32 s61, 4\n\ts_cbranch_scc1 .Lc0_ub\n.Lc0_ub_b:\n\t"
  "v_mfma_f32_16x16x32_f16 v[208:211], v[92:95], v[168:171], v[208:211]\n\tds_read_b128 v[108:111], v18 offset:2240\n\tds_read_b128 v[112:115], v18 offset:2752\n\t"
  "v_mfma_f32_16x16x32_f16 v[212:215], v[92:95], v[172:175], v[212:215]\n\tds_read_b128 v[128:131], v18 offset:3264\n\tds_read_b128 v[132:135], v18 offset:3776\n\t"
  "v_mfma_f32_16x16x32_f16 v[208:211], v[96:99], v[164:167], v[208:211]\n\t"
  "v_mfma_f32_16x16x32_f16 v[212:215], v[96:99], v[168:171], v[212:215]\n\t"
  "v_mfma_f32_16x16x32_f16 v[176:179], v[96:99], v[172:175], v[176:179]\n\tds_read_b128 v[136:139], v18 offset:2176\n\tds_read_b128 v[140:143], v18 offset:2688\n\t"
  "v_mfma_f32_16x16x32_f16 v[208:211], v[116:119], v[160:163], v[208:211]\n\tds_read_b128 v[144:147], v18 offset:3200\n\tds_read_b128 v[148:151], v18 offset:3712\n\t"
  "v_mfma_f32_16x16x32_f16 v[212:215], v[116:119], v[164:167], v[212:215]\n\t"
  "v_mfma_f32_16x16x32_f16 v[176:179], v[116:119], v[168:171], v[176:179]\n\t"
  "v_mfma_f32_16x16x32_f16 v[180:183], v[116:119], v[172:175], v[180:183]\n\ts_waitcnt lgkmcnt(6)\n\t"
  "v_mfma_f32_16x16x32_f16 v[212:215], v[120:123], v[160:163], v[212:215]\n\tds_read_b128 v[116:119], v18 offset:2112\n\tds_read_b128 v[152:155], v18 offset:2624\n\t"
  "v_mfma_f32_16x16x32_f16 v[176:179], v[120:123], v[164:167], v[176:179]\n\tds_read_b128 v[156:159], v18 offset:3136\n\tds_read_b128 v[172:175], v18 offset:3648\n\t"
  "v_mfma_f32_16x16x32_f16 v[180:183], v[120:123], v[168:171], v[180:183]\n\t"
  "v_mfma_f32_16x16x32_f16 v[176:179], v[102:105], v[160:163], v[176:179]\n\t"
  "v_mfma_f32_16x16x32_f16 v[180:183], v[102:105], v[164:167], v[180:183]\n\t"
  "v_mfma_f32_16x16x32_f16 v[180:183], v[124:127], v[160:163], v[180:183]\n\tds_read_b128 v[104:107], v18 offset:2048\n\tds_read_b128 v[120:123], v18 offset:2560\n\t"
  "v_mfma_f32_16x16x32_f16 v[212:215], v[40:43], v[108:111], v[212:215]\n\tds_read_b128 v[124:127], v18 offset:3072\n\tds_read_b128 v[160:163], v18 offset:3584\n\t"
  "v_mfma_f32_16x16x32_f16 v[176:179], v[44:47], v[108:111], v[176:179]\n\t"
  "v_mfma_f32_16x16x32_f16 v[212:215], v[36:39], v[112:115], v[212:215]\n\t"
  "v_mfma_f32_16x16x32_f16 v[176:179], v[40:43], v[112:115], v[176:179]\n\ts_waitcnt lgkmcnt(4)\n\t"
  "v_mfma_f32_16x16x32_f16 v[176:179], v[36:39], v[128:131], v[176:179]\n\t"
  "v_mfma_f32_16x16x32_f16 v[212:215], v[56:59], v[136:139], v[212:215]\n\t"
  "v_mfma_f32_16x16x32_f16 v[176:179], v[60:63], v[136:139], v[176:179]\n\t"
  "v_mfma_f32_16x16x32_f16 v[212:215], v[52:55], v[140:143], v[212:215]\n\t"
  "v_mfma_f32_16x16x32_f16 v[176:179], v[56:59], v[140:143], v[176:179]\n\t"
  "v_mfma_f32_16x16x32_f16 v[176:179], v[52:55], v[144:147], v[176:179]\n\t"
  "v_mfma_f32_16x16x32_f16 v[212:215], v[72:75], v[116:119], v[212:215]\n\tds_read_b128 v[164:167], v19 offset:4096\n\tds_read_b128 v[168:171], v19 offset:7168\n\t"
  "v_mfma_f32_16x16x32_f16 v[176:179], v[76:79], v[116:119], v[176:179]\n\t"
  "v_mfma_f32_16x16x32_f16 v[212:215], v[68:71], v[152:155], v[212:215]\n\t"
  "v_mfma_f32_16x16x32_f16 v[176:179], v[72:75], v[152:155], v[176:179]\n\t"
  "v_mfma_f32_16x16x32_f16 v[176:179], v[68:71], v[156:159], v[176:179]\n\ts_waitcnt lgkmcnt(2)\n\t"
  "v_mfma_f32_16x16x32_f16 v[212:215], v[88:91], v[104:107], v[212:215]\n\t"
  "v_mfma_f32_16x16x32_f16 v[176:179], v[92:95], v[104:107], v[176:179]\n\t"
  "v_mfma_f32_16x16x32_f16 v[212:215], v[84:87], v[120:123], v[212:215]\n\t"
  "v_mfma_f32_16x16x32_f16 v[176:179], v[88:91], v[120:123], v[176:179]\n\tds_read_b32 v101, v27 offset:64\n\ts_waitcnt lgkmcnt(1)\n\t"
  "v_mfma_f32_16x16x32_f16 v[176:179], v[84:87], v[124:127], v[176:179]\n\tds_write_b128 v20, v[212:215] offset:57344\n\tv_perm_b32 v164, v164, v164, s60\n\t"
  "v_mfma_f32_16x16x32_f16 v[208:211], v[36:39], v[108:111], v[208:211]\n\ts_setprio 3\n\tbuffer_load_dwordx4 v[28:31], v23, s[56:59], 0 offen sc1\n\tbuffer_load_dwordx4 v[32:35], v24, s[56:59], 0 offen sc1\n\ts_setprio 0\n\t"
  "v_mfma_f32_16x16x32_f16 v[180:183], v[48:51], v[108:111], v[180:183]\n\tds_write_b128 v20, v[176:179] offset:58368\n\tv_perm_b32 v165, v165, v165, s60\n\t"
  "v_mfma_f32_16x16x32_f16 v[180:183], v[44:47], v[112:115], v[180:183]\n\ts_mov_b64 exec, 1\n\tds_add_u32 v27, v100 offset:16\n\ts_mov_b64 exec, -1\n\t"
  "v_mfma_f32_16x16x32_f16 v[180:183], v[40:43], v[128:131], v[180:183]\n\tv_perm_b32 v166, v166, v166, s60\n\tv_perm_b32 v167, v167, v167, s60\n\t"
  "v_mfma_f32_16x16x32_f16 v[180:183], v[36:39], v[132:135], v[180:183]\n\tv_perm_b32 v168, v168, v168, s60\n\tv_perm_b32 v169, v169, v169, s60\n\t"
  "v_mfma_f32_16x16x32_f16 v[208:211], v[52:55], v[136:139], v[208:211]\n\tv_perm_b32 v170, v170, v170, s60\n\tv_perm_b32 v171, v171, v171, s60\n\t"
  "v_mfma_f32_16x16x32_f16 v[180:183], v[64:67], v[136:139], v[180:183]\n\ts_waitcnt lgkmcnt(3)\n\tv_readfirstlane_b32 s61, v101\n\t"
  "v_mfma_f32_16x16x32_f16 v[180:183], v[60:63], v[140:143], v[180:183]\n\t"
  "v_mfma_f32_16x16x32_f16 v[180:183], v[56:59], v[144:147], v[180:183]\n\ts_cmp_lt_u32 s61, 4\n\ts_cbranch_scc1 .Lc0_p3\n.Lc0_p3_b:\n\t"
  "v_mfma_f32_16x16x32_f16 v[180:183], v[52:55], v[148:151], v[180:183]\n\tds_read_b32 v36, v27 offset:16\n\tds_read_b128 v[40:43], v20 offset:59392\n\tds_read_b128 v[44:47], v20 offset:60416\n\t"
  "v_mfma_f32_16x16x32_f16 v[208:211], v[68:71], v[116:119], v[208:211]\n\t"
  "v_mfma_f32_16x16x32_f16 v[180:183], v[80:83], v[116:119], v[180:183]\n\t"
  "v_mfma_f32_16x16x32_f16 v[180:183], v[76:79], v[152:155], v[180:183]\n\t"
  "v_mfma_f32_16x16x32_f16 v[180:183], v[72:75], v[156:159], v[180:183]\n\t"
  "v_mfma_f32_16x16x32_f16 v[180:183], v[68:71], v[172:175], v[180:183]\n\t"
  "v_mfma_f32_16x16x32_f16 v[208:211], v[84:87], v[104:107], v[208:211]\n\t"
  "v_mfma_f32_16x16x32_f16 v[180:183], v[96:99], v[104:107], v[180:183]\n\t"
  "v_mfma_f32_16x16x32_f16 v[180:183], v[92:95], v[120:123], v[180:183]\n\ts_waitcnt lgkmcnt(0)\n\tv_readfirstlane_b32 s61, v36\n\tds_read_b32 v25, v27 offset:32\n\t"
  "v_mfma_f32_16x16x32_f16 v[180:183], v[88:91], v[124:127], v[180:183]\n\ts_cmp_lt_u32 s61, 4\n\ts_cbranch_scc1 .Lc0_x4\n.Lc0_x4_b:\n\t"
  "v_mfma_f32_16x16x32_f16 v[180:183], v[84:87], v[160:163], v[180:183]\n\tv_fmac_f32 v164, v40, v26\n\tv_fmac_f32 v165, v41, v26\n\tv_fmac_f32 v166, v42, v26\n\t"
  "v_fmac_f32 v167, v43, v26\n\tv_fmac_f32 v168, v44, v26\n\tv_fmac_f32 v169, v45, v26\n\tv_fmac_f32 v170, v46, v26\n\t"
  "v_fmac_f32 v171, v47, v26\n\tv_fmac_f32 v164, v208, v26\n\tv_fmac_f32 v165, v209, v26\n\tv_fmac_f32 v166, v210, v26\n\t"
  "v_fmac_f32 v167, v211, v26\n\tv_cvt_pk_bf16_f32 v36, v164, v165\n\tv_cvt_pk_bf16_f32 v37, v166, v167\n\tv_fmac_f32 v168, v180, v26\n\t"
  "v_fmac_f32 v169, v181, v26\n\tv_fmac_f32 v170, v182, v26\n\tv_fmac_f32 v171, v183, v26\n\tv_cvt_pk_bf16_f32 v38, v168, v169\n\t"
  "v_cvt_pk_bf16_f32 v39, v170, v171\n\ts_nop 0\n\tv_permlane16_swap_b32 v36, v38\n\tv_permlane16_swap_b32 v37, v39\n\t"
  "s_setprio 3\n\tbuffer_store_dwordx4 v[36:39], v22, s[56:59], 0 offen sc1\n\ts_setprio 0\n\ts_waitcnt lgkmcnt(0)\n\ts_nop 0\n\t"
  "s_branch .Lc_end\n.Lc_odd:\n\t"
  "ds_read_b64 v[36:37], v16 offset:3776\n\tds_read_b64 v[38:39], v17 offset:3776\n\tds_read_b64 v[40:41], v16 offset:3264\n\tds_read_b64 v[42:43], v17 offset:3264\n\t"
  "ds_read_b64 v[44:45], v16 offset:2752\n\tds_read_b64 v[46:47], v17 offset:2752\n\tds_read_b64 v[48:49], v16 offset:2240\n\tds_read_b64 v[50:51], v17 offset:2240\n\t"
  "ds_read_b64 v[52:53], v16 offset:3712\n\tds_read_b64 v[54:55], v17 offset:3712\n\tds_read_b64 v[56:57], v16 offset:3200\n\tds_read_b64 v[58:59], v17 offset:3200\n\t"
  "ds_read_b64 v[60:61], v16 offset:2688\n\tds_read_b64 v[62:63], v17 offset:2688\n\tds_read_b64 v[64:65], v16 offset:2176\n\tds_read_b64 v[66:67], v17 offset:2176\n\t"
  "ds_read_b64 v[68:69], v16 offset:3648\n\tds_read_b64 v[70:71], v17 offset:3648\n\tds_read_b64 v[72:73], v16 offset:3136\n\tds_read_b64 v[74:75], v17 offset:3136\n\t"
  "ds_read_b64 v[76:77], v16 offset:2624\n\tds_read_b64 v[78:79], v17 offset:2624\n\tds_read_b64 v[80:81], v16 offset:2112\n\tds_read_b64 v[82:83], v17 offset:2112\n\t"
  "ds_read_b64 v[84:85], v16 offset:3584\n\tds_read_b64 v[86:87], v17 offset:3584\n\tds_read_b64 v[88:89], v16 offset:3072\n\tds_read_b64 v[90:91], v17 offset:3072\n\t"
  "ds_read_b64 v[92:93], v16 offset:2560\n\tds_read_b64 v[94:95], v17 offset:2560\n\tds_read_b64 v[96:97], v16 offset:2048\n\tds_read_b64 v[98:99], v17 offset:2048\n\t"
  "ds_read_b32 v26, v21\n\tv_and_b32 v27, -8, v21\n\tv_mov_b32 v100, 1\n\t"
  CST(6)
  "s_mov_b32 vcc_hi, 0x1000000\n\t.Lc1_u:\n\tds_read_b32 v101, v27 offset:56\n\ts_sub_u32 vcc_hi, vcc_hi, 1\n\ts_cselect_b32 vcc_lo, 0, 4\n\ts_waitcnt lgkmcnt(0)\n\tv_readfirstlane_b32 s61, v101\n\ts_nop 1\n\ts_cmp_lt_u32 s61, vcc_lo\n\ts_cbranch_scc1 .Lc1_u\n\t"
  CST(7)
  "ds_read_b128 v[102:105], v18 offset:192\n\tds_read_b128 v[106:109], v18 offset:704\n\tds_read_b128 v[110:113], v18 offset:1216\n\tds_read_b128 v[114:117], v18 offset:1728\n\t"
  "ds_read_b128 v[120:123], v18 offset:128\n\tds_read_b128 v[124:127], v18 offset:640\n\tds_read_b128 v[128:131], v18 offset:1152\n\tds_read_b128 v[132:135], v18 offset:1664\n\t"
  "ds_read_b128 v[136:139], v18 offset:64\n\ts_waitcnt lgkmcnt(8)\n\t"
  "v_mfma_f32_16x16x32_f16 v[140:143], v[36:39], v[102:105], 0\n\tds_read_b128 v[144:147], v18 offset:576\n\t"
  "v_mfma_f32_16x16x32_f16 v[148:151], v[48:51], v[102:105], 0\n\tds_read_b128 v[152:155], v18 offset:1088\n\ts_waitcnt lgkmcnt(9)\n\t"
  "v_mfma_f32_16x16x32_f16 v[148:151], v[44:47], v[106:109], v[148:151]\n\tds_read_b128 v[156:159], v18 offset:1600\n\ts_waitcnt lgkmcnt(3)\n\t"
  "v_mfma_f32_16x16x32_f16 v[148:151], v[40:43], v[110:113], v[148:151]\n\tds_read_b128 v[160:163], v18 offset:0\n\tds_read_b32 v101, v27 offset:48\n\t"
  "v_mfma_f32_16x16x32_f16 v[148:151], v[36:39], v[114:117], v[148:151]\n\tds_read_b128 v[164:167], v18 offset:512\n\t"
  "v_mfma_f32_16x16x32_f16 v[140:143], v[52:55], v[120:123], v[140:143]\n\tds_read_b128 v[168:171], v18 offset:1024\n\t"
  "v_mfma_f32_16x16x32_f16 v[148:151], v[64:67], v[120:123], v[148:151]\n\tds_read_b128 v[172:175], v18 offset:1536\n\t"
  "v_mfma_f32_16x16x32_f16 v[148:151], v[60:63], v[124:127], v[148:151]\n\t"
  "v_mfma_f32_16x16x32_f16 v[148:151], v[56:59], v[128:131], v[148:151]\n\t"
  "v_mfma_f32_16x16x32_f16 v[148:151], v[52:55], v[132:135], v[148:151]\n\t"
  "v_mfma_f32_16x16x32_f16 v[140:143], v[68:71], v[136:139], v[140:143]\n\tds_read_b128 v[176:179], v19 offset:1024\n\tds_read_b128 v[180:183], v19 offset:2048\n\t"
  "v_mfma_f32_16x16x32_f16 v[148:151], v[80:83], v[136:139], v[148:151]\n\ts_waitcnt lgkmcnt(5)\n\tv_readfirstlane_b32 s61, v101\n\t"
  "v_mfma_f32_16x16x32_f16 v[148:151], v[76:79], v[144:147], v[148:151]\n\t"
  "v_mfma_f32_16x16x32_f16 v[148:151], v[72:75], v[152:155], v[148:151]\n\ts_cmp_lt_u32 s61, 4\n\ts_cbranch_scc1 .Lc1_kh\n.Lc1_kh_b:\n\t"
  "v_mfma_f32_16x16x32_f16 v[148:151], v[68:71], v[156:159], v[148:151]\n\tds_read_b64 v[184:185], v16 offset:1728\n\tds_read_b64 v[186:187], v17 offset:1728\n\t"
  "v_mfma_f32_16x16x32_f16 v[140:143], v[84:87], v[160:163], v[140:143]\n\tds_read_b64 v[188:189], v16 offset:1216\n\tds_read_b64 v[190:191], v17 offset:1216\n\t"
  "v_mfma_f32_16x16x32_f16 v[148:151], v[96:99], v[160:163], v[148:151]\n\tds_read_b64 v[192:193], v16 offset:704\n\ts_waitcnt lgkmcnt(7)\n\t"
  "v_mfma_f32_16x16x32_f16 v[148:151], v[92:95], v[164:167], v[148:151]\n\tds_read_b64 v[194:195], v17 offset:704\n\tds_read_b64 v[196:197], v16 offset:192\n\tds_read_b64 v[198:199], v17 offset:192\n\t"
  "v_mfma_f32_16x16x32_f16 v[148:151], v[88:91], v[168:171], v[148:151]\n\ts_waitcnt lgkmcnt(8)\n\tv_perm_b32 v176, v176, v176, s60\n\t"
  "v_mfma_f32_16x16x32_f16 v[148:151], v[84:87], v[172:175], v[148:151]\n\tds_write_b128 v20, v[140:143] offset:2048\n\tv_perm_b32 v177, v177, v177, s60\n\t"
  "v_mfma_f32_16x16x32_f16 v[200:203], v[40:43], v[102:105], 0\n\tv_perm_b32 v178, v178, v178, s60\n\tv_perm_b32 v179, v179, v179, s60\n\t"
  "v_mfma_f32_16x16x32_f16 v[140:143], v[44:47], v[102:105], 0\n\tds_write_b128 v20, v[148:151] offset:3072\n\tv_perm_b32 v180, v180, v180, s60\n\t"
  "v_mfma_f32_16x16x32_f16 v[200:203], v[36:39], v[106:109], v[200:203]\n\ts_mov_b64 exec, 1\n\tds_add_u32 v21, v100 offset:8\n\ts_mov_b64 exec, -1\n\t"
  "v_mfma_f32_16x16x32_f16 v[140:143], v[40:43], v[106:109], v[140:143]\n\tv_perm_b32 v181, v181, v181, s60\n\tv_perm_b32 v182, v182, v182, s60\n\t"
  "v_mfma_f32_16x16x32_f16 v[140:143], v[36:39], v[110:113], v[140:143]\n\tv_perm_b32 v183, v183, v183, s60\n\t"
  "v_mfma_f32_16x16x32_f16 v[200:203], v[56:59], v[120:123], v[200:203]\n\t"
  "v_mfma_f32_16x16x32_f16 v[140:143], v[60:63], v[120:123], v[140:143]\n\t"
  "v_mfma_f32_16x16x32_f16 v[200:203], v[52:55], v[124:127], v[200:203]\n\t"
  "v_mfma_f32_16x16x32_f16 v[140:143], v[56:59], v[124:127], v[140:143]\n\t"
  "v_mfma_f32_16x16x32_f16 v[140:143], v[52:55], v[128:131], v[140:143]\n\t"
  "v_mfma_f32_16x16x32_f16 v[200:203], v[72:75], v[136:139], v[200:203]\n\t"
  "v_mfma_f32_16x16x32_f16 v[140:143], v[76:79], v[136:139], v[140:143]\n\tds_read_b32 v101, v21 offset:8\n\tds_read_b128 v[148:151], v20 offset:0\n\tds_read_b128 v[204:207], v20 offset:1024\n\t"
  "v_mfma_f32_16x16x32_f16 v[200:203], v[68:71], v[144:147], v[200:203]\n\t"
  "v_mfma_f32_16x16x32_f16 v[140:143], v[72:75], v[144:147], v[140:143]\n\t"
  "v_mfma_f32_16x16x32_f16 v[140:143], v[68:71], v[152:155], v[140:143]\n\t"
  "v_mfma_f32_16x16x32_f16 v[200:203], v[88:91], v[160:163], v[200:203]\n\t"
  "v_mfma_f32_16x16x32_f16 v[140:143], v[92:95], v[160:163], v[140:143]\n\t"
  "v_mfma_f32_16x16x32_f16 v[200:203], v[84:87], v[164:167], v[200:203]\n\t"
  "v_mfma_f32_16x16x32_f16 v[140:143], v[88:91], v[164:167], v[140:143]\n\t"
  "v_mfma_f32_16x16x32_f16 v[140:143], v[84:87], v[168:171], v[140:143]\n\ts_waitcnt lgkmcnt(0)\n\tv_readfirstlane_b32 s61, v101\n\t"
  "v_mfma_f32_16x16x32_f16 v[208:211], v[40:43], v[114:117], 0\n\t"
  "v_mfma_f32_16x16x32_f16 v[208:211], v[44:47], v[110:113], v[208:211]\n\ts_cmp_lt_u32 s61, 2\n\ts_cbranch_scc1 .Lc1_x0\n.Lc1_x0_b:\n\tv_fmac_f32 v176, v148, v26\n\tv_fmac_f32 v177, v149, v26\n\t"
  "v_fmac_f32 v178, v150, v26\n\t"
  "v_mfma_f32_16x16x32_f16 v[212:215], v[44:47], v[114:117], 0\n\tv_fmac_f32 v179, v151, v26\n\tv_fmac_f32 v180, v204, v26\n\tv_fmac_f32 v181, v205, v26\n\t"
  "v_fmac_f32 v182, v206, v26\n\tv_fmac_f32 v183, v207, v26\n\t"
  "v_mfma_f32_16x16x32_f16 v[208:211], v[48:51], v[106:109], v[208:211]\n\tv_fmac_f32 v176, v200, v26\n\tv_fmac_f32 v177, v201, v26\n\t"
  "v_mfma_f32_16x16x32_f16 v[212:215], v[48:51], v[110:113], v[212:215]\n\tv_fmac_f32 v178, v202, v26\n\tv_fmac_f32 v179, v203, v26\n\tv_cvt_pk_bf16_f32 v148, v176, v177\n\t"
  "v_cvt_pk_bf16_f32 v149, v178, v179\n\tv_fmac_f32 v180, v140, v26\n\t"
  "v_mfma_f32_16x16x32_f16 v[176:179], v[48:51], v[114:117], 0\n\tv_fmac_f32 v181, v141, v26\n\tv_fmac_f32 v182, v142, v26\n\tv_fmac_f32 v183, v143, v26\n\t"
  "v_cvt_pk_bf16_f32 v150, v180, v181\n\tv_cvt_pk_bf16_f32 v151, v182, v183\n\t"
  "v_mfma_f32_16x16x32_f16 v[208:211], v[184:187], v[102:105], v[208:211]\n\tv_permlane16_swap_b32 v148, v150\n\tv_permlane16_swap_b32 v149, v151\n\ts_setprio 3\n\tbuffer_store_dwordx4 v[148:151], v22, s[56:59], 0 offen sc1\n\ts_setprio 0\n\t"
  CST(8)
  "v_mfma_f32_16x16x32_f16 v[212:215], v[184:187], v[106:109], v[212:215]\n\tds_read_b64 v[140:141], v16 offset:1664\n\tds_read_b64 v[142:143], v17 offset:1664\n\t"
  "v_mfma_f32_16x16x32_f16 v[176:179], v[184:187], v[110:113], v[176:179]\n\tds_read_b64 v[148:149], v16 offset:1152\n\tds_read_b64 v[150:151], v17 offset:1152\n\t"
  "v_mfma_f32_16x16x32_f16 v[180:183], v[184:187], v[114:117], 0\n\tds_read_b64 v[116:117], v16 offset:640\n\tds_read_b64 v[118:119], v17 offset:640\n\t"
  "v_mfma_f32_16x16x32_f16 v[212:215], v[188:191], v[102:105], v[212:215]\n\t"
  "v_mfma_f32_16x16x32_f16 v[176:179], v[188:191], v[106:109], v[176:179]\n\tds_read_b64 v[184:185], v16 offset:128\n\tds_read_b64 v[186:187], v17 offset:128\n\t"
  "v_mfma_f32_16x16x32_f16 v[180:183], v[188:191], v[110:113], v[180:183]\n\t"
  "v_mfma_f32_16x16x32_f16 v[176:179], v[192:195], v[102:105], v[176:179]\n\t"
  "v_mfma_f32_16x16x32_f16 v[180:183], v[192:195], v[106:109], v[180:183]\n\t"
  "v_mfma_f32_16x16x32_f16 v[180:183], v[196:199], v[102:105], v[180:183]\n\t"
  "v_mfma_f32_16x16x32_f16 v[208:211], v[56:59], v[132:135], v[208:211]\n\t"
  "v_mfma_f32_16x16x32_f16 v[208:211], v[60:63], v[128:131], v[208:211]\n\t"
  "v_mfma_f32_16x16x32_f16 v[212:215], v[60:63], v[132:135], v[212:215]\n\tds_read_b64 v[102:103], v16 offset:1600\n\tds_read_b64 v[104:105], v17 offset:1600\n\t"
  "v_mfma_f32_16x16x32_f16 v[208:211], v[64:67], v[124:127], v[208:211]\n\t"
  "v_mfma_f32_16x16x32_f16 v[212:215], v[64:67], v[128:131], v[212:215]\n\t"
  "v_mfma_f32_16x16x32_f16 v[176:179], v[64:67], v[132:135], v[176:179]\n\ts_waitcnt lgkmcnt(2)\n\t"
  "v_mfma_f32_16x16x32_f16 v[208:211], v[140:143], v[120:123], v[208:211]\n\tds_read_b64 v[108:109], v16 offset:1088\n\tds_read_b64 v[110:111], v17 offset:1088\n\t"
  "v_mfma_f32_16x16x32_f16 v[212:215], v[140:143], v[124:127], v[212:215]\n\t"
  "v_mfma_f32_16x16x32_f16 v[176:179], v[140:143], v[128:131], v[176:179]\n\t"
  "v_mfma_f32_16x16x32_f16 v[180:183], v[140:143], v[132:135], v[180:183]\n\tds_read_b64 v[112:113], v16 offset:576\n\tds_read_b64 v[114:115], v17 offset:576\n\t"
  "v_mfma_f32_16x16x32_f16 v[212:215], v[148:151], v[120:123], v[212:215]\n\t"
  "v_mfma_f32_16x16x32_f16 v[176:179], v[148:151], v[124:127], v[176:179]\n\tds_read_b64 v[132:133], v16 offset:64\n\tds_read_b64 v[134:135], v17 offset:64\n\t"
  "v_mfma_f32_16x16x32_f16 v[180:183], v[148:151], v[128:131], v[180:183]\n\t"
  "v_mfma_f32_16x16x32_f16 v[176:179], v[116:119], v[120:123], v[176:179]\n\t"
  "v_mfma_f32_16x16x32_f16 v[180:183], v[116:119], v[124:127], v[180:183]\n\tv_add_u32 v22, 0x101000, v22\n\t"
  "v_mfma_f32_16x16x32_f16 v[180:183], v[184:187], v[120:123], v[180:183]\n\t"
  "v_mfma_f32_16x16x32_f16 v[208:211], v[72:75], v[156:159], v[208:211]\n\t"
  "v_mfma_f32_16x16x32_f16 v[208:211], v[76:79], v[152:155], v[208:211]\n\t"
  "v_mfma_f32_16x16x32_f16 v[212:215], v[76:79], v[156:159], v[212:215]\n\tds_read_b64 v[116:117], v16 offset:1536\n\tds_read_b64 v[118:119], v17 offset:1536\n\t"
  "v_mfma_f32_16x16x32_f16 v[208:211], v[80:83], v[144:147], v[208:211]\n\t"
  "v_mfma_f32_16x16x32_f16 v[212:215], v[80:83], v[152:155], v[212:215]\n\t"
  "v_mfma_f32_16x16x32_f16 v[176:179], v[80:83], v[156:159], v[176:179]\n\ts_waitcnt lgkmcnt(2)\n\t"
  "v_mfma_f32_16x16x32_f16 v[208:211], v[102:105], v[136:139], v[208:211]\n\tds_read_b32 v101, v27 offset:52\n\tds_read_b64 v[120:121], v16 offset:1024\n\t"
  "v_mfma_f32_16x16x32_f16 v[212:215], v[102:105], v[144:147], v[212:215]\n\tds_read_b64 v[122:123], v17 offset:1024\n\t"
  "v_mfma_f32_16x16x32_f16 v[176:179], v[102:105], v[152:155], v[176:179]\n\t"
  "v_mfma_f32_16x16x32_f16 v[180:183], v[102:105], v[156:159], v[180:183]\n\tds_read_b64 v[102:103], v16 offset:512\n\tds_read_b64 v[104:105], v17 offset:512\n\t"
  "v_mfma_f32_16x16x32_f16 v[212:215], v[108:111], v[136:139], v[212:215]\n\t"
  "v_mfma_f32_16x16x32_f16 v[176:179], v[108:111], v[144:147], v[176:179]\n\tds_read_b64 v[124:125], v16 offset:0\n\tds_read_b64 v[126:127], v17 offset:0\n\t"
  "v_mfma_f32_16x16x32_f16 v[180:183], v[108:111], v[152:155], v[180:183]\n\t"
  "v_mfma_f32_16x16x32_f16 v[176:179], v[112:115], v[136:139], v[176:179]\n\t"
  "v_mfma_f32_16x16x32_f16 v[180:183], v[112:115], v[144:147], v[180:183]\n\ts_waitcnt lgkmcnt(5)\n\tv_readfirstlane_b32 s61, v101\n\t"
  "v_mfma_f32_16x16x32_f16 v[180:183], v[132:135], v[136:139], v[180:183]\n\t"
  "v_mfma_f32_16x16x32_f16 v[208:211], v[88:91], v[172:175], v[208:211]\n\ts_cmp_lt_u32 s61, 4\n\ts_cbranch_scc1 .Lc1_ub\n.Lc1_ub_b:\n\t"
  "v_mfma_f32_16x16x32_f16 v[208:211], v[92:95], v[168:171], v[208:211]\n\tds_read_b128 v[108:111], v18 offset:2240\n\tds_read_b128 v[112:115], v18 offset:2752\n\t"
  "v_mfma_f32_16x16x32_f16 v[212:215], v[92:95], v[172:175], v[212:215]\n\tds_read_b128 v[128:131], v18 offset:3264\n\tds_read_b128 v[132:135], v18 offset:3776\n\t"
  "v_mfma_f32_16x16x32_f16 v[208:211], v[96:99], v[164:167], v[208:211]\n\t"
  "v_mfma_f32_16x16x32_f16 v[212:215], v[96:99], v[168:171], v[212:215]\n\t"
  "v_mfma_f32_16x16x32_f16 v[176:179], v[96:99], v[172:175], v[176:179]\n\tds_read_b128 v[136:139], v18 offset:2176\n\tds_read_b128 v[140:143], v18 offset:2688\n\t"
  "v_mfma_f32_16x16x32_f16 v[208:211], v[116:119], v[160:163], v[208:211]\n\tds_read_b128 v[144:147], v18 offset:3200\n\tds_read_b128 v[148:151], v18 offset:3712\n\t"
  "v_mfma_f32_16x16x32_f16 v[212:215], v[116:119], v[164:167], v[212:215]\n\t"
  "v_mfma_f32_16x16x32_f16 v[176:179], v[116:119], v[168:171], v[176:179]\n\t"
  "v_mfma_f32_16x16x32_f16 v[180:183], v[116:119], v[172:175], v[180:183]\n\ts_waitcnt lgkmcnt(6)\n\t"
  "v_mfma_f32_16x16x32_f16 v[212:215], v[120:123], v[160:163], v[212:215]\n\tds_read_b128 v[116:119], v18 offset:2112\n\tds_read_b128 v[152:155], v18 offset:2624\n\t"
  "v_mfma_f32_16x16x32_f16 v[176:179], v[120:123], v[164:167], v[176:179]\n\tds_read_b128 v[156:159], v18 offset:3136\n\tds_read_b128 v[172:175], v18 offset:3648\n\t"
  "v_mfma_f32_16x16x32_f16 v[180:183], v[120:123], v[168:171], v[180:183]\n\t"
  "v_mfma_f32_16x16x32_f16 v[176:179], v[102:105], v[160:163], v[176:179]\n\t"
  "v_mfma_f32_16x16x32_f16 v[180:183], v[102:105], v[164:167], v[180:183]\n\t"
  "v_mfma_f32_16x16x32_f16 v[180:183], v[124:127], v[160:163], v[180:183]\n\tds_read_b128 v[104:107], v18 offset:2048\n\tds_read_b128 v[120:123], v18 offset:2560\n\t"
  "v_mfma_f32_16x16x32_f16 v[208:211], v[36:39], v[108:111], v[208:211]\n\tds_read_b128 v[124:127], v18 offset:3072\n\tds_read_b128 v[160:163], v18 offset:3584\n\t"
  "v_mfma_f32_16x16x32_f16 v[180:183], v[48:51], v[108:111], v[180:183]\n\t"
  "v_mfma_f32_16x16x32_f16 v[180:183], v[44:47], v[112:115], v[180:183]\n\ts_waitcnt lgkmcnt(6)\n\t"
  "v_mfma_f32_16x16x32_f16 v[180:183], v[40:43], v[128:131], v[180:183]\n\t"
  "v_mfma_f32_16x16x32_f16 v[180:183], v[36:39], v[132:135], v[180:183]\n\t"
  "v_mfma_f32_16x16x32_f16 v[208:211], v[52:55], v[136:139], v[208:211]\n\t"
  "v_mfma_f32_16x16x32_f16 v[180:183], v[64:67], v[136:139], v[180:183]\n\t"
  "v_mfma_f32_16x16x32_f16 v[180:183], v[60:63], v[140:143], v[180:183]\n\t"
  "v_mfma_f32_16x16x32_f16 v[180:183], v[56:59], v[144:147], v[180:183]\n\t"
  "v_mfma_f32_16x16x32_f16 v[180:183], v[52:55], v[148:151], v[180:183]\n\t"
  "v_mfma_f32_16x16x32_f16 v[208:211], v[68:71], v[116:119], v[208:211]\n\tds_read_b128 v[48:51], v19 offset:5120\n\tds_read_b128 v[64:67], v19 offset:6144\n\t"
  "v_mfma_f32_16x16x32_f16 v[180:183], v[80:83], v[116:119], v[180:183]\n\t"
  "v_mfma_f32_16x16x32_f16 v[180:183], v[76:79], v[152:155], v[180:183]\n\ts_waitcnt lgkmcnt(2)\n\t"
  "v_mfma_f32_16x16x32_f16 v[180:183], v[72:75], v[156:159], v[180:183]\n\t"
  "v_mfma_f32_16x16x32_f16 v[180:183], v[68:71], v[172:175], v[180:183]\n\t"
  "v_mfma_f32_16x16x32_f16 v[208:211], v[84:87], v[104:107], v[208:211]\n\t"
  "v_mfma_f32_16x16x32_f16 v[180:183], v[96:99], v[104:107], v[180:183]\n\t"
  "v_mfma_f32_16x16x32_f16 v[180:183], v[92:95], v[120:123], v[180:183]\n\t"
  "v_mfma_f32_16x16x32_f16 v[180:183], v[88:91], v[124:127], v[180:183]\n\tds_read_b32 v101, v27 offset:64\n\ts_waitcnt lgkmcnt(1)\n\t"
  "v_mfma_f32_16x16x32_f16 v[180:183], v[84:87], v[160:163], v[180:183]\n\tds_write_b128 v20, v[208:211] offset:59392\n\tv_perm_b32 v48, v48, v48, s60\n\t"
  "v_mfma_f32_16x16x32_f16 v[212:215], v[40:43], v[108:111], v[212:215]\n\ts_setprio 3\n\tbuffer_load_dwordx4 v[28:31], v23, s[56:59], 0 offen sc1\n\tbuffer_load_dwordx4 v[32:35], v24, s[56:59], 0 offen sc1\n\ts_setprio 0\n\t"
  "v_mfma_f32_16x16x32_f16 v[176:179], v[44:47], v[108:111], v[176:179]\n\tds_write_b128 v20, v[180:183] offset:60416\n\tv_perm_b32 v49, v49, v49, s60\n\t"
  "v_mfma_f32_16x16x32_f16 v[212:215], v[36:39], v[112:115], v[212:215]\n\ts_mov_b64 exec, 1\n\tds_add_u32 v27, v100 offset:16\n\ts_mov_b64 exec, -1\n\t"
  "v_mfma_f32_16x16x32_f16 v[176:179], v[40:43], v[112:115], v[176:179]\n\tv_perm_b32 v50, v50, v50, s60\n\tv_perm_b32 v51, v51, v51, s60\n\t"
  "v_mfma_f32_16x16x32_f16 v[176:179], v[36:39], v[128:131], v[176:179]\n\tv_perm_b32 v64, v64, v64, s60\n\tv_perm_b32 v65, v65, v65, s60\n\t"
  "v_mfma_f32_16x16x32_f16 v[212:215], v[56:59], v[136:139], v[212:215]\n\tv_perm_b32 v66, v66, v66, s60\n\tv_perm_b32 v67, v67, v67, s60\n\t"
  "v_mfma_f32_16x16x32_f16 v[176:179], v[60:63], v[136:139], v[176:179]\n\ts_waitcnt lgkmcnt(3)\n\tv_readfirstlane_b32 s61, v101\n\t"
  "v_mfma_f32_16x16x32_f16 v[212:215], v[52:55], v[140:143], v[212:215]\n\t"
  "v_mfma_f32_16x16x32_f16 v[176:179], v[56:59], v[140:143], v[176:179]\n\ts_cmp_lt_u32 s61, 4\n\ts_cbranch_scc1 .Lc1_p3\n.Lc1_p3_b:\n\t"
  "v_mfma_f32_16x16x32_f16 v[176:179], v[52:55], v[144:147], v[176:179]\n\tds_read_b32 v36, v27 offset:16\n\tds_read_b128 v[40:43], v20 offset:57344\n\tds_read_b128 v[44:47], v20 offset:58368\n\t"
  "v_mfma_f32_16x16x32_f16 v[212:215], v[72:75], v[116:119], v[212:215]\n\t"
  "v_mfma_f32_16x16x32_f16 v[176:179], v[76:79], v[116:119], v[176:179]\n\t"
  "v_mfma_f32_16x16x32_f16 v[212:215], v[68:71], v[152:155], v[212:215]\n\t"
  "v_mfma_f32_16x16x32_f16 v[176:179], v[72:75], v[152:155], v[176:179]\n\t"
  "v_mfma_f32_16x16x32_f16 v[176:179], v[68:71], v[156:159], v[176:179]\n\t"
  "v_mfma_f32_16x16x32_f16 v[212:215], v[88:91], v[104:107], v[212:215]\n\t"
  "v_mfma_f32_16x16x32_f16 v[176:179], v[92:95], v[104:107], v[176:179]\n\t"
  "v_mfma_f32_16x16x32_f16 v[212:215], v[84:87], v[120:123], v[212:215]\n\ts_waitcnt lgkmcnt(0)\n\tv_readfirstlane_b32 s61, v36\n\tds_read_b32 v25, v27 offset:32\n\t"
  "v_mfma_f32_16x16x32_f16 v[176:179], v[88:91], v[120:123], v[176:179]\n\ts_cmp_lt_u32 s61, 4\n\ts_cbranch_scc1 .Lc1_x4\n.Lc1_x4_b:\n\t"
  "v_mfma_f32_16x16x32_f16 v[176:179], v[84:87], v[124:127], v[176:179]\n\tv_fmac_f32 v48, v40, v26\n\tv_fmac_f32 v49, v41, v26\n\tv_fmac_f32 v50, v42, v26\n\t"
  "v_fmac_f32 v51, v43, v26\n\tv_fmac_f32 v64, v44, v26\n\tv_fmac_f32 v65, v45, v26\n\tv_fmac_f32 v66, v46, v26\n\t"
  "v_fmac_f32 v67, v47, v26\n\tv_fmac_f32 v48, v212, v26\n\tv_fmac_f32 v49, v213, v26\n\tv_fmac_f32 v50, v214, v26\n\t"
  "v_fmac_f32 v51, v215, v26\n\tv_cvt_pk_bf16_f32 v36, v48, v49\n\tv_cvt_pk_bf16_f32 v37, v50, v51\n\tv_fmac_f32 v64, v176, v26\n\t"
  "v_fmac_f32 v65, v177, v26\n\tv_fmac_f32 v66, v178, v26\n\tv_fmac_f32 v67, v179, v26\n\tv_cvt_pk_bf16_f32 v38, v64, v65\n\t"
  "v_cvt_pk_bf16_f32 v39, v66, v67\n\ts_nop 0\n\tv_permlane16_swap_b32 v36, v38\n\tv_permlane16_swap_b32 v37, v39\n\t"
  "s_setprio 3\n\tbuffer_store_dwordx4 v[36:39], v22, s[56:59], 0 offen sc1\n\ts_setprio 0\n\ts_waitcnt lgkmcnt(0)\n\ts_nop 0\n\t"
  ".Lc_end:\n\ts_waitcnt vmcnt(1) lgkmcnt(0)"
  : C1_OPS, "=&{v[28:31]}"(fl), "=&{v[32:35]}"(wfl), "=&{v25}"(g2)
  : "{s[56:59]}"(wrs), "{s60}"((a >> 1) ? 0x03020c0cu : 0x01000c0cu), [a] "s"(a)
  : "memory", "scc", "vcc", "s61", "v26", "v27", "v36", "v37", "v38", "v39", "v40", "v41", "v42", "v43", "v44", "v45", "v46", "v47", "v48", "v49", "v50", "v51", "v52", "v53", "v54", "v55", "v56", "v57", "v58", "v59", "v60", "v61", "v62", "v63", "v64", "v65", "v66", "v67", "v68", "v69", "v70", "v71", "v72", "v73", "v74", "v75", "v76", "v77", "v78", "v79", "v80", "v81", "v82", "v83", "v84", "v85", "v86", "v87", "v88", "v89", "v90", "v91", "v92", "v93", "v94", "v95", "v96", "v97", "v98", "v99", "v100", "v101", "v102", "v103", "v104", "v105", "v106", "v107", "v108", "v109", "v110", "v111", "v112", "v113", "v114", "v115", "v116", "v117", "v118", "v119", "v120", "v121", "v122", "v123", "v124", "v125", "v126", "v127", "v128", "v129", "v130", "v131", "v132", "v133", "v134", "v135", "v136", "v137", "v138", "v139", "v140", "v141", "v142", "v143", "v144", "v145", "v146", "v147", "v148", "v149", "v150", "v151", "v152", "v153", "v154", "v155", "v156", "v157", "v158", "v159", "v160", "v161", "v162", "v163", "v164", "v165", "v166", "v167", "v168", "v169", "v170", "v171", "v172", "v173", "v174", "v175", "v176", "v177", "v178", "v179", "v180", "v181", "v182", "v183", "v184", "v185", "v186", "v187", "v188", "v189", "v190", "v191", "v192", "v193", "v194", "v195", "v196", "v197", "v198", "v199", "v200", "v201", "v202", "v203", "v204", "v205", "v206", "v207", "v208", "v209", "v210", "v211", "v212", "v213", "v214", "v215", "v216", "v217", "v218", "v219", "v220", "v221", "v222", "v223");
  ST(0, 9);
}

struct XTake { u32 p, sw, idx; bf16x8 one; u64 m0, m1, m2; };
typedef __attribute__((ext_vector_type(16))) short bf16x16;
__device__ __forceinline__ void x_pre(XTake& X, int a, int lane) {
  X.p = (u32)(L_TA + a * 8192 + lane * 8);
  X.sw = (u32)(L_SP + ((lane & 15) * 4 + a) * 4 + ((lane >> 4) == ((lane >> 2) & 3) ? 768 : 0));
  X.idx = (lane & 1) ? 0xEEEEu : 0x4444u;
  X.one = __builtin_bit_cast(bf16x8, (u32x4){0x3F803F80u, 0x3F803F80u, 0x3F803F80u, 0x3F803F80u});
  X.m0 = 0xAAAAAAAAAAAAAAAAull; X.m1 = 0xCCCCCCCCCCCCCCCCull; X.m2 = 0xF0000F0000F0000Full;
  asm volatile("" : "+v"(X.p), "+v"(X.sw), "+v"(X.idx), "+v"(X.one));
  asm volatile("" : "+s"(X.m0), "+s"(X.m1), "+s"(X.m2));
}
template <int H, int NY = 0, bool ALL = false>
__device__ __forceinline__ void x_take(const XTake& X, char* smem, rsrc_t wrs, int xin, int rb, int a, int lane, u32 sent = 0xFFFFFFFFu) {
  constexpr int tile = H ? L_TB : L_TA, NT = H ? 3 : 2;
  const LAS char* p = (const LAS char*)(size_t)(H ? opaque(X.p + (L_TB - L_TA)) : X.p);
  const LAS char* ph = (const LAS char*)(size_t)(H ? opaque(X.p + (L_TH - L_TA) - a * 4096) : 0u);
  LAS float* spw = (LAS float*)(size_t)X.sw + H * 384;
  float s = 0.f, dv[NT];
  bf16x16 xf[NT][2];
  f32x4 ss[NT], sg[NT];
  auto ld_ = [&](int o, int j) __attribute__((always_inline)) {
    if (o < 2) xf[o][j] = __builtin_shufflevector(ld_tr8<1024>(p + o * 512 + j * 4096), ld_tr8<1024>(p + o * 512 + j * 4096 + 2048), 0, 1, 2, 3, 4, 5, 6, 7, 8, 9, 10, 11, 12, 13, 14, 15);
    else xf[o][j] = __builtin_shufflevector(ld_tr8<512>(ph + j * 2048), ld_tr8<512>(ph + j * 2048 + 1024), 0, 1, 2, 3, 4, 5, 6, 7, 8, 9, 10, 11, 12, 13, 14, 15);
    SB();
  };
  auto gr_ = [&](int o, int j) __attribute__((always_inline)) {
#define XLO(v_) __builtin_shufflevector(v_, v_, 0, 1, 2, 3, 4, 5, 6, 7)
#define XHI(v_) __builtin_shufflevector(v_, v_, 8, 9, 10, 11, 12, 13, 14, 15)
    if (j == 0) asm volatile("v_mfma_f32_16x16x32_bf16 %0, %1, %1, 0" : "=&v"(sg[o]) : "v"(XLO(xf[o][0])));
    else asm volatile("v_mfma_f32_16x16x32_bf16 %0, %1, %1, %0" : "+v"(sg[o]) : "v"(XLO(xf[o][1])));
    asm volatile("v_mfma_f32_16x16x32_bf16 %0, %1, %1, %0" : "+v"(sg[o]) : "v"(XHI(xf[o][j])));
    SB();
  };
  auto sm_ = [&](int o, int j) __attribute__((always_inline)) {
    asm volatile("v_smfmac_f32_16x16x64_bf16 %0, %1, %2, %3" : "+v"(ss[o]) : "v"(X.one), "v"(xf[o][j]), "v"(X.idx));
    SB();
  };
  auto fin_ = [&](int o, auto V_) __attribute__((always_inline)) {
    float t;
    float& d = dv[o];
    asm volatile("v_cndmask_b32_e64 %0, %3, %4, %9\n\tv_cndmask_b32_e64 %1, %5, %6, %9\n\tv_cndmask_b32_e64 %0, %0, %1, %10\n\t.if %12 >= 0\n\ts_nop %12\n\t.endif\n\t"
                 "v_add_f32 %1, %7, %8\n\tv_cndmask_b32_e64 %0, %1, %0, %11\n\tv_add_f32 %2, %2, %1"
                 : "=&v"(d), "=&v"(t), "+v"(s) : "v"(sg[o][0]), "v"(sg[o][1]), "v"(sg[o][2]), "v"(sg[o][3]), "v"(ss[o][0]), "v"(ss[o][1]),
                   "s"(X.m0), "s"(X.m1), "s"(X.m2), "n"(decltype(V_)::value - 1), "v"(ss[o]));
    SB();
  };
  auto zero_ = [&]() __attribute__((always_inline)) {
#pragma unroll
    for (int o = 0; o < NT; ++o) { ss[o] = (f32x4){0.f, 0.f, 0.f, 0.f}; asm volatile("" : "+v"(ss[o])); }
    asm volatile("" : "+v"(s));
  };
  auto first_ = [&]() __attribute__((always_inline)) { gr_(0, 0); sm_(0, 0); gr_(1, 0); sm_(1, 0); };
  auto second_ = [&]() __attribute__((always_inline)) {
    gr_(0, 1); sm_(0, 1); gr_(1, 1); fin_(0, IC<0>{}); sm_(1, 1);
    if (NT == 2) { fin_(1, IC<5>{}); spw[0] = dv[0]; spw[64] = dv[1]; }
  };
  auto third_ = [&]() __attribute__((always_inline)) {
    gr_(2, 0); fin_(1, IC<0>{}); gr_(2, 1);
    spw[0] = dv[0]; spw[64] = dv[1];
    SB();
    sm_(2, 0); sm_(2, 1); fin_(NT - 1, IC<5>{});
    spw[128] = dv[NT - 1];
  };
  zero_();
  SB();
  if (H) {
    const u32 sa = opaque((u32)(L_TB + a * 8192 + (lane >> 4) * 1024 + (lane & 15) * 32 + ((lane >> 3) & 1) * 512 + ((lane >> 4) & 3) * 8 + 4));
#pragma unroll 1
    for (unsigned spins = 0; spins <= SPIN_CAP; ++spins) {
      x_dma(wrs, xin, rb, tile, a, lane);
      asm volatile("s_waitcnt vmcnt(8)" ::: "memory");
      if (__builtin_amdgcn_ballot_w64(*(const LAS u32*)(size_t)sa == sent) == 0ull) break;
      FAILC(20);
    }
  } else asm volatile("s_waitcnt vmcnt(%0)" :: "n"(4 + NY) : "memory");
  if (H && B6 == 0) { __builtin_amdgcn_s_barrier(); ST(xin != OFF_X1, 14); asm volatile("" ::: "memory"); }
  ld_(0, 0); ld_(1, 0);
  if (!ALL && !(H && B6 == 1)) first_();
  asm volatile("s_waitcnt vmcnt(%0)" :: "n"(4 * (NT - 2) + NY) : "memory");
  if (H && B6) { __builtin_amdgcn_s_barrier(); ST(xin != OFF_X1, 14); asm volatile("" ::: "memory"); }
  ld_(0, 1); ld_(1, 1);
  if (ALL || (H && B6 == 1)) first_();
  second_();
  if (NT == 3) {
    asm volatile("s_waitcnt vmcnt(%0)" :: "n"(NY) : "memory");
    ld_(2, 0); ld_(2, 1);
    third_();
  }
  if (__builtin_expect(__builtin_amdgcn_ballot_w64(s != s) != 0ull, 0)) {
    for (unsigned spins = 0;; ++spins) {
      FAILC((xin == OFF_X1 ? 1 : 9) + H);
      if (H) *(u32*)(smem + L_MI + M_RD) = xin == OFF_X1 ? 1 : 2;
      x_dma(wrs, xin, rb, tile, a, lane);
      asm volatile("s_waitcnt vmcnt(0)" ::: "memory");
      s = 0.f;
#pragma unroll
      for (int o = 0; o < 2 * NT; ++o) ld_(o >> 1, o & 1);
      zero_();
      first_(); second_();
      if (NT == 3) third_();
      if (!__any(s != s)) break;
      bool st = false;
#pragma unroll 1
      for (int e = 0; e < 4 * NT; ++e) { const u32x4 v = *(const u32x4*)(smem + (e < 8 ? tile + a * 8192 : L_TH + a * 4096 - 8192) + lane * 16 + e * 1024); st |= v[1] == sent || v[3] == sent; }
      if (!__any(st) || spins > SPIN_CAP) break;
    }
  }
}
#define PIN(x_) asm volatile("" : "+v"(x_))
struct HaloMsg { u32x4 k, v; };
__device__ __forceinline__ void halo_ld(HaloMsg& h, rsrc_t wrs, int ho) { h.k = ldw(wrs, ho); h.v = ldw(wrs, ho + 1024); }
__device__ __forceinline__ bool halo_ok(const HaloMsg& h, u32 tag) {
  return __builtin_amdgcn_ballot_w64((opaque((h.k[1] ^ tag) | (h.k[3] ^ tag)) | opaque((h.v[1] ^ tag) | (h.v[3] ^ tag))) != 0u) == 0ull;
}
struct AttC { f32x4 mp, mo, mf; u32x2 one; u32 sl, su; };
__device__ __forceinline__ void att_pre(AttC& C, int lane, bool first) {
  const float NINF = -__builtin_inff();
  const u64 pm[4] = {0x0fff00ff000f0000ull, 0x3fff03ff003f0003ull, 0xf000ff00fff0ffffull, 0xc000fc00ffc0fffcull};
#pragma unroll
  for (int e = 0; e < 4; ++e) {
    const u64 par = (e & 1) ? 0xAAAAAAAAAAAAAAAAull : 0x5555555555555555ull;
    C.mp[e] = __builtin_amdgcn_inverse_ballot_w64(pm[e >> 1] & par) ? 0.f : NINF;
    C.mo[e] = __builtin_amdgcn_inverse_ballot_w64(pm[2 + (e >> 1)] & par) ? 0.f : NINF;
    C.mf[e] = first ? NINF : C.mp[e];
  }
  C.one = (u32x2){0x3F803F80u, 0x3F803F80u};
  C.sl = (lane & 1) ? 0x01000c0cu : 0x0c0c0100u; C.su = (lane & 1) ? 0x03020c0cu : 0x0c0c0302u;
  asm volatile("" : "+v"(C.mp), "+v"(C.mo), "+v"(C.mf), "+v"(C.one), "+v"(C.sl), "+v"(C.su));
}
template <class F>
__device__ __forceinline__ void attention(const AttC& C, u32 qp, HaloMsg& hm, int ho, u32 tag, rsrc_t wrs, int oo, int fid, F mid) {
  u32x4 vv, ow;
  u32x2 q0;
  f32x4 l0, o1;
  float inv;
#define SOFT(a_, b_, c_, d_, F1_, F2_, W_, V_, O_)                                                                                                      \
  F1_ "v_permlane16_swap_b32 v232, v233\n\tv_max_f32 v234, v232, v233\n\tv_max_f32 v235, v232, v233\n\t"                                                \
  F2_ "v_permlane32_swap_b32 v234, v235\n\tv_max_f32 v232, v234, v235\n\tv_mul_f32 v232, 0xbeb8aa3b, v232\n\t"                                          \
  "v_fmamk_f32 " a_ ", " a_ ", 0x3eb8aa3b, v232\n\tv_fmamk_f32 " b_ ", " b_ ", 0x3eb8aa3b, v232\n\t"                                                    \
  "v_fmamk_f32 " c_ ", " c_ ", 0x3eb8aa3b, v232\n\tv_fmamk_f32 " d_ ", " d_ ", 0x3eb8aa3b, v232\n\t"                                                    \
  "v_exp_f32 " a_ ", " a_ "\n\tv_exp_f32 " b_ ", " b_ "\n\tv_exp_f32 " c_ ", " c_ "\n\tv_exp_f32 " d_ ", " d_ "\n\t"                                    \
  "v_cvt_pk_bf16_f32 v232, " a_ ", " b_ "\n\tv_cvt_pk_bf16_f32 v233, " c_ ", " d_ "\n\t"                                                                \
  "v_perm_b32 v226, v232, v232, %[sl]\n\tv_perm_b32 v227, v232, v232, %[su]\n\t"                                                                        \
  "v_mfma_f32_16x16x16_bf16 v[236:239], %[one], v[232:233], 0\n\t"                                                                                      \
  "v_perm_b32 v224, v233, v233, %[sl]\n\tv_perm_b32 v225, v233, v233, %[su]\n\t" W_ "\n\ts_nop 0\n\t"                                                   \
  "v_mfma_f32_16x16x32_bf16 " O_ ", " V_ ", v[224:227], 0\n\t"
  asm volatile(
      "ds_read2st64_b32 v[214:215], %[qp] offset0:4 offset1:5\n\tds_read2st64_b32 v[216:217], %[qp] offset0:6 offset1:7\n\t"
      "ds_read2st64_b32 v[212:213], %[qp] offset0:0 offset1:1\n\tds_read2st64_b32 v[218:219], %[qp] offset0:10 offset1:11\n\t"
      "ds_read2st64_b32 v[200:201], %[qp] offset0:2 offset1:3\n\tds_read2st64_b32 v[202:203], %[qp] offset0:8 offset1:9\n\t"
      "s_waitcnt lgkmcnt(4)\n\tv_mfma_f32_16x16x16_bf16 v[220:223], v[216:217], v[214:215], %[mp]\n\t"
      "s_waitcnt lgkmcnt(3)\n\tv_mfma_f32_16x16x16_bf16 v[224:227], v[212:213], v[214:215], %[mo]\n\t"
      "s_waitcnt lgkmcnt(2)\n\tv_mfma_f32_16x16x16_bf16 v[228:231], v[216:217], v[218:219], %[mo]\n\t"
      "v_max_f32 v220, v220, v221\n\tv_max_f32 v221, v222, v223\n\tv_max_f32 v232, v220, v221\n\t"
      "v_max_f32 v222, v224, v225\n\tv_max_f32 v223, v226, v227\n\t"
      "v_max3_f32 v233, v232, v222, v223\n\tv_max3_f32 v232, v232, v222, v223\n\t"
      SOFT("v220", "v221", "v222", "v223", "s_nop 0\n\tv_max_f32 v228, v228, v229\n\t", "v_max_f32 v229, v230, v231\n\tv_max_f32 v230, v228, v229\n\t", "s_waitcnt lgkmcnt(0)", "v[200:203]", "v[244:247]")
      "v_rcp_f32 v248, v236"
      : "=&{v[200:203]}"(vv), "=&{v[218:219]}"(q0), "=&{v[228:231]}"(l0), "=&{v[244:247]}"(o1), "=&{v248}"(inv)
      : [qp] "v"(qp), [mp] "v"(C.mp), [mo] "v"(C.mo), [one] "v"(C.one), [sl] "v"(C.sl), [su] "v"(C.su)
      : "memory", "v212", "v213", "v214", "v215", "v216", "v217", "v220", "v221", "v222", "v223", "v224", "v225", "v226", "v227", "v232", "v233", "v234", "v235", "v236", "v237", "v238", "v239");
  mid();
  {
    u32 n = SPIN_CAP;
    asm volatile("1:\n\tv_bitop3_b32 v232, v209, v211, %[tg] bitop3:0x7e\n\tv_bitop3_b32 v233, v205, v207, %[tg] bitop3:0x7e\n\tv_or_b32 v232, v232, v233\n\tv_cmp_ne_u32 vcc, 0, v232\n\t"
                 "s_cbranch_vccz 2f\n\ts_sleep 2\n\tbuffer_load_dwordx4 v[208:211], %[ho], %[rs], 0 offen sc1\n\tbuffer_load_dwordx4 v[204:207], %[ho], %[rs], 0 offen offset:1024 sc1\n\t"
                 "s_sub_u32 %[n], %[n], 1\n\ts_waitcnt vmcnt(0)\n\ts_cbranch_scc0 1b\n2:"
                 : "+{v[208:211]}"(hm.k), "+{v[204:207]}"(hm.v), [n] "+s"(n) : [tg] "s"(tag), [ho] "v"(ho), [rs] "s"(wrs) : "memory", "vcc", "scc", "v232", "v233");
  }
  asm volatile(
      "v_mov_b32 v209, v210\n\tv_mov_b32 v205, v206\n\ts_nop 0\n\tv_mfma_f32_16x16x16_bf16 v[220:223], v[208:209], v[218:219], %[mf]\n\t"
      "v_mul_f32 v244, v244, v248\n\tv_mul_f32 v245, v245, v248\n\tv_mul_f32 v246, v246, v248\n\tv_mul_f32 v247, v247, v248\n\t"
      "v_cvt_pk_bf16_f32 v242, v244, v245\n\tv_cvt_pk_bf16_f32 v243, v246, v247\n\ts_nop 1\n\t"
      "v_max_f32 v220, v220, v221\n\tv_max_f32 v221, v222, v223\n\t"
      "v_max3_f32 v233, v230, v220, v221\n\tv_max3_f32 v232, v230, v220, v221\n\t"
      SOFT("v220", "v221", "v228", "v229", "s_nop 1\n\t", "s_nop 1\n\t", "s_nop 0", "v[202:205]", "v[244:247]")
      "v_rcp_f32 v248, v236\n\ts_nop 6\n\t"
      "v_mul_f32 v244, v244, v248\n\tv_mul_f32 v245, v245, v248\n\tv_mul_f32 v246, v246, v248\n\tv_mul_f32 v247, v247, v248\n\t"
      "v_cvt_pk_bf16_f32 v240, v244, v245\n\tv_cvt_pk_bf16_f32 v241, v246, v247\n\ts_nop 0\n\t"
      "v_permlane16_swap_b32 v240, v242\n\tv_permlane16_swap_b32 v241, v243"
      : "=&{v[240:243]}"(ow), "+{v[208:211]}"(hm.k), "+{v[204:207]}"(hm.v), "+{v[228:231]}"(l0), "+{v[244:247]}"(o1), "+{v248}"(inv)
      : "{v[200:203]}"(vv), "{v[218:219]}"(q0), [mf] "v"(C.mf), [one] "v"(C.one), [sl] "v"(C.sl), [su] "v"(C.su)
      : "v220", "v221", "v222", "v223", "v224", "v225", "v226", "v227", "v232", "v233", "v234", "v235", "v236", "v237", "v238", "v239");
  stw(wrs, oo, ow);
  asm volatile("s_nop 1" :: "v"(ow) : "memory");
}
__device__ __forceinline__ void o_dma(rsrc_t wrs, int og, int ot, int a, int lane) {
  dma4k(wrs, lane * 16, og + a * 4096, ot + a * 4096);
  dma4k(wrs, lane * 16, og + 16384 + a * 4096, ot + 16384 + a * 4096);
}
__device__ __forceinline__ void o_get(rsrc_t wrs, int vo, int g0, int g1, int ot, u32 sa, u32 mark, int fid) {
#pragma unroll 1
  for (unsigned spins = 0; spins <= SPIN_CAP; ++spins) {
    dma4k(wrs, vo, g0, ot);
    dma4k(wrs, vo, g1, ot + 16384);
    asm volatile("s_waitcnt vmcnt(4)" ::: "memory");
    if (__builtin_amdgcn_ballot_w64(*(const LAS u32*)(size_t)sa == mark) == 0ull) break;
    FAILC(fid);
  }
}
template <bool L1, class F>
__device__ __forceinline__ void wo_mma2(f32x4 (&acc)[2], char* smem, int ot, rsrc_t wrs, int og, int a, int lane, const bf16x8 (&wof)[16], u32 cnt, const f32x4 (&r0)[2], const u32 (&qa)[4], u32 sent, F late) {
  u32x4 of[32];
#define LDO2(s_) of[s_] = *(const LAS u32x4*)((const LAS char*)(size_t)qa[(s_) >> 3] + (ot - L_TA) + ((s_) & 1) * 16384 + (((s_) >> 1) & 3) * 1024)
#define WMM(s_) do { SB(); if ((s_) < 2) asm volatile("v_mfma_f32_16x16x32_bf16 %0, %1, %2, %3" : "=&v"(acc[(s_) & 1]) \
        : "v"(L1 ? __builtin_bit_cast(bf16x8, of[s_]) : wof[0]), "v"(L1 ? wof[0] : __builtin_bit_cast(bf16x8, of[s_])), "v"(r0[(s_) & 1])); \
      else if (L1) asm volatile("v_mfma_f32_16x16x32_bf16 %0, %1, %2, %0" : "+v"(acc[(s_) & 1]) : "v"(of[s_]), "v"(wof[(s_) >> 1])); \
      else asm volatile("v_mfma_f32_16x16x32_bf16 %0, %1, %2, %0" : "+v"(acc[(s_) & 1]) : "v"(wof[(s_) >> 1]), "v"(of[s_])); SB(); } while (0)
  u32 mx = 0, cv;
#pragma unroll
  for (int s = 0; s < 8; ++s) { LDO2(s); SB(); }
#pragma unroll
  for (int s = 0; s < 3; ++s) {
    WMM(s);
#pragma unroll
    for (int e = 3 * s; e < 3 * s + 3 && e < 8; ++e) mx = max(max(mx, of[e][1]), of[e][3]);
    asm volatile("" : "+v"(mx));
  }
  if (__builtin_expect(!__all(mx < 0xFF810000u), 0)) {
    for (unsigned spins = 0; spins <= SPIN_CAP; ++spins) {
      FAILC((L1 ? 5 : 13) + (ot == L_TB));
      __builtin_amdgcn_s_sleep(2);
      o_dma(wrs, og, ot, a, lane);
      asm volatile("s_waitcnt vmcnt(0)" ::: "memory");
      bool st = false;
#pragma unroll
      for (int s = 0; s < 8; ++s) { LDO2(s); st |= of[s][1] == sent || of[s][3] == sent; }
      if (!__any(st)) break;
    }
    WMM(0); WMM(1); WMM(2);
    asm volatile("s_nop 7" : "+v"(acc[0]), "+v"(acc[1]));
  }
  cnt_add(smem, C_OT, lane);
  cv = *(const volatile LAS u32*)(size_t)(u32)(L_MI + C_OT);
  SB();
#pragma unroll
  for (int s = 3; s < WS5; ++s) WMM(s);
  {
    u32 n = 1u << 24;
    asm volatile("1:\n\tv_readfirstlane_b32 vcc_lo, %0\n\ts_sub_u32 %1, %1, 1\n\ts_cbranch_scc1 2f\n\ts_cmp_ge_u32 vcc_lo, %3\n\ts_cbranch_scc1 2f\n\tds_read_b32 %0, %2\n\ts_waitcnt lgkmcnt(0)\n\ts_branch 1b\n2:"
                 : "+v"(cv), "+s"(n) : "v"(L_MI + C_OT), "s"(cnt) : "memory", "vcc", "scc");
  }
#pragma unroll
  for (int s = 8; s < WS5 + 8; ++s) { LDO2(s); SB(); }
#pragma unroll
  for (int s = WS5; s < 32; ++s) {
    WMM(s);
    if (s + 8 < 32) LDO2(s + 8);
    if (s == 24) late();
  }
  asm volatile("s_nop 7" : "+v"(acc[0]), "+v"(acc[1]));
}
template <class F>
__device__ __forceinline__ void wo_last(f32x4 (&acc)[2], char* smem, rsrc_t wrs, int og, int a, int lane, const bf16x8 (&wof)[16], const f32x4 (&r0)[2], const u32 (&qa)[4], u32 sent, F put) {
  u32x4 of[32];
#define LDO3(q_, o_, s_) of[s_] = *(const LAS u32x4*)((const LAS char*)(size_t)q_[((s_) >> 2) & 3] + (o_) + ((s_) >> 4) * 16384 + ((s_) & 3) * 1024)
#define WM3(s_) do { SB(); if (((s_) & 15) == 0) asm volatile("v_mfma_f32_16x16x32_bf16 %0, %1, %2, %3" : "=&v"(acc[(s_) >> 4]) : "v"(wof[0]), "v"(of[s_]), "v"(r0[(s_) >> 4])); \
      else asm volatile("v_mfma_f32_16x16x32_bf16 %0, %1, %2, %0" : "+v"(acc[(s_) >> 4]) : "v"(wof[(s_) & 15]), "v"(of[s_])); SB(); } while (0)
  u32 qb[4];
#pragma unroll
  for (int e = 0; e < 4; ++e) qb[e] = qa[e];
  SB();
  __builtin_amdgcn_s_barrier();
  ST(1, 19);
#pragma unroll 1
  for (u32 pass = 0;; ++pass) {
    asm volatile("" : "+v"(qb[0]), "+v"(qb[1]), "+v"(qb[2]), "+v"(qb[3]) :: "memory");
#pragma unroll
    for (int s = 0; s < WLD; ++s) { LDO3(qb, L_TB - L_TA, s); SB(); }
#pragma unroll
    for (int s = 0; s < 32; ++s) {
      WM3(s);
      if (s + WLD == 16) asm volatile("s_cmp_lg_u32 %0, 0\n\ts_cbranch_scc1 1f\n\ts_barrier\n1:" :: "s"(__builtin_amdgcn_readfirstlane(pass)) : "memory", "scc");
      if (s + WLD < 32) LDO3(qb, L_TB - L_TA, s + WLD);
    }
    asm volatile("s_nop 7" : "+v"(acc[0]), "+v"(acc[1]));
    put();
    SB();
    if (__builtin_expect(__builtin_amdgcn_ballot_w64(__builtin_isunordered(acc[0][0], acc[1][0])) == 0ull, 1)) break;
    asm volatile("s_waitcnt vmcnt(0)" ::: "memory");
    FAILC(14);
    if (pass == 0) {
      lds_barrier();
      const u32 fx = *(const u32*)(smem + L_MI + M_FX);
#pragma unroll
      for (int e = 0; e < 4; ++e) qb[e] -= ((fx >> (8 * ((a + e) & 3))) & 1u) * (L_TB - L_TA);
    } else {
      u32 m = 1;
#pragma unroll 1
      for (int s = 0; s < 32; ++s) {
        const u32x4 v = *(const LAS u32x4*)(size_t)((s & 8 ? (s & 4 ? qb[3] : qb[2]) : (s & 4 ? qb[1] : qb[0])) + (L_TB - L_TA) + (s >> 4) * 16384 + (s & 3) * 1024);
        m = min(min(m, v[1] ^ sent), v[3] ^ sent);
      }
      if (__builtin_amdgcn_ballot_w64(m == 0u) == 0ull || pass > SPIN_CAP) break;
      lds_barrier();
      o_dma(wrs, og, __builtin_amdgcn_readfirstlane(qb[0]) + (L_TB - L_TA) - a * 4096, a, lane);
      asm volatile("s_waitcnt vmcnt(0)" ::: "memory");
      lds_barrier();
    }
  }
}
struct WoPre { f32x4 r[2]; int so; u32 xs, ao; };
template <bool L1>
__device__ __forceinline__ void wo_pre(WoPre& P, const u32x2 (&res)[2], char* ws, int H, int rb, int hb, int a, int lane) {
  const int i16 = lane & 15, kg = lane >> 4, R0 = 1024 * H + 32 * rb;
#pragma unroll
  for (int t = 0; t < 2; ++t) P.r[t] = (f32x4){lo2f(res[t][0]), hi2f(res[t][0]), lo2f(res[t][1]), hi2f(res[t][1])};
  if (L1) {
    P.so = OFF_X2 + ((32 * hb + 8 * a + (i16 >> 1)) * XS_ + R0 + 16 * (lane & 1) + 4 * kg) * 4;
    P.xs = (u32)(L_XS + H * 512 + lane * 8);
    P.ao = (u32)(OFF_ST2 + ((R0 & ~255) + ((lane >> 1) & 3) * 64 + ((R0 >> 2) & 63) + 4 * (lane & 1) + kg) * 16 + (lane & 8));
    asm volatile("" : "+v"(P.r[0]), "+v"(P.r[1]), "+v"(P.so), "+v"(P.xs), "+v"(P.ao));
  } else {
    P.so = ((R0 + 16 * (kg & 1) + i16) * 256 + 32 * hb + 8 * a + 4 * (kg >> 1)) * 4;
    asm volatile("" : "+v"(P.r[0]), "+v"(P.r[1]), "+v"(P.so));
  }
}
template <bool L1>
__device__ __forceinline__ void wo_epi(const f32x4 (&acc)[2], const WoPre& P, char* smem, rsrc_t wrs, char* ws, u32* out, int H, int lane, u32 ONES) {
  if (L1) {
    f32x4 lo_, hi_;
    pair_x(lo_, hi_, acc[0], acc[1]);
    u32x4 v;
#pragma unroll
    for (int e = 0; e < 4; ++e) v[e] = pk2(lo_[e], hi_[e]);
    asm volatile("" : "+v"(v));
    stw(wrs, P.so, v);
    SB();
    float sv, q0, q1, q2, q3;
#define RD_(d_, a_, c_, m_) "v_add_f32_dpp " d_ ", " a_ ", " a_ " " c_ " row_mask:0xf bank_mask:" m_ "\n\t"
    asm volatile("v_dot2_f32_bf16 %5, %13, %17, 0\n\tv_dot2_f32_bf16 %6, %14, %17, 0\n\tv_dot2_f32_bf16 %7, %15, %17, 0\n\tv_dot2_f32_bf16 %8, %16, %17, 0\n\t"
                 "v_dot2_f32_bf16 %9, %13, %13, 0\n\tv_dot2_f32_bf16 %10, %14, %14, 0\n\tv_dot2_f32_bf16 %11, %15, %15, 0\n\tv_dot2_f32_bf16 %12, %16, %16, 0\n\t"
                 RD_("%1", "%5", "row_ror:8", "0x3") RD_("%2", "%6", "row_ror:8", "0x3") RD_("%3", "%7", "row_ror:8", "0x3") RD_("%4", "%8", "row_ror:8", "0x3")
                 RD_("%1", "%9", "row_ror:8", "0xc") RD_("%2", "%10", "row_ror:8", "0xc") RD_("%3", "%11", "row_ror:8", "0xc") RD_("%4", "%12", "row_ror:8", "0xc")
                 RD_("%5", "%1", "row_shl:4", "0x5") RD_("%6", "%2", "row_shl:4", "0x5") RD_("%5", "%3", "row_shr:4", "0xa") RD_("%6", "%4", "row_shr:4", "0xa")
                 "s_nop 0\n\t"
                 RD_("%1", "%5", "quad_perm:[2,3,0,1]", "0xf") RD_("%2", "%6", "quad_perm:[2,3,0,1]", "0xf")
                 "v_cndmask_b32_e64 %0, %1, %2, %18"
                 : "=&v"(sv), "=&v"(q0), "=&v"(q1), "=&v"(q2), "=&v"(q3), "=&v"(lo_[0]), "=&v"(lo_[1]), "=&v"(lo_[2]), "=&v"(lo_[3]), "=&v"(hi_[0]), "=&v"(hi_[1]), "=&v"(hi_[2]), "=&v"(hi_[3])
                 : "v"(v[0]), "v"(v[1]), "v"(v[2]), "v"(v[3]), "v"(ONES), "s"(0xCCCCCCCCCCCCCCCCull));
    const float hf = __builtin_floorf(sv * 0.5f);
    const u32 lo = (u32)(fmaf(sv, 0.5f, -hf) * 4294967296.0f);
    const u64 mine = ((u64)(u32)(int)hf << 32) | (u64)((lo & ~63u) | 1u);
    const u64 old = __hip_atomic_fetch_add((LAS u64*)(size_t)P.xs, mine, __ATOMIC_RELAXED, __HIP_MEMORY_SCOPE_WORKGROUP);
    if (((u32)old & 63u) == 3u) __hip_atomic_fetch_add((GAS u64*)((GAS char*)ws + P.ao), old + mine, __ATOMIC_RELAXED, __HIP_MEMORY_SCOPE_AGENT);
  } else {
    auto r0s = __builtin_amdgcn_permlane16_swap(pk2(acc[0][0], acc[0][1]), pk2(acc[1][0], acc[1][1]), false, false);
    auto r1s = __builtin_amdgcn_permlane16_swap(pk2(acc[0][2], acc[0][3]), pk2(acc[1][2], acc[1][3]), false, false);
    const rsrc_t ors = __builtin_amdgcn_make_buffer_rsrc((void*)out, 0, 2 * MB_, 0x00020000);
    if (H == 0) __builtin_amdgcn_raw_buffer_store_b128((u32x4){r0s[0], r1s[0], r0s[1], r1s[1]}, ors, P.so, 0, OUTA);
    else __builtin_amdgcn_raw_buffer_store_b128((u32x4){r0s[0], r1s[0], r0s[1], r1s[1]}, ors, P.so, 0, OUTB);
  }
}
template <bool L1>
__device__ __forceinline__ void res_ld(u32x2 (&res)[2], const char* smem, int tile, int hb, int a, int lane) {
#pragma unroll
  for (int t = 0; t < 2; ++t) {
    const int o = tile + t * 512 + (4 * hb + a) * 1024;
    if (L1) res[t] = *(const u32x2*)(smem + o + (lane & 15) * 32 + (lane >> 4) * 8);
    else res[t] = __builtin_bit_cast(u32x2, __builtin_amdgcn_ds_read_tr16_b64_v4i16((LAS bf16x4*)(size_t)(u32)(o + lane * 8)));
  }
}
template <bool L1>
__device__ __forceinline__ void a_blayer(char* smem, rsrc_t wrs, char* ws, u32* out, int t, int a, bf16x8 (&wof)[16], u32 tag, u32x4 wfl, ConvK& ck, XTake& xt, AttC& ac) {
  const u32 t9 = L1 ? 0u : KADD(2, now_(), D9);
  t = (int)opaque((u32)t);
  const int lane = t & 63, i16 = lane & 15, kg = lane >> 4;
  const int XIN = L1 ? OFF_X1 : OFF_X3, OBUF = L1 ? OFF_O1 : OFF_O2;
  const int bid = BIDX;
  const int rb = RB_OF(bid), hb = HB_OF(bid);
  if (!L1) { x_pre(xt, a, lane); SB(); x_take<0, 0, true>(xt, smem, wrs, XIN, rb, a, lane); SB(); }
  const int og = OBUF + rb * 32768;
  u32x2 resa[2], resb[2];
  const int wfo = OFF_ST0 + 8192 + (hb * 4 + a) * 128 + (lane & 3) * 16;
#define LDWO(e_) wof[e_] = __builtin_bit_cast(bf16x8, ldws(wrs, ((hb * 64 + a) * 64 + lane) * 16, OFF_WO + ((4 * a + (e_)) & 15) * 4096))
  auto ld_wo = [&](int e0, int e1) __attribute__((always_inline)) {
#pragma unroll
    for (int e = e0; e < e1; ++e) LDWO(e);
    __builtin_amdgcn_sched_barrier(0);
  };
  const u32 mark = x0_mark(tag), sent = L1 ? mark : 0xFFFFFFFFu;
  if (L1) {
    tags_wait(wfl, wrs, wfo, tag, 6); ld_wo(0, WOE);
    SB();
    x_pre(xt, a, lane); SB();
    x_take<0, WOE, true>(xt, smem, wrs, XIN, rb, a, lane, sent);
  }
  ST(!L1, 10);
  SB();
  lds_barrier();
  ST(!L1, 11);
  const u32 t5 = L1 ? 0u : KADD(20, now_(), D5);
  res_ld<L1>(resa, smem, L_TA, hb, a, lane);
  auto pf_x3 = [&]() __attribute__((always_inline)) {
    const u32x4 SENT = {0xFFFFFFFFu, 0xFFFFFFFFu, 0xFFFFFFFFu, 0xFFFFFFFFu};
    const int xo = OFF_X3 + (8 * a + (lane >> 3)) * XBS + ((bid & 7) * 32 + (bid >> 3)) * 128 + (lane & 7) * 16;
    stw(wrs, xo, SENT); stw(wrs, xo + 32 * XBS, SENT);
  };
  const int oo = (int)opaque((u32)(OBUF + (((rb * 2 + (kg & 1)) * 16 + hb * 2 + (a >> 1)) * 64 + (2 * (a & 1) + (kg >> 1)) * 16 + i16) * 16));
  const u32 ONES = opaque(0x3F803F80u);
  att_pre(ac, lane, rb == 0);
  if (L1) pf_x3();
  if (L1) SB();
  const int ho = (int)opaque((u32)((rb == 0 ? WSB : KVO(L1, rb, hb * 4 + a)) + lane * 16));
  const u32 qp = opaque((u32)(L_QB + a * 3072 + lane * 4));
  const int tso = (int)opaque((u32)(OFF_TS + rb * 128 + (lane & 7) * 8)), tsw = (int)opaque((u32)(t == hb ? tso : WSB));
  HaloMsg hm;
  SB();
  if (L1) KSLP(1, SLP_XB1); else gate(tmax(t9, t5));
  ST(!L1, 12);
  x_take<1, 0, false>(xt, smem, wrs, XIN, 32 + rb, a, lane, sent);
  ST(!L1, 13);
  if (L1) SB();
  lds_barrier();
  ST(!L1, 15);
  if (L1) KSLP(3, SLP_H1); else KSLP(4, SLP_H2);
  halo_ld(hm, wrs, ho);
  u32 t7 = 0, tq = 0;
  if (!L1) {
    t7 = (u32)__builtin_amdgcn_s_memrealtime();
    tq = KADD(6, now_(), DQ2);
    __builtin_amdgcn_raw_buffer_store_b64((u32x2){t7, tag}, wrs, tsw, 0, 16);
  }
  SB();
  attention(ac, qp, hm, ho, rb == 0 ? 0u : L1 ? mark : tag, wrs, oo, L1 ? 3 : 11, [&]() __attribute__((always_inline)) { if (L1) SB(); });
  const u32 to = L1 ? 0u : KADD(21, now_(), DO2);
  ST(!L1, 16);
  if (L1) __builtin_amdgcn_raw_buffer_store_b32(tag, wrs, (bid | t) == 0 ? OFF_EP : WSB, 0, 16);
  if (L1) ld_wo(WOE, 16);
  res_ld<L1>(resb, smem, L_TB, hb, a, lane);
  cnt_add(smem, C_OB, lane);
  WoPre wp, wq;
  u32 qa[4];
#pragma unroll
  for (int e = 0; e < 4; ++e) qa[e] = opaque((u32)(L_TA + ((a + e) & 3) * 4096 + lane * 16));
  wo_pre<L1>(wp, resa, ws, 0, rb, hb, a, lane);
  wo_pre<L1>(wq, resb, ws, 1, rb, hb, a, lane);
  const u32 sa = opaque((u32)(L_TA + a * 4096 + lane * 64 + (lane & 14) * 4 + 4));
  SB();
  if (L1) KSLP(5, SLP_OA1); else gate(tmax(tq, to));
  ST(!L1, 17);
  u32x2 ts;
  if (!L1) asm volatile("buffer_load_dwordx2 %0, %1, %2, 0 offen sc1" : "=&{v[200:201]}"(ts) : "v"(tso), "s"(wrs) : "memory");
  o_get(wrs, lane * 16, og + a * 4096, og + 16384 + a * 4096, L_TA + a * 4096, sa, mark, L1 ? 21 : 23);
  if (!L1) {
    int d;
    asm volatile("v_subrev_u32 %0, %2, v200\n\tv_cmp_eq_u32 vcc, %3, v201\n\tv_min_i32 %0, 0x96, %0\n\tv_cndmask_b32 %0, 64, %0, vcc\n\t"
                 "s_nop 1\n\tv_max_i32_dpp %0, %0, %0 quad_perm:[1,0,3,2] row_mask:0xf bank_mask:0xf\n\ts_nop 1\n\tv_max_i32_dpp %0, %0, %0 quad_perm:[2,3,0,1] row_mask:0xf bank_mask:0xf\n\t"
                 "s_nop 1\n\tv_max_i32_dpp %0, %0, %0 row_half_mirror row_mask:0xf bank_mask:0xf\n\tv_max_i32 %0, 0, %0\n\tv_mul_u32_u24 %0, 20, %0"
                 : "=&v"(d), "+{v[200:201]}"(ts) : "s"(t7), "s"(tag) : "vcc", "memory");
    *(int*)(smem + L_MI + M_DL) = d;
    cnt_add(smem, C_OV, lane);
  }
  u32 ob = 0;
  int ob0 = og + MB_ + a * 4096, ob1 = ob0 + 16384;
  if (L1) asm volatile("" : "+s"(ob0), "+s"(ob1));
  asm volatile("s_waitcnt vmcnt(0)" ::: "memory");
  f32x4 acc[2];
  wo_mma2<L1>(acc, smem, L_TA, wrs, og, a, lane, wof, L1 ? 4 : 12, wp.r, qa, mark, [&]() __attribute__((always_inline)) { if (L1) ob = *(const volatile LAS u32*)(size_t)(u32)(L_MI + C_OB); });
  ST(!L1, 18);
  wo_epi<L1>(acc, wp, smem, wrs, ws, out, 0, lane, ONES);
  if (L1) {
    cnt_add(smem, C_X2, lane);
    if (__builtin_expect((u32)__builtin_amdgcn_readfirstlane(ob) < 8u, 0)) cnt_spin(smem, C_OB, 8);
    dma4k(wrs, lane * 16, ob0, L_TB + a * 4096);
    dma4k(wrs, lane * 16, ob1, L_TB + 16384 + a * 4096);
    a_conv_pre(ck, a, lane);
    ST(0, 19);
    asm volatile("s_waitcnt vmcnt(0)" ::: "memory");
    wo_mma2<L1>(acc, smem, L_TB, wrs, og + MB_, a, lane, wof, 8, wq.r, qa, mark, []() {});
    wo_epi<L1>(acc, wq, smem, wrs, ws, out, 1, lane, ONES);
  } else wo_last(acc, smem, wrs, og + MB_, a, lane, wof, wq.r, qa, mark, [&]() __attribute__((always_inline)) { wo_epi<L1>(acc, wq, smem, wrs, ws, out, 1, lane, ONES); });
  ST(!L1, 20); if (L1) ST(1, 1);
}

#define WREGS(X) \
  X(0, 76, 77, 78, 79) X(1, 80, 81, 82, 83) X(2, 84, 85, 86, 87) X(3, 88, 89, 90, 91) X(4, 92, 93, 94, 95) X(5, 96, 97, 98, 99) \
  X(6, 100, 101, 102, 103) X(7, 104, 105, 106, 107) X(8, 108, 109, 110, 111) X(9, 112, 113, 114, 115) X(10, 116, 117, 118, 119) X(11, 120, 121, 122, 123) \
  X(12, 124, 125, 126, 127) X(13, 128, 129, 130, 131) X(14, 132, 133, 134, 135) X(15, 136, 137, 138, 139) X(16, 140, 141, 142, 143) X(17, 144, 145, 146, 147) \
  X(18, 148, 149, 150, 151) X(19, 152, 153, 154, 155) X(20, 156, 157, 158, 159) X(21, 160, 161, 162, 163) X(22, 164, 165, 166, 167) X(23, 168, 169, 170, 171) \
  X(24, 172, 173, 174, 175) X(25, 176, 177, 178, 179) X(26, 180, 181, 182, 183) X(27, 184, 185, 186, 187) X(28, 188, 189, 190, 191) X(29, 192, 193, 194, 195) \
  X(30, 196, 197, 198, 199) X(31, 200, 201, 202, 203) X(32, 204, 205, 206, 207) X(33, 208, 209, 210, 211) X(34, 212, 213, 214, 215) X(35, 216, 217, 218, 219) \
  X(36, 220, 221, 222, 223) X(37, 224, 225, 226, 227) X(38, 228, 229, 230, 231) X(39, 232, 233, 234, 235) X(40, 236, 237, 238, 239) X(41, 240, 241, 242, 243) \
  X(42, 244, 245, 246, 247) X(43, 248, 249, 250, 251) X(44, 252, 253, 254, 255) X(45, 64, 65, 66, 67) X(46, 68, 69, 70, 71) X(47, 72, 73, 74, 75)
__device__ __forceinline__ void w_dma(rsrc_t r, int voff, int vof2, int soff, int lds) {
  asm volatile("s_mov_b32 m0, %3\n\ts_nop 0\n\tbuffer_load_dwordx4 %0, %1, %2 offen lds\n\t"
               "s_add_u32 m0, m0, 3072\n\ts_nop 0\n\tbuffer_load_dwordx4 %0, %1, %2 offen offset:1024 lds\n\t"
               "s_add_u32 m0, m0, 3072\n\ts_nop 0\n\tbuffer_load_dwordx4 %4, %1, %2 offen offset:2048 lds"
               :: "v"(voff), "s"(r), "s"(soff), "s"(lds), "v"(vof2) : "memory", "scc");
}
template <int I>
__device__ __forceinline__ void w_ld(bf16x8& r, int vo, rsrc_t rs) {
#define X(n_, a_, b_, c_, d_) if constexpr (I == n_) asm volatile("buffer_load_dwordx4 %0, %1, %2, %3 offen offset:%4 sc1" : "={v[" #a_ ":" #d_ "]}"(r) : "v"(vo), "s"(rs), "s"(OFF_WQ + (I / 3) * 98304), "n"((I % 3) * 1024) : "memory");
  WREGS(X)
#undef X
}
template <int I, int O>
__device__ __forceinline__ void w_get(bf16x8& r, u32 a) {
#define X(n_, a_, b_, c_, d_) if constexpr (I == n_) asm volatile("ds_read_b64_tr_b16 v[" #a_ ":" #b_ "], %1 offset:%2\n\tds_read_b64_tr_b16 v[" #c_ ":" #d_ "], %1 offset:%3" : "=&{v[" #a_ ":" #d_ "]}"(r) : "v"(a), "n"(O), "n"(O + 2048) : "memory");
  WREGS(X)
#undef X
}
template <int I>
__device__ __forceinline__ void w_pin(const bf16x8& r) {
#define X(n_, a_, b_, c_, d_) if constexpr (I == n_) asm volatile("" :: "{v[" #a_ ":" #d_ "]}"(r));
  WREGS(X)
#undef X
}
template <int I>
__device__ __forceinline__ void w_sum(f32x4& d, const bf16x8& one, u32 idx, const bf16x8 (&wf)[48]) {
  w_pin<I>(wf[I]); w_pin<(I + 1) % 48>(wf[(I + 1) % 48]);
  asm volatile("v_smfmac_f32_16x16x64_bf16 %0, %1, v[%3:%3+7], %2" : "+v"(d) : "v"(one), "v"(idx), "n"(I < 45 ? 76 + 4 * I : 64 + 4 * (I - 45)));
  w_pin<I>(wf[I]); w_pin<(I + 1) % 48>(wf[(I + 1) % 48]);
}
struct GPre { u32 ap, l4, fr, fq, sp, wx, mo, oo, tg, ly; };
template <bool L1>
__device__ __forceinline__ void g_pre(GPre& P, int g, u32 tag) {
  const int lane = lane_id(), bid = BIDX;
  const int rb = RB_OF(bid), hb = HB_OF(bid);
  P.mo = (u32)KVO(L1, (rb + 1) & 31, hb * 4 + g);
  P.oo = (u32)((L1 ? OFF_O1 : OFF_O2) + MB_ + ((rb * 32 + hb * 2 + (g >> 1)) * 64 + 32 * (g & 1)) * 16);
  P.tg = L1 ? x0_mark(tag) : tag;
  P.ly = L1 ? 1 : 2;
  asm volatile("" : "+{s60}"(P.mo), "+{s61}"(P.oo), "+{s62}"(P.tg), "+{s63}"(P.ly));
  P.ap = (u32)(L_TA + lane * 8);
  P.l4 = (u32)(lane * 4);
  P.fr = (u32)(L_FS + g * 768 + (lane & 15) * 4);
  P.fq = (u32)(L_FS + g * 768 + (lane >> 4) * 16);
  P.sp = (u32)(L_SP + (lane & 31) * 16);
  P.wx = (u32)(L_WX + g * 3072 + lane * 16);
  asm volatile("" : "+{v0}"(P.ap), "+{v1}"(P.l4), "+{v2}"(P.fr), "+{v3}"(P.fq), "+{v4}"(P.sp), "+{v5}"(P.wx));
}
template <bool L1, class F>
__device__ __forceinline__ void g_blayer(char* smem, rsrc_t wrs, int g, u32 tag, bf16x8 (&wf)[48], GPre& P, F mid) {
  if (!L1) ST(1, 10);
  auto pin = [&](auto I_) __attribute__((always_inline)) { w_pin<decltype(I_)::value>(wf[decltype(I_)::value]); };
  static_for<0, 45>(pin);
#define G_OPS : "+{v0}"(P.ap), "+{v1}"(P.l4), "+{v2}"(P.fr), "+{v3}"(P.fq), "+{v4}"(P.sp), "+{v5}"(P.wx) \
  : "{s[56:59]}"(wrs), "{s60}"(P.mo), "{s61}"(P.oo), "{s62}"(P.tg), "{s63}"(P.ly), "{s64}"(L_QB + g * 3072), "{s65}"(L_FS + g * 768), "{s66}"(L_WX + g * 3072) \
  : "memory", "scc", "vcc", "v6", "v7", "v8", "v9", "v10", "v11", "v12", "v13", "v14", "v15", "v16", "v17", "v18", "v19", "v20", "v21", "v22", "v23", "v24", "v25", "v26", "v27", "v28", "v29", "v30", "v31", "v32", "v33", "v34", "v35", "v36", "v37", "v38", "v39", "v40", "v41", "v42", "v43", "v44", "v45", "v46", "v47", "v48", "v49", "v50", "v51", "v52", "v53", "v54", "v55", "v56", "v57", "v58", "v59", "v60", "v61", "v62", "v63", "v64", "v65", "v66", "v67", "v68", "v69", "v70", "v71", "v72", "v73", "v74", "v75", "s54", "s55", "s67", "s68", "s69", "s70", "s71", "s72", "s73", "s74", "s75", "s76", "s77"
  if constexpr (L1) asm volatile(".Lg_body:\n\t"
  "ds_read_b128 v[56:59], v5 offset:1024\n\tds_read_b128 v[40:43], v5 offset:2048\n\tds_read_b128 v[16:19], v5 offset:0\n\t"
  "s_waitcnt lgkmcnt(3)\n\ts_barrier\n\t" GST(11)
  "ds_read_b64_tr_b16 v[28:29], v0 offset:31232\n\tds_read_b64_tr_b16 v[30:31], v0 offset:32256\n\tds_read_b64_tr_b16 v[48:49], v0 offset:512\n\tds_read_b64_tr_b16 v[50:51], v0 offset:1536\n\t"
  "ds_read_b128 v[64:67], v4\n\tds_read_b128 v[72:75], v4 offset:768\n\tds_read_b64_tr_b16 v[36:37], v0 offset:2560\n\tds_read_b64_tr_b16 v[38:39], v0 offset:3584\n\t"
  "ds_read_b64_tr_b16 v[24:25], v0 offset:4608\n\tds_read_b64_tr_b16 v[26:27], v0 offset:5632\n\ts_mov_b32 s68, 0xaaaaaaaa\n\ts_mov_b32 s69, 0xaaaaaaaa\n\t"
  "s_mov_b32 s70, 0xf0000\n\ts_mov_b32 s71, 0xfff00ff\n\ts_mov_b32 s72, 0x3f0003\n\ts_mov_b32 s73, 0x3fff03ff\n\t"
  "s_mov_b32 s74, 0xfff0ffff\n\ts_mov_b32 s75, 0xf000ff00\n\ts_mov_b32 s76, 0xffc0fffc\n\ts_mov_b32 s77, 0xc000fc00\n\t"
  "s_mov_b32 s67, 0x0c0c0100\n\ts_mov_b32 s55, 0x0c0c0302\n\ts_waitcnt lgkmcnt(8)\n\t"
  "v_mfma_f32_16x16x32_bf16 v[12:15], v[56:59], v[28:31], 0\n\tds_read_b64_tr_b16 v[4:5], v0 offset:6656\n\t"
  "v_mfma_f32_16x16x32_bf16 v[56:59], v[28:31], v[40:43], 0\n\tds_read_b64_tr_b16 v[6:7], v0 offset:7680\n\t"
  "v_mfma_f32_16x16x32_bf16 v[68:71], v[16:19], v[28:31], 0\n\ts_waitcnt lgkmcnt(2)\n\t"
  "v_mfma_f32_16x16x32_bf16 v[12:15], v[80:83], v[48:51], v[12:15]\n\tds_read_b64_tr_b16 v[44:45], v0 offset:8704\n\tv_add_f32 v64, v64, v65\n\t"
  "v_mfma_f32_16x16x32_bf16 v[56:59], v[48:51], v[84:87], v[56:59]\n\tds_read_b64_tr_b16 v[46:47], v0 offset:9728\n\tv_add_f32 v66, v66, v67\n\t"
  "v_mfma_f32_16x16x32_bf16 v[68:71], v[76:79], v[48:51], v[68:71]\n\tv_add_f32 v72, v72, v73\n\tv_add_f32 v64, v64, v66\n\t"
  "v_mfma_f32_16x16x32_bf16 v[12:15], v[92:95], v[36:39], v[12:15]\n\tds_read_b64_tr_b16 v[40:41], v0 offset:10752\n\tv_add_f32 v74, v74, v75\n\t"
  "v_mfma_f32_16x16x32_bf16 v[56:59], v[36:39], v[96:99], v[56:59]\n\tds_read_b64_tr_b16 v[42:43], v0 offset:11776\n\tv_mul_f32 v65, 0x3b000000, v64\n\t"
  "v_mfma_f32_16x16x32_bf16 v[68:71], v[88:91], v[36:39], v[68:71]\n\tv_add_f32 v72, v72, v74\n\tv_mul_f32_e64 v66, -v65, v65\n\t"
  "v_mfma_f32_16x16x32_bf16 v[12:15], v[104:107], v[24:27], v[12:15]\n\tds_read_b64_tr_b16 v[8:9], v0 offset:12800\n\tv_fmac_f32 v66, 0x3b000000, v72\n\t"
  "v_mfma_f32_16x16x32_bf16 v[56:59], v[24:27], v[108:111], v[56:59]\n\tds_read_b64_tr_b16 v[10:11], v0 offset:13824\n\tv_max_f32 v66, 0, v66\n\t"
  "v_mfma_f32_16x16x32_bf16 v[68:71], v[100:103], v[24:27], v[68:71]\n\tv_add_f32 v66, 0x3727c5ac, v66\n\ts_waitcnt lgkmcnt(4)\n\t"
  "v_mfma_f32_16x16x32_bf16 v[12:15], v[116:119], v[4:7], v[12:15]\n\tds_read_b64_tr_b16 v[28:29], v0 offset:14848\n\tv_add_u32 v67, s65, v1\n\t"
  "v_mfma_f32_16x16x32_bf16 v[56:59], v[4:7], v[120:123], v[56:59]\n\tds_read_b64_tr_b16 v[30:31], v0 offset:15872\n\t"
  "v_mfma_f32_16x16x32_bf16 v[68:71], v[112:115], v[4:7], v[68:71]\n\tv_rsq_f32 v66, v66\n\t"
  "v_mfma_f32_16x16x32_bf16 v[12:15], v[128:131], v[44:47], v[12:15]\n\tds_read_b64_tr_b16 v[4:5], v0 offset:16896\n\tds_write2st64_b32 v67, v66, v65 offset1:1\n\t"
  "v_mfma_f32_16x16x32_bf16 v[56:59], v[44:47], v[132:135], v[56:59]\n\tds_read_b64_tr_b16 v[6:7], v0 offset:17920\n\tds_read_b128 v[24:27], v3 offset:576\n\t"
  "v_mfma_f32_16x16x32_bf16 v[68:71], v[124:127], v[44:47], v[68:71]\n\tds_read2_b32 v[44:45], v2 offset0:64 offset1:80\n\ts_waitcnt lgkmcnt(7)\n\t"
  "v_mfma_f32_16x16x32_bf16 v[12:15], v[140:143], v[40:43], v[12:15]\n\tds_read_b64_tr_b16 v[16:17], v0 offset:18944\n\tds_read_b128 v[36:39], v3 offset:256\n\t"
  "v_mfma_f32_16x16x32_bf16 v[56:59], v[40:43], v[144:147], v[56:59]\n\tds_read_b64_tr_b16 v[18:19], v0 offset:19968\n\tds_read_b128 v[32:35], v3 offset:512\n\t"
  "v_mfma_f32_16x16x32_bf16 v[68:71], v[136:139], v[40:43], v[68:71]\n\tds_read_b32 v47, v2 offset:640\n\t"
  "v_mfma_f32_16x16x32_bf16 v[12:15], v[152:155], v[8:11], v[12:15]\n\tds_read_b64_tr_b16 v[20:21], v0 offset:20992\n\t"
  "v_mfma_f32_16x16x32_bf16 v[56:59], v[8:11], v[156:159], v[56:59]\n\tds_read_b64_tr_b16 v[22:23], v0 offset:22016\n\t"
  "v_mfma_f32_16x16x32_bf16 v[68:71], v[148:151], v[8:11], v[68:71]\n\ts_waitcnt lgkmcnt(9)\n\t"
  "v_mfma_f32_16x16x32_bf16 v[12:15], v[164:167], v[28:31], v[12:15]\n\tds_read_b64_tr_b16 v[64:65], v0 offset:23040\n\ts_waitcnt lgkmcnt(8)\n\t"
  "v_mfma_f32_16x16x32_bf16 v[56:59], v[28:31], v[168:171], v[56:59]\n\tds_read_b64_tr_b16 v[66:67], v0 offset:24064\n\tv_mul_f32_e64 v52, -v44, v24\n\t"
  "v_mfma_f32_16x16x32_bf16 v[68:71], v[160:163], v[28:31], v[68:71]\n\tv_mul_f32_e64 v53, -v44, v25\n\tv_mul_f32_e64 v54, -v44, v26\n\t"
  "v_mfma_f32_16x16x32_bf16 v[12:15], v[176:179], v[4:7], v[12:15]\n\tds_read_b64_tr_b16 v[48:49], v0 offset:25088\n\tv_mul_f32_e64 v55, -v44, v27\n\t"
  "v_mfma_f32_16x16x32_bf16 v[56:59], v[4:7], v[180:183], v[56:59]\n\tds_read_b64_tr_b16 v[50:51], v0 offset:26112\n\ts_waitcnt lgkmcnt(5)\n\t"
  "v_mfma_f32_16x16x32_bf16 v[68:71], v[172:175], v[4:7], v[68:71]\n\tv_mul_f32_e64 v72, -v36, v47\n\tv_mul_f32_e64 v73, -v37, v47\n\t"
  "v_mfma_f32_16x16x32_bf16 v[12:15], v[188:191], v[16:19], v[12:15]\n\t"
  GST(20)
  "ds_read_b64_tr_b16 v[8:9], v0 offset:27136\n\tds_read2_b32 v[40:41], v2 offset0:0 offset1:16\n\t"
  "v_mfma_f32_16x16x32_bf16 v[56:59], v[16:19], v[192:195], v[56:59]\n\tds_read_b64_tr_b16 v[10:11], v0 offset:28160\n\tv_mul_f32_e64 v74, -v38, v47\n\t"
  "v_mfma_f32_16x16x32_bf16 v[68:71], v[184:187], v[16:19], v[68:71]\n\tv_mul_f32_e64 v75, -v39, v47\n\ts_waitcnt lgkmcnt(5)\n\t"
  "v_mfma_f32_16x16x32_bf16 v[12:15], v[200:203], v[20:23], v[12:15]\n\tds_read_b64_tr_b16 v[16:17], v0 offset:29184\n\tv_mul_f32_e64 v60, -v44, v32\n\t"
  "v_mfma_f32_16x16x32_bf16 v[56:59], v[20:23], v[204:207], v[56:59]\n\tds_read_b64_tr_b16 v[18:19], v0 offset:30208\n\tv_mul_f32_e64 v61, -v44, v33\n\t"
  "v_mfma_f32_16x16x32_bf16 v[68:71], v[196:199], v[20:23], v[68:71]\n\tv_mul_f32_e64 v62, -v44, v34\n\tv_mul_f32_e64 v63, -v44, v35\n\t"
  "v_mfma_f32_16x16x32_bf16 v[12:15], v[212:215], v[64:67], v[12:15]\n\tds_read_b64_tr_b16 v[36:37], v0 offset:0\n\t"
  "v_mfma_f32_16x16x32_bf16 v[56:59], v[64:67], v[216:219], v[56:59]\n\tds_read_b64_tr_b16 v[38:39], v0 offset:1024\n\t"
  "v_mfma_f32_16x16x32_bf16 v[68:71], v[208:211], v[64:67], v[68:71]\n\ts_waitcnt lgkmcnt(4)\n\t"
  "v_mfma_f32_16x16x32_bf16 v[12:15], v[224:227], v[48:51], v[12:15]\n\tds_read_b64_tr_b16 v[4:5], v0 offset:2048\n\t"
  "v_mfma_f32_16x16x32_bf16 v[56:59], v[48:51], v[228:231], v[56:59]\n\tds_read_b64_tr_b16 v[6:7], v0 offset:3072\n\tv_lshl_add_u32 v43, v1, 2, s60\n\t"
  "v_mfma_f32_16x16x32_bf16 v[68:71], v[220:223], v[48:51], v[68:71]\n\t"
  "v_mfma_f32_16x16x32_bf16 v[12:15], v[236:239], v[8:11], v[12:15]\n\tds_read_b64_tr_b16 v[20:21], v0 offset:4096\n\tds_read_b128 v[48:51], v3 offset:320\n\t"
  "v_mfma_f32_16x16x32_bf16 v[56:59], v[8:11], v[240:243], v[56:59]\n\tds_read_b64_tr_b16 v[22:23], v0 offset:5120\n\t"
  "v_mfma_f32_16x16x32_bf16 v[68:71], v[232:235], v[8:11], v[68:71]\n\tds_read_b128 v[8:11], v3 offset:64\n\ts_waitcnt lgkmcnt(6)\n\t"
  "v_mfma_f32_16x16x32_bf16 v[12:15], v[248:251], v[16:19], v[12:15]\n\tds_read_b64_tr_b16 v[28:29], v0 offset:6144\n\t"
  "v_mfma_f32_16x16x32_bf16 v[56:59], v[16:19], v[252:255], v[56:59]\n\tds_read_b64_tr_b16 v[30:31], v0 offset:7168\n\t"
  "v_mfma_f32_16x16x32_bf16 v[68:71], v[244:247], v[16:19], v[68:71]\n\tv_fma_f32 v12, -v45, v24, v12\n\tv_fma_f32 v13, -v45, v25, v13\n\t"
  "v_mfma_f32_16x16x32_bf16 v[52:55], v[80:83], v[36:39], v[52:55]\n\tds_read_b64_tr_b16 v[16:17], v0 offset:8192\n\tv_fma_f32 v14, -v45, v26, v14\n\t"
  "v_mfma_f32_16x16x32_bf16 v[72:75], v[36:39], v[84:87], v[72:75]\n\tds_read_b64_tr_b16 v[18:19], v0 offset:9216\n\tv_fma_f32 v15, -v45, v27, v15\n\t"
  "v_mfma_f32_16x16x32_bf16 v[60:63], v[76:79], v[36:39], v[60:63]\n\tv_mul_f32 v12, v12, v41\n\ts_waitcnt lgkmcnt(5)\n\t"
  "v_mfma_f32_16x16x32_bf16 v[52:55], v[92:95], v[4:7], v[52:55]\n\tds_read_b64_tr_b16 v[64:65], v0 offset:10240\n\tv_mul_f32 v13, v13, v41\n\t"
  "v_mfma_f32_16x16x32_bf16 v[72:75], v[4:7], v[96:99], v[72:75]\n\tds_read_b64_tr_b16 v[66:67], v0 offset:11264\n\tv_mul_f32 v14, v14, v41\n\t"
  "v_mfma_f32_16x16x32_bf16 v[60:63], v[88:91], v[4:7], v[60:63]\n\t"
  GST(17)
  "v_mul_f32 v15, v15, v41\n\tv_cvt_pk_bf16_f32 v12, v12, v13\n\t"
  "v_mfma_f32_16x16x32_bf16 v[52:55], v[104:107], v[20:23], v[52:55]\n\tds_read_b64_tr_b16 v[4:5], v0 offset:12288\n\tv_cvt_pk_bf16_f32 v14, v14, v15\n\t"
  "v_mfma_f32_16x16x32_bf16 v[72:75], v[20:23], v[108:111], v[72:75]\n\tds_read_b64_tr_b16 v[6:7], v0 offset:13312\n\tv_mov_b32 v13, s62\n\t"
  "v_mfma_f32_16x16x32_bf16 v[60:63], v[100:103], v[20:23], v[60:63]\n\tv_mov_b32 v15, s62\n\ts_waitcnt lgkmcnt(4)\n\t"
  "v_mfma_f32_16x16x32_bf16 v[52:55], v[116:119], v[28:31], v[52:55]\n\tds_read_b64_tr_b16 v[20:21], v0 offset:14336\n\tv_fma_f32 v56, -v48, v47, v56\n\t"
  "v_mfma_f32_16x16x32_bf16 v[72:75], v[28:31], v[120:123], v[72:75]\n\tds_read_b64_tr_b16 v[22:23], v0 offset:15360\n\tv_fma_f32 v57, -v49, v47, v57\n\t"
  "v_mfma_f32_16x16x32_bf16 v[60:63], v[112:115], v[28:31], v[60:63]\n\tv_fma_f32 v58, -v50, v47, v58\n\tv_fma_f32 v59, -v51, v47, v59\n\t"
  "v_mfma_f32_16x16x32_bf16 v[52:55], v[128:131], v[16:19], v[52:55]\n\tds_read_b64_tr_b16 v[36:37], v0 offset:16384\n\tv_mul_f32 v56, v56, v8\n\t"
  "v_mfma_f32_16x16x32_bf16 v[72:75], v[16:19], v[132:135], v[72:75]\n\tds_read_b64_tr_b16 v[38:39], v0 offset:17408\n\tv_mul_f32 v57, v57, v9\n\t"
  "v_mfma_f32_16x16x32_bf16 v[60:63], v[124:127], v[16:19], v[60:63]\n\tv_mul_f32 v58, v58, v10\n\ts_waitcnt lgkmcnt(4)\n\t"
  "v_mfma_f32_16x16x32_bf16 v[52:55], v[140:143], v[64:67], v[52:55]\n\tds_read_b64_tr_b16 v[16:17], v0 offset:18432\n\tv_mul_f32 v59, v59, v11\n\t"
  "v_mfma_f32_16x16x32_bf16 v[72:75], v[64:67], v[144:147], v[72:75]\n\tds_read_b64_tr_b16 v[18:19], v0 offset:19456\n\tv_cvt_pk_bf16_f32 v56, v56, v57\n\t"
  "v_mfma_f32_16x16x32_bf16 v[60:63], v[136:139], v[64:67], v[60:63]\n\tv_cvt_pk_bf16_f32 v58, v58, v59\n\tv_mov_b32 v57, s62\n\t"
  "v_mfma_f32_16x16x32_bf16 v[52:55], v[152:155], v[4:7], v[52:55]\n\tds_read_b64_tr_b16 v[8:9], v0 offset:20480\n\tv_mov_b32 v59, s62\n\t"
  "v_mfma_f32_16x16x32_bf16 v[72:75], v[4:7], v[156:159], v[72:75]\n\tds_read_b64_tr_b16 v[10:11], v0 offset:21504\n\tv_fma_f32 v68, -v45, v32, v68\n\t"
  "v_mfma_f32_16x16x32_bf16 v[60:63], v[148:151], v[4:7], v[60:63]\n\tv_add_u32 v46, s64, v1\n\ts_waitcnt lgkmcnt(4)\n\t"
  "v_mfma_f32_16x16x32_bf16 v[52:55], v[164:167], v[20:23], v[52:55]\n\tds_read_b64_tr_b16 v[48:49], v0 offset:22528\n\tv_fma_f32 v69, -v45, v33, v69\n\t"
  "v_mfma_f32_16x16x32_bf16 v[72:75], v[20:23], v[168:171], v[72:75]\n\tds_read_b64_tr_b16 v[50:51], v0 offset:23552\n\tv_fma_f32 v70, -v45, v34, v70\n\t"
  "v_mfma_f32_16x16x32_bf16 v[60:63], v[160:163], v[20:23], v[60:63]\n\tv_fma_f32 v71, -v45, v35, v71\n\tv_mul_f32 v68, v68, v41\n\t"
  "v_mfma_f32_16x16x32_bf16 v[52:55], v[176:179], v[36:39], v[52:55]\n\tds_read_b64_tr_b16 v[28:29], v0 offset:24576\n\tv_mul_f32 v69, v69, v41\n\t"
  "v_mfma_f32_16x16x32_bf16 v[72:75], v[36:39], v[180:183], v[72:75]\n\tds_read_b64_tr_b16 v[30:31], v0 offset:25600\n\tv_mul_f32 v70, v70, v41\n\t"
  "v_mfma_f32_16x16x32_bf16 v[60:63], v[172:175], v[36:39], v[60:63]\n\tv_mul_f32 v71, v71, v41\n\ts_waitcnt lgkmcnt(4)\n\t"
  "v_mfma_f32_16x16x32_bf16 v[52:55], v[188:191], v[16:19], v[52:55]\n\tds_read_b64_tr_b16 v[20:21], v0 offset:26624\n\tv_cvt_pk_bf16_f32 v68, v68, v69\n\t"
  "v_mfma_f32_16x16x32_bf16 v[72:75], v[16:19], v[192:195], v[72:75]\n\tds_read_b64_tr_b16 v[22:23], v0 offset:27648\n\tv_cvt_pk_bf16_f32 v70, v70, v71\n\t"
  "v_mfma_f32_16x16x32_bf16 v[60:63], v[184:187], v[16:19], v[60:63]\n\t"
  "v_mfma_f32_16x16x32_bf16 v[52:55], v[200:203], v[8:11], v[52:55]\n\t"
  GST(13)
  "ds_read_b64_tr_b16 v[36:37], v0 offset:28672\n\t"
  "v_mfma_f32_16x16x32_bf16 v[72:75], v[8:11], v[204:207], v[72:75]\n\ts_barrier\n\tds_read_b64_tr_b16 v[38:39], v0 offset:29696\n\t"
  "v_mfma_f32_16x16x32_bf16 v[60:63], v[196:199], v[8:11], v[60:63]\n\tbuffer_store_dwordx4 v[12:15], v43, s[56:59], 0 offen offset:0 sc1\n\ts_waitcnt lgkmcnt(4)\n\t"
  "v_mfma_f32_16x16x32_bf16 v[52:55], v[212:215], v[48:51], v[52:55]\n\tds_read_b64_tr_b16 v[64:65], v0 offset:30720\n\tbuffer_store_dwordx4 v[56:59], v43, s[56:59], 0 offen offset:1024 sc1\n\t"
  "v_mfma_f32_16x16x32_bf16 v[72:75], v[48:51], v[216:219], v[72:75]\n\tds_read_b64_tr_b16 v[66:67], v0 offset:31744\n\tv_lshl_add_u32 v4, v1, 2, s66\n\tv_add_u32 v0, 0x8000, v0\n\t"
  "v_mfma_f32_16x16x32_bf16 v[60:63], v[208:211], v[48:51], v[60:63]\n\tds_read_b128 v[42:45], v4 offset:1024\n\tds_read_b128 v[8:11], v4 offset:2048\n\t"
  "v_mfma_f32_16x16x32_bf16 v[52:55], v[224:227], v[28:31], v[52:55]\n\tds_read_b64_tr_b16 v[16:17], v0 offset:0\n\tds_read_b128 v[48:51], v4 offset:0\n\t"
  "v_mfma_f32_16x16x32_bf16 v[72:75], v[28:31], v[228:231], v[72:75]\n\tds_read_b64_tr_b16 v[18:19], v0 offset:1024\n\tv_min_u32 v41, 188, v1\n\t"
  "v_mfma_f32_16x16x32_bf16 v[60:63], v[220:223], v[28:31], v[60:63]\n\tds_read_b128 v[4:7], v3\n\ts_waitcnt lgkmcnt(8)\n\t"
  "v_mfma_f32_16x16x32_bf16 v[52:55], v[236:239], v[20:23], v[52:55]\n\tds_read_b64_tr_b16 v[28:29], v0 offset:2048\n\tv_lshlrev_b32 v41, 2, v41\n\t"
  "v_mfma_f32_16x16x32_bf16 v[72:75], v[20:23], v[240:243], v[72:75]\n\tds_read_b64_tr_b16 v[30:31], v0 offset:3072\n\tv_add_u32 v41, 0x22c00, v41\n\t"
  "v_mfma_f32_16x16x32_bf16 v[60:63], v[232:235], v[20:23], v[60:63]\n\t"
  "v_mfma_f32_16x16x32_bf16 v[52:55], v[248:251], v[36:39], v[52:55]\n\tds_read_b64_tr_b16 v[20:21], v0 offset:8192\n\t"
  "v_mfma_f32_16x16x32_bf16 v[72:75], v[36:39], v[252:255], v[72:75]\n\tds_read_b64_tr_b16 v[22:23], v0 offset:9216\n\t"
  "v_mfma_f32_16x16x32_bf16 v[60:63], v[244:247], v[36:39], v[60:63]\n\ts_waitcnt lgkmcnt(5)\n\t"
  "v_mfma_f32_16x16x32_bf16 v[52:55], v[42:45], v[64:67], v[52:55]\n\tds_read_b64_tr_b16 v[42:43], v0 offset:10240\n\t"
  "v_mfma_f32_16x16x32_bf16 v[72:75], v[64:67], v[8:11], v[72:75]\n\tds_read_b64_tr_b16 v[44:45], v0 offset:11264\n\t"
  "v_mfma_f32_16x16x32_bf16 v[60:63], v[48:51], v[64:67], v[60:63]\n\t"
  GST(12)
  "v_mul_f32 v52, v52, v40\n\tv_mul_f32 v53, v53, v40\n\t"
  "v_mfma_f32_16x16x32_bf16 v[8:11], v[80:83], v[16:19], 0\n\tds_read_b64_tr_b16 v[36:37], v0 offset:16384\n\tv_mul_f32 v54, v54, v40\n\t"
  "v_mfma_f32_16x16x32_bf16 v[48:51], v[16:19], v[84:87], 0\n\tds_read_b64_tr_b16 v[38:39], v0 offset:17408\n\tv_mul_f32 v55, v55, v40\n\t"
  "v_mfma_f32_16x16x32_bf16 v[64:67], v[76:79], v[16:19], 0\n\tv_cvt_pk_bf16_f32 v52, v52, v53\n\ts_waitcnt lgkmcnt(4)\n\t"
  "v_mfma_f32_16x16x32_bf16 v[8:11], v[92:95], v[28:31], v[8:11]\n\tds_read_b64_tr_b16 v[16:17], v0 offset:18432\n\tv_cvt_pk_bf16_f32 v54, v54, v55\n\t"
  "v_mfma_f32_16x16x32_bf16 v[48:51], v[28:31], v[96:99], v[48:51]\n\tds_read_b64_tr_b16 v[18:19], v0 offset:19456\n\tv_mul_f32 v60, v60, v40\n\t"
  "v_mfma_f32_16x16x32_bf16 v[64:67], v[88:91], v[28:31], v[64:67]\n\tv_mul_f32 v61, v61, v40\n\tv_mul_f32 v62, v62, v40\n\t"
  "v_mfma_f32_16x16x32_bf16 v[8:11], v[128:131], v[20:23], v[8:11]\n\tds_read_b64_tr_b16 v[28:29], v0 offset:24576\n\tds_write2st64_b32 v46, v12, v14 offset0:0 offset1:1\n\tds_write2st64_b32 v46, v52, v54 offset0:6 offset1:7\n\t"
  "v_mfma_f32_16x16x32_bf16 v[48:51], v[20:23], v[132:135], v[48:51]\n\tds_read_b64_tr_b16 v[30:31], v0 offset:25600\n\tds_write2st64_b32 v46, v56, v58 offset0:2 offset1:3\n\t"
  "v_mfma_f32_16x16x32_bf16 v[64:67], v[124:127], v[20:23], v[64:67]\n\tds_write2st64_b32 v46, v68, v70 offset0:4 offset1:5\n\tv_mul_f32 v63, v63, v40\n\tv_cvt_pk_bf16_f32 v60, v60, v61\n\t"
  "v_cvt_pk_bf16_f32 v62, v62, v63\n\ts_waitcnt lgkmcnt(8)\n\t"
  "v_mfma_f32_16x16x32_bf16 v[8:11], v[140:143], v[42:45], v[8:11]\n\tds_read_b64_tr_b16 v[12:13], v0 offset:26624\n\tds_write2st64_b32 v46, v60, v62 offset0:10 offset1:11\n\t"
  "v_mfma_f32_16x16x32_bf16 v[48:51], v[42:45], v[144:147], v[48:51]\n\tds_read_b64_tr_b16 v[14:15], v0 offset:27648\n\tv_mul_f32 v72, v72, v4\n\t"
  "v_mfma_f32_16x16x32_bf16 v[64:67], v[136:139], v[42:45], v[64:67]\n\tv_mul_f32 v73, v73, v5\n\tv_mul_f32 v74, v74, v6\n\tv_mul_f32 v75, v75, v7\n\t"
  "v_cvt_pk_bf16_f32 v72, v72, v73\n\tv_cvt_pk_bf16_f32 v74, v74, v75\n\t"
  "v_mfma_f32_16x16x32_bf16 v[8:11], v[176:179], v[36:39], v[8:11]\n\tds_write2st64_b32 v46, v72, v74 offset0:8 offset1:9\n\t"
  "v_mfma_f32_16x16x32_bf16 v[48:51], v[36:39], v[180:183], v[48:51]\n\t"
  "v_mfma_f32_16x16x32_bf16 v[64:67], v[172:175], v[36:39], v[64:67]\n\tv_and_b32 v44, 0x40, v1\n\ts_waitcnt lgkmcnt(4)\n\t"
  "v_mfma_f32_16x16x32_bf16 v[8:11], v[188:191], v[16:19], v[8:11]\n\tv_lshl_add_u32 v44, v44, 8, s61\n\tv_and_b32 v45, 0x3c, v1\n\t"
  "v_mfma_f32_16x16x32_bf16 v[48:51], v[16:19], v[192:195], v[48:51]\n\ts_waitcnt lgkmcnt(0)\n\ts_barrier\n\tds_read_b32 v62, v41 offset:5700\n\t"
  "ds_read_b128 v[72:75], v41\n\tds_read_b128 v[52:55], v41 offset:768\n\t"
  "v_mfma_f32_16x16x32_bf16 v[64:67], v[184:187], v[16:19], v[64:67]\n\tds_read_b64_tr_b16 v[40:41], v0 offset:4096\n\tds_read_b64_tr_b16 v[42:43], v0 offset:5120\n\t"
  "v_mfma_f32_16x16x32_bf16 v[8:11], v[224:227], v[28:31], v[8:11]\n\tds_read_b64_tr_b16 v[20:21], v0 offset:6144\n\tds_read_b64_tr_b16 v[22:23], v0 offset:7168\n\t"
  "v_mfma_f32_16x16x32_bf16 v[48:51], v[28:31], v[228:231], v[48:51]\n\tds_read_b64_tr_b16 v[36:37], v0 offset:12288\n\tv_lshl_add_u32 v44, v45, 2, v44\n\t"
  "v_mfma_f32_16x16x32_bf16 v[64:67], v[220:223], v[28:31], v[64:67]\n\tds_read_b64_tr_b16 v[38:39], v0 offset:13312\n\tv_and_b32 v45, 0x80, v1\n\t"
  "v_mfma_f32_16x16x32_bf16 v[8:11], v[236:239], v[12:15], v[8:11]\n\tds_read_b64_tr_b16 v[28:29], v0 offset:14336\n\tv_lshl_add_u32 v44, v45, 1, v44\n\t"
  "v_mfma_f32_16x16x32_bf16 v[48:51], v[12:15], v[240:243], v[48:51]\n\tds_read_b64_tr_b16 v[30:31], v0 offset:15360\n\t"
  "v_mfma_f32_16x16x32_bf16 v[64:67], v[232:235], v[12:15], v[64:67]\n\ts_waitcnt lgkmcnt(9)\n\ts_waitcnt lgkmcnt(6)\n\t"
  "v_mfma_f32_16x16x32_bf16 v[8:11], v[104:107], v[40:43], v[8:11]\n\tds_read_b64_tr_b16 v[68:69], v0 offset:20480\n\tv_readfirstlane_b32 s54, v62\n\t"
  "v_mfma_f32_16x16x32_bf16 v[48:51], v[40:43], v[108:111], v[48:51]\n\tds_read_b64_tr_b16 v[70:71], v0 offset:21504\n\tv_add_f32 v72, v72, v73\n\t"
  "v_mfma_f32_16x16x32_bf16 v[64:67], v[100:103], v[40:43], v[64:67]\n\ts_cmp_eq_u32 s54, s63\n\ts_cbranch_scc1 .Lg_redo\n.Lg_back:\n\ts_waitcnt lgkmcnt(5)\n\t"
  "v_mfma_f32_16x16x32_bf16 v[8:11], v[116:119], v[20:23], v[8:11]\n\tds_read_b64_tr_b16 v[56:57], v0 offset:22528\n\tv_add_f32 v74, v74, v75\n\t"
  "v_mfma_f32_16x16x32_bf16 v[48:51], v[20:23], v[120:123], v[48:51]\n\tds_read_b64_tr_b16 v[58:59], v0 offset:23552\n\tv_add_f32 v52, v52, v53\n\t"
  "v_mfma_f32_16x16x32_bf16 v[64:67], v[112:115], v[20:23], v[64:67]\n\tv_lshl_add_u32 v45, v1, 2, s66\n\ts_waitcnt lgkmcnt(4)\n\t"
  "v_mfma_f32_16x16x32_bf16 v[8:11], v[152:155], v[36:39], v[8:11]\n\tds_read_b64_tr_b16 v[16:17], v0 offset:30720\n\tv_add_f32 v72, v72, v74\n\t"
  "v_mfma_f32_16x16x32_bf16 v[48:51], v[36:39], v[156:159], v[48:51]\n\tds_read_b64_tr_b16 v[18:19], v0 offset:31744\n\tds_read_b128 v[20:23], v45 offset:1024\n\t"
  "v_mfma_f32_16x16x32_bf16 v[64:67], v[148:151], v[36:39], v[64:67]\n\tv_add_f32 v54, v54, v55\n\tv_mul_f32 v73, 0x3b000000, v72\n\t"
  "v_mfma_f32_16x16x32_bf16 v[8:11], v[164:167], v[28:31], v[8:11]\n\tds_read_b64_tr_b16 v[60:61], v0 offset:28672\n\tv_add_f32 v52, v52, v54\n\t"
  "v_mfma_f32_16x16x32_bf16 v[48:51], v[28:31], v[168:171], v[48:51]\n\tds_read_b64_tr_b16 v[62:63], v0 offset:29696\n\tv_mul_f32_e64 v74, -v73, v73\n\tv_fmac_f32 v74, 0x3b000000, v52\n\t"
  "v_max_f32 v74, 0, v74\n\tv_add_f32 v74, 0x3727c5ac, v74\n\t"
  "v_mfma_f32_16x16x32_bf16 v[64:67], v[160:163], v[28:31], v[64:67]\n\tv_rsq_f32 v74, v74\n\tv_add_u32 v75, s65, v1\n\tds_write2st64_b32 v75, v74, v73 offset1:1\n\t"
  "ds_read_b128 v[4:7], v45 offset:2048\n\ts_waitcnt lgkmcnt(6)\n\t"
  "v_mfma_f32_16x16x32_bf16 v[8:11], v[200:203], v[68:71], v[8:11]\n\tds_read_b64_tr_b16 v[12:13], v0 offset:49152\n\tds_read2_b32 v[72:73], v2 offset0:64 offset1:80\n\tds_read_b128 v[28:31], v45 offset:0\n\t"
  "v_mfma_f32_16x16x32_bf16 v[48:51], v[68:71], v[204:207], v[48:51]\n\tds_read_b64_tr_b16 v[14:15], v0 offset:49664\n\t"
  "v_mfma_f32_16x16x32_bf16 v[64:67], v[196:199], v[68:71], v[64:67]\n\tds_read_b64_tr_b16 v[52:53], v0 offset:50176\n\t"
  "v_mfma_f32_16x16x32_bf16 v[8:11], v[212:215], v[56:59], v[8:11]\n\tds_read_b64_tr_b16 v[54:55], v0 offset:50688\n\t"
  "v_mfma_f32_16x16x32_bf16 v[48:51], v[56:59], v[216:219], v[48:51]\n\tds_read_b64_tr_b16 v[68:69], v0 offset:51200\n\t"
  "v_mfma_f32_16x16x32_bf16 v[64:67], v[208:211], v[56:59], v[64:67]\n\tds_read_b64_tr_b16 v[70:71], v0 offset:51712\n\ts_waitcnt lgkmcnt(10)\n\t"
  "v_mfma_f32_16x16x32_bf16 v[8:11], v[20:23], v[16:19], v[8:11]\n\tds_read_b64_tr_b16 v[40:41], v0 offset:52224\n\ts_waitcnt lgkmcnt(9)\n\t"
  "v_mfma_f32_16x16x32_bf16 v[48:51], v[16:19], v[4:7], v[48:51]\n\tds_read_b64_tr_b16 v[42:43], v0 offset:52736\n\ts_waitcnt lgkmcnt(7)\n\t"
  "v_mfma_f32_16x16x32_bf16 v[64:67], v[28:31], v[16:19], v[64:67]\n\tds_read_b64_tr_b16 v[36:37], v0 offset:53248\n\tds_read_b128 v[16:19], v3 offset:256\n\t"
  "v_mfma_f32_16x16x32_bf16 v[8:11], v[248:251], v[60:63], v[8:11]\n\tds_read_b64_tr_b16 v[38:39], v0 offset:53760\n\t"
  "v_mfma_f32_16x16x32_bf16 v[48:51], v[60:63], v[252:255], v[48:51]\n\tds_read_b64_tr_b16 v[56:57], v0 offset:54272\n\t"
  "v_mfma_f32_16x16x32_bf16 v[64:67], v[244:247], v[60:63], v[64:67]\n\tds_read_b64_tr_b16 v[58:59], v0 offset:54784\n\ts_waitcnt lgkmcnt(8)\n\t"
  "v_mfma_f32_16x16x32_bf16 v[60:63], v[80:83], v[12:15], 0\n\t"
  GST(18)
  "ds_read_b64_tr_b16 v[28:29], v0 offset:55296\n\tv_fma_f32 v8, -v72, v24, v8\n\t"
  "v_mfma_f32_16x16x32_bf16 v[20:23], v[12:15], v[84:87], 0\n\tds_read_b64_tr_b16 v[30:31], v0 offset:55808\n\tv_fma_f32 v9, -v72, v25, v9\n\t"
  "v_mfma_f32_16x16x32_bf16 v[60:63], v[92:95], v[52:55], v[60:63]\n\tds_read_b64_tr_b16 v[4:5], v0 offset:56320\n\tv_fma_f32 v10, -v72, v26, v10\n\t"
  "v_mfma_f32_16x16x32_bf16 v[20:23], v[52:55], v[96:99], v[20:23]\n\tds_read_b64_tr_b16 v[6:7], v0 offset:56832\n\ts_waitcnt lgkmcnt(7)\n\t"
  "v_mfma_f32_16x16x32_bf16 v[60:63], v[104:107], v[68:71], v[60:63]\n\tds_read_b64_tr_b16 v[52:53], v0 offset:57344\n\tv_fma_f32 v11, -v72, v27, v11\n\t"
  "v_mfma_f32_16x16x32_bf16 v[20:23], v[68:71], v[108:111], v[20:23]\n\tds_read_b64_tr_b16 v[54:55], v0 offset:57856\n\tv_cvt_pk_bf16_f32 v74, v8, v9\n\t"
  "v_mfma_f32_16x16x32_bf16 v[60:63], v[116:119], v[40:43], v[60:63]\n\tds_read_b64_tr_b16 v[8:9], v0 offset:58368\n\tv_cvt_pk_bf16_f32 v75, v10, v11\n\t"
  "v_mfma_f32_16x16x32_bf16 v[20:23], v[40:43], v[120:123], v[20:23]\n\tds_read_b64_tr_b16 v[10:11], v0 offset:58880\n\ts_waitcnt lgkmcnt(7)\n\t"
  "v_mfma_f32_16x16x32_bf16 v[60:63], v[128:131], v[36:39], v[60:63]\n\tds_read_b64_tr_b16 v[68:69], v0 offset:59392\n\tv_fma_f32 v64, -v72, v32, v64\n\t"
  "v_mfma_f32_16x16x32_bf16 v[20:23], v[36:39], v[132:135], v[20:23]\n\tds_read_b64_tr_b16 v[70:71], v0 offset:59904\n\tv_fma_f32 v65, -v72, v33, v65\n\t"
  "v_mfma_f32_16x16x32_bf16 v[60:63], v[140:143], v[56:59], v[60:63]\n\tds_read_b64_tr_b16 v[40:41], v0 offset:60416\n\tv_fma_f32 v66, -v72, v34, v66\n\t"
  "v_mfma_f32_16x16x32_bf16 v[20:23], v[56:59], v[144:147], v[20:23]\n\tds_read_b64_tr_b16 v[42:43], v0 offset:60928\n\ts_waitcnt lgkmcnt(7)\n\t"
  "v_mfma_f32_16x16x32_bf16 v[60:63], v[152:155], v[28:31], v[60:63]\n\tds_read_b64_tr_b16 v[36:37], v0 offset:61440\n\tv_fma_f32 v67, -v72, v35, v67\n\t"
  "v_mfma_f32_16x16x32_bf16 v[20:23], v[28:31], v[156:159], v[20:23]\n\tds_read_b64_tr_b16 v[38:39], v0 offset:61952\n\tv_cvt_pk_bf16_f32 v28, v64, v65\n\t"
  "v_mfma_f32_16x16x32_bf16 v[60:63], v[164:167], v[4:7], v[60:63]\n\tds_read_b64_tr_b16 v[64:65], v0 offset:62464\n\tv_cvt_pk_bf16_f32 v29, v66, v67\n\t"
  "v_mfma_f32_16x16x32_bf16 v[20:23], v[4:7], v[168:171], v[20:23]\n\tds_read_b64_tr_b16 v[66:67], v0 offset:62976\n\ts_waitcnt lgkmcnt(7)\n\t"
  "v_mfma_f32_16x16x32_bf16 v[60:63], v[176:179], v[52:55], v[60:63]\n\tds_read_b64_tr_b16 v[56:57], v0 offset:63488\n\tv_fma_f32 v48, -v16, v47, v48\n\t"
  "v_mfma_f32_16x16x32_bf16 v[20:23], v[52:55], v[180:183], v[20:23]\n\tds_read_b64_tr_b16 v[58:59], v0 offset:64000\n\tv_fma_f32 v49, -v17, v47, v49\n\t"
  "v_mfma_f32_16x16x32_bf16 v[60:63], v[188:191], v[8:11], v[60:63]\n\tds_read_b64_tr_b16 v[52:53], v0 offset:64512\n\tv_lshl_add_u32 v31, v1, 2, s66\n\t"
  "v_mfma_f32_16x16x32_bf16 v[20:23], v[8:11], v[192:195], v[20:23]\n\tds_read_b64_tr_b16 v[54:55], v0 offset:65024\n\ts_waitcnt lgkmcnt(7)\n\t"
  "v_mfma_f32_16x16x32_bf16 v[60:63], v[200:203], v[68:71], v[60:63]\n\tds_read_b64_tr_b16 v[8:9], v0 offset:31232\n\tv_fma_f32 v50, -v18, v47, v50\n\t"
  "v_mfma_f32_16x16x32_bf16 v[20:23], v[68:71], v[204:207], v[20:23]\n\tds_read_b64_tr_b16 v[10:11], v0 offset:32256\n\tv_fma_f32 v51, -v19, v47, v51\n\t"
  "v_mfma_f32_16x16x32_bf16 v[60:63], v[212:215], v[40:43], v[60:63]\n\tv_cvt_pk_bf16_f32 v70, v48, v49\n\tv_cvt_pk_bf16_f32 v71, v50, v51\n\tds_read_b128 v[48:51], v31 offset:1024\n\t"
  "v_mfma_f32_16x16x32_bf16 v[20:23], v[40:43], v[216:219], v[20:23]\n\tds_read_b64_tr_b16 v[12:13], v0 offset:512\n\tds_read_b128 v[16:19], v31 offset:2048\n\ts_waitcnt lgkmcnt(8)\n\t"
  "v_mfma_f32_16x16x32_bf16 v[60:63], v[224:227], v[36:39], v[60:63]\n\tds_read_b64_tr_b16 v[14:15], v0 offset:1536\n\tds_read_b32 v68, v2 offset:384\n\tv_mul_f32_e64 v4, -v73, v24\n\t"
  "v_mul_f32_e64 v5, -v73, v25\n\tv_mul_f32_e64 v6, -v73, v26\n\t"
  "v_mfma_f32_16x16x32_bf16 v[20:23], v[36:39], v[228:231], v[20:23]\n\tv_mul_f32_e64 v7, -v73, v27\n\tds_read_b128 v[40:43], v31 offset:0\n\t"
  "v_mfma_f32_16x16x32_bf16 v[60:63], v[236:239], v[64:67], v[60:63]\n\tds_read_b64_tr_b16 v[36:37], v0 offset:2560\n\tv_mul_f32_e64 v30, -v73, v32\n\t"
  "v_mfma_f32_16x16x32_bf16 v[20:23], v[64:67], v[240:243], v[20:23]\n\tds_read_b64_tr_b16 v[38:39], v0 offset:3584\n\ts_waitcnt lgkmcnt(8)\n\t"
  "v_mfma_f32_16x16x32_bf16 v[60:63], v[248:251], v[56:59], v[60:63]\n\tv_mul_f32_e64 v31, -v73, v33\n\tv_mul_f32_e64 v32, -v73, v34\n\tv_mul_f32_e64 v33, -v73, v35\n\t"
  "v_mfma_f32_16x16x32_bf16 v[20:23], v[56:59], v[252:255], v[20:23]\n\tds_read_b64_tr_b16 v[64:65], v0 offset:4608\n\ts_waitcnt lgkmcnt(6)\n\t"
  "v_mfma_f32_16x16x32_bf16 v[60:63], v[48:51], v[52:55], v[60:63]\n\tds_read_b64_tr_b16 v[66:67], v0 offset:5632\n\tds_read2_b32 v[34:35], v2 offset0:0 offset1:16\n\t"
  "v_mfma_f32_16x16x32_bf16 v[20:23], v[52:55], v[16:19], v[20:23]\n\t"
  "v_mfma_f32_16x16x32_bf16 v[4:7], v[48:51], v[8:11], v[4:7]\n\t"
  GST(19)
  "ds_read_b64_tr_b16 v[56:57], v0 offset:6656\n\tv_and_or_b32 v46, v1, 4, v3\n\tds_read2_b32 v[72:73], v46 offset0:32 offset1:34\n\tds_read2_b32 v[52:53], v46 offset0:0 offset1:2\n\t"
  "v_mov_b32 v55, 0xff800000\n\ts_waitcnt lgkmcnt(7)\n\t"
  "v_mfma_f32_16x16x32_bf16 v[30:33], v[40:43], v[8:11], v[30:33]\n\tds_read_b64_tr_b16 v[58:59], v0 offset:7680\n\tv_cndmask_b32_e64 v54, v55, 0, s[70:71]\n\tv_cndmask_b32_e64 v45, v55, 0, s[72:73]\n\t"
  "v_cndmask_b32_e64 v2, v55, 0, s[74:75]\n\tv_cndmask_b32_e64 v55, v55, 0, s[76:77]\n\t"
  "v_mfma_f32_16x16x32_bf16 v[40:43], v[8:11], v[16:19], 0\n\tv_fma_f32 v60, -v68, v24, v60\n\tv_fma_f32 v61, -v68, v25, v61\n\t"
  "v_mfma_f32_16x16x32_bf16 v[4:7], v[80:83], v[12:15], v[4:7]\n\tds_read_b64_tr_b16 v[8:9], v0 offset:8704\n\tv_fma_f32 v62, -v68, v26, v62\n\t"
  "v_mfma_f32_16x16x32_bf16 v[30:33], v[76:79], v[12:15], v[30:33]\n\tds_read_b64_tr_b16 v[10:11], v0 offset:9728\n\tv_mfma_f32_16x16x16_bf16 v[48:51], v[74:75], v[28:29], 0\n\tv_fma_f32 v63, -v68, v27, v63\n\t"
  "v_cvt_pk_bf16_f32 v60, v60, v61\n\t"
  "v_mfma_f32_16x16x32_bf16 v[40:43], v[12:15], v[84:87], v[40:43]\n\tv_cvt_pk_bf16_f32 v61, v62, v63\n\ts_waitcnt lgkmcnt(3)\n\t"
  "v_mfma_f32_16x16x32_bf16 v[4:7], v[92:95], v[36:39], v[4:7]\n\tds_read_b64_tr_b16 v[12:13], v0 offset:10752\n\tv_mfma_f32_16x16x16_bf16 v[24:27], v[60:61], v[28:29], 0\n\tv_cndmask_b32_e64 v29, v48, v49, s[68:69]\n\t"
  "v_fma_f32 v29, v29, v52, v2\n\t"
  "v_mfma_f32_16x16x32_bf16 v[30:33], v[88:91], v[36:39], v[30:33]\n\tds_read_b64_tr_b16 v[14:15], v0 offset:11776\n\tv_cndmask_b32_e64 v28, v50, v51, s[68:69]\n\t"
  "v_mfma_f32_16x16x32_bf16 v[40:43], v[36:39], v[96:99], v[40:43]\n\tds_read_b128 v[16:19], v3 offset:384\n\tv_fma_f32 v28, v28, v53, v55\n\t"
  "v_mfma_f32_16x16x32_bf16 v[4:7], v[104:107], v[64:67], v[4:7]\n\tds_read_b64_tr_b16 v[36:37], v0 offset:12800\n\tv_max_f32 v60, v29, v28\n\t"
  "v_mfma_f32_16x16x32_bf16 v[30:33], v[100:103], v[64:67], v[30:33]\n\tds_read_b64_tr_b16 v[38:39], v0 offset:13824\n\tv_cndmask_b32_e64 v50, v24, v25, s[68:69]\n\t"
  "v_mfma_f32_16x16x32_bf16 v[40:43], v[64:67], v[108:111], v[40:43]\n\tv_fma_f32 v50, v50, v72, v54\n\ts_waitcnt lgkmcnt(5)\n\t"
  "v_mfma_f32_16x16x32_bf16 v[4:7], v[116:119], v[56:59], v[4:7]\n\tds_read_b64_tr_b16 v[64:65], v0 offset:14848\n\tv_cndmask_b32_e64 v48, v26, v27, s[68:69]\n\t"
  "v_mfma_f32_16x16x32_bf16 v[30:33], v[112:115], v[56:59], v[30:33]\n\tds_read_b64_tr_b16 v[66:67], v0 offset:15872\n\tv_fma_f32 v48, v48, v73, v45\n\t"
  "v_mfma_f32_16x16x32_bf16 v[40:43], v[56:59], v[120:123], v[40:43]\n\tv_lshlrev_b32 v51, 2, v1\n\tv_alignbit_b32 v49, s67, s67, v51\n\t"
  "v_mfma_f32_16x16x32_bf16 v[4:7], v[128:131], v[8:11], v[4:7]\n\tds_read_b64_tr_b16 v[24:25], v0 offset:16896\n\tv_alignbit_b32 v51, s55, s55, v51\n\t"
  "v_mfma_f32_16x16x32_bf16 v[30:33], v[124:127], v[8:11], v[30:33]\n\tds_read_b64_tr_b16 v[26:27], v0 offset:17920\n\tv_mov_b32 v58, 0x3f803f80\n\t"
  "v_mfma_f32_16x16x32_bf16 v[40:43], v[8:11], v[132:135], v[40:43]\n\tv_mov_b32 v59, 0x3f803f80\n\ts_waitcnt lgkmcnt(4)\n\t"
  "v_mfma_f32_16x16x32_bf16 v[4:7], v[140:143], v[12:15], v[4:7]\n\tds_read_b64_tr_b16 v[8:9], v0 offset:18944\n\tv_mul_f32 v34, 0x3eb8aa3b, v34\n\t"
  "v_mfma_f32_16x16x32_bf16 v[30:33], v[136:139], v[12:15], v[30:33]\n\tds_read_b64_tr_b16 v[10:11], v0 offset:19968\n\tv_max3_f32 v61, v60, v50, v48\n\t"
  "v_mfma_f32_16x16x32_bf16 v[40:43], v[12:15], v[144:147], v[40:43]\n\tv_mul_f32 v35, 0x3eb8aa3b, v35\n\tv_max3_f32 v60, v60, v50, v48\n\t"
  "v_mfma_f32_16x16x32_bf16 v[4:7], v[152:155], v[36:39], v[4:7]\n\tds_read_b64_tr_b16 v[12:13], v0 offset:20992\n\tv_fma_f32 v20, -v16, v47, v20\n\t"
  "v_mfma_f32_16x16x32_bf16 v[30:33], v[148:151], v[36:39], v[30:33]\n\tds_read_b64_tr_b16 v[14:15], v0 offset:22016\n\tv_fma_f32 v21, -v17, v47, v21\n\t"
  "v_mfma_f32_16x16x32_bf16 v[40:43], v[36:39], v[156:159], v[40:43]\n\tv_fma_f32 v22, -v18, v47, v22\n\ts_waitcnt lgkmcnt(4)\n\t"
  "v_mfma_f32_16x16x32_bf16 v[4:7], v[164:167], v[64:67], v[4:7]\n\tds_read_b64_tr_b16 v[16:17], v0 offset:23040\n\tv_fma_f32 v23, -v19, v47, v23\n\t"
  "v_mfma_f32_16x16x32_bf16 v[30:33], v[160:163], v[64:67], v[30:33]\n\tds_read_b64_tr_b16 v[18:19], v0 offset:24064\n\tv_cvt_pk_bf16_f32 v68, v20, v21\n\t"
  "v_mfma_f32_16x16x32_bf16 v[40:43], v[64:67], v[168:171], v[40:43]\n\tv_permlane16_swap_b32 v60, v61\n\t"
  "v_mfma_f32_16x16x32_bf16 v[4:7], v[176:179], v[24:27], v[4:7]\n\tds_read_b64_tr_b16 v[36:37], v0 offset:25088\n\tv_max_f32 v62, v60, v61\n\t"
  "v_mfma_f32_16x16x32_bf16 v[30:33], v[172:175], v[24:27], v[30:33]\n\tds_read_b64_tr_b16 v[38:39], v0 offset:26112\n\tv_max_f32 v63, v60, v61\n\t"
  "v_mfma_f32_16x16x32_bf16 v[40:43], v[24:27], v[180:183], v[40:43]\n\tds_read_b64_tr_b16 v[24:25], v0 offset:27136\n\ts_waitcnt lgkmcnt(5)\n\t"
  "v_mfma_f32_16x16x32_bf16 v[4:7], v[188:191], v[8:11], v[4:7]\n\tds_read_b64_tr_b16 v[26:27], v0 offset:28160\n\tv_cvt_pk_bf16_f32 v69, v22, v23\n\t"
  "v_mfma_f32_16x16x32_bf16 v[30:33], v[184:187], v[8:11], v[30:33]\n\tds_read_b64_tr_b16 v[20:21], v0 offset:29184\n\tds_read2_b32 v[56:57], v46 offset0:16 offset1:18\n\t"
  "v_mfma_f32_16x16x32_bf16 v[40:43], v[8:11], v[192:195], v[40:43]\n\tds_read_b64_tr_b16 v[22:23], v0 offset:30208\n\tv_permlane32_swap_b32 v62, v63\n\t"
  "v_mfma_f32_16x16x32_bf16 v[4:7], v[200:203], v[12:15], v[4:7]\n\tv_max_f32 v60, v62, v63\n\tv_mul_f32_e64 v60, -v60, v34\n\t"
  "v_mfma_f32_16x16x32_bf16 v[30:33], v[196:199], v[12:15], v[30:33]\n\tv_fma_f32 v50, v50, v34, v60\n\tv_fma_f32 v48, v48, v34, v60\n\t"
  "v_mfma_f32_16x16x32_bf16 v[40:43], v[12:15], v[204:207], v[40:43]\n\tv_fma_f32 v29, v29, v34, v60\n\ts_waitcnt lgkmcnt(5)\n\t"
  "v_mfma_f32_16x16x32_bf16 v[4:7], v[212:215], v[16:19], v[4:7]\n\tv_fma_f32 v28, v28, v34, v60\n\tds_read_b128 v[64:67], v3 offset:320\n\t"
  "v_mfma_f32_16x16x32_bf16 v[30:33], v[208:211], v[16:19], v[30:33]\n\tv_exp_f32 v50, v50\n\t"
  "v_mfma_f32_16x16x32_bf16 v[40:43], v[16:19], v[216:219], v[40:43]\n\tv_exp_f32 v48, v48\n\t"
  "v_mfma_f32_16x16x32_bf16 v[4:7], v[224:227], v[36:39], v[4:7]\n\tv_exp_f32 v29, v29\n\t"
  "v_mfma_f32_16x16x32_bf16 v[30:33], v[220:223], v[36:39], v[30:33]\n\tv_cvt_pk_bf16_f32 v60, v50, v48\n\ts_waitcnt lgkmcnt(1)\n\t"
  "v_mfma_f32_16x16x32_bf16 v[4:7], v[236:239], v[24:27], v[4:7]\n\tv_exp_f32 v28, v28\n\t"
  "v_mfma_f32_16x16x32_bf16 v[30:33], v[232:235], v[24:27], v[30:33]\n\tv_cvt_pk_bf16_f32 v61, v29, v28\n\tv_mul_f32 v50, v50, v72\n\t"
  "v_mfma_f32_16x16x32_bf16 v[4:7], v[248:251], v[20:23], v[4:7]\n\tv_mul_f32 v48, v48, v73\n\tv_mfma_f32_16x16x16_bf16 v[8:11], v[58:59], v[60:61], 0\n\tv_mul_f32 v29, v29, v52\n\t"
  "v_mul_f32 v28, v28, v53\n\t"
  "v_mfma_f32_16x16x32_bf16 v[30:33], v[244:247], v[20:23], v[30:33]\n\tv_cvt_pk_bf16_f32 v4, v4, v5\n\tv_cvt_pk_bf16_f32 v5, v6, v7\n\t"
  "v_mfma_f32_16x16x32_bf16 v[40:43], v[36:39], v[228:231], v[40:43]\n\tv_cvt_pk_bf16_f32 v50, v50, v48\n\tv_cvt_pk_bf16_f32 v29, v29, v28\n\t"
  "v_mfma_f32_16x16x32_bf16 v[40:43], v[24:27], v[240:243], v[40:43]\n\tv_cvt_pk_bf16_f32 v30, v30, v31\n\tv_cvt_pk_bf16_f32 v31, v32, v33\n\t"
  "v_mfma_f32_16x16x32_bf16 v[40:43], v[20:23], v[252:255], v[40:43]\n\t"
  GST(14)
  "v_perm_b32 v32, v50, v50, v49\n\tv_mfma_f32_16x16x16_bf16 v[12:15], v[4:5], v[30:31], 0\n\tv_mfma_f32_16x16x16_bf16 v[4:7], v[74:75], v[30:31], 0\n\ts_waitcnt lgkmcnt(0)\n\t"
  "v_fma_f32 v40, -v64, v47, v40\n\tv_fma_f32 v41, -v65, v47, v41\n\tv_fma_f32 v42, -v66, v47, v42\n\tv_cndmask_b32_e64 v28, v12, v13, s[68:69]\n\t"
  "v_fma_f32 v28, v28, v56, v2\n\tv_cndmask_b32_e64 v31, v14, v15, s[68:69]\n\tv_fma_f32 v31, v31, v57, v55\n\tv_max_f32 v64, v28, v31\n\t"
  "v_cndmask_b32_e64 v46, v4, v5, s[68:69]\n\tv_fma_f32 v46, v46, v52, v54\n\tv_cndmask_b32_e64 v34, v6, v7, s[68:69]\n\tv_fma_f32 v34, v34, v53, v45\n\t"
  "v_max3_f32 v65, v64, v46, v34\n\tv_max3_f32 v64, v64, v46, v34\n\tv_fma_f32 v43, -v67, v47, v43\n\tv_cvt_pk_bf16_f32 v72, v40, v41\n\t"
  "v_permlane16_swap_b32 v64, v65\n\tv_max_f32 v66, v64, v65\n\tv_max_f32 v67, v64, v65\n\tv_cvt_pk_bf16_f32 v73, v42, v43\n\t"
  "v_perm_b32 v33, v50, v50, v51\n\tv_permlane32_swap_b32 v66, v67\n\tv_max_f32 v64, v66, v67\n\tv_mul_f32_e64 v64, -v64, v35\n\t"
  "v_fma_f32 v46, v46, v35, v64\n\tv_fma_f32 v34, v34, v35, v64\n\tv_fma_f32 v28, v28, v35, v64\n\tv_fma_f32 v31, v31, v35, v64\n\t"
  "v_exp_f32 v46, v46\n\tv_exp_f32 v34, v34\n\tv_exp_f32 v28, v28\n\tv_exp_f32 v31, v31\n\t"
  "v_cvt_pk_bf16_f32 v64, v46, v34\n\tv_cvt_pk_bf16_f32 v65, v28, v31\n\tv_mul_f32 v46, v46, v52\n\tv_mul_f32 v34, v34, v53\n\t"
  "v_mfma_f32_16x16x16_bf16 v[40:43], v[58:59], v[64:65], 0\n\tv_mul_f32 v28, v28, v56\n\tv_mul_f32 v31, v31, v57\n\tv_cvt_pk_bf16_f32 v46, v46, v34\n\t"
  "v_cvt_pk_bf16_f32 v28, v28, v31\n\tv_perm_b32 v20, v46, v46, v49\n\tv_perm_b32 v34, v29, v29, v49\n\tv_perm_b32 v21, v46, v46, v51\n\t"
  "v_perm_b32 v35, v29, v29, v51\n\tv_perm_b32 v22, v28, v28, v49\n\tv_perm_b32 v23, v28, v28, v51\n\t"
  "v_mfma_f32_16x16x32_bf16 v[4:7], v[68:71], v[32:35], 0\n\tv_rcp_f32 v8, v8\n\t"
  "v_mfma_f32_16x16x32_bf16 v[24:27], v[70:73], v[20:23], 0\n\tv_rcp_f32 v40, v40\n\ts_nop 1\n\tv_mul_f32 v4, v4, v8\n\t"
  "v_mul_f32 v5, v5, v8\n\tv_mul_f32 v6, v6, v8\n\tv_mul_f32 v7, v7, v8\n\tv_cvt_pk_bf16_f32 v28, v4, v5\n\t"
  "v_mul_f32 v24, v24, v40\n\tv_mul_f32 v25, v25, v40\n\tv_mul_f32 v26, v26, v40\n\tv_mul_f32 v27, v27, v40\n\t"
  "v_cvt_pk_bf16_f32 v30, v24, v25\n\tv_cvt_pk_bf16_f32 v31, v26, v27\n\tv_cvt_pk_bf16_f32 v29, v6, v7\n\tv_permlane16_swap_b32 v28, v30\n\t"
  "s_nop 0\n\tv_permlane16_swap_b32 v29, v31\n\tbuffer_store_dwordx4 v[28:31], v44, s[56:59], 0 offen sc1\n\t"
  "s_cmp_eq_u32 s63, 2\n\ts_cbranch_scc1 .Lg_ret\n\ts_nop 0"
  G_OPS);
  else asm volatile("s_branch .Lg_body\n.Lg_ret:\n\ts_nop 0" G_OPS);
  static_for<0, 45>(pin);
  ST(!L1, 16);
  if (L1) {
    KSLP(7, SLP_OB1);
    cnt_add(smem, C_OB, 0);
    mid();
    cnt_wait(smem, C_X2, 4);
    return;
  }
  u32 t16 = KADD(8, now_(), DB2);
  cnt_add(smem, C_OB, 0);
  const int bid = BIDX, lane = lane_id();
  int og0 = OFF_O2 + MB_ + RB_OF(bid) * 32768 + g * 4096, og1 = og0 + 16384, vo = lane * 16;
  u32 sa = (u32)(L_TB + g * 4096 + lane * 64 + (lane & 14) * 4 + 4);
  asm volatile("" : "+s"(og0), "+s"(og1), "+v"(vo), "+v"(sa));
  u32 dl;
  for (unsigned n = 0; n < (1u << 24); ++n) {
    asm volatile("" ::: "memory");
    const u32 c0 = *(const volatile LAS u32*)(size_t)(u32)(L_MI + C_OV), c1 = *(const volatile LAS u32*)(size_t)(u32)(L_MI + C_OB);
    dl = *(const volatile LAS u32*)(size_t)(u32)(L_MI + M_DL);
    if ((u32)__builtin_amdgcn_readfirstlane(min(c0 + 11u, c1)) >= 16u) break;
  }
  gate(t16 + dl);
  ST(1, 15);
  const u32 mark = x0_mark(tag);
  o_get(wrs, vo, og0, og1, L_TB + g * 4096, sa, mark, 25);
  asm volatile("s_barrier\n\ts_waitcnt vmcnt(0)\n\ts_barrier" ::: "memory");
  if (__any(*(const LAS u32*)(size_t)(sa + 16384) == mark)) {
    FAILC(12);
    for (unsigned spins = 0; spins <= SPIN_CAP; ++spins) {
      dma4k(wrs, vo, og0, L_TA + g * 4096);
      dma4k(wrs, vo, og1, L_TA + 16384 + g * 4096);
      asm volatile("s_waitcnt vmcnt(0)" ::: "memory");
      if (!__any(*(const LAS u32*)(size_t)(sa - (L_TB - L_TA)) == mark || *(const LAS u32*)(size_t)(sa - (L_TB - L_TA) + 16384) == mark)) break;
    }
    smem[L_MI + M_FX + g] = 1;
    asm volatile("s_waitcnt lgkmcnt(0)" ::: "memory");
  }
}

using u32x8 = __attribute__((ext_vector_type(8))) unsigned;
using u32x16 = __attribute__((ext_vector_type(16))) unsigned;
__global__ __launch_bounds__(512, 2) KATTR void k_mega(const u16*, const u16*, const u16*, const u16*, const u16*, const u16*, const u16*, const u16*,
                                                       const u16*, const u16*, const u16*, char*, u32*) {
  extern __shared__ __attribute__((aligned(16))) char smem[];
  u32x16 ka;
  u32x8 kb;
  u32x2 kc;
  asm volatile("s_load_dwordx16 %0, %3, 0x0\n\ts_load_dwordx8 %1, %3, 0x40\n\ts_load_dwordx2 %2, %3, 0x60"
               : "=&s"(ka), "=&s"(kb), "=&s"(kc) : "s"(__builtin_amdgcn_kernarg_segment_ptr()) : "memory");
  const int tid = TIDX, lane = tid & 63;
  const int w = __builtin_amdgcn_readfirstlane(tid >> 6);
  const int bid = BIDX;
  ST_INIT(); ST(0, 0); ST(1, 0);
  if (w == 0) {
    u64 pc;
    int clen;
    asm volatile("s_getpc_b64 %0\n.Lcw_base:\n\ts_mov_b32 %1, .Lcw_end-.Lcw_base-4" : "=s"(pc), "=s"(clen));
    asm volatile("s_mov_b32 m0, %2\n\ts_nop 0\n\tglobal_load_lds_dword %0, %1" :: "v"(min(min(lane, 15) * 128 + (bid >> 3) * 2048, clen)), "s"(pc), "s"(L_FS) : "memory");
  }
  const int kp = (bid & 7) * 32 + (bid >> 3), hb = HB_OF(bid);
  const int c = (w >> 1) & 1, g = w & 3;
  u32 po = (u32)(kp * 512 + (tid & 255) * 2);
  const int pb = (bid & 7) + 8 * (bid >> 6), ps = (bid >> 3) & 7, gr = pb * 8 + ps;
  u32 xo = (u32)((w < 4 ? (pb * 64 + 16 * w) * 1024 + ps * 128 : gr * 8192 + g * 2048) + (lane & 7) * 16 + (lane >> 3) * (w < 4 ? 2048 : 128));
  asm volatile("" : "+v"(po), "+v"(xo) :: "memory");
  asm volatile("s_waitcnt lgkmcnt(0)" : "+s"(ka), "+s"(kb), "+s"(kc) :: "memory");
  ST_FIX();
#define KP(T_, v_, i_) ((T_)(((u64)v_[2 * (i_) + 1] << 32) | v_[2 * (i_)]))
  const u16 *x0 = KP(const u16*, ka, 0), *A_re = KP(const u16*, ka, 1), *A_im = KP(const u16*, ka, 2), *B_re = KP(const u16*, ka, 3), *B_im = KP(const u16*, ka, 4);
  const u16 *C_re = KP(const u16*, ka, 5), *C_im = KP(const u16*, ka, 6), *log_dt = KP(const u16*, ka, 7), *Dskip = KP(const u16*, kb, 0), *wqkv = KP(const u16*, kb, 1), *wo = KP(const u16*, kb, 2);
  char* ws = KP(char*, kb, 3);
  u32* out = KP(u32*, kc, 0);
#define LD_A() asm volatile("global_load_ushort v248, %0, %1\n\tglobal_load_ushort v249, %0, %2" :: "v"(po), "s"(A_re), "s"(A_im) : "memory", "v248", "v249")
  u32 ldtw = 0, dskw = 0;
  if (w < 4) {
    asm volatile("s_load_dword %0, %2, %4\n\ts_load_dword %1, %3, %4" : "=&s"(ldtw), "=&s"(dskw) : "s"(log_dt), "s"(Dskip), "s"(4 * kp) : "memory");
    LD_A();
  }
  asm volatile("global_load_dwordx4 v[240:243], %0, %1\n\tglobal_load_dwordx4 v[244:247], %0, %1 offset:1024\n\tglobal_load_dword v254, %2, %3 sc1"
               :: "v"(xo), "s"(x0), "v"(OFF_EP), "s"(ws) : "memory", "v240", "v254");
  if (w >= 4) {
    asm volatile("global_load_ushort v250, %0, %1\n\tglobal_load_ushort v251, %0, %2\n\tglobal_load_ushort v252, %0, %3\n\tglobal_load_ushort v253, %0, %4"
                 :: "v"(po), "s"(B_re), "s"(B_im), "s"(C_re), "s"(C_im) : "memory", "v250", "v253");
    LD_A();
  }
  __builtin_amdgcn_sched_barrier(0);
  const rsrc_t wrs = __builtin_amdgcn_make_buffer_rsrc((void*)ws, 0, WSB, 0x00020000);
  if (w >= 4) {
    asm volatile("s_mov_b32 m0, %2\n\ts_nop 0\n\tglobal_load_lds_dword %0, %1" :: "v"((bid * 16 + g * 4 + min(lane, 3)) * 128), "s"(wqkv), "s"(L_FS + w * 256) : "memory");
    u32x4 zz = {0, 0, 0, 0};
    asm volatile("" : "+v"(zz));
    const int z0 = (int)opaque((u32)(L_U + (lane >> 5) * 4608 + (lane & 31) * 16)), z1 = (int)opaque((u32)(L_MI + min((tid & 255) * 16 - 1024, 1088)));
    *(u32x4*)(smem + (g == 0 ? z0 : z1)) = zz;
    asm volatile("s_nop 1\n\ts_waitcnt lgkmcnt(0)" :: "v"(zz) : "memory");
  }
  if (w >= 4) asm volatile("s_load_dword %0, %1, %2" : "=s"(ldtw) : "s"(log_dt), "s"(4 * kp) : "memory");
  __builtin_amdgcn_sched_barrier(0);
#define XT_GET(x_) asm volatile("v_mov_b64 %0, v[240:241]\n\tv_mov_b64 %1, v[242:243]\n\tv_mov_b64 %2, v[244:245]\n\tv_mov_b64 %3, v[246:247]" \
                   : "=v"(x_[0].xy), "=v"(x_[0].zw), "=v"(x_[1].xy), "=v"(x_[1].zw) :: "memory")
  const int ftb = c ? L_TA : 0;
#define PUT(e_, r_, i_) *(LAS u32*)(wa + (e_) * FTS) = pkh(r_, i_)
#define CMR(a_, b_) fmaf(a_[0], b_[0], -(a_[1] * b_[1]))
#define CMI(a_, b_) fmaf(a_[0], b_[1], a_[1] * b_[0])
#define CMUL(a_, b_) (f32x2){CMR(a_, b_), CMI(a_, b_)}
  struct P0A { u32 gad[12], sad[8], ua, ur; };
  auto p0_addr = [&](P0A& A, int lane, int ut) __attribute__((always_inline)) {
    const int i = lane & 15, kk = lane >> 4;
    const u32 sb = (u32)((L_KT - KTO + c * TBS - 0x7E00) & 0xFFFF) * 0x10001u;
    const float f0 = __uint_as_float(opaque(__float_as_uint((float)((ut < 2 ? 1023 : 2047) - 512 * (w & 1) - 8 * lane))));
#pragma unroll
    for (int e = 0; e < 6; ++e) { const u32 ga = tb_addr(pk2(f0 - (float)(2 * e), f0 - (float)(2 * e + 1)), sb); A.gad[2 * e] = ga & 0xFFFFu; A.gad[2 * e + 1] = ga >> 16; }
    if (ut < 2) {
      A.ua = (u32)(ftb + i * FTS + kk * 16); A.ur = A.ua + ut * 16 * FTS;
      const int a0 = 128 * (i >> 3) + (i & 7), a1 = i < 8 ? 256 + 128 * (i >> 2) + 2 * (i & 3) : 128 * (i >> 1) + 4 * (i & 1), a2 = min(1024 + 128 * i, 2064);
#pragma unroll
      for (int t = 0; t < 2; ++t) {
        const float st = (t && i >= 8) ? 16.f : 8.f;
        const float fb = (float)(t ? a2 : ut == 0 ? a0 : a1) + st * (float)(4 * kk);
        const u32 s0 = tb_addr(pk2(fb, fb + st), sb), s1 = tb_addr(pk2(fb + 2.f * st, fb + 3.f * st), sb);
        A.sad[4 * t] = s0 & 0xFFFFu; A.sad[4 * t + 1] = s0 >> 16; A.sad[4 * t + 2] = s1 & 0xFFFFu; A.sad[4 * t + 3] = s1 >> 16;
      }
      asm volatile("" : "+v"(A.ua), "+v"(A.ur), "+v"(A.sad[0]), "+v"(A.sad[1]), "+v"(A.sad[2]), "+v"(A.sad[3]), "+v"(A.sad[4]), "+v"(A.sad[5]), "+v"(A.sad[6]), "+v"(A.sad[7]));
    }
    asm volatile("" : "+v"(A.gad[0]), "+v"(A.gad[1]), "+v"(A.gad[2]), "+v"(A.gad[3]), "+v"(A.gad[4]), "+v"(A.gad[5]), "+v"(A.gad[6]), "+v"(A.gad[7]), "+v"(A.gad[8]), "+v"(A.gad[9]), "+v"(A.gad[10]));
    __builtin_amdgcn_sched_barrier(0);
  };
  auto k_unit = [&](const P0A& A, float dk, bool lag0) __attribute__((always_inline)) {
    f16x8 fr[8], fc[8];
    const f32x2 bd = *(const f32x2*)(smem + L_P0 + 512 + c * 8);
    __builtin_amdgcn_sched_barrier(0);
#pragma unroll
    for (int e = 0; e < 8; ++e) {
      fc[e] = *(const LAS f16x8*)(size_t)(A.ua + e * 64 + 41 * FTS);
      fr[e] = *(const LAS f16x8*)(size_t)(A.ur + e * 64);
      __builtin_amdgcn_sched_barrier(0);
    }
    f16x8 f2[8];
    if (!(w & 1)) {
#pragma unroll
      for (int e = 0; e < 8; ++e) { f2[e] = *(const LAS f16x8*)(size_t)(A.ua + e * 64 + 32 * FTS); __builtin_amdgcn_sched_barrier(0); }
    }
    const int ec = bexp(bd[0] + bd[1] + fabsf(dk));
    const float f0 = __uint_as_float((u32)max(127 + bexp(bd[0]) - ec, 0) << 23), f1 = __uint_as_float((u32)max(127 + bexp(bd[1]) - ec, 0) << 23);
    const float d0s = lag0 ? dk * __uint_as_float((u32)(268 - ec) << 23) : 0.f;
    auto unit = [&](const f16x8 (&fr)[8], u32x4 sa, float add) __attribute__((always_inline)) {
      f32x4 k0 = {0.f, 0.f, 0.f, 0.f}, k1 = k0;
#pragma unroll
      for (int e = 0; e < 4; ++e) k0 = __builtin_amdgcn_mfma_f32_16x16x32_f16(fc[e], fr[e], k0, 0, 0, 0);
#pragma unroll
      for (int e = 4; e < 8; ++e) k1 = __builtin_amdgcn_mfma_f32_16x16x32_f16(fc[e], fr[e], k1, 0, 0, 0);
      k0 *= f0; k0[0] += add;
      const u32 h01 = pkh(fmaf(k1[0], f1, k0[0]), fmaf(k1[1], f1, k0[1])), h23 = pkh(fmaf(k1[2], f1, k0[2]), fmaf(k1[3], f1, k0[3]));
      *(LAS u16*)((LAS char*)(size_t)sa[0] + KTO) = (u16)h01; *(LAS u16*)((LAS char*)(size_t)sa[1] + KTO) = (u16)(h01 >> 16);
      *(LAS u16*)((LAS char*)(size_t)sa[2] + KTO) = (u16)h23; *(LAS u16*)((LAS char*)(size_t)sa[3] + KTO) = (u16)(h23 >> 16);
    };
    if (!(w & 1)) {
      unit(fr, (u32x4){A.sad[0], A.sad[1], A.sad[2], A.sad[3]}, d0s);
      unit(f2, (u32x4){A.sad[4], A.sad[5], A.sad[6], A.sad[7]}, 0.f);
    } else {
      unit(fr, (u32x4){A.sad[0], A.sad[1], A.sad[2], A.sad[3]}, 0.f);
      *(float*)(smem + L_MI + 4 * c) = __uint_as_float((u32)(ec - 14) << 23);
    }
  };
  auto k_get = [&](u32 (&kh)[11], const P0A& A) __attribute__((always_inline)) {
#pragma unroll
    for (int e = 0; e < 11; ++e) kh[e] = *(const LAS u16*)((const LAS char*)(size_t)A.gad[e] + KTO);
    __builtin_amdgcn_sched_barrier(0);
  };
  auto k_copies = [&](const u32 (&kh)[11], int lane, bool low) __attribute__((always_inline)) {
    u32 kw[6];
#pragma unroll
    for (int e = 0; e < 5; ++e) kw[e] = __builtin_amdgcn_perm(kh[2 * e + 1], kh[2 * e], 0x05040100u);
    kw[5] = kh[10];
    if (low && (w & 1) && lane == 63) { kw[4] = 0; kw[5] = 0; }
    build_copies(smem, c, (w & 1) * 64 + lane + (low ? 128 : 0), kw);
    __builtin_amdgcn_sched_barrier(0);
  };
  __builtin_amdgcn_sched_barrier(0);
  int w2 = w;
  asm volatile("" : "+s"(w2));
  if (w < 4) {
  P0A pa;
  p0_addr(pa, lane, w & 1);
  LAS char* wa = (LAS char*)(size_t)opaque((u32)(ftb + (tid & 127) * 4));
  u32 tg;
  const bool lo = pb < 16;
  const int xc8 = lane >> 3, xrp = lane & 7;
  const int xts = lo ? 128 : XTS, xt2 = 2 * xts;
  const int xto = (int)opaque((u32)(lo ? OFF_X1 + (pb * 4 + w) * XBS + (ps * 32 + 4 * xc8) * 128 + xrp * 16
                                       : XTO(ps * 32 + 4 * xc8) + ((pb & 15) * 64 + 16 * w + 2 * xrp) * 8));
  auto xpub = [&]() __attribute__((always_inline)) {
    u32x4 q[4];
    u32x4 xt[2];
    __builtin_amdgcn_sched_barrier(0);
    asm volatile("s_waitcnt vmcnt(0)\n\tv_add_u32 %0, 1, v254" : "=v"(tg) :: "memory");
    XT_GET(xt);
    const int sl = ((lane & 7) * 8 + (lane >> 3)) * 4;
#pragma unroll
    for (int e = 0; e < 8; ++e) xt[e & 1][e >> 1] = (u32)__builtin_amdgcn_ds_bpermute(sl, (int)xt[e & 1][e >> 1]);
#pragma unroll
    for (int e = 0; e < 4; ++e) q[e] = (u32x4){xt[0][e], x0_mark(tg), xt[1][e], x0_mark(tg)};
    asm volatile("" : "+v"(q[0]), "+v"(q[1]), "+v"(q[2]), "+v"(q[3]));
#pragma unroll
    for (int e = 0; e < 4; ++e) stw(wrs, xto + (e & 1) * xts + (e >> 1) * xt2, q[e]);
    asm volatile("s_nop 1" :: "v"(q[0]), "v"(q[1]), "v"(q[2]), "v"(q[3]) : "memory");
    ST(0, 3);
    __builtin_amdgcn_sched_barrier(0);
  };
  u32 ha, hz, hs, ht;
  C1Pre cp;
  int xs0, xs1, xl0, xl1, xv;
  u32 za;
  auto setup1 = [&]() __attribute__((always_inline)) {
    SB();
    const int t = (w & 1) * 64 + lane;
    ha = (u32)(L_KT + c * TBS - 0x7E00 + 2 * (0x44FF - t)); hz = ha - (t == 127 ? 2 : 0);
    hs = (lane & 1) ? 0x01000302u : 0x03020302u; ht = (lane & 1) ? 0x01000100u : 0x01000302u;
    asm volatile("" : "+v"(ha), "+v"(hz), "+v"(hs), "+v"(ht));
    SB();
  };
  auto setup2 = [&]() __attribute__((always_inline)) {
    SB();
    c1_pre(cp, kp, w, lane);
    xs0 = OFF_X1 + RB_OF(bid) * XBS + w * 8192; xs1 = xs0 + 4096; xl0 = L_TA + w * 8192; xl1 = xl0 + 4096; xv = x_lane(lane);
    za = (u32)(((lane >> 1) & 7) * CSTR + 4096 + (lane & 1) * 16);
    asm volatile("" : "+s"(xs0), "+s"(xs1), "+s"(xl0), "+s"(xl1), "+v"(xv), "+v"(za));
    SB();
  };
  {
    float ar, ai;
#define B0() do { SB(); __builtin_amdgcn_s_barrier(); SB(); } while (0)
    B0();
    asm volatile("s_waitcnt vmcnt(3)\n\tv_lshlrev_b32 %0, 16, v248\n\tv_lshlrev_b32 %1, 16, v249" : "=v"(ar), "=v"(ai) :: "memory");
    asm volatile("s_waitcnt lgkmcnt(0)" : "+s"(ldtw), "+s"(dskw) :: "memory");
    ST(0, 1);
    const float dt = rbf(__expf(c ? hi2f(ldtw) : lo2f(ldtw)));
    const float zr = dt * ar * 1.4426950408889634f, zi = dt * ai * 0.15915494309189535f;
    float cs = __builtin_amdgcn_exp2f(fmaxf(zr, 0.f) * -1928.f);
    f32x2 P[8], Q1, R[17];
    { float a_, b_; cexp2(zr, zi, a_, b_); P[1] = (f32x2){a_, b_}; cexp2(zr * 128.f, zi * 128.f, a_, b_); Q1 = (f32x2){a_, b_}; }
    asm volatile("" : "+v"(cs), "+v"(P[1]), "+v"(Q1));
    xpub();
    P[2] = CMUL(P[1], P[1]);
    const f32x2 Q2 = CMUL(Q1, Q1);
    R[1] = Q1 * cs; R[2] = Q2 * cs;
    P[3] = CMUL(P[2], P[1]); P[4] = CMUL(P[2], P[2]);
    const f32x2 Q4 = CMUL(Q2, Q2);
    R[3] = CMUL(R[1], Q2); R[4] = Q4 * cs;
    P[5] = CMUL(P[4], P[1]); P[6] = CMUL(P[3], P[3]); P[7] = CMUL(P[4], P[3]);
#pragma unroll
    for (int j = 5; j <= 16; ++j) R[j] = CMUL(R[j - 4], Q4);
    asm volatile("" : "+v"(R[13]), "+v"(R[14]), "+v"(R[15]), "+v"(R[16]), "+v"(P[7]), "+v"(P[6]), "+v"(P[5]));
    PUT(0, cs, 0.f); PUT(8, R[1][0], R[1][1]);
#pragma unroll
    for (int r = 1; r < 8; ++r) { PUT(r, P[r][0] * cs, P[r][1] * cs); PUT(8 + r, CMR(R[1], P[r]), CMI(R[1], P[r])); }
    asm volatile("" ::: "memory");
#pragma unroll
    for (int j = 2; j < 4; ++j) {
      PUT(16 + 4 * (j - 2), R[j][0], R[j][1]);
#pragma unroll
      for (int r = 1; r < 4; ++r) PUT(16 + 4 * (j - 2) + r, CMR(R[j], P[2 * r]), CMI(R[j], P[2 * r]));
    }
#pragma unroll
    for (int j = 4; j < 8; ++j) { PUT(24 + 2 * (j - 4), R[j][0], R[j][1]); PUT(25 + 2 * (j - 4), CMR(R[j], P[4]), CMI(R[j], P[4])); }
#pragma unroll
    for (int j = 8; j <= 16; ++j) PUT(24 + j, R[j][0], R[j][1]);
    __builtin_amdgcn_sched_barrier(0);
  }
  ST(0, 2);
  cnt_add(smem, C_F, lane);
  u32 stag = __builtin_amdgcn_readfirstlane(tg), g2;
  asm volatile("" : "+s"(stag));
  cnt_spin(smem, C_F, 8);
  k_unit(pa, c ? hi2f(dskw) : lo2f(dskw), (tid & 127) == 0);
  cnt_add(smem, C_A, lane);
  setup1();
  setup2();
  cnt_spin(smem, C_A, 4);
  ST(0, 4);
  {
    u32 kl[11];
    k_get(kl, pa);
    const u32 h0 = *(const LAS u16*)(size_t)ha, h1 = *(const LAS u16*)(size_t)(ha + 2), h2 = *(const LAS u16*)(size_t)hz;
    SB();
    u32x4 zz = {0, 0, 0, 0};
    asm volatile("" : "+v"(zz));
    *(LAS u32x4*)(size_t)za = zz;
    k_copies(kl, lane, true);
    asm volatile("" :: "v"(zz));
    cnt_add(smem, C_A, lane);
    ST(0, 5);
    {
      const u32 p = h0 | (h1 << 16), hh = __builtin_amdgcn_perm(p, p, 0x03020302u), ll = __builtin_amdgcn_perm(p, p, 0x01000100u);
      const u32 hx = __builtin_amdgcn_perm(p, p, hs), xl = __builtin_amdgcn_perm(p, p, ht);
      u32x4 c0 = {hh, hx, ll, ll}, c1 = {hh, xl, ll, ll}, c2 = {hx, ll, ll, ll}, c3 = {xl, ll, ll, h0 | (h2 << 16)};
      asm volatile("" : "+v"(c0), "+v"(c1), "+v"(c2), "+v"(c3));
      char* cop = smem + c * 4 * CSTR + ((w & 1) * 64 + lane) * 16;
      *(u32x4*)(cop + 0 * CSTR) = c0; *(u32x4*)(cop + 1 * CSTR) = c1; *(u32x4*)(cop + 2 * CSTR) = c2; *(u32x4*)(cop + 3 * CSTR) = c3;
      asm volatile("s_nop 1" :: "v"(c0), "v"(c1), "v"(c2), "v"(c3) : "memory");
      cnt_add(smem, C_KH, lane);
    }
  }
  cnt_spin(smem, C_A, 8);
  bf16x8 wof[16];
  u32x4 pfl, wfl;
  XTake xt;
  AttC ac;
  a_conv1(wrs, w, cp, pfl, wfl, g2);
  asm volatile("" : "+v"(pfl), "+v"(wfl));
  SB();
  KSLP(15, SLP_XA1);
  tags_wait(pfl, wrs, OFF_ST0 + tid * 16, stag, 19);
  dma4k(wrs, xv, xs0, xl0);
  dma4k(wrs, xv, xs1, xl1);
  KSLP(14, SLP_WE);
  SB();
  ConvK ck;
  a_blayer<true>(smem, wrs, ws, out, tid, w, wof, stag, wfl, ck, xt, ac);
  ST(1, 2);
  a_conv<false>(smem, wrs, RB_OF(bid), w, ck);
  a_blayer<false>(smem, wrs, ws, out, tid, w, wof, stag, wfl, ck, xt, ac);
  ST_OUT();
  }
  if (w2 >= 4) {
    const int lane = lane_id();
    LAS char* wa = (LAS char*)(size_t)opaque((u32)(ftb + (w & 1) * 256 + lane * 4));
    u32 tag;
    u32 st0, st1;
    int pbo;
    auto cols = [&]() __attribute__((always_inline)) {
    {
      float ar, ai, br, bi, cr, ci;
      asm volatile("v_lshlrev_b32 %0, 16, v248\n\tv_lshlrev_b32 %1, 16, v249\n\tv_lshlrev_b32 %2, 16, v250\n\t"
                   "v_lshlrev_b32 %3, 16, v251\n\tv_lshlrev_b32 %4, 16, v252\n\tv_lshlrev_b32 %5, 16, v253"
                   : "=v"(ar), "=v"(ai), "=v"(br), "=v"(bi), "=v"(cr), "=v"(ci) :: "memory");
      asm volatile("s_waitcnt lgkmcnt(0)" : "+s"(ldtw) :: "memory");
      const float dt = rbf(__expf(c ? hi2f(ldtw) : lo2f(ldtw)));
      const float dbr = dt * br, dbi = dt * bi;
      const float zr = dt * ar * 1.4426950408889634f, zi = dt * ai * 0.15915494309189535f;
      const float zp = fmaxf(zr, 0.f), g1 = __builtin_amdgcn_exp2f(zp * 1928.f);
      const float wr = (cr * dbr - ci * dbi) * g1, wi = (cr * dbi + ci * dbr) * g1;
      float bd = (fabsf(wr) + fabsf(wi)) * __builtin_amdgcn_exp2f(zp * 120.f);
      f32x2 S1, C[16];
      { float a_, b_; cexp2(zr * 8.f, zi * 8.f, a_, b_); S1 = (f32x2){a_, b_}; }
      bd = dpp_add<0x111, 0xf>(bd); bd = dpp_add<0x112, 0xf>(bd);
      const f32x2 S2 = CMUL(S1, S1);
      bd = dpp_add<0x114, 0xf>(bd); bd = dpp_add<0x118, 0xf>(bd);
      const f32x2 S4 = CMUL(S2, S2);
      bd = dpp_add<0x142, 0xa>(bd); bd = dpp_add<0x143, 0xc>(bd);
      bd = __int_as_float(__builtin_amdgcn_readlane(__float_as_int(bd), 63));
      *(float*)(smem + L_P0 + 512 + g * 4) = bd;
      const float sc = __uint_as_float((u32)(268 - bexp(bd)) << 23);
      C[0] = (f32x2){wr * sc, wi * sc};
      C[1] = CMUL(C[0], S1); C[2] = CMUL(C[0], S2); C[3] = CMUL(C[1], S2);
#pragma unroll
      for (int e = 4; e < 16; ++e) C[e] = CMUL(C[e - 4], S4);
#pragma unroll
      for (int e = 0; e < 16; ++e) *(LAS u32*)(wa + (41 + e) * FTS) = pkhn(C[e][0], C[e][1]);
    }
      cnt_add(smem, C_F, lane);
      ST(0, 2);
    };
    auto stats = [&]() __attribute__((always_inline)) {
    {
      const u32 ONES = opaque(0x3F803F80u);
      u32x4 xt[2];
      XT_GET(xt);
      float sm[2] = {0.f, 0.f}, sq[2] = {0.f, 0.f};
#pragma unroll
      for (int e = 0; e < 8; ++e) { sm[e & 1] = dot2acc(xt[e & 1][e >> 1], ONES, sm[e & 1]); sq[e & 1] = dot2acc(xt[e & 1][e >> 1], xt[e & 1][e >> 1], sq[e & 1]); }
      const bool b0 = lane & 1, b1 = lane & 2;
      const float v0 = (b0 ? sm[1] : sm[0]) + dpp_mov<DPP_XOR1>(b0 ? sm[0] : sm[1]), v1 = (b0 ? sq[1] : sq[0]) + dpp_mov<DPP_XOR1>(b0 ? sq[0] : sq[1]);
      float v = (b1 ? v1 : v0) + dpp_mov<DPP_XOR2>(b1 ? v0 : v1);
      v += dpp_mov<0x124>(v); v += dpp_mov<DPP_ROR8>(v);
      auto r = __builtin_amdgcn_permlane16_swap(__float_as_uint(v), __float_as_uint(v), false, false);
      v = __uint_as_float(r[0]) + __uint_as_float(r[1]);
      auto q = __builtin_amdgcn_permlane32_swap(__float_as_uint(v), __float_as_uint(v), false, false);
      v = (__uint_as_float(q[0]) + __uint_as_float(q[1])) * (1.f / 512.f);
      st0 = pack_stats(v, fmaxf(dpp_mov<DPP_XOR2>(v) - v * v, 0.f));
      st1 = dpp_u<DPP_XOR1>(st0);
    }
    };
    auto publish = [&]() __attribute__((always_inline)) {
      asm volatile("v_add_u32 %0, 1, v254" : "=v"(tag) :: "memory");
      stw(wrs, pbo, (u32x4){st0, tag, st1, tag});
      __builtin_amdgcn_sched_barrier(0);
    };
#define VMW(n_) asm volatile("s_waitcnt vmcnt(%0)" :: "n"(n_) : "memory")
    constexpr int PTN = 2;
    const int ut = 16 + (bid >> 3), us = ut / 3, um = ut % 3;
    const rsrc_t qrs = __builtin_amdgcn_make_buffer_rsrc((void*)wqkv, 0, 1572864, 0x00020000), ors = __builtin_amdgcn_make_buffer_rsrc((void*)wo, 0, 524288, 0x00020000);
    const int wv0 = (8 * g + (lane >> 3)) * 3072 + (((lane & 7) ^ ((lane >> 3) & 6)) << 4);
#define W_DMA1(r_, v_, s_, l_) asm volatile("s_mov_b32 m0, %3\n\ts_nop 0\n\tbuffer_load_dwordx4 %0, %1, %2 offen lds" :: "v"(v_), "s"(r_), "s"(s_), "s"(l_) : "memory")
    W_DMA1(qrs, wv0, us * 98304 + um * 1024 + (bid & 7) * 128, L_WR + g * 1024);
    const int hs = (3 - (int)(kb[2] >> 7)) & 7, xt_ = bid - 117, xs = (xt_ / 3) * 98304 + (xt_ % 3) * 1024;
    const bool xp = bid >= 128 && bid < 133;
    W_DMA1(xp ? qrs : ors, xp ? wv0 : ((bid >> 3) * 32 + 8 * g + (lane >> 3)) * 1024 + (((lane & 7) ^ (((lane >> 3) & 2) | ((g & 1) << 2))) << 4), xp ? xs + hs * 128 : (bid & 7) * 128, L_WR + 4096 + g * 1024);
    B0();
    const int ln = lane;
    pbo = (int)opaque((u32)(lane == 0 ? OFF_SG + gr * 64 + g * 16 : WSB));
    const int wvo = (int)opaque((u32)(wv0 + hb * 128)), wvx = (int)opaque((u32)(hb == hs ? WSB : wvo));
#define WSO(s_) ((s_) < 4 ? 40960 + (s_) * 12288 : L_WX - L_WS)
    auto wraw = [&](auto S_) __attribute__((always_inline)) { constexpr int s = decltype(S_)::value; w_dma(qrs, s < 4 ? wvo : wvx, s < 3 ? wvo : wvx, s * 98304, L_WS + WSO(s) + g * 1024); };
    __builtin_amdgcn_s_setprio(1);
    VMW(8 + PTN);
    ST(0, 1);
    stats();
    VMW(7 + PTN);
    publish();
    ST(0, 10);
    VMW(2 + PTN);
    cols();
    SB();
    __builtin_amdgcn_s_setprio(0);
    static_for<0, R1D>(wraw);
    VMW(3 * R1D + 1); cnt_add(smem, C_B, lane);
    SB();
    const u32 wga = opaque((u32)(L_WS + (ln >> 2) * 128 + (((2 * g + ((ln & 3) >> 1)) ^ ((ln >> 2) & 6)) << 4) + (ln & 1) * 8)), wgb = opaque(wga + 53248u);
    const u32 to = opaque(xp ? wga + (L_WR - L_WS + 4096) : (u32)(L_WR + 4096 + (8 * (lane >> 4) + ((lane >> 2) & 3)) * 128 + (((2 * g + ((lane & 3) >> 1)) ^ (((lane >> 2) & 2) | ((lane >> 2) & 4))) << 4) + (lane & 1) * 8));
    const u32 tp = opaque(to + (xp ? 2048 : 512));
    const int us0 = (int)opaque((u32)(OFF_WQ + us * 98304 + ((bid & 7) * 4 + g) * 3072 + um * 1024 + lane * 16));
    const int us1 = (int)opaque((u32)((bid < 128 ? OFF_WO + (((bid & 7) * 16 + (bid >> 3)) * 4 + g) * 1024 : xp ? OFF_WQ + xs + (hs * 4 + g) * 3072 : WSB) + lane * 16));
    cnt_spin(smem, C_B, 4);
    {
#define TR4(a_) __builtin_amdgcn_ds_read_tr16_b64_v4i16((LAS bf16x4*)(size_t)(a_))
      const bf16x4 q0 = TR4(wgb + (L_WR - L_WS - 53248)), q1 = TR4(wgb + (L_WR - L_WS - 53248 + 2048)), q2 = TR4(to), q3 = TR4(tp);
      cnt_add(smem, C_B, lane);
      stw(wrs, us0, __builtin_bit_cast(u32x4, __builtin_shufflevector(q0, q1, 0, 1, 2, 3, 4, 5, 6, 7)));
      stw(wrs, us1, __builtin_bit_cast(u32x4, __builtin_shufflevector(q2, q3, 0, 1, 2, 3, 4, 5, 6, 7)));
      SB();
    }
    static_for<R1D, R1F>(wraw);
    SB();
    const u32 stag = __builtin_amdgcn_readfirstlane(tag);
    const int fs0 = OFF_ST0 + 4096 + ((bid & 7) * 4 + g) * 128 + (bid >> 3) * 4, fs1 = bid < 128 ? fs0 + 4096 : bid < 136 ? OFF_ST0 + 8192 + (hs * 4 + g) * 128 + (bid - 112) * 4 : WSB;
    const int fso = (int)opaque((u32)(lane == 0 ? fs0 : lane == 1 ? fs1 : WSB));
    const int wbo = (int)opaque((u32)((hb * 4 + g) * 3072 + ln * 16));
    const int fo = (int)opaque((u32)(OFF_ST0 + 4096 + (hb * 4 + g) * 128 + (hb == hs && (ln & 8) ? 4096 + 64 + (ln & 1) * 16 : (ln & 7) * 16)));
    const int r0 = 256 * g + 2 * ln;
    const int sto = (int)opaque((u32)(OFF_SG + r0 * 8)), xto = (int)opaque((u32)(XTO(kp) + r0 * 8));
    const int xlo = (int)opaque((u32)(OFF_X1 + (16 * g + (ln >> 3)) * XBS + kp * 128 + (ln & 7) * 16));
    const u32 uw = opaque((u32)(L_U + (256 + r0) * 2)), xw = opaque((u32)(L_X + r0 * 4));
    const int mso = (int)opaque((u32)(ln == 0 ? OFF_ST0 + kp * 16 + g * 4 : WSB));
    const int p2o = (int)opaque((u32)(OFF_X2 + (kp * XS_ + 256 * g + 4 * ln) * 4)), pso = (int)opaque((u32)(ln < 2 ? OFF_ST2 + (512 * g + 2 * bid + ln) * 16 : WSB));
    SB();
    VMW(3 * (R1F - R1D));
    __builtin_amdgcn_raw_buffer_store_b32(tag, wrs, fso, 0, 16);
    ST(0, 6);
    SB();
    static_for<R1F, 5>(wraw);
    W_DMA1(qrs, wvx, 5 * 98304, L_SP + g * 1024);
    bf16x8 wf[48];
    GPre gp;
    {
      auto wget = [&](auto S_) __attribute__((always_inline)) {
        constexpr int s = decltype(S_)::value, o = WSO(s) >= 53248 ? WSO(s) - 53248 : WSO(s);
        w_get<3 * s, o>(wf[3 * s], WSO(s) >= 53248 ? wgb : wga);
        w_get<3 * s + 1, o + 4096>(wf[3 * s + 1], WSO(s) >= 53248 ? wgb : wga);
        w_get<3 * s + 2, o + 8192>(wf[3 * s + 2], WSO(s) >= 53248 ? wgb : wga);
      };
      const u32 mk = x0_mark(stag);
      auto wld = [&](auto I_) __attribute__((always_inline)) { w_ld<decltype(I_)::value>(wf[decltype(I_)::value], wbo, wrs); };
      u32x4 gs[4], gx[4];
#define G_LD(e_) do { gs[e_] = ldws(wrs, sto + ((e_) & 1) * 1024, ((e_) >> 1) * 8192); gx[e_] = (e_) < 2 ? ldws(wrs, xlo, (e_) * 8 * XBS) : ldw(wrs, xto + ((e_) - 2) * 1024); } while (0)
#define G_B2(v_, t_) opaque((v_[1] ^ (t_)) | (v_[3] ^ (t_)))
#define G_OK(h_) (__builtin_amdgcn_ballot_w64(((G_B2(gs[2 * (h_)], stag) | G_B2(gs[2 * (h_) + 1], stag)) | (G_B2(gx[2 * (h_)], mk) | G_B2(gx[2 * (h_) + 1], mk))) != 0u) == 0ull)
      auto take = [&](auto H_) __attribute__((always_inline)) {
        constexpr int H = decltype(H_)::value;
        if (__builtin_expect(!G_OK(H), 0)) {
          for (unsigned spins = 0;; ++spins) {
            FAILC(H ? 4 : 0);
            __builtin_amdgcn_s_sleep(SLP_U);
            asm volatile("" ::: "memory");
            G_LD(2 * H); G_LD(2 * H + 1);
            if (G_OK(H) || spins > SPIN_CAP) break;
          }
        }
#pragma unroll
        for (int e = 2 * H; e < 2 * H + 2; ++e) {
          const float rs0 = __builtin_amdgcn_rcpf(hi2f(gs[e][0])), rs1 = __builtin_amdgcn_rcpf(hi2f(gs[e][2])), n0 = -lo2f(gs[e][0]) * rs0, n1 = -lo2f(gs[e][2]) * rs1;
          const int r = (e & 1) * 128 + (e >> 1) * 1024;
          *(LAS u32*)(size_t)(uw + r * 2) = pkh(fmaf(lo2f(gx[e][0]), rs0, n0), fmaf(lo2f(gx[e][2]), rs1, n1));
          *(LAS u32*)(size_t)(uw + r * 2 + 4608) = pkh(fmaf(hi2f(gx[e][0]), rs0, n0), fmaf(hi2f(gx[e][2]), rs1, n1));
          *(LAS u32x2*)(size_t)(xw + r * 4) = (u32x2){gx[e][0], gx[e][2]};
        }
        __builtin_amdgcn_sched_barrier(0);
      };
      KSLP(16, SLP_G);
      asm volatile("" ::: "memory");
#pragma unroll
      for (int e = 0; e < 4; ++e) G_LD(e);
      __builtin_amdgcn_sched_barrier(0);
      ST(0, 5);
      f32x4 cs[3];
      u32x4 SENT = {0xFFFFFFFFu, 0xFFFFFFFFu, 0xFFFFFFFFu, 0xFFFFFFFFu};
      const u32 o1 = ((ln >> 1) & 1) == (ln >> 5) ? 0x3F803F80u : 0u;
      bf16x8 one8 = __builtin_bit_cast(bf16x8, (u32x4){o1, o1, o1, o1});
      u32 sidx = (ln & 1) ? 0xEEEEu : 0x4444u;
      cs[0] = cs[1] = cs[2] = (f32x4){0.f, 0.f, 0.f, 0.f};
      asm volatile("" : "+v"(one8), "+v"(sidx), "+v"(cs[0]), "+v"(cs[1]), "+v"(cs[2]), "+v"(SENT));
      auto wsum = [&](auto I_) __attribute__((always_inline)) { constexpr int i = 2 * decltype(I_)::value + 1; w_sum<i>(cs[i % 3], one8, sidx, wf); };
      cnt_spin(smem, C_B, 8);
      __builtin_amdgcn_s_setprio(1);
      VMW(8);
      cnt_add(smem, C_W, ln);
      u32x4 fl;
      asm volatile("" : "+v"(gs[0]), "+v"(gs[1]), "+v"(gx[0]), "+v"(gx[1]) :: "memory");
      take(IC<0>{});
      cnt_add(smem, C_U, ln);
      __builtin_amdgcn_raw_buffer_store_b32(tag, wrs, mso, 0, 16);
      ST(0, 3);
      stw(wrs, p2o, SENT); __builtin_amdgcn_raw_buffer_store_b128(SENT, wrs, p2o, 4096, 16);
      stw(wrs, pso, __builtin_bit_cast(u32x4, cs[0]));
      asm volatile("s_nop 1" :: "v"(SENT) : "memory");
      SB();
      auto rest = [&]() __attribute__((always_inline)) {
        cnt_spin(smem, C_W, 4);
        static_for<0, 5>(wget);
        w_get<15, L_SP - L_WS - 53248>(wf[15], wgb);
        cnt_add(smem, C_B, ln);
        take(IC<1>{});
        cnt_add(smem, C_P2, ln);
        ST(0, 4);
      };
      rest();
      { KSLP(17, SLP_FL); fl = ldw(wrs, fo); SB(); asm volatile("s_waitcnt lgkmcnt(0)" ::: "memory"); static_for<0, 7>(wsum); g_pre<true>(gp, g, stag); }
      asm volatile("" : "+v"(gs[2]), "+v"(gs[3]), "+v"(gx[2]), "+v"(gx[3]), "+v"(fl) :: "memory");
      tags_wait(fl, wrs, fo, stag, 15);
      ST(0, 7);
      asm volatile("s_cmp_lg_u32 %5, %6\n\ts_cbranch_scc1 1f\n\tbuffer_load_dwordx4 %0, %7, %8, %9 offen offset:2048 sc1\n\t"
                   "buffer_load_dwordx4 %1, %7, %8, %10 offen sc1\n\tbuffer_load_dwordx4 %2, %7, %8, %10 offen offset:1024 sc1\n\tbuffer_load_dwordx4 %3, %7, %8, %10 offen offset:2048 sc1\n\t"
                   "buffer_load_dwordx4 %4, %7, %8, %11 offen sc1\n1:"
                   : "+{v[120:123]}"(wf[11]), "+{v[124:127]}"(wf[12]), "+{v[128:131]}"(wf[13]), "+{v[132:135]}"(wf[14]), "+{v[136:139]}"(wf[15])
                   : "s"(hb), "s"(hs), "v"(wbo), "s"(wrs), "s"(OFF_WQ + 3 * 98304), "s"(OFF_WQ + 4 * 98304), "s"(OFF_WQ + 5 * 98304) : "memory", "scc");
      static_for<45, 48>(wld);
      static_for<16, 16 + H1>(wld);
      VMW(3 + H1);
      cnt_add(smem, C_P3, ln);
      static_for<16 + H1, 45>(wld);
      __builtin_amdgcn_s_setprio(0);
      ST(0, 9);
      cnt_spin(smem, C_B, 12);
      VMW(29);
#pragma unroll
      for (int e = 0; e < 3; ++e) *(LAS bf16x8*)(size_t)(gp.wx + e * 1024) = wf[45 + e];
      static_for<22, 24>(wsum);
      asm volatile("s_cmp_lg_u32 %4, %5\n\ts_cbranch_scc1 1f\n\tv_smfmac_f32_16x16x64_bf16 %0, %2, v[120:127], %3\n\tv_smfmac_f32_16x16x64_bf16 %1, %2, v[128:135], %3\n1:"
                   : "+v"(cs[2]), "+v"(cs[1]) : "v"(one8), "v"(sidx), "s"(hb), "s"(hs), "{v[120:123]}"(wf[11]), "{v[124:127]}"(wf[12]), "{v[128:131]}"(wf[13]), "{v[132:135]}"(wf[14]) : "scc");
      VMW(12); static_for<7, 16>(wsum);
      VMW(6); static_for<16, 19>(wsum);
      VMW(2); static_for<19, 21>(wsum);
      VMW(0); static_for<21, 22>(wsum);
      asm volatile("s_nop 7" : "+v"(cs[0]), "+v"(cs[1]), "+v"(cs[2]));
#pragma unroll
      for (int e = 0; e < 3; ++e) *(LAS float*)(size_t)(gp.fr + 512 + e * 64) = (cs[e][0] + cs[e][1]) + (cs[(e + 2) % 3][2] + cs[(e + 2) % 3][3]);
      asm volatile("" ::: "memory");
      ST(0, 8);
    }
    {
      ConvPre pa2, pb2;
      u32x4 xa, xb;
      int ln2, l2, xo2, xo, so;
      u32 ua, rp0, rp1;
      float isc;
      g_blayer<true>(smem, wrs, g, stag, wf, gp, [&]() __attribute__((always_inline)) {
        ln2 = lane_id();
        l2 = 256 * g + 4 * ln2;
        xo2 = (int)opaque((u32)(OFF_X3 + (8 * g + (ln2 >> 3)) * XBS + kp * 128 + (ln2 & 1) * 64 + (ln2 & 6) * 8));
        xo = (int)opaque((u32)(OFF_X2 + (kp * XS_ + l2) * 4)); so = (int)opaque((u32)(OFF_ST2 + (256 * g + ln2) * 16));
        ua = opaque((u32)(L_U + (256 + l2) * 2));
        rp0 = opaque((u32)(L_REDA + (ln2 & 1) * 8192 + (g * 64 + (ln2 ^ ((ln2 >> 3) & 3))) * 16)); rp1 = opaque(rp0 ^ 16u);
        isc = *(const float*)(smem + L_MI + 4 * (ln2 & 1));
        asm volatile("" : "+v"(isc));
      });
#define VM0() __builtin_amdgcn_s_waitcnt(0x0F70)
      KSLP(9, SLP_C2);
      u32x4 gg[4];
      ST(1, 2);
      xa = ldw(wrs, xo);
      asm volatile("" : "+v"(xa));
      if (__builtin_expect(!x2_ok(xa), 0)) { FAILC(16); __builtin_amdgcn_s_sleep(SLP_XP); xa = x2_ld(wrs, kp, l2); x2_poll(wrs, kp, l2, xa); VM0(); }
      KSLP(10, SLP_S);
      asm volatile("" ::: "memory");
#pragma unroll
      for (int e = 0; e < 4; ++e) gg[e] = ldw(wrs, so + e * 1024);
      SB();
      conv_unp(pa2, xa);
      asm volatile("" : "+v"(gg[0]), "+v"(gg[1]), "+v"(gg[2]), "+v"(gg[3]));
      if (__builtin_expect(!st2_ok(gg), 0)) { FAILC(7); __builtin_amdgcn_s_sleep(SLP_SP); st2_ld(wrs, l2, gg); st2_poll(wrs, l2, gg); VM0(); }
      conv2_in<false>(smem, ua, pa2, xa, gg);
      ST(1, 3);
      lds_barrier();
      ST(1, 7);
      conv_res(pa2);
      const int so2 = (int)opaque((u32)(so + 16384));
      auto rq_b = [&]() __attribute__((always_inline)) {
        KSLP(11, SLP_C3);
        asm volatile("" ::: "memory");
        xb = ldws(wrs, xo, 4096);
        SB();
#pragma unroll
        for (int e = 0; e < 4; ++e) gg[e] = ldw(wrs, so2 + e * 1024);
        SB();
      };
      rq_b();
      __builtin_amdgcn_s_barrier();
      asm volatile("" ::: "memory");
      ST(1, 8);
      stw(wrs, xo2, conv_epi(rp0, rp1, 0, isc, pa2));
      SB();
      asm volatile("" : "+v"(xb), "+v"(gg[0]), "+v"(gg[1]), "+v"(gg[2]), "+v"(gg[3]) :: "memory");
      ST(1, 4);
      if (__builtin_expect(!x2st_ok(xb, gg), 0)) {
        FAILC(18);
        xb = x2_ld(wrs, kp, l2 + 1024);
        st2_ld(wrs, l2 + 1024, gg);
        if (!x2_ok(xb)) {
          x2_poll(wrs, kp, l2 + 1024, xb);
          st2_ld(wrs, l2 + 1024, gg);
        }
        st2_poll(wrs, l2 + 1024, gg);
        VM0();
      }
      ST(1, 1);
      const int rb = RB_OF(bid);
      if (XPB == 2) { x_dma(wrs, OFF_X3, rb, L_TA, g, ln2); SB(); }
      conv2_in<true>(smem, ua + 2048, pb2, xb, gg);
      ST(1, 5);
      lds_barrier();
      ST(1, 6);
      if (XPB == 1) { KSLP(12, SLP_XA2); x_dma(wrs, OFF_X3, rb, L_TA, g, ln2); SB(); }
      conv_res(pb2);
      if (XPB == 0) { KSLP(12, SLP_XA2); x_dma(wrs, OFF_X3, rb, L_TA, g, ln2); }
      SB();
      g_pre<false>(gp, g, stag);
      const int xo3 = (int)opaque((u32)(xo2 + 32 * XBS));
      if (GPE) __builtin_amdgcn_s_setprio(GPE);
      asm volatile("s_waitcnt vmcnt(0)\n\ts_barrier" ::: "memory");
      ST(1, 9);
      u32x4 ov = conv_epi(rp0, rp1, 16384, isc, pb2);
      stw(wrs, xo3, ov);
      if (GPE) __builtin_amdgcn_s_setprio(0);
      asm volatile("s_nop 1" :: "v"(ov));
    }
    g_blayer<false>(smem, wrs, g, stag, wf, gp, []() {});

    ST_OUT();
  }
}

__device__ __attribute__((used, noinline)) void k_text_end() {
  asm volatile(
  ".Lg_redo:\n\ts_waitcnt lgkmcnt(0)\n\t"
  "ds_read_b64_tr_b16 v[20:21], v0 offset:0\n\tds_read_b64_tr_b16 v[22:23], v0 offset:1024\n\ts_waitcnt lgkmcnt(0)\n\t"
  "v_mfma_f32_16x16x32_bf16 v[8:11], v[80:83], v[20:23], 0\n\tv_mfma_f32_16x16x32_bf16 v[48:51], v[20:23], v[84:87], 0\n\tv_mfma_f32_16x16x32_bf16 v[64:67], v[76:79], v[20:23], 0\n\t"
  "ds_read_b64_tr_b16 v[20:21], v0 offset:2048\n\tds_read_b64_tr_b16 v[22:23], v0 offset:3072\n\ts_waitcnt lgkmcnt(0)\n\t"
  "v_mfma_f32_16x16x32_bf16 v[8:11], v[92:95], v[20:23], v[8:11]\n\tv_mfma_f32_16x16x32_bf16 v[48:51], v[20:23], v[96:99], v[48:51]\n\tv_mfma_f32_16x16x32_bf16 v[64:67], v[88:91], v[20:23], v[64:67]\n\t"
  "ds_read_b64_tr_b16 v[20:21], v0 offset:8192\n\tds_read_b64_tr_b16 v[22:23], v0 offset:9216\n\ts_waitcnt lgkmcnt(0)\n\t"
  "v_mfma_f32_16x16x32_bf16 v[8:11], v[128:131], v[20:23], v[8:11]\n\tv_mfma_f32_16x16x32_bf16 v[48:51], v[20:23], v[132:135], v[48:51]\n\tv_mfma_f32_16x16x32_bf16 v[64:67], v[124:127], v[20:23], v[64:67]\n\t"
  "ds_read_b64_tr_b16 v[20:21], v0 offset:10240\n\tds_read_b64_tr_b16 v[22:23], v0 offset:11264\n\ts_waitcnt lgkmcnt(0)\n\t"
  "v_mfma_f32_16x16x32_bf16 v[8:11], v[140:143], v[20:23], v[8:11]\n\tv_mfma_f32_16x16x32_bf16 v[48:51], v[20:23], v[144:147], v[48:51]\n\tv_mfma_f32_16x16x32_bf16 v[64:67], v[136:139], v[20:23], v[64:67]\n\t"
  "ds_read_b64_tr_b16 v[20:21], v0 offset:16384\n\tds_read_b64_tr_b16 v[22:23], v0 offset:17408\n\ts_waitcnt lgkmcnt(0)\n\t"
  "v_mfma_f32_16x16x32_bf16 v[8:11], v[176:179], v[20:23], v[8:11]\n\tv_mfma_f32_16x16x32_bf16 v[48:51], v[20:23], v[180:183], v[48:51]\n\tv_mfma_f32_16x16x32_bf16 v[64:67], v[172:175], v[20:23], v[64:67]\n\t"
  "ds_read_b64_tr_b16 v[20:21], v0 offset:18432\n\tds_read_b64_tr_b16 v[22:23], v0 offset:19456\n\ts_waitcnt lgkmcnt(0)\n\t"
  "v_mfma_f32_16x16x32_bf16 v[8:11], v[188:191], v[20:23], v[8:11]\n\tv_mfma_f32_16x16x32_bf16 v[48:51], v[20:23], v[192:195], v[48:51]\n\tv_mfma_f32_16x16x32_bf16 v[64:67], v[184:187], v[20:23], v[64:67]\n\t"
  "ds_read_b64_tr_b16 v[20:21], v0 offset:24576\n\tds_read_b64_tr_b16 v[22:23], v0 offset:25600\n\ts_waitcnt lgkmcnt(0)\n\t"
  "v_mfma_f32_16x16x32_bf16 v[8:11], v[224:227], v[20:23], v[8:11]\n\tv_mfma_f32_16x16x32_bf16 v[48:51], v[20:23], v[228:231], v[48:51]\n\tv_mfma_f32_16x16x32_bf16 v[64:67], v[220:223], v[20:23], v[64:67]\n\t"
  "ds_read_b64_tr_b16 v[20:21], v0 offset:26624\n\tds_read_b64_tr_b16 v[22:23], v0 offset:27648\n\ts_waitcnt lgkmcnt(0)\n\t"
  "v_mfma_f32_16x16x32_bf16 v[8:11], v[236:239], v[20:23], v[8:11]\n\tv_mfma_f32_16x16x32_bf16 v[48:51], v[20:23], v[240:243], v[48:51]\n\tv_mfma_f32_16x16x32_bf16 v[64:67], v[232:235], v[20:23], v[64:67]\n\t"
  "ds_read_b64_tr_b16 v[20:21], v0 offset:4096\n\tds_read_b64_tr_b16 v[22:23], v0 offset:5120\n\ts_waitcnt lgkmcnt(0)\n\t"
  "v_mfma_f32_16x16x32_bf16 v[8:11], v[104:107], v[20:23], v[8:11]\n\tv_mfma_f32_16x16x32_bf16 v[48:51], v[20:23], v[108:111], v[48:51]\n\tv_mfma_f32_16x16x32_bf16 v[64:67], v[100:103], v[20:23], v[64:67]\n\t"
  "ds_read_b64_tr_b16 v[20:21], v0 offset:6144\n\tds_read_b64_tr_b16 v[22:23], v0 offset:7168\n\tds_read_b64_tr_b16 v[36:37], v0 offset:12288\n\tds_read_b64_tr_b16 v[38:39], v0 offset:13312\n\tds_read_b64_tr_b16 v[28:29], v0 offset:14336\n\tds_read_b64_tr_b16 v[30:31], v0 offset:15360\n\tds_read_b64_tr_b16 v[68:69], v0 offset:20480\n\tds_read_b64_tr_b16 v[70:71], v0 offset:21504\n\t"
  "s_waitcnt lgkmcnt(0)\n\ts_branch .Lg_back\n\t"
  ".Lc0_kh:\n\ts_mov_b32 vcc_hi, 0x1000000\n\t.Lc0_kh_s:\n\tds_read_b32 v101, v27 offset:48\n\ts_sub_u32 vcc_hi, vcc_hi, 1\n\ts_cselect_b32 vcc_lo, 0, 4\n\ts_waitcnt lgkmcnt(0)\n\tv_readfirstlane_b32 s61, v101\n\ts_nop 1\n\ts_cmp_lt_u32 s61, vcc_lo\n\ts_cbranch_scc1 .Lc0_kh_s\n\t"
  "s_waitcnt lgkmcnt(0)\n\ts_branch .Lc0_kh_b\n\t"
  ".Lc0_x0:\n\ts_mov_b32 vcc_hi, 0x1000000\n\t.Lc0_x0_s:\n\tds_read_b32 v101, v21 offset:8\n\ts_sub_u32 vcc_hi, vcc_hi, 1\n\ts_cselect_b32 vcc_lo, 0, 2\n\ts_waitcnt lgkmcnt(0)\n\tv_readfirstlane_b32 s61, v101\n\ts_nop 1\n\ts_cmp_lt_u32 s61, vcc_lo\n\ts_cbranch_scc1 .Lc0_x0_s\n\t"
  "ds_read_b128 v[144:147], v20 offset:2048\n\tds_read_b128 v[204:207], v20 offset:3072\n\ts_waitcnt lgkmcnt(0)\n\ts_branch .Lc0_x0_b\n\t"
  ".Lc0_ub:\n\ts_mov_b32 vcc_hi, 0x1000000\n\t.Lc0_ub_s:\n\tds_read_b32 v101, v27 offset:52\n\ts_sub_u32 vcc_hi, vcc_hi, 1\n\ts_cselect_b32 vcc_lo, 0, 4\n\ts_waitcnt lgkmcnt(0)\n\tv_readfirstlane_b32 s61, v101\n\ts_nop 1\n\ts_cmp_lt_u32 s61, vcc_lo\n\ts_cbranch_scc1 .Lc0_ub_s\n\t"
  "s_waitcnt lgkmcnt(0)\n\ts_branch .Lc0_ub_b\n\t"
  ".Lc0_p3:\n\ts_mov_b32 vcc_hi, 0x1000000\n\t.Lc0_p3_s:\n\tds_read_b32 v101, v27 offset:64\n\ts_sub_u32 vcc_hi, vcc_hi, 1\n\ts_cselect_b32 vcc_lo, 0, 4\n\ts_waitcnt lgkmcnt(0)\n\tv_readfirstlane_b32 s61, v101\n\ts_nop 1\n\ts_cmp_lt_u32 s61, vcc_lo\n\ts_cbranch_scc1 .Lc0_p3_s\n\t"
  "s_waitcnt lgkmcnt(0)\n\ts_branch .Lc0_p3_b\n\t"
  ".Lc0_x4:\n\ts_mov_b32 vcc_hi, 0x1000000\n\t.Lc0_x4_s:\n\tds_read_b32 v36, v27 offset:16\n\ts_sub_u32 vcc_hi, vcc_hi, 1\n\ts_cselect_b32 vcc_lo, 0, 4\n\ts_waitcnt lgkmcnt(0)\n\tv_readfirstlane_b32 s61, v36\n\ts_nop 1\n\ts_cmp_lt_u32 s61, vcc_lo\n\ts_cbranch_scc1 .Lc0_x4_s\n\t"
  "ds_read_b128 v[40:43], v20 offset:59392\n\tds_read_b128 v[44:47], v20 offset:60416\n\ts_waitcnt lgkmcnt(0)\n\ts_branch .Lc0_x4_b\n\t"
  ".Lc1_kh:\n\ts_mov_b32 vcc_hi, 0x1000000\n\t.Lc1_kh_s:\n\tds_read_b32 v101, v27 offset:48\n\ts_sub_u32 vcc_hi, vcc_hi, 1\n\ts_cselect_b32 vcc_lo, 0, 4\n\ts_waitcnt lgkmcnt(0)\n\tv_readfirstlane_b32 s61, v101\n\ts_nop 1\n\ts_cmp_lt_u32 s61, vcc_lo\n\ts_cbranch_scc1 .Lc1_kh_s\n\t"
  "s_waitcnt lgkmcnt(0)\n\ts_branch .Lc1_kh_b\n\t"
  ".Lc1_x0:\n\ts_mov_b32 vcc_hi, 0x1000000\n\t.Lc1_x0_s:\n\tds_read_b32 v101, v21 offset:8\n\ts_sub_u32 vcc_hi, vcc_hi, 1\n\ts_cselect_b32 vcc_lo, 0, 2\n\ts_waitcnt lgkmcnt(0)\n\tv_readfirstlane_b32 s61, v101\n\ts_nop 1\n\ts_cmp_lt_u32 s61, vcc_lo\n\ts_cbranch_scc1 .Lc1_x0_s\n\t"
  "ds_read_b128 v[148:151], v20 offset:0\n\tds_read_b128 v[204:207], v20 offset:1024\n\ts_waitcnt lgkmcnt(0)\n\ts_branch .Lc1_x0_b\n\t"
  ".Lc1_ub:\n\ts_mov_b32 vcc_hi, 0x1000000\n\t.Lc1_ub_s:\n\tds_read_b32 v101, v27 offset:52\n\ts_sub_u32 vcc_hi, vcc_hi, 1\n\ts_cselect_b32 vcc_lo, 0, 4\n\ts_waitcnt lgkmcnt(0)\n\tv_readfirstlane_b32 s61, v101\n\ts_nop 1\n\ts_cmp_lt_u32 s61, vcc_lo\n\ts_cbranch_scc1 .Lc1_ub_s\n\t"
  "s_waitcnt lgkmcnt(0)\n\ts_branch .Lc1_ub_b\n\t"
  ".Lc1_p3:\n\ts_mov_b32 vcc_hi, 0x1000000\n\t.Lc1_p3_s:\n\tds_read_b32 v101, v27 offset:64\n\ts_sub_u32 vcc_hi, vcc_hi, 1\n\ts_cselect_b32 vcc_lo, 0, 4\n\ts_waitcnt lgkmcnt(0)\n\tv_readfirstlane_b32 s61, v101\n\ts_nop 1\n\ts_cmp_lt_u32 s61, vcc_lo\n\ts_cbranch_scc1 .Lc1_p3_s\n\t"
  "s_waitcnt lgkmcnt(0)\n\ts_branch .Lc1_p3_b\n\t"
  ".Lc1_x4:\n\ts_mov_b32 vcc_hi, 0x1000000\n\t.Lc1_x4_s:\n\tds_read_b32 v36, v27 offset:16\n\ts_sub_u32 vcc_hi, vcc_hi, 1\n\ts_cselect_b32 vcc_lo, 0, 4\n\ts_waitcnt lgkmcnt(0)\n\tv_readfirstlane_b32 s61, v36\n\ts_nop 1\n\ts_cmp_lt_u32 s61, vcc_lo\n\ts_cbranch_scc1 .Lc1_x4_s\n\t"
  "ds_read_b128 v[40:43], v20 offset:57344\n\tds_read_b128 v[44:47], v20 offset:58368\n\ts_waitcnt lgkmcnt(0)\n\ts_branch .Lc1_x4_b\n\t"
  ".Lcw_end:");
}

extern "C" void kernel_launch(void* const* d_in, const int* in_sizes, int n_in, void* d_out, int out_size,
                              void* d_ws, size_t ws_size, hipStream_t stream) {
  (void)hipFuncSetAttribute((const void*)k_mega, hipFuncAttributeMaxDynamicSharedMemorySize, MEGA_LDS);
  k_mega<<<256, 512, MEGA_LDS, stream>>>((const u16*)d_in[0], (const u16*)d_in[1], (const u16*)d_in[2], (const u16*)d_in[3],
                                         (const u16*)d_in[4], (const u16*)d_in[5], (const u16*)d_in[6], (const u16*)d_in[7],
                                         (const u16*)d_in[8], (const u16*)d_in[10], (const u16*)d_in[11], (char*)d_ws,
                                         (u32*)d_out);
}
#if defined(__HIP_DEVICE_COMPILE__)
#pragma clang attribute pop
#endif
```

```cpp
#if defined(__HIP_DEVICE_COMPILE__)
#pragma clang attribute push (__attribute__((target("no-packed-fp32-ops"))), apply_to = function)
#endif
#include <hip/hip_runtime.h>
#include <hip/hip_bf16.h>
#include <stdint.h>
#define TIDX ((int)__builtin_amdgcn_workitem_id_x())
#define BIDX ((int)__builtin_amdgcn_workgroup_id_x())
#define ST(set, k)
#define ST_INIT()
#define ST_OUT()
#define KATTR
#define FAILC(k)

#define ST_FIX()
#define CST(k)
#define GST(k)
typedef unsigned short u16;
typedef unsigned int u32;
typedef unsigned long long u64;
using bf16x8 = __attribute__((ext_vector_type(8))) short;
using bf16x4 = __attribute__((ext_vector_type(4))) short;
using f16x8 = __attribute__((ext_vector_type(8))) _Float16;
using f16x4 = __attribute__((ext_vector_type(4))) _Float16;
using f32x2 = __attribute__((ext_vector_type(2))) float;
using f32x4 = __attribute__((ext_vector_type(4))) float;
using u32x2 = __attribute__((ext_vector_type(2))) unsigned;
using u32x4 = __attribute__((ext_vector_type(4))) unsigned;
#define LAS __attribute__((address_space(3)))
#define GAS __attribute__((address_space(1)))

#define D_ 512
#define NS_ 128
#define XS_ 2080

#define MB_ (1 << 20)
#define XTO(kp_) (OFF_O1 + (((kp_) >> 4) & 1) * MB_ + ((kp_) >> 5) * 131072 + ((kp_) & 15) * 8192)
#define XTS 8192
#define RB_OF(b_) (((b_) & 7) * 4 + ((b_) >> 6))
#define HB_OF(b_) (((b_) >> 3) & 7)
#define OFF_ST0 (5 * MB_)
#define OFF_TS (OFF_ST0 + 16384)
#define OFF_SG (OFF_ST0 + 32768)
#define OFF_ST2 (OFF_ST0 + 65536)
#define OFF_EP (OFF_ST0 + 131072)
#define OFF_WQ (9 * MB_ - 262144)
#define OFF_WO (10 * MB_ + 393216)
#define OFF_X1 (11 * MB_)
#define OFF_O1 (14 * MB_)
#define OFF_X2 (16 * MB_)
#define OFF_X3 (19 * MB_)
#define OFF_O2 (22 * MB_)
#define OFF_KV2 (26 * MB_)
#define KVO(L1_, rb_, h_) (((L1_) ? OFF_O2 : OFF_KV2) + (((h_) >> 3) & 1) * MB_ + (rb_) * 32768 + (((h_) >> 2) & 1) * 16384 + ((h_) >> 4) * 8192 + ((h_) & 3) * 2048)
#define WSB (32 * MB_)
#define XBS 32896
#define SPIN_CAP (1u << 14)
#define B6 0
#define OUTA 2
#define OUTB 0
#define WLD 8
#define SLP_FL 4
#define SLP_WE 0
#define R1F 3
#define D9 1450
#define WS5 5
#define SLP_B4 4
#define H1 9
#define DB2 450
#define D5 350
#define DQ2 0
#define DO2 900
#define SLP_U 2
#define SLP_F 2
#define SLP_G 0
#define R1D 1
#define WOE 4
#define SLP_H1 0
#define SLP_H2 2
#define WO0 0
#define SLP_XA2 6
#define CPF 5
#define SLP_XP 2
#define SLP_SP 4
#define SLP_S 6
#define SLP_XA1 4
#define SLP_XB1 6
#define SLP_OB1 6
#define SLP_OA1 7
#define SLP_C2 3
#define SLP_C3 15

__device__ __forceinline__ float bf2f(u16 v) { return __uint_as_float(((u32)v) << 16); }
typedef __attribute__((ext_vector_type(2))) __bf16 bf16x2v;
__device__ __forceinline__ u32 pk2(float a, float b) {
  f32x2 f = {a, b};
  bf16x2v v = __builtin_convertvector(f, bf16x2v);
  return __builtin_bit_cast(u32, v);
}
__device__ __forceinline__ float rbf(float f) { return __uint_as_float(pk2(f, 0.f) << 16); }
__device__ __forceinline__ float lo2f(u32 w) { return __uint_as_float(w << 16); }
__device__ __forceinline__ float hi2f(u32 w) { return __uint_as_float(w & 0xFFFF0000u); }
template <int CTRL>
__device__ __forceinline__ float dpp_mov(float v) {
  return __int_as_float(__builtin_amdgcn_update_dpp(0, __float_as_int(v), CTRL, 0xf, 0xf, false));
}
#define DPP_XOR1 0xB1
#define DPP_XOR2 0x4E
#define DPP_ROR8 0x128
template <int CTRL, int RMASK>
__device__ __forceinline__ float dpp_add(float v) {
  return v + __int_as_float(__builtin_amdgcn_update_dpp(0, __float_as_int(v), CTRL, RMASK, 0xf, false));
}
__device__ __forceinline__ void wave_sum3(float& a, float& b, float& c) {
#define WS3(CT_, RM_) a = dpp_add<CT_, RM_>(a); b = dpp_add<CT_, RM_>(b); c = dpp_add<CT_, RM_>(c); __builtin_amdgcn_sched_barrier(0);
  WS3(0x111, 0xf) WS3(0x112, 0xf) WS3(0x114, 0xf) WS3(0x118, 0xf) WS3(0x142, 0xa) WS3(0x143, 0xc)
  a = __int_as_float(__builtin_amdgcn_readlane(__float_as_int(a), 63));
  b = __int_as_float(__builtin_amdgcn_readlane(__float_as_int(b), 63));
  c = __int_as_float(__builtin_amdgcn_readlane(__float_as_int(c), 63));
}
__device__ __forceinline__ float rmax(float a, float b) {
  float r;
  asm("v_max_f32 %0, %1, %2" : "=v"(r) : "v"(a), "v"(b));
  return r;
}
__device__ __forceinline__ void lds_barrier() {
  asm volatile("s_waitcnt lgkmcnt(0)" ::: "memory");
  __builtin_amdgcn_s_barrier();
  asm volatile("" ::: "memory");
}
__device__ __forceinline__ void cexp2(float re2, float rev, float& orr, float& oi) {
  float e = __builtin_amdgcn_exp2f(re2);
  rev = __builtin_amdgcn_fractf(rev);
  float s = __builtin_amdgcn_sinf(rev);
  float c = __builtin_amdgcn_cosf(rev);
  orr = e * c;
  oi = e * s;
}
__device__ __forceinline__ void cexp3(float re2, float rev, float& e, float& c, float& s) {
  e = __builtin_amdgcn_exp2f(re2);
  rev = __builtin_amdgcn_fractf(rev);
  s = __builtin_amdgcn_sinf(rev); c = __builtin_amdgcn_cosf(rev);
}
__device__ __forceinline__ float dot2acc(u32 a, u32 b, float c) {
  return __builtin_amdgcn_fdot2_f32_bf16(__builtin_bit_cast(bf16x2v, a), __builtin_bit_cast(bf16x2v, b), c, false);
}
__device__ __forceinline__ u32 opaque(u32 v) { asm volatile("" : "+v"(v)); return v; }
__device__ __forceinline__ int lane_id() { return (int)__builtin_amdgcn_mbcnt_hi(~0u, __builtin_amdgcn_mbcnt_lo(~0u, opaque(0u))); }
__device__ __forceinline__ u32 pack_stats(float mean, float var) {
  float vb = rbf(var);
  float ve = rbf(vb + 1e-5f);
  float sd = __builtin_amdgcn_sqrtf(ve);
  return pk2(mean, sd);
}

#define SB() __builtin_amdgcn_sched_barrier(0)

#define KADD(id_, t_, v_) kadd_<(v_)>(t_)
#define KSLP(id_, v_) do { if ((v_) > 0) __builtin_amdgcn_s_sleep(v_); } while (0)
template <int V>
__device__ __forceinline__ u32 kadd_(u32 t) { u32 r; asm("s_add_i32 %0, %1, %2" : "=s"(r) : "s"(__builtin_amdgcn_readfirstlane(t)), "n"(V) : "scc"); return r; }
template <int V> struct IC { static constexpr int value = V; };
template <int B, int E, class F>
__device__ __forceinline__ void static_for(F& f) { if constexpr (B < E) { f(IC<B>{}); static_for<B + 1, E>(f); } }
typedef __amdgpu_buffer_rsrc_t rsrc_t;
__device__ __forceinline__ u32x4 ldw(rsrc_t r, int off) { return __builtin_amdgcn_raw_buffer_load_b128(r, off, 0, 16); }
__device__ __forceinline__ u32x4 ldws(rsrc_t r, int off, int soff) { return __builtin_amdgcn_raw_buffer_load_b128(r, off, soff, 16); }
__device__ __forceinline__ void stw(rsrc_t r, int off, u32x4 v) { __builtin_amdgcn_raw_buffer_store_b128(v, r, off, 0, 16); }
__device__ __forceinline__ u32 umax4(u32 m, u32x4 v) { return max(max(max(m, v[0]), v[1]), max(v[2], v[3])); }
template <int CTRL>
__device__ __forceinline__ u32 dpp_u(u32 v) { return (u32)__builtin_amdgcn_update_dpp(0, (int)v, CTRL, 0xf, 0xf, false); }

#define CSTR 4160
#define L_U 33280
#define L_TA 42496
#define L_TB (L_TA + 32768)
#define L_QB (L_TB + 32768)
#define L_TH (L_QB + 16384)
#define L_SP (L_QB + 32768)
#define L_FS (L_SP + 4096)
#define L_MI (L_FS + 3072)
#define L_XS (L_MI + 80)
#define L_WX (L_XS + 1024)
#define MEGA_LDS (L_WX + 12288)
#define L_REDA L_QB
#define L_REDB (L_QB + 16384)
#define L_XC L_REDB
#define L_X (L_REDB + 8192)
#define TBS 3136
#define L_WR (L_QB + 24576)
#define L_WS L_TA
#define L_KT L_TB
#define KTO 65024
#define FTS 528
#define L_P0 (L_FS + 2048)
#define STAT_SCALE 33554432.0f
#define C_OB 20
#define C_OT 24
#define M_FX 72
#define C_X2 28
#define C_G2 32
#define C_A 36
#define C_B 40
#define C_F 44
#define C_KH 48
#define C_P2 52
#define C_XC 8
#define C_P3 64
#define M_RD 68
#define M_DL 76
#define C_OV 28
#define C_U 56
#define C_W 60

typedef __attribute__((ext_vector_type(2))) _Float16 f16x2v;
typedef __attribute__((ext_vector_type(2))) short i16x2;
__device__ __forceinline__ u32 pkh(float a, float b) {
  u32 r;
  asm("v_cvt_pk_f16_f32 %0, %1, %2" : "=v"(r) : "v"(a), "v"(b));
  return r;
}
__device__ __forceinline__ u32 pkhn(float a, float b) {
  u32 r;
  asm("v_cvt_pk_f16_f32 %0, %1, -%2" : "=v"(r) : "v"(a), "v"(b));
  return r;
}
__device__ __forceinline__ u32 tb_addr(u32 bits2, u32 base2) {
  i16x2 b = __builtin_elementwise_max(__builtin_bit_cast(i16x2, bits2), (i16x2){0x3F00, 0x3F00});
  u32 r;
  asm("v_pk_mad_u16 %0, %1, 2, %2 op_sel_hi:[1,0,1]" : "=v"(r) : "v"(b), "s"(base2));
  return r;
}
__device__ __forceinline__ int bexp(float b) { return min(max((int)((__float_as_uint(b) >> 23) & 0xFF), 20), 240); }
__device__ __forceinline__ void cnt_add(char* smem, int off, int lane) {
  asm volatile("s_mov_b64 exec, 1\n\tds_add_u32 %0, %1\n\ts_mov_b64 exec, -1" :: "v"(L_MI + off), "v"(1u) : "memory");
}
__device__ __forceinline__ void cnt_wait(char* smem, int off, u32 target) {
#pragma unroll 1
  for (unsigned n = 0; n < (1u << 23) && (u32)__builtin_amdgcn_readfirstlane(__hip_atomic_load((LAS u32*)(smem + L_MI + off), __ATOMIC_RELAXED, __HIP_MEMORY_SCOPE_WORKGROUP)) < target; ++n)
    __builtin_amdgcn_s_sleep(2);
  asm volatile("" ::: "memory");
}
__device__ __forceinline__ void cnt_spin(char* smem, int off, u32 target) {
  for (unsigned n = 0; n < (1u << 24) && (u32)__builtin_amdgcn_readfirstlane(__hip_atomic_load((LAS u32*)(smem + L_MI + off), __ATOMIC_RELAXED, __HIP_MEMORY_SCOPE_WORKGROUP)) < target; ++n) {}
  asm volatile("" ::: "memory");
}
__device__ __forceinline__ void cnt_spin2(int o0, u32 t0, int o1, u32 t1) {
  for (unsigned n = 0; n < (1u << 24); ++n) {
    asm volatile("" ::: "memory");
    const u32 c0 = *(const volatile LAS u32*)(size_t)(u32)(L_MI + o0), c1 = *(const volatile LAS u32*)(size_t)(u32)(L_MI + o1);
    if (__builtin_amdgcn_readfirstlane(min((int)(c0 - t0), (int)(c1 - t1))) >= 0) break;
  }
  asm volatile("" ::: "memory");
}
__device__ __forceinline__ u32 now_() { return (u32)__builtin_amdgcn_s_memtime(); }
__device__ __forceinline__ u32 tmax(u32 a, u32 b) { return (int)(a - b) > 0 ? a : b; }
__device__ __forceinline__ void gate(u32 t) {
  u32 n = 256;
  asm volatile("1:\n\ts_memtime vcc\n\ts_sub_u32 %0, %0, 1\n\ts_waitcnt lgkmcnt(0)\n\ts_cbranch_scc1 2f\n\ts_sub_i32 vcc_lo, %1, vcc_lo\n\ts_cmp_lt_i32 vcc_lo, 1\n\ts_cbranch_scc1 2f\n\t"
               "s_cmp_lt_i32 vcc_lo, 64\n\ts_cbranch_scc1 1b\n\ts_sleep 1\n\ts_cmp_lt_i32 vcc_lo, 0x200\n\ts_cbranch_scc1 1b\n\ts_sleep 6\n\ts_branch 1b\n2:"
               : "+s"(n) : "s"(__builtin_amdgcn_readfirstlane(t)) : "memory", "vcc", "scc");
}
__device__ __forceinline__ u32 x0_mark(u32 tag) { return 0xFF807F80u + (((tag & 31u) + 1u) << 16) + ((tag >> 5) & 31u) + 1u; }
__device__ __forceinline__ bool tags_ok(const u32x4& f, u32 tag) {
  return __builtin_amdgcn_ballot_w64((opaque((f[0] ^ tag) | (f[1] ^ tag)) | opaque((f[2] ^ tag) | (f[3] ^ tag))) != 0u) == 0ull;
}
__device__ __forceinline__ void tags_wait(u32x4& f, rsrc_t wrs, int fo, u32 tag, int fid) {
  if (__builtin_expect(!tags_ok(f, tag), 0)) {
    for (unsigned spins = 0; spins <= SPIN_CAP; ++spins) {
      FAILC(fid);
      __builtin_amdgcn_s_sleep(SLP_F);
      asm volatile("" ::: "memory");
      if (tags_ok(ldw(wrs, fo), tag)) break;
    }
    __builtin_amdgcn_s_waitcnt(0x0F70);
  }
  asm volatile("" ::: "memory");
}
__device__ __forceinline__ void build_copies(char* smem, int cc, int ch, const u32 (&kw)[6]) {
  char* cop = smem + cc * 4 * CSTR + ch * 16;
  u32 sh[5];
#pragma unroll
  for (int k = 0; k < 5; ++k) sh[k] = __builtin_amdgcn_alignbit(kw[k + 1], kw[k], 16);
  u32x4 c0 = {kw[0], kw[1], kw[2], kw[3]}, c1 = {sh[0], sh[1], sh[2], sh[3]}, c2 = {kw[1], kw[2], kw[3], kw[4]}, c3 = {sh[1], sh[2], sh[3], sh[4]};
  asm volatile("" : "+v"(c0), "+v"(c1), "+v"(c2), "+v"(c3));
  *(u32x4*)(cop + 0 * CSTR) = c0; *(u32x4*)(cop + 1 * CSTR) = c1; *(u32x4*)(cop + 2 * CSTR) = c2; *(u32x4*)(cop + 3 * CSTR) = c3;
  asm volatile("s_nop 1" :: "v"(c0), "v"(c1), "v"(c2), "v"(c3) : "memory");
}
struct ConvPre { f32x4 x0, x1; };
__device__ __forceinline__ void conv_unp(ConvPre& P, u32x4 xw) {
#pragma unroll
  for (int e = 0; e < 4; ++e) { P.x0[e] = lo2f(xw[e]); P.x1[e] = hi2f(xw[e]); }
  asm volatile("" : "+v"(P.x0), "+v"(P.x1));
}
__device__ __forceinline__ u32x4 x2_ld(rsrc_t wrs, int kp, int l0) { return ldw(wrs, OFF_X2 + (kp * XS_ + l0) * 4); }
__device__ __forceinline__ bool x2_ok(const u32x4& xw) { return __all(umax4(0u, xw) != 0xFFFFFFFFu); }
__device__ __forceinline__ void x2_poll(rsrc_t wrs, int kp, int l0, u32x4& xw) {
  for (unsigned spins = 0; !x2_ok(xw) && spins <= SPIN_CAP; ++spins) {
    FAILC(17);
    __builtin_amdgcn_s_sleep(SLP_XP);
    asm volatile("" ::: "memory");
    xw = x2_ld(wrs, kp, l0);
  }
}
__device__ __forceinline__ void st2_ld(rsrc_t wrs, int l0, u32x4 (&g)[4]) {
#pragma unroll
  for (int e = 0; e < 4; ++e) g[e] = ldw(wrs, OFF_ST2 + ((l0 & ~255) + ((l0 >> 2) & 63)) * 16 + e * 1024);
}
__device__ __forceinline__ bool st2_ok(const u32x4 (&g)[4]) {
  u32 ok = 32u;
#pragma unroll
  for (int e = 0; e < 4; ++e) ok &= g[e][0] & g[e][2];
  return __all(ok != 0u);
}
__device__ __forceinline__ bool x2st_ok(const u32x4& xw, const u32x4 (&g)[4]) {
  u32 ok = umax4(0u, xw) != 0xFFFFFFFFu ? 32u : 0u;
#pragma unroll
  for (int e = 0; e < 4; ++e) ok &= g[e][0] & g[e][2];
  return __all(ok != 0u);
}
__device__ __forceinline__ void st2_poll(rsrc_t wrs, int l0, u32x4 (&g)[4]) {
  for (unsigned spins = 0; !st2_ok(g) && spins <= SPIN_CAP; ++spins) {
    FAILC(8);
    __builtin_amdgcn_s_sleep(SLP_SP);
    asm volatile("" ::: "memory");
    st2_ld(wrs, l0, g);
  }
}
template <bool UNP>
__device__ __forceinline__ void conv2_in(char* smem, u32 ua, ConvPre& P, u32x4 xw, u32x4 (&g)[4]) {
  f32x4 rs, nm;
#pragma unroll
  for (int e = 0; e < 4; ++e) {
    const float c1 = 4294967296.0f / (64.0f * 512.0f * STAT_SCALE), c2 = 1.0f / 4294967296.0f;
    const float t1 = fmaf((float)g[e][0], c2, (float)(int)g[e][1]), t2 = fmaf((float)g[e][2], c2, (float)(int)g[e][3]);
    const float m1 = -c1 * t1;
    rs[e] = __builtin_amdgcn_rsqf(fmaf(rmax(fmaf(m1, t1, t2), 0.f), c1, 1e-5f));
    nm[e] = m1 * rs[e];
  }
  if (UNP) conv_unp(P, xw);
  u32x2 u0 = {pkh(fmaf(P.x0[0], rs[0], nm[0]), fmaf(P.x0[1], rs[1], nm[1])), pkh(fmaf(P.x0[2], rs[2], nm[2]), fmaf(P.x0[3], rs[3], nm[3]))};
  u32x2 u1 = {pkh(fmaf(P.x1[0], rs[0], nm[0]), fmaf(P.x1[1], rs[1], nm[1])), pkh(fmaf(P.x1[2], rs[2], nm[2]), fmaf(P.x1[3], rs[3], nm[3]))};
  asm volatile("" : "+v"(u0), "+v"(u1));
  *(LAS u32x2*)(size_t)ua = u0;
  *(LAS u32x2*)(size_t)(ua + 4608) = u1;
  asm volatile("s_nop 1" :: "v"(u0), "v"(u1) : "memory");
}
__device__ __forceinline__ void pair_x(f32x4& lo, f32x4& hi, const f32x4& a, const f32x4& b) {
#define PXD " quad_perm:[1,0,3,2] row_mask:0xf bank_mask:0xf\n\t"
  asm volatile("s_mov_b64 vcc, %16\n\ts_nop 0\n\t"
               "v_cndmask_b32_dpp %0, %12, %8, vcc" PXD "v_cndmask_b32_dpp %1, %13, %9, vcc" PXD "v_cndmask_b32_dpp %2, %14, %10, vcc" PXD "v_cndmask_b32_dpp %3, %15, %11, vcc" PXD
               "s_not_b64 vcc, vcc\n\t"
               "v_cndmask_b32_dpp %4, %8, %12, vcc" PXD "v_cndmask_b32_dpp %5, %9, %13, vcc" PXD "v_cndmask_b32_dpp %6, %10, %14, vcc" PXD "v_cndmask_b32_dpp %7, %11, %15, vcc" PXD
               : "=&v"(lo[0]), "=&v"(lo[1]), "=&v"(lo[2]), "=&v"(lo[3]), "=&v"(hi[0]), "=&v"(hi[1]), "=&v"(hi[2]), "=&v"(hi[3])
               : "v"(a[0]), "v"(a[1]), "v"(a[2]), "v"(a[3]), "v"(b[0]), "v"(b[1]), "v"(b[2]), "v"(b[3]), "s"(0x5555555555555555ull) : "vcc", "scc");
}
__device__ __forceinline__ void conv_res(ConvPre& P) {
  f32x4 lo, hi;
  pair_x(lo, hi, P.x0, P.x1);
  P.x0 = lo; P.x1 = hi;
}
__device__ __forceinline__ u32x4 conv_epi(u32 rp0, u32 rp1, int off, float isc, const ConvPre& P) {
  const f32x4 a0 = *(const LAS f32x4*)(size_t)(rp0 + off), a1 = *(const LAS f32x4*)(size_t)(rp0 + off + 4096), b0 = *(const LAS f32x4*)(size_t)(rp1 + off), b1 = *(const LAS f32x4*)(size_t)(rp1 + off + 4096);
  __builtin_amdgcn_sched_barrier(0);
  u32x4 r;
#pragma unroll
  for (int e = 0; e < 2; ++e) {
    r[e] = pk2(fmaf(a1[2 * e], isc, fmaf(a0[2 * e], isc, P.x0[2 * e])), fmaf(a1[2 * e + 1], isc, fmaf(a0[2 * e + 1], isc, P.x0[2 * e + 1])));
    r[2 + e] = pk2(fmaf(b1[2 * e], isc, fmaf(b0[2 * e], isc, P.x1[2 * e])), fmaf(b1[2 * e + 1], isc, fmaf(b0[2 * e + 1], isc, P.x1[2 * e + 1])));
  }
  return r;
}
__device__ __forceinline__ void dma4k(rsrc_t r, int voff, int soff, int lds) {
  asm volatile("s_mov_b32 m0, %3\n\ts_nop 0\n\t"
               "buffer_load_dwordx4 %0, %1, %2 offen sc1 lds\n\t"
               "buffer_load_dwordx4 %0, %1, %2 offen offset:1024 sc1 lds\n\t"
               "buffer_load_dwordx4 %0, %1, %2 offen offset:2048 sc1 lds\n\t"
               "buffer_load_dwordx4 %0, %1, %2 offen offset:3072 sc1 lds"
               :: "v"(voff), "s"(r), "s"(soff), "s"(lds) : "memory");
}
__device__ __forceinline__ void dma1k(rsrc_t r, int voff, int soff, int lds) {
  asm volatile("s_mov_b32 m0, %3\n\ts_nop 0\n\tbuffer_load_dwordx4 %0, %1, %2 offen sc1 lds" :: "v"(voff), "s"(r), "s"(soff), "s"(lds) : "memory");
}
__device__ __forceinline__ int x_lane(int lane) { return ((lane & 31) >> 1) * 64 + (lane >> 5) * 32 + (lane & 1) * 16; }
__device__ __forceinline__ void x_dma(rsrc_t wrs, int xin, int rb, int tile, int a, int lane) {
  const int vo = x_lane(lane);
  dma4k(wrs, vo, xin + rb * XBS + a * 8192, tile + a * 8192);
  dma4k(wrs, vo, xin + rb * XBS + a * 8192 + 4096, tile + a * 8192 + 4096);
  if (tile == L_TB) {
    int so = xin + (rb - 1) * XBS + a * 8192;
    asm volatile("s_mov_b32 m0, %3\n\ts_nop 0\n\tbuffer_load_dwordx4 %1, %2, %0 offen sc1 lds\n\t"
                 "s_sub_u32 m0, m0, 1024\n\ts_nop 0\n\tbuffer_load_dwordx4 %1, %2, %0 offen offset:2048 sc1 lds\n\t"
                 "s_add_u32 m0, m0, 3072\n\ts_add_u32 %0, %0, 4096\n\tbuffer_load_dwordx4 %1, %2, %0 offen sc1 lds\n\t"
                 "s_sub_u32 m0, m0, 1024\n\ts_nop 0\n\tbuffer_load_dwordx4 %1, %2, %0 offen offset:2048 sc1 lds"
                 : "+s"(so) : "v"((lane >> 1) * 64 + 32 + (lane & 1) * 16), "s"(wrs), "s"(L_TH + a * 4096) : "memory", "scc");
  }
}
template <int S>
__device__ __forceinline__ bf16x8 ld_tr8(const LAS char* p) {
  const bf16x4 lo = __builtin_amdgcn_ds_read_tr16_b64_v4i16((LAS bf16x4*)p), hi = __builtin_amdgcn_ds_read_tr16_b64_v4i16((LAS bf16x4*)(p + S));
  return __builtin_shufflevector(lo, hi, 0, 1, 2, 3, 4, 5, 6, 7);
}

struct ConvK { u32 ah, ah2, bp, ra; f16x8 k[16]; };
__device__ __forceinline__ void a_conv_pre(ConvK& C, int a, int lane) {
  lane = (int)opaque((u32)lane);
  const int c = a >> 1, qq = a & 1, j16 = lane & 15, kg = lane >> 4;
  const int y00 = 2031 - j16 + 8 * kg - 128 * qq;
  const int sg = y00 & 3;
  C.ah = opaque((u32)(c * 4 * CSTR + sg * CSTR + 2 * (y00 - sg) - 3776));
  C.ah2 = opaque(C.ah + 8);
  C.bp = opaque((u32)(L_U + c * 4608 + (256 + 16 * j16 - 16 + 8 * kg - 128 * qq) * 2 - 192));
  C.ra = opaque((u32)(L_REDA + (a * 256 + 4 * j16 + (kg ^ ((j16 >> 1) & 3) ^ c)) * 16));
#pragma unroll
  for (int e = 0; e < 16; ++e) {
    const int ao = 3776 - (512 * (e & 3) + 64 * (e >> 2));
    const f16x4 h0 = *(const LAS f16x4*)(size_t)(C.ah + ao), h1 = *(const LAS f16x4*)(size_t)(C.ah2 + ao);
    C.k[e] = __builtin_shufflevector(h0, h1, 0, 1, 2, 3, 4, 5, 6, 7);
    __builtin_amdgcn_sched_barrier(0);
  }
#pragma unroll
  for (int e = 0; e < 16; ++e) asm volatile("" : "+v"(C.k[e]));
}
template <bool L1>
__device__ __forceinline__ void a_conv(char* smem, rsrc_t wrs, int rb, int a, const ConvK& C) {
  const LAS char* ah = (const LAS char*)(size_t)C.ah;
  const LAS char* ah2 = (const LAS char*)(size_t)C.ah2;
  const LAS char* bp = (const LAS char*)(size_t)C.bp;
  LAS f32x4* ra = (LAS f32x4*)(size_t)C.ra;
  f32x4 acc[8];
#pragma unroll
  for (int e = 0; e < 8; ++e) acc[e] = (f32x4){0.f, 0.f, 0.f, 0.f};
  f16x8 bf[4][8], fh[4][8];
#define S_RR(s_) ((s_) < 16 ? (s_) >> 2 : (s_) < 44 ? ((s_) - 16) / 7 : ((s_) - 44) >> 2)
#define S_MU(s_) ((s_) < 16 ? (s_) & 3 : (s_) < 44 ? ((s_) - 16) % 7 + 1 : ((s_) - 44) & 3)
#define S_NEW(s_) ((s_) < 16 || ((s_) < 44 && S_MU(s_) >= 4))
#define LDA(s_) do { const int ao_ = 3776 - (512 * S_MU(s_) + 64 * S_RR(s_));                                        \
      f16x4 h0_ = *(const LAS f16x4*)(ah + ao_), h1_ = *(const LAS f16x4*)(ah2 + ao_);                                \
      fh[S_RR(s_)][S_MU(s_)] = __builtin_shufflevector(h0_, h1_, 0, 1, 2, 3, 4, 5, 6, 7); } while (0)
#define LDB(r_, t_) bf[r_][t_] = *(const LAS f16x8*)(bp + 512 * (t_) + 192 - 64 * (r_))
#define CSTEPS(s0_, s1_)                                                                                               \
  _Pragma("unroll") for (int st = s0_; st < s1_; ++st) {                                                               \
    const int rr = S_RR(st), mu = S_MU(st);                                                                           \
    if (S_NEW(st + CPF) && (L1 || st + CPF >= 16)) LDA(st + CPF);                                                     \
    if (st < 4) { LDB(2 + (st >> 1), 2 * (st & 1)); LDB(2 + (st >> 1), 2 * (st & 1) + 1); }                           \
    if (st >= 44 && st < 48) { LDB(2 + ((st - 44) >> 1), 4 + 2 * (st & 1)); LDB(2 + ((st - 44) >> 1), 5 + 2 * (st & 1)); } \
    __builtin_amdgcn_sched_barrier(0);                                                                                \
    _Pragma("unroll") for (int cb = 0; cb < 8; ++cb) {                                                                \
      const int t = cb - mu;                                                                                          \
      const bool on = st < 16 ? (cb < 4 && t >= 0) : st < 44 ? (cb >= 4 && t >= 0 && t < 4) : (t >= 4);               \
      if (on) acc[cb] = __builtin_amdgcn_mfma_f32_16x16x32_f16(fh[rr][mu], bf[rr][t < 0 ? 0 : t], acc[cb], 0, 0, 0);  \
    }                                                                                                                 \
    __builtin_amdgcn_sched_barrier(0);                                                                                \
    if (st >= 13 && st < 16) ra[(st - 13) * 64] = acc[st - 13];                                                       \
    if (st >= 57) ra[1024 + (st - 57) * 64] = acc[4 + st - 57];                                                       \
  }
#pragma unroll
  for (int e = 0; e < 16; ++e) fh[e >> 2][e & 3] = C.k[e];
  __builtin_amdgcn_s_barrier();
  asm volatile("" ::: "memory");
  ST(!L1, 7);
#pragma unroll
  for (int e = 0; e < CPF; ++e) { if (L1) LDA(e); __builtin_amdgcn_sched_barrier(0); if (e < 2) { LDB(e, 0); __builtin_amdgcn_sched_barrier(0); LDB(e, 1); __builtin_amdgcn_sched_barrier(0); LDB(e, 2); __builtin_amdgcn_sched_barrier(0); LDB(e, 3); __builtin_amdgcn_sched_barrier(0); } }
  __builtin_amdgcn_sched_barrier(0);
  CSTEPS(0, 16)
  CSTEPS(16, 17)
  ra[3 * 64] = acc[3];
  __builtin_amdgcn_sched_barrier(0);
  asm volatile("" ::: "memory");
  CSTEPS(17, 18)
  if (L1) asm volatile("s_waitcnt vmcnt(0)" ::: "memory");
  asm volatile("s_waitcnt lgkmcnt(2)" ::: "memory");
  __builtin_amdgcn_s_barrier();
  asm volatile("" ::: "memory");
  ST(!L1, 8);
  CSTEPS(18, 44)
  if (!L1) ST(1, 5);
  if (!L1) __builtin_amdgcn_s_barrier();
  if (!L1) ST(1, 6);
  asm volatile("" ::: "memory");
#pragma unroll
  for (int e = 0; e < 8; ++e) { LDB(e >> 2, 4 + (e & 3)); __builtin_amdgcn_sched_barrier(0); }
  CSTEPS(44, 60)
  ra[1024 + 3 * 64] = acc[7];
  lds_barrier();
  ST(!L1, 9);
}

struct C1Pre { u32 ah, ah2, bp, xp, xc, cn; int so, fo0, fo1; };
#define C1_OPS "+{v16}"(C.ah), "+{v17}"(C.ah2), "+{v18}"(C.bp), "+{v19}"(C.xp), "+{v20}"(C.xc), "+{v21}"(C.cn), "+{v22}"(C.so), "+{v23}"(C.fo0), "+{v24}"(C.fo1)
__device__ __forceinline__ void c1_pre(C1Pre& C, int kp, int a, int lane) {
  lane = (int)opaque((u32)lane);
  const int c = a >> 1, j16 = lane & 15, kg = lane >> 4, P = a & 1;
  const int y0 = 2031 - j16 + 8 * kg - 128 * P, sg = y0 & 3;
  C.ah = (u32)(c * 4 * CSTR + sg * CSTR + 2 * (y0 - sg) - 3776);
  C.ah2 = C.ah + 8;
  C.bp = (u32)(L_U + c * 4608 + (240 + 16 * j16 + 8 * kg - 128 * P) * 2 - 192);
  C.xp = (u32)(L_X + j16 * 64 + kg * 16);
  C.xc = (u32)(L_TA + 24576 + c * 4096 + lane * 16);
  C.cn = (u32)(L_MI + 4 * c);
  C.so = OFF_X1 + (8 * ((kg & 1) ? 3 - P : P) + (j16 >> 1)) * XBS + kp * 128 + c * 64 + (j16 & 1) * 32 + (kg >> 1) * 16;
  C.fo0 = OFF_ST0 + (a * 64 + lane) * 16;
  C.fo1 = OFF_ST0 + 8192 + (HB_OF(BIDX) * 4 + a) * 128 + (lane & 3) * 16;
  asm volatile("" : C1_OPS);
}
static_assert(L_XC - L_TA - 24576 == 57344 && C_XC == 8 && C_KH == 48 && C_P2 == 52 && C_U == 56 && C_P3 == 64 && C_G2 == 32 && L_MI % 8 == 0, "generated offsets");
__device__ __forceinline__ void a_conv1(rsrc_t wrs, int a, C1Pre& C, u32x4& fl, u32x4& wfl, u32& g2) {
  asm volatile("s_bitcmp1_b32 %[a], 0\n\ts_cbranch_scc1 .Lc_odd\n\t"
  "ds_read_b64 v[36:37], v16 offset:3776\n\tds_read_b64 v[38:39], v17 offset:3776\n\tds_read_b64 v[40:41], v16 offset:3264\n\tds_read_b64 v[42:43], v17 offset:3264\n\t"
  "ds_read_b64 v[44:45], v16 offset:2752\n\tds_read_b64 v[46:47], v17 offset:2752\n\tds_read_b64 v[48:49], v16 offset:2240\n\tds_read_b64 v[50:51], v17 offset:2240\n\t"
  "ds_read_b64 v[52:53], v16 offset:3712\n\tds_read_b64 v[54:55], v17 offset:3712\n\tds_read_b64 v[56:57], v16 offset:3200\n\tds_read_b64 v[58:59], v17 offset:3200\n\t"
  "ds_read_b64 v[60:61], v16 offset:2688\n\tds_read_b64 v[62:63], v17 offset:2688\n\tds_read_b64 v[64:65], v16 offset:2176\n\tds_read_b64 v[66:67], v17 offset:2176\n\t"
  "ds_read_b64 v[68:69], v16 offset:3648\n\tds_read_b64 v[70:71], v17 offset:3648\n\tds_read_b64 v[72:73], v16 offset:3136\n\tds_read_b64 v[74:75], v17 offset:3136\n\t"
  "ds_read_b64 v[76:77], v16 offset:2624\n\tds_read_b64 v[78:79], v17 offset:2624\n\tds_read_b64 v[80:81], v16 offset:2112\n\tds_read_b64 v[82:83], v17 offset:2112\n\t"
  "ds_read_b64 v[84:85], v16 offset:3584\n\tds_read_b64 v[86:87], v17 offset:3584\n\tds_read_b64 v[88:89], v16 offset:3072\n\tds_read_b64 v[90:91], v17 offset:3072\n\t"
  "ds_read_b64 v[92:93], v16 offset:2560\n\tds_read_b64 v[94:95], v17 offset:2560\n\tds_read_b64 v[96:97], v16 offset:2048\n\tds_read_b64 v[98:99], v17 offset:2048\n\t"
  "ds_read_b32 v26, v21\n\tv_and_b32 v27, -8, v21\n\tv_mov_b32 v100, 1\n\t"
  CST(6)
  "s_mov_b32 vcc_hi, 0x1000000\n\t.Lc0_u:\n\tds_read_b32 v101, v27 offset:56\n\ts_sub_u32 vcc_hi, vcc_hi, 1\n\ts_cselect_b32 vcc_lo, 0, 4\n\ts_waitcnt lgkmcnt(0)\n\tv_readfirstlane_b32 s61, v101\n\ts_nop 1\n\ts_cmp_lt_u32 s61, vcc_lo\n\ts_cbranch_scc1 .Lc0_u\n\t"
  CST(7)
  "ds_read_b128 v[102:105], v18 offset:192\n\tds_read_b128 v[106:109], v18 offset:704\n\tds_read_b128 v[110:113], v18 offset:1216\n\tds_read_b128 v[116:119], v18 offset:128\n\t"
  "ds_read_b128 v[120:123], v18 offset:640\n\tds_read_b128 v[124:127], v18 offset:1152\n\tds_read_b128 v[128:131], v18 offset:64\n\ts_waitcnt lgkmcnt(6)\n\t"
  "v_mfma_f32_16x16x32_f16 v[132:135], v[40:43], v[102:105], 0\n\tds_read_b128 v[136:139], v18 offset:576\n\tds_read_b128 v[140:143], v18 offset:1728\n\t"
  "v_mfma_f32_16x16x32_f16 v[144:147], v[44:47], v[102:105], 0\n\tds_read_b128 v[148:151], v18 offset:1088\n\ts_waitcnt lgkmcnt(8)\n\t"
  "v_mfma_f32_16x16x32_f16 v[132:135], v[36:39], v[106:109], v[132:135]\n\tds_read_b128 v[152:155], v18 offset:1664\n\tds_read_b128 v[156:159], v18 offset:1600\n\t"
  "v_mfma_f32_16x16x32_f16 v[144:147], v[40:43], v[106:109], v[144:147]\n\tds_read_b128 v[160:163], v18 offset:0\n\tds_read_b32 v101, v27 offset:48\n\ts_waitcnt lgkmcnt(7)\n\t"
  "v_mfma_f32_16x16x32_f16 v[144:147], v[36:39], v[110:113], v[144:147]\n\tds_read_b128 v[164:167], v18 offset:512\n\t"
  "v_mfma_f32_16x16x32_f16 v[132:135], v[56:59], v[116:119], v[132:135]\n\tds_read_b128 v[168:171], v18 offset:1024\n\t"
  "v_mfma_f32_16x16x32_f16 v[144:147], v[60:63], v[116:119], v[144:147]\n\tds_read_b128 v[172:175], v18 offset:1536\n\t"
  "v_mfma_f32_16x16x32_f16 v[132:135], v[52:55], v[120:123], v[132:135]\n\t"
  "v_mfma_f32_16x16x32_f16 v[144:147], v[56:59], v[120:123], v[144:147]\n\t"
  "v_mfma_f32_16x16x32_f16 v[144:147], v[52:55], v[124:127], v[144:147]\n\t"
  "v_mfma_f32_16x16x32_f16 v[132:135], v[72:75], v[128:131], v[132:135]\n\tds_read_b128 v[176:179], v19 offset:0\n\tds_read_b128 v[180:183], v19 offset:3072\n\t"
  "v_mfma_f32_16x16x32_f16 v[144:147], v[76:79], v[128:131], v[144:147]\n\ts_waitcnt lgkmcnt(5)\n\tv_readfirstlane_b32 s61, v101\n\t"
  "v_mfma_f32_16x16x32_f16 v[132:135], v[68:71], v[136:139], v[132:135]\n\t"
  "v_mfma_f32_16x16x32_f16 v[144:147], v[72:75], v[136:139], v[144:147]\n\ts_cmp_lt_u32 s61, 4\n\ts_cbranch_scc1 .Lc0_kh\n.Lc0_kh_b:\n\t"
  "v_mfma_f32_16x16x32_f16 v[144:147], v[68:71], v[148:151], v[144:147]\n\tds_read_b64 v[184:185], v16 offset:1728\n\tds_read_b64 v[186:187], v17 offset:1728\n\t"
  "v_mfma_f32_16x16x32_f16 v[132:135], v[88:91], v[160:163], v[132:135]\n\tds_read_b64 v[188:189], v16 offset:1216\n\tds_read_b64 v[190:191], v17 offset:1216\n\t"
  "v_mfma_f32_16x16x32_f16 v[144:147], v[92:95], v[160:163], v[144:147]\n\tds_read_b64 v[192:193], v16 offset:704\n\ts_waitcnt lgkmcnt(7)\n\t"
  "v_mfma_f32_16x16x32_f16 v[132:135], v[84:87], v[164:167], v[132:135]\n\tds_read_b64 v[194:195], v17 offset:704\n\tds_read_b64 v[196:197], v16 offset:192\n\tds_read_b64 v[198:199], v17 offset:192\n\t"
  "v_mfma_f32_16x16x32_f16 v[144:147], v[88:91], v[164:167], v[144:147]\n\ts_waitcnt lgkmcnt(8)\n\tv_perm_b32 v176, v176, v176, s60\n\t"
  "v_mfma_f32_16x16x32_f16 v[144:147], v[84:87], v[168:171], v[144:147]\n\tds_write_b128 v20, v[132:135] offset:0\n\tv_perm_b32 v177, v177, v177, s60\n\t"
  "v_mfma_f32_16x16x32_f16 v[200:203], v[36:39], v[102:105], 0\n\tv_perm_b32 v178, v178, v178, s60\n\tv_perm_b32 v179, v179, v179, s60\n\t"
  "v_mfma_f32_16x16x32_f16 v[132:135], v[48:51], v[102:105], 0\n\tds_write_b128 v20, v[144:147] offset:1024\n\tv_perm_b32 v180, v180, v180, s60\n\t"
  "v_mfma_f32_16x16x32_f16 v[132:135], v[44:47], v[106:109], v[132:135]\n\ts_mov_b64 exec, 1\n\tds_add_u32 v21, v100 offset:8\n\ts_mov_b64 exec, -1\n\t"
  "v_mfma_f32_16x16x32_f16 v[132:135], v[40:43], v[110:113], v[132:135]\n\tv_perm_b32 v181, v181, v181, s60\n\tv_perm_b32 v182, v182, v182, s60\n\t"
  "v_mfma_f32_16x16x32_f16 v[132:135], v[36:39], v[140:143], v[132:135]\n\tv_perm_b32 v183, v183, v183, s60\n\t"
  "v_mfma_f32_16x16x32_f16 v[200:203], v[52:55], v[116:119], v[200:203]\n\t"
  "v_mfma_f32_16x16x32_f16 v[132:135], v[64:67], v[116:119], v[132:135]\n\t"
  "v_mfma_f32_16x16x32_f16 v[132:135], v[60:63], v[120:123], v[132:135]\n\t"
  "v_mfma_f32_16x16x32_f16 v[132:135], v[56:59], v[124:127], v[132:135]\n\t"
  "v_mfma_f32_16x16x32_f16 v[132:135], v[52:55], v[152:155], v[132:135]\n\t"
  "v_mfma_f32_16x16x32_f16 v[200:203], v[68:71], v[128:131], v[200:203]\n\t"
  "v_mfma_f32_16x16x32_f16 v[132:135], v[80:83], v[128:131], v[132:135]\n\tds_read_b32 v101, v21 offset:8\n\tds_read_b128 v[144:147], v20 offset:2048\n\tds_read_b128 v[204:207], v20 offset:3072\n\t"
  "v_mfma_f32_16x16x32_f16 v[132:135], v[76:79], v[136:139], v[132:135]\n\t"
  "v_mfma_f32_16x16x32_f16 v[132:135], v[72:75], v[148:151], v[132:135]\n\t"
  "v_mfma_f32_16x16x32_f16 v[132:135], v[68:71], v[156:159], v[132:135]\n\t"
  "v_mfma_f32_16x16x32_f16 v[200:203], v[84:87], v[160:163], v[200:203]\n\t"
  "v_mfma_f32_16x16x32_f16 v[132:135], v[96:99], v[160:163], v[132:135]\n\t"
  "v_mfma_f32_16x16x32_f16 v[132:135], v[92:95], v[164:167], v[132:135]\n\t"
  "v_mfma_f32_16x16x32_f16 v[132:135], v[88:91], v[168:171], v[132:135]\n\t"
  "v_mfma_f32_16x16x32_f16 v[132:135], v[84:87], v[172:175], v[132:135]\n\ts_waitcnt lgkmcnt(0)\n\tv_readfirstlane_b32 s61, v101\n\t"
  "v_mfma_f32_16x16x32_f16 v[208:211], v[40:43], v[140:143], 0\n\t"
  "v_mfma_f32_16x16x32_f16 v[208:211], v[44:47], v[110:113], v[208:211]\n\ts_cmp_lt_u32 s61, 2\n\ts_cbranch_scc1 .Lc0_x0\n.Lc0_x0_b:\n\tv_fmac_f32 v176, v144, v26\n\tv_fmac_f32 v177, v145, v26\n\t"
  "v_fmac_f32 v178, v146, v26\n\t"
  "v_mfma_f32_16x16x32_f16 v[212:215], v[44:47], v[140:143], 0\n\tv_fmac_f32 v179, v147, v26\n\tv_fmac_f32 v180, v204, v26\n\tv_fmac_f32 v181, v205, v26\n\t"
  "v_fmac_f32 v182, v206, v26\n\tv_fmac_f32 v183, v207, v26\n\t"
  "v_mfma_f32_16x16x32_f16 v[208:211], v[48:51], v[106:109], v[208:211]\n\tv_fmac_f32 v176, v200, v26\n\tv_fmac_f32 v177, v201, v26\n\t"
  "v_mfma_f32_16x16x32_f16 v[212:215], v[48:51], v[110:113], v[212:215]\n\tv_fmac_f32 v178, v202, v26\n\tv_fmac_f32 v179, v203, v26\n\tv_cvt_pk_bf16_f32 v144, v176, v177\n\t"
  "v_cvt_pk_bf16_f32 v145, v178, v179\n\tv_fmac_f32 v180, v132, v26\n\t"
  "v_mfma_f32_16x16x32_f16 v[176:179], v[48:51], v[140:143], 0\n\tv_fmac_f32 v181, v133, v26\n\tv_fmac_f32 v182, v134, v26\n\tv_fmac_f32 v183, v135, v26\n\t"
  "v_cvt_pk_bf16_f32 v146, v180, v181\n\tv_cvt_pk_bf16_f32 v147, v182, v183\n\t"
  "v_mfma_f32_16x16x32_f16 v[208:211], v[184:187], v[102:105], v[208:211]\n\tv_permlane16_swap_b32 v144, v146\n\tv_permlane16_swap_b32 v145, v147\n\ts_setprio 3\n\tbuffer_store_dwordx4 v[144:147], v22, s[56:59], 0 offen sc1\n\ts_setprio 0\n\t"
  CST(8)
  "v_mfma_f32_16x16x32_f16 v[212:215], v[184:187], v[106:109], v[212:215]\n\tds_read_b64 v[132:133], v16 offset:1664\n\tds_read_b64 v[134:135], v17 offset:1664\n\t"
  "v_mfma_f32_16x16x32_f16 v[176:179], v[184:187], v[110:113], v[176:179]\n\tds_read_b64 v[144:145], v16 offset:1152\n\tds_read_b64 v[146:147], v17 offset:1152\n\t"
  "v_mfma_f32_16x16x32_f16 v[180:183], v[184:187], v[140:143], 0\n\tds_read_b64 v[140:141], v16 offset:640\n\tds_read_b64 v[142:143], v17 offset:640\n\t"
  "v_mfma_f32_16x16x32_f16 v[212:215], v[188:191], v[102:105], v[212:215]\n\t"
  "v_mfma_f32_16x16x32_f16 v[176:179], v[188:191], v[106:109], v[176:179]\n\tds_read_b64 v[184:185], v16 offset:128\n\tds_read_b64 v[186:187], v17 offset:128\n\t"
  "v_mfma_f32_16x16x32_f16 v[180:183], v[188:191], v[110:113], v[180:183]\n\t"
  "v_mfma_f32_16x16x32_f16 v[176:179], v[192:195], v[102:105], v[176:179]\n\t"
  "v_mfma_f32_16x16x32_f16 v[180:183], v[192:195], v[106:109], v[180:183]\n\t"
  "v_mfma_f32_16x16x32_f16 v[180:183], v[196:199], v[102:105], v[180:183]\n\t"
  "v_mfma_f32_16x16x32_f16 v[208:211], v[56:59], v[152:155], v[208:211]\n\t"
  "v_mfma_f32_16x16x32_f16 v[208:211], v[60:63], v[124:127], v[208:211]\n\t"
  "v_mfma_f32_16x16x32_f16 v[212:215], v[60:63], v[152:155], v[212:215]\n\tds_read_b64 v[102:103], v16 offset:1600\n\tds_read_b64 v[104:105], v17 offset:1600\n\t"
  "v_mfma_f32_16x16x32_f16 v[208:211], v[64:67], v[120:123], v[208:211]\n\t"
  "v_mfma_f32_16x16x32_f16 v[212:215], v[64:67], v[124:127], v[212:215]\n\t"
  "v_mfma_f32_16x16x32_f16 v[176:179], v[64:67], v[152:155], v[176:179]\n\ts_waitcnt lgkmcnt(2)\n\t"
  "v_mfma_f32_16x16x32_f16 v[208:211], v[132:135], v[116:119], v[208:211]\n\tds_read_b64 v[108:109], v16 offset:1088\n\tds_read_b64 v[110:111], v17 offset:1088\n\t"
  "v_mfma_f32_16x16x32_f16 v[212:215], v[132:135], v[120:123], v[212:215]\n\t"
  "v_mfma_f32_16x16x32_f16 v[176:179], v[132:135], v[124:127], v[176:179]\n\t"
  "v_mfma_f32_16x16x32_f16 v[180:183], v[132:135], v[152:155], v[180:183]\n\tds_read_b64 v[112:113], v16 offset:576\n\tds_read_b64 v[114:115], v17 offset:576\n\t"
  "v_mfma_f32_16x16x32_f16 v[212:215], v[144:147], v[116:119], v[212:215]\n\t"
  "v_mfma_f32_16x16x32_f16 v[176:179], v[144:147], v[120:123], v[176:179]\n\tds_read_b64 v[132:133], v16 offset:64\n\tds_read_b64 v[134:135], v17 offset:64\n\t"
  "v_mfma_f32_16x16x32_f16 v[180:183], v[144:147], v[124:127], v[180:183]\n\t"
  "v_mfma_f32_16x16x32_f16 v[176:179], v[140:143], v[116:119], v[176:179]\n\t"
  "v_mfma_f32_16x16x32_f16 v[180:183], v[140:143], v[120:123], v[180:183]\n\tv_add_u32 v22, 0x101000, v22\n\t"
  "v_mfma_f32_16x16x32_f16 v[180:183], v[184:187], v[116:119], v[180:183]\n\t"
  "v_mfma_f32_16x16x32_f16 v[208:211], v[72:75], v[156:159], v[208:211]\n\t"
  "v_mfma_f32_16x16x32_f16 v[208:211], v[76:79], v[148:151], v[208:211]\n\t"
  "v_mfma_f32_16x16x32_f16 v[212:215], v[76:79], v[156:159], v[212:215]\n\tds_read_b64 v[116:117], v16 offset:1536\n\tds_read_b64 v[118:119], v17 offset:1536\n\t"
  "v_mfma_f32_16x16x32_f16 v[208:211], v[80:83], v[136:139], v[208:211]\n\t"
  "v_mfma_f32_16x16x32_f16 v[212:215], v[80:83], v[148:151], v[212:215]\n\t"
  "v_mfma_f32_16x16x32_f16 v[176:179], v[80:83], v[156:159], v[176:179]\n\ts_waitcnt lgkmcnt(2)\n\t"
  "v_mfma_f32_16x16x32_f16 v[208:211], v[102:105], v[128:131], v[208:211]\n\tds_read_b32 v101, v27 offset:52\n\tds_read_b64 v[120:121], v16 offset:1024\n\t"
  "v_mfma_f32_16x16x32_f16 v[212:215], v[102:105], v[136:139], v[212:215]\n\tds_read_b64 v[122:123], v17 offset:1024\n\t"
  "v_mfma_f32_16x16x32_f16 v[176:179], v[102:105], v[148:151], v[176:179]\n\t"
  "v_mfma_f32_16x16x32_f16 v[180:183], v[102:105], v[156:159], v[180:183]\n\tds_read_b64 v[102:103], v16 offset:512\n\tds_read_b64 v[104:105], v17 offset:512\n\t"
  "v_mfma_f32_16x16x32_f16 v[212:215], v[108:111], v[128:131], v[212:215]\n\t"
  "v_mfma_f32_16x16x32_f16 v[176:179], v[108:111], v[136:139], v[176:179]\n\tds_read_b64 v[124:125], v16 offset:0\n\tds_read_b64 v[126:127], v17 offset:0\n\t"
  "v_mfma_f32_16x16x32_f16 v[180:183], v[108:111], v[148:151], v[180:183]\n\t"
  "v_mfma_f32_16x16x32_f16 v[176:179], v[112:115], v[128:131], v[176:179]\n\t"
  "v_mfma_f32_16x16x32_f16 v[180:183], v[112:115], v[136:139], v[180:183]\n\ts_waitcnt lgkmcnt(5)\n\tv_readfirstlane_b32 s61, v101\n\t"
  "v_mfma_f32_16x16x32_f16 v[180:183], v[132:135], v[128:131], v[180:183]\n\t"
  "v_mfma_f32_16x16x32_f16 v[208:211], v[88:91], v[172:175], v[208:211]\n\ts_cmp_lt_u32 s61, 4\n\ts_cbranch_scc1 .Lc0_ub\n.Lc0_ub_b:\n\t"
  "v_mfma_f32_16x16x32_f16 v[208:211], v[92:95], v[168:171], v[208:211]\n\tds_read_b128 v[108:111], v18 offset:2240\n\tds_read_b128 v[112:115], v18 offset:2752\n\t"
  "v_mfma_f32_16x16x32_f16 v[212:215], v[92:95], v[172:175], v[212:215]\n\tds_read_b128 v[128:131], v18 offset:3264\n\tds_read_b128 v[132:135], v18 offset:3776\n\t"
  "v_mfma_f32_16x16x32_f16 v[208:211], v[96:99], v[164:167], v[208:211]\n\t"
  "v_mfma_f32_16x16x32_f16 v[212:215], v[96:99], v[168:171], v[212:215]\n\t"
  "v_mfma_f32_16x16x32_f16 v[176:179], v[96:99], v[172:175], v[176:179]\n\tds_read_b128 v[136:139], v18 offset:2176\n\tds_read_b128 v[140:143], v18 offset:2688\n\t"
  "v_mfma_f32_16x16x32_f16 v[208:211], v[116:119], v[160:163], v[208:211]\n\tds_read_b128 v[144:147], v18 offset:3200\n\tds_read_b128 v[148:151], v18 offset:3712\n\t"
  "v_mfma_f32_16x16x32_f16 v[212:215], v[116:119], v[164:167], v[212:215]\n\t"
  "v_mfma_f32_16x16x32_f16 v[176:179], v[116:119], v[168:171], v[176:179]\n\t"
  "v_mfma_f32_16x16x32_f16 v[180:183], v[116:119], v[172:175], v[180:183]\n\ts_waitcnt lgkmcnt(6)\n\t"
  "v_mfma_f32_16x16x32_f16 v[212:215], v[120:123], v[160:163], v[212:215]\n\tds_read_b128 v[116:119], v18 offset:2112\n\tds_read_b128 v[152:155], v18 offset:2624\n\t"
  "v_mfma_f32_16x16x32_f16 v[176:179], v[120:123], v[164:167], v[176:179]\n\tds_read_b128 v[156:159], v18 offset:3136\n\tds_read_b128 v[172:175], v18 offset:3648\n\t"
  "v_mfma_f32_16x16x32_f16 v[180:183], v[120:123], v[168:171], v[180:183]\n\t"
  "v_mfma_f32_16x16x32_f16 v[176:179], v[102:105], v[160:163], v[176:179]\n\t"
  "v_mfma_f32_16x16x32_f16 v[180:183], v[102:105], v[164:167], v[180:183]\n\t"
  "v_mfma_f32_16x16x32_f16 v[180:183], v[124:127], v[160:163], v[180:183]\n\tds_read_b128 v[104:107], v18 offset:2048\n\tds_read_b128 v[120:123], v18 offset:2560\n\t"
  "v_mfma_f32_16x16x32_f16 v[212:215], v[40:43], v[108:111], v[212:215]\n\tds_read_b128 v[124:127], v18 offset:3072\n\tds_read_b128 v[160:163], v18 offset:3584\n\t"
  "v_mfma_f32_16x16x32_f16 v[176:179], v[44:47], v[108:111], v[176:179]\n\t"
  "v_mfma_f32_16x16x32_f16 v[212:215], v[36:39], v[112:115], v[212:215]\n\t"
  "v_mfma_f32_16x16x32_f16 v[176:179], v[40:43], v[112:115], v[176:179]\n\ts_waitcnt lgkmcnt(4)\n\t"
  "v_mfma_f32_16x16x32_f16 v[176:179], v[36:39], v[128:131], v[176:179]\n\t"
  "v_mfma_f32_16x16x32_f16 v[212:215], v[56:59], v[136:139], v[212:215]\n\t"
  "v_mfma_f32_16x16x32_f16 v[176:179], v[60:63], v[136:139], v[176:179]\n\t"
  "v_mfma_f32_16x16x32_f16 v[212:215], v[52:55], v[140:143], v[212:215]\n\t"
  "v_mfma_f32_16x16x32_f16 v[176:179], v[56:59], v[140:143], v[176:179]\n\t"
  "v_mfma_f32_16x16x32_f16 v[176:179], v[52:55], v[144:147], v[176:179]\n\t"
  "v_mfma_f32_16x16x32_f16 v[212:215], v[72:75], v[116:119], v[212:215]\n\tds_read_b128 v[164:167], v19 offset:4096\n\tds_read_b128 v[168:171], v19 offset:7168\n\t"
  "v_mfma_f32_16x16x32_f16 v[176:179], v[76:79], v[116:119], v[176:179]\n\t"
  "v_mfma_f32_16x16x32_f16 v[212:215], v[68:71], v[152:155], v[212:215]\n\t"
  "v_mfma_f32_16x16x32_f16 v[176:179], v[72:75], v[152:155], v[176:179]\n\t"
  "v_mfma_f32_16x16x32_f16 v[176:179], v[68:71], v[156:159], v[176:179]\n\ts_waitcnt lgkmcnt(2)\n\t"
  "v_mfma_f32_16x16x32_f16 v[212:215], v[88:91], v[104:107], v[212:215]\n\t"
  "v_mfma_f32_16x16x32_f16 v[176:179], v[92:95], v[104:107], v[176:179]\n\t"
  "v_mfma_f32_16x16x32_f16 v[212:215], v[84:87], v[120:123], v[212:215]\n\t"
  "v_mfma_f32_16x16x32_f16 v[176:179], v[88:91], v[120:123], v[176:179]\n\tds_read_b32 v101, v27 offset:64\n\ts_waitcnt lgkmcnt(1)\n\t"
  "v_mfma_f32_16x16x32_f16 v[176:179], v[84:87], v[124:127], v[176:179]\n\tds_write_b128 v20, v[212:215] offset:57344\n\tv_perm_b32 v164, v164, v164, s60\n\t"
  "v_mfma_f32_16x16x32_f16 v[208:211], v[36:39], v[108:111], v[208:211]\n\ts_setprio 3\n\tbuffer_load_dwordx4 v[28:31], v23, s[56:59], 0 offen sc1\n\tbuffer_load_dwordx4 v[32:35], v24, s[56:59], 0 offen sc1\n\ts_setprio 0\n\t"
  "v_mfma_f32_16x16x32_f16 v[180:183], v[48:51], v[108:111], v[180:183]\n\tds_write_b128 v20, v[176:179] offset:58368\n\tv_perm_b32 v165, v165, v165, s60\n\t"
  "v_mfma_f32_16x16x32_f16 v[180:183], v[44:47], v[112:115], v[180:183]\n\ts_mov_b64 exec, 1\n\tds_add_u32 v27, v100 offset:16\n\ts_mov_b64 exec, -1\n\t"
  "v_mfma_f32_16x16x32_f16 v[180:183], v[40:43], v[128:131], v[180:183]\n\tv_perm_b32 v166, v166, v166, s60\n\tv_perm_b32 v167, v167, v167, s60\n\t"
  "v_mfma_f32_16x16x32_f16 v[180:183], v[36:39], v[132:135], v[180:183]\n\tv_perm_b32 v168, v168, v168, s60\n\tv_perm_b32 v169, v169, v169, s60\n\t"
  "v_mfma_f32_16x16x32_f16 v[208:211], v[52:55], v[136:139], v[208:211]\n\tv_perm_b32 v170, v170, v170, s60\n\tv_perm_b32 v171, v171, v171, s60\n\t"
  "v_mfma_f32_16x16x32_f16 v[180:183], v[64:67], v[136:139], v[180:183]\n\ts_waitcnt lgkmcnt(3)\n\tv_readfirstlane_b32 s61, v101\n\t"
  "v_mfma_f32_16x16x32_f16 v[180:183], v[60:63], v[140:143], v[180:183]\n\t"
  "v_mfma_f32_16x16x32_f16 v[180:183], v[56:59], v[144:147], v[180:183]\n\ts_cmp_lt_u32 s61, 4\n\ts_cbranch_scc1 .Lc0_p3\n.Lc0_p3_b:\n\t"
  "v_mfma_f32_16x16x32_f16 v[180:183], v[52:55], v[148:151], v[180:183]\n\tds_read_b32 v36, v27 offset:16\n\tds_read_b128 v[40:43], v20 offset:59392\n\tds_read_b128 v[44:47], v20 offset:60416\n\t"
  "v_mfma_f32_16x16x32_f16 v[208:211], v[68:71], v[116:119], v[208:211]\n\t"
  "v_mfma_f32_16x16x32_f16 v[180:183], v[80:83], v[116:119], v[180:183]\n\t"
  "v_mfma_f32_16x16x32_f16 v[180:183], v[76:79], v[152:155], v[180:183]\n\t"
  "v_mfma_f32_16x16x32_f16 v[180:183], v[72:75], v[156:159], v[180:183]\n\t"
  "v_mfma_f32_16x16x32_f16 v[180:183], v[68:71], v[172:175], v[180:183]\n\t"
  "v_mfma_f32_16x16x32_f16 v[208:211], v[84:87], v[104:107], v[208:211]\n\t"
  "v_mfma_f32_16x16x32_f16 v[180:183], v[96:99], v[104:107], v[180:183]\n\t"
  "v_mfma_f32_16x16x32_f16 v[180:183], v[92:95], v[120:123], v[180:183]\n\ts_waitcnt lgkmcnt(0)\n\tv_readfirstlane_b32 s61, v36\n\tds_read_b32 v25, v27 offset:32\n\t"
  "v_mfma_f32_16x16x32_f16 v[180:183], v[88:91], v[124:127], v[180:183]\n\ts_cmp_lt_u32 s61, 4\n\ts_cbranch_scc1 .Lc0_x4\n.Lc0_x4_b:\n\t"
  "v_mfma_f32_16x16x32_f16 v[180:183], v[84:87], v[160:163], v[180:183]\n\tv_fmac_f32 v164, v40, v26\n\tv_fmac_f32 v165, v41, v26\n\tv_fmac_f32 v166, v42, v26\n\t"
  "v_fmac_f32 v167, v43, v26\n\tv_fmac_f32 v168, v44, v26\n\tv_fmac_f32 v169, v45, v26\n\tv_fmac_f32 v170, v46, v26\n\t"
  "v_fmac_f32 v171, v47, v26\n\tv_fmac_f32 v164, v208, v26\n\tv_fmac_f32 v165, v209, v26\n\tv_fmac_f32 v166, v210, v26\n\t"
  "v_fmac_f32 v167, v211, v26\n\tv_cvt_pk_bf16_f32 v36, v164, v165\n\tv_cvt_pk_bf16_f32 v37, v166, v167\n\tv_fmac_f32 v168, v180, v26\n\t"
  "v_fmac_f32 v169, v181, v26\n\tv_fmac_f32 v170, v182, v26\n\tv_fmac_f32 v171, v183, v26\n\tv_cvt_pk_bf16_f32 v38, v168, v169\n\t"
  "v_cvt_pk_bf16_f32 v39, v170, v171\n\ts_nop 0\n\tv_permlane16_swap_b32 v36, v38\n\tv_permlane16_swap_b32 v37, v39\n\t"
  "s_setprio 3\n\tbuffer_store_dwordx4 v[36:39], v22, s[56:59], 0 offen sc1\n\ts_setprio 0\n\ts_waitcnt lgkmcnt(0)\n\ts_nop 0\n\t"
  "s_branch .Lc_end\n.Lc_odd:\n\t"
  "ds_read_b64 v[36:37], v16 offset:3776\n\tds_read_b64 v[38:39], v17 offset:3776\n\tds_read_b64 v[40:41], v16 offset:3264\n\tds_read_b64 v[42:43], v17 offset:3264\n\t"
  "ds_read_b64 v[44:45], v16 offset:2752\n\tds_read_b64 v[46:47], v17 offset:2752\n\tds_read_b64 v[48:49], v16 offset:2240\n\tds_read_b64 v[50:51], v17 offset:2240\n\t"
  "ds_read_b64 v[52:53], v16 offset:3712\n\tds_read_b64 v[54:55], v17 offset:3712\n\tds_read_b64 v[56:57], v16 offset:3200\n\tds_read_b64 v[58:59], v17 offset:3200\n\t"
  "ds_read_b64 v[60:61], v16 offset:2688\n\tds_read_b64 v[62:63], v17 offset:2688\n\tds_read_b64 v[64:65], v16 offset:2176\n\tds_read_b64 v[66:67], v17 offset:2176\n\t"
  "ds_read_b64 v[68:69], v16 offset:3648\n\tds_read_b64 v[70:71], v17 offset:3648\n\tds_read_b64 v[72:73], v16 offset:3136\n\tds_read_b64 v[74:75], v17 offset:3136\n\t"
  "ds_read_b64 v[76:77], v16 offset:2624\n\tds_read_b64 v[78:79], v17 offset:2624\n\tds_read_b64 v[80:81], v16 offset:2112\n\tds_read_b64 v[82:83], v17 offset:2112\n\t"
  "ds_read_b64 v[84:85], v16 offset:3584\n\tds_read_b64 v[86:87], v17 offset:3584\n\tds_read_b64 v[88:89], v16 offset:3072\n\tds_read_b64 v[90:91], v17 offset:3072\n\t"
  "ds_read_b64 v[92:93], v16 offset:2560\n\tds_read_b64 v[94:95], v17 offset:2560\n\tds_read_b64 v[96:97], v16 offset:2048\n\tds_read_b64 v[98:99], v17 offset:2048\n\t"
  "ds_read_b32 v26, v21\n\tv_and_b32 v27, -8, v21\n\tv_mov_b32 v100, 1\n\t"
  CST(6)
  "s_mov_b32 vcc_hi, 0x1000000\n\t.Lc1_u:\n\tds_read_b32 v101, v27 offset:56\n\ts_sub_u32 vcc_hi, vcc_hi, 1\n\ts_cselect_b32 vcc_lo, 0, 4\n\ts_waitcnt lgkmcnt(0)\n\tv_readfirstlane_b32 s61, v101\n\ts_nop 1\n\ts_cmp_lt_u32 s61, vcc_lo\n\ts_cbranch_scc1 .Lc1_u\n\t"
  CST(7)
  "ds_read_b128 v[102:105], v18 offset:192\n\tds_read_b128 v[106:109], v18 offset:704\n\tds_read_b128 v[110:113], v18 offset:1216\n\tds_read_b128 v[114:117], v18 offset:1728\n\t"
  "ds_read_b128 v[120:123], v18 offset:128\n\tds_read_b128 v[124:127], v18 offset:640\n\tds_read_b128 v[128:131], v18 offset:1152\n\tds_read_b128 v[132:135], v18 offset:1664\n\t"
  "ds_read_b128 v[136:139], v18 offset:64\n\ts_waitcnt lgkmcnt(8)\n\t"
  "v_mfma_f32_16x16x32_f16 v[140:143], v[36:39], v[102:105], 0\n\tds_read_b128 v[144:147], v18 offset:576\n\t"
  "v_mfma_f32_16x16x32_f16 v[148:151], v[48:51], v[102:105], 0\n\tds_read_b128 v[152:155], v18 offset:1088\n\ts_waitcnt lgkmcnt(9)\n\t"
  "v_mfma_f32_16x16x32_f16 v[148:151], v[44:47], v[106:109], v[148:151]\n\tds_read_b128 v[156:159], v18 offset:1600\n\ts_waitcnt lgkmcnt(3)\n\t"
  "v_mfma_f32_16x16x32_f16 v[148:151], v[40:43], v[110:113], v[148:151]\n\tds_read_b128 v[160:163], v18 offset:0\n\tds_read_b32 v101, v27 offset:48\n\t"
  "v_mfma_f32_16x16x32_f16 v[148:151], v[36:39], v[114:117], v[148:151]\n\tds_read_b128 v[164:167], v18 offset:512\n\t"
  "v_mfma_f32_16x16x32_f16 v[140:143], v[52:55], v[120:123], v[140:143]\n\tds_read_b128 v[168:171], v18 offset:1024\n\t"
  "v_mfma_f32_16x16x32_f16 v[148:151], v[64:67], v[120:123], v[148:151]\n\tds_read_b128 v[172:175], v18 offset:1536\n\t"
  "v_mfma_f32_16x16x32_f16 v[148:151], v[60:63], v[124:127], v[148:151]\n\t"
  "v_mfma_f32_16x16x32_f16 v[148:151], v[56:59], v[128:131], v[148:151]\n\t"
  "v_mfma_f32_16x16x32_f16 v[148:151], v[52:55], v[132:135], v[148:151]\n\t"
  "v_mfma_f32_16x16x32_f16 v[140:143], v[68:71], v[136:139], v[140:143]\n\tds_read_b128 v[176:179], v19 offset:1024\n\tds_read_b128 v[180:183], v19 offset:2048\n\t"
  "v_mfma_f32_16x16x32_f16 v[148:151], v[80:83], v[136:139], v[148:151]\n\ts_waitcnt lgkmcnt(5)\n\tv_readfirstlane_b32 s61, v101\n\t"
  "v_mfma_f32_16x16x32_f16 v[148:151], v[76:79], v[144:147], v[148:151]\n\t"
  "v_mfma_f32_16x16x32_f16 v[148:151], v[72:75], v[152:155], v[148:151]\n\ts_cmp_lt_u32 s61, 4\n\ts_cbranch_scc1 .Lc1_kh\n.Lc1_kh_b:\n\t"
  "v_mfma_f32_16x16x32_f16 v[148:151], v[68:71], v[156:159], v[148:151]\n\tds_read_b64 v[184:185], v16 offset:1728\n\tds_read_b64 v[186:187], v17 offset:1728\n\t"
  "v_mfma_f32_16x16x32_f16 v[140:143], v[84:87], v[160:163], v[140:143]\n\tds_read_b64 v[188:189], v16 offset:1216\n\tds_read_b64 v[190:191], v17 offset:1216\n\t"
  "v_mfma_f32_16x16x32_f16 v[148:151], v[96:99], v[160:163], v[148:151]\n\tds_read_b64 v[192:193], v16 offset:704\n\ts_waitcnt lgkmcnt(7)\n\t"
  "v_mfma_f32_16x16x32_f16 v[148:151], v[92:95], v[164:167], v[148:151]\n\tds_read_b64 v[194:195], v17 offset:704\n\tds_read_b64 v[196:197], v16 offset:192\n\tds_read_b64 v[198:199], v17 offset:192\n\t"
  "v_mfma_f32_16x16x32_f16 v[148:151], v[88:91], v[168:171], v[148:151]\n\ts_waitcnt lgkmcnt(8)\n\tv_perm_b32 v176, v176, v176, s60\n\t"
  "v_mfma_f32_16x16x32_f16 v[148:151], v[84:87], v[172:175], v[148:151]\n\tds_write_b128 v20, v[140:143] offset:2048\n\tv_perm_b32 v177, v177, v177, s60\n\t"
  "v_mfma_f32_16x16x32_f16 v[200:203], v[40:43], v[102:105], 0\n\tv_perm_b32 v178, v178, v178, s60\n\tv_perm_b32 v179, v179, v179, s60\n\t"
  "v_mfma_f32_16x16x32_f16 v[140:143], v[44:47], v[102:105], 0\n\tds_write_b128 v20, v[148:151] offset:3072\n\tv_perm_b32 v180, v180, v180, s60\n\t"
  "v_mfma_f32_16x16x32_f16 v[200:203], v[36:39], v[106:109], v[200:203]\n\ts_mov_b64 exec, 1\n\tds_add_u32 v21, v100 offset:8\n\ts_mov_b64 exec, -1\n\t"
  "v_mfma_f32_16x16x32_f16 v[140:143], v[40:43], v[106:109], v[140:143]\n\tv_perm_b32 v181, v181, v181, s60\n\tv_perm_b32 v182, v182, v182, s60\n\t"
  "v_mfma_f32_16x16x32_f16 v[140:143], v[36:39], v[110:113], v[140:143]\n\tv_perm_b32 v183, v183, v183, s60\n\t"
  "v_mfma_f32_16x16x32_f16 v[200:203], v[56:59], v[120:123], v[200:203]\n\t"
  "v_mfma_f32_16x16x32_f16 v[140:143], v[60:63], v[120:123], v[140:143]\n\t"
  "v_mfma_f32_16x16x32_f16 v[200:203], v[52:55], v[124:127], v[200:203]\n\t"
  "v_mfma_f32_16x16x32_f16 v[140:143], v[56:59], v[124:127], v[140:143]\n\t"
  "v_mfma_f32_16x16x32_f16 v[140:143], v[52:55], v[128:131], v[140:143]\n\t"
  "v_mfma_f32_16x16x32_f16 v[200:203], v[72:75], v[136:139], v[200:203]\n\t"
  "v_mfma_f32_16x16x32_f16 v[140:143], v[76:79], v[136:139], v[140:143]\n\tds_read_b32 v101, v21 offset:8\n\tds_read_b128 v[148:151], v20 offset:0\n\tds_read_b128 v[204:207], v20 offset:1024\n\t"
  "v_mfma_f32_16x16x32_f16 v[200:203], v[68:71], v[144:147], v[200:203]\n\t"
  "v_mfma_f32_16x16x32_f16 v[140:143], v[72:75], v[144:147], v[140:143]\n\t"
  "v_mfma_f32_16x16x32_f16 v[140:143], v[68:71], v[152:155], v[140:143]\n\t"
  "v_mfma_f32_16x16x32_f16 v[200:203], v[88:91], v[160:163], v[200:203]\n\t"
  "v_mfma_f32_16x16x32_f16 v[140:143], v[92:95], v[160:163], v[140:143]\n\t"
  "v_mfma_f32_16x16x32_f16 v[200:203], v[84:87], v[164:167], v[200:203]\n\t"
  "v_mfma_f32_16x16x32_f16 v[140:143], v[88:91], v[164:167], v[140:143]\n\t"
  "v_mfma_f32_16x16x32_f16 v[140:143], v[84:87], v[168:171], v[140:143]\n\ts_waitcnt lgkmcnt(0)\n\tv_readfirstlane_b32 s61, v101\n\t"
  "v_mfma_f32_16x16x32_f16 v[208:211], v[40:43], v[114:117], 0\n\t"
  "v_mfma_f32_16x16x32_f16 v[208:211], v[44:47], v[110:113], v[208:211]\n\ts_cmp_lt_u32 s61, 2\n\ts_cbranch_scc1 .Lc1_x0\n.Lc1_x0_b:\n\tv_fmac_f32 v176, v148, v26\n\tv_fmac_f32 v177, v149, v26\n\t"
  "v_fmac_f32 v178, v150, v26\n\t"
  "v_mfma_f32_16x16x32_f16 v[212:215], v[44:47], v[114:117], 0\n\tv_fmac_f32 v179, v151, v26\n\tv_fmac_f32 v180, v204, v26\n\tv_fmac_f32 v181, v205, v26\n\t"
  "v_fmac_f32 v182, v206, v26\n\tv_fmac_f32 v183, v207, v26\n\t"
  "v_mfma_f32_16x16x32_f16 v[208:211], v[48:51], v[106:109], v[208:211]\n\tv_fmac_f32 v176, v200, v26\n\tv_fmac_f32 v177, v201, v26\n\t"
  "v_mfma_f32_16x16x32_f16 v[212:215], v[48:51], v[110:113], v[212:215]\n\tv_fmac_f32 v178, v202, v26\n\tv_fmac_f32 v179, v203, v26\n\tv_cvt_pk_bf16_f32 v148, v176, v177\n\t"
  "v_cvt_pk_bf16_f32 v149, v178, v179\n\tv_fmac_f32 v180, v140, v26\n\t"
  "v_mfma_f32_16x16x32_f16 v[176:179], v[48:51], v[114:117], 0\n\tv_fmac_f32 v181, v141, v26\n\tv_fmac_f32 v182, v142, v26\n\tv_fmac_f32 v183, v143, v26\n\t"
  "v_cvt_pk_bf16_f32 v150, v180, v181\n\tv_cvt_pk_bf16_f32 v151, v182, v183\n\t"
  "v_mfma_f32_16x16x32_f16 v[208:211], v[184:187], v[102:105], v[208:211]\n\tv_permlane16_swap_b32 v148, v150\n\tv_permlane16_swap_b32 v149, v151\n\ts_setprio 3\n\tbuffer_store_dwordx4 v[148:151], v22, s[56:59], 0 offen sc1\n\ts_setprio 0\n\t"
  CST(8)
  "v_mfma_f32_16x16x32_f16 v[212:215], v[184:187], v[106:109], v[212:215]\n\tds_read_b64 v[140:141], v16 offset:1664\n\tds_read_b64 v[142:143], v17 offset:1664\n\t"
  "v_mfma_f32_16x16x32_f16 v[176:179], v[184:187], v[110:113], v[176:179]\n\tds_read_b64 v[148:149], v16 offset:1152\n\tds_read_b64 v[150:151], v17 offset:1152\n\t"
  "v_mfma_f32_16x16x32_f16 v[180:183], v[184:187], v[114:117], 0\n\tds_read_b64 v[116:117], v16 offset:640\n\tds_read_b64 v[118:119], v17 offset:640\n\t"
  "v_mfma_f32_16x16x32_f16 v[212:215], v[188:191], v[102:105], v[212:215]\n\t"
  "v_mfma_f32_16x16x32_f16 v[176:179], v[188:191], v[106:109], v[176:179]\n\tds_read_b64 v[184:185], v16 offset:128\n\tds_read_b64 v[186:187], v17 offset:128\n\t"
  "v_mfma_f32_16x16x32_f16 v[180:183], v[188:191], v[110:113], v[180:183]\n\t"
  "v_mfma_f32_16x16x32_f16 v[176:179], v[192:195], v[102:105], v[176:179]\n\t"
  "v_mfma_f32_16x16x32_f16 v[180:183], v[192:195], v[106:109], v[180:183]\n\t"
  "v_mfma_f32_16x16x32_f16 v[180:183], v[196:199], v[102:105], v[180:183]\n\t"
  "v_mfma_f32_16x16x32_f16 v[208:211], v[56:59], v[132:135], v[208:211]\n\t"
  "v_mfma_f32_16x16x32_f16 v[208:211], v[60:63], v[128:131], v[208:211]\n\t"
  "v_mfma_f32_16x16x32_f16 v[212:215], v[60:63], v[132:135], v[212:215]\n\tds_read_b64 v[102:103], v16 offset:1600\n\tds_read_b64 v[104:105], v17 offset:1600\n\t"
  "v_mfma_f32_16x16x32_f16 v[208:211], v[64:67], v[124:127], v[208:211]\n\t"
  "v_mfma_f32_16x16x32_f16 v[212:215], v[64:67], v[128:131], v[212:215]\n\t"
  "v_mfma_f32_16x16x32_f16 v[176:179], v[64:67], v[132:135], v[176:179]\n\ts_waitcnt lgkmcnt(2)\n\t"
  "v_mfma_f32_16x16x32_f16 v[208:211], v[140:143], v[120:123], v[208:211]\n\tds_read_b64 v[108:109], v16 offset:1088\n\tds_read_b64 v[110:111], v17 offset:1088\n\t"
  "v_mfma_f32_16x16x32_f16 v[212:215], v[140:143], v[124:127], v[212:215]\n\t"
  "v_mfma_f32_16x16x32_f16 v[176:179], v[140:143], v[128:131], v[176:179]\n\t"
  "v_mfma_f32_16x16x32_f16 v[180:183], v[140:143], v[132:135], v[180:183]\n\tds_read_b64 v[112:113], v16 offset:576\n\tds_read_b64 v[114:115], v17 offset:576\n\t"
  "v_mfma_f32_16x16x32_f16 v[212:215], v[148:151], v[120:123], v[212:215]\n\t"
  "v_mfma_f32_16x16x32_f16 v[176:179], v[148:151], v[124:127], v[176:179]\n\tds_read_b64 v[132:133], v16 offset:64\n\tds_read_b64 v[134:135], v17 offset:64\n\t"
  "v_mfma_f32_16x16x32_f16 v[180:183], v[148:151], v[128:131], v[180:183]\n\t"
  "v_mfma_f32_16x16x32_f16 v[176:179], v[116:119], v[120:123], v[176:179]\n\t"
  "v_mfma_f32_16x16x32_f16 v[180:183], v[116:119], v[124:127], v[180:183]\n\tv_add_u32 v22, 0x101000, v22\n\t"
  "v_mfma_f32_16x16x32_f16 v[180:183], v[184:187], v[120:123], v[180:183]\n\t"
  "v_mfma_f32_16x16x32_f16 v[208:211], v[72:75], v[156:159], v[208:211]\n\t"
  "v_mfma_f32_16x16x32_f16 v[208:211], v[76:79], v[152:155], v[208:211]\n\t"
  "v_mfma_f32_16x16x32_f16 v[212:215], v[76:79], v[156:159], v[212:215]\n\tds_read_b64 v[116:117], v16 offset:1536\n\tds_read_b64 v[118:119], v17 offset:1536\n\t"
  "v_mfma_f32_16x16x32_f16 v[208:211], v[80:83], v[144:147], v[208:211]\n\t"
  "v_mfma_f32_16x16x32_f16 v[212:215], v[80:83], v[152:155], v[212:215]\n\t"
  "v_mfma_f32_16x16x32_f16 v[176:179], v[80:83], v[156:159], v[176:179]\n\ts_waitcnt lgkmcnt(2)\n\t"
  "v_mfma_f32_16x16x32_f16 v[208:211], v[102:105], v[136:139], v[208:211]\n\tds_read_b32 v101, v27 offset:52\n\tds_read_b64 v[120:121], v16 offset:1024\n\t"
  "v_mfma_f32_16x16x32_f16 v[212:215], v[102:105], v[144:147], v[212:215]\n\tds_read_b64 v[122:123], v17 offset:1024\n\t"
  "v_mfma_f32_16x16x32_f16 v[176:179], v[102:105], v[152:155], v[176:179]\n\t"
  "v_mfma_f32_16x16x32_f16 v[180:183], v[102:105], v[156:159], v[180:183]\n\tds_read_b64 v[102:103], v16 offset:512\n\tds_read_b64 v[104:105], v17 offset:512\n\t"
  "v_mfma_f32_16x16x32_f16 v[212:215], v[108:111], v[136:139], v[212:215]\n\t"
  "v_mfma_f32_16x16x32_f16 v[176:179], v[108:111], v[144:147], v[176:179]\n\tds_read_b64 v[124:125], v16 offset:0\n\tds_read_b64 v[126:127], v17 offset:0\n\t"
  "v_mfma_f32_16x16x32_f16 v[180:183], v[108:111], v[152:155], v[180:183]\n\t"
  "v_mfma_f32_16x16x32_f16 v[176:179], v[112:115], v[136:139], v[176:179]\n\t"
  "v_mfma_f32_16x16x32_f16 v[180:183], v[112:115], v[144:147], v[180:183]\n\ts_waitcnt lgkmcnt(5)\n\tv_readfirstlane_b32 s61, v101\n\t"
  "v_mfma_f32_16x16x32_f16 v[180:183], v[132:135], v[136:139], v[180:183]\n\t"
  "v_mfma_f32_16x16x32_f16 v[208:211], v[88:91], v[172:175], v[208:211]\n\ts_cmp_lt_u32 s61, 4\n\ts_cbranch_scc1 .Lc1_ub\n.Lc1_ub_b:\n\t"
  "v_mfma_f32_16x16x32_f16 v[208:211], v[92:95], v[168:171], v[208:211]\n\tds_read_b128 v[108:111], v18 offset:2240\n\tds_read_b128 v[112:115], v18 offset:2752\n\t"
  "v_mfma_f32_16x16x32_f16 v[212:215], v[92:95], v[172:175], v[212:215]\n\tds_read_b128 v[128:131], v18 offset:3264\n\tds_read_b128 v[132:135], v18 offset:3776\n\t"
  "v_mfma_f32_16x16x32_f16 v[208:211], v[96:99], v[164:167], v[208:211]\n\t"
  "v_mfma_f32_16x16x32_f16 v[212:215], v[96:99], v[168:171], v[212:215]\n\t"
  "v_mfma_f32_16x16x32_f16 v[176:179], v[96:99], v[172:175], v[176:179]\n\tds_read_b128 v[136:139], v18 offset:2176\n\tds_read_b128 v[140:143], v18 offset:2688\n\t"
  "v_mfma_f32_16x16x32_f16 v[208:211], v[116:119], v[160:163], v[208:211]\n\tds_read_b128 v[144:147], v18 offset:3200\n\tds_read_b128 v[148:151], v18 offset:3712\n\t"
  "v_mfma_f32_16x16x32_f16 v[212:215], v[116:119], v[164:167], v[212:215]\n\t"
  "v_mfma_f32_16x16x32_f16 v[176:179], v[116:119], v[168:171], v[176:179]\n\t"
  "v_mfma_f32_16x16x32_f16 v[180:183], v[116:119], v[172:175], v[180:183]\n\ts_waitcnt lgkmcnt(6)\n\t"
  "v_mfma_f32_16x16x32_f16 v[212:215], v[120:123], v[160:163], v[212:215]\n\tds_read_b128 v[116:119], v18 offset:2112\n\tds_read_b128 v[152:155], v18 offset:2624\n\t"
  "v_mfma_f32_16x16x32_f16 v[176:179], v[120:123], v[164:167], v[176:179]\n\tds_read_b128 v[156:159], v18 offset:3136\n\tds_read_b128 v[172:175], v18 offset:3648\n\t"
  "v_mfma_f32_16x16x32_f16 v[180:183], v[120:123], v[168:171], v[180:183]\n\t"
  "v_mfma_f32_16x16x32_f16 v[176:179], v[102:105], v[160:163], v[176:179]\n\t"
  "v_mfma_f32_16x16x32_f16 v[180:183], v[102:105], v[164:167], v[180:183]\n\t"
  "v_mfma_f32_16x16x32_f16 v[180:183], v[124:127], v[160:163], v[180:183]\n\tds_read_b128 v[104:107], v18 offset:2048\n\tds_read_b128 v[120:123], v18 offset:2560\n\t"
  "v_mfma_f32_16x16x32_f16 v[208:211], v[36:39], v[108:111], v[208:211]\n\tds_read_b128 v[124:127], v18 offset:3072\n\tds_read_b128 v[160:163], v18 offset:3584\n\t"
  "v_mfma_f32_16x16x32_f16 v[180:183], v[48:51], v[108:111], v[180:183]\n\t"
  "v_mfma_f32_16x16x32_f16 v[180:183], v[44:47], v[112:115], v[180:183]\n\ts_waitcnt lgkmcnt(6)\n\t"
  "v_mfma_f32_16x16x32_f16 v[180:183], v[40:43], v[128:131], v[180:183]\n\t"
  "v_mfma_f32_16x16x32_f16 v[180:183], v[36:39], v[132:135], v[180:183]\n\t"
  "v_mfma_f32_16x16x32_f16 v[208:211], v[52:55], v[136:139], v[208:211]\n\t"
  "v_mfma_f32_16x16x32_f16 v[180:183], v[64:67], v[136:139], v[180:183]\n\t"
  "v_mfma_f32_16x16x32_f16 v[180:183], v[60:63], v[140:143], v[180:183]\n\t"
  "v_mfma_f32_16x16x32_f16 v[180:183], v[56:59], v[144:147], v[180:183]\n\t"
  "v_mfma_f32_16x16x32_f16 v[180:183], v[52:55], v[148:151], v[180:183]\n\t"
  "v_mfma_f32_16x16x32_f16 v[208:211], v[68:71], v[116:119], v[208:211]\n\tds_read_b128 v[48:51], v19 offset:5120\n\tds_read_b128 v[64:67], v19 offset:6144\n\t"
  "v_mfma_f32_16x16x32_f16 v[180:183], v[80:83], v[116:119], v[180:183]\n\t"
  "v_mfma_f32_16x16x32_f16 v[180:183], v[76:79], v[152:155], v[180:183]\n\ts_waitcnt lgkmcnt(2)\n\t"
  "v_mfma_f32_16x16x32_f16 v[180:183], v[72:75], v[156:159], v[180:183]\n\t"
  "v_mfma_f32_16x16x32_f16 v[180:183], v[68:71], v[172:175], v[180:183]\n\t"
  "v_mfma_f32_16x16x32_f16 v[208:211], v[84:87], v[104:107], v[208:211]\n\t"
  "v_mfma_f32_16x16x32_f16 v[180:183], v[96:99], v[104:107], v[180:183]\n\t"
  "v_mfma_f32_16x16x32_f16 v[180:183], v[92:95], v[120:123], v[180:183]\n\t"
  "v_mfma_f32_16x16x32_f16 v[180:183], v[88:91], v[124:127], v[180:183]\n\tds_read_b32 v101, v27 offset:64\n\ts_waitcnt lgkmcnt(1)\n\t"
  "v_mfma_f32_16x16x32_f16 v[180:183], v[84:87], v[160:163], v[180:183]\n\tds_write_b128 v20, v[208:211] offset:59392\n\tv_perm_b32 v48, v48, v48, s60\n\t"
  "v_mfma_f32_16x16x32_f16 v[212:215], v[40:43], v[108:111], v[212:215]\n\ts_setprio 3\n\tbuffer_load_dwordx4 v[28:31], v23, s[56:59], 0 offen sc1\n\tbuffer_load_dwordx4 v[32:35], v24, s[56:59], 0 offen sc1\n\ts_setprio 0\n\t"
  "v_mfma_f32_16x16x32_f16 v[176:179], v[44:47], v[108:111], v[176:179]\n\tds_write_b128 v20, v[180:183] offset:60416\n\tv_perm_b32 v49, v49, v49, s60\n\t"
  "v_mfma_f32_16x16x32_f16 v[212:215], v[36:39], v[112:115], v[212:215]\n\ts_mov_b64 exec, 1\n\tds_add_u32 v27, v100 offset:16\n\ts_mov_b64 exec, -1\n\t"
  "v_mfma_f32_16x16x32_f16 v[176:179], v[40:43], v[112:115], v[176:179]\n\tv_perm_b32 v50, v50, v50, s60\n\tv_perm_b32 v51, v51, v51, s60\n\t"
  "v_mfma_f32_16x16x32_f16 v[176:179], v[36:39], v[128:131], v[176:179]\n\tv_perm_b32 v64, v64, v64, s60\n\tv_perm_b32 v65, v65, v65, s60\n\t"
  "v_mfma_f32_16x16x32_f16 v[212:215], v[56:59], v[136:139], v[212:215]\n\tv_perm_b32 v66, v66, v66, s60\n\tv_perm_b32 v67, v67, v67, s60\n\t"
  "v_mfma_f32_16x16x32_f16 v[176:179], v[60:63], v[136:139], v[176:179]\n\ts_waitcnt lgkmcnt(3)\n\tv_readfirstlane_b32 s61, v101\n\t"
  "v_mfma_f32_16x16x32_f16 v[212:215], v[52:55], v[140:143], v[212:215]\n\t"
  "v_mfma_f32_16x16x32_f16 v[176:179], v[56:59], v[140:143], v[176:179]\n\ts_cmp_lt_u32 s61, 4\n\ts_cbranch_scc1 .Lc1_p3\n.Lc1_p3_b:\n\t"
  "v_mfma_f32_16x16x32_f16 v[176:179], v[52:55], v[144:147], v[176:179]\n\tds_read_b32 v36, v27 offset:16\n\tds_read_b128 v[40:43], v20 offset:57344\n\tds_read_b128 v[44:47], v20 offset:58368\n\t"
  "v_mfma_f32_16x16x32_f16 v[212:215], v[72:75], v[116:119], v[212:215]\n\t"
  "v_mfma_f32_16x16x32_f16 v[176:179], v[76:79], v[116:119], v[176:179]\n\t"
  "v_mfma_f32_16x16x32_f16 v[212:215], v[68:71], v[152:155], v[212:215]\n\t"
  "v_mfma_f32_16x16x32_f16 v[176:179], v[72:75], v[152:155], v[176:179]\n\t"
  "v_mfma_f32_16x16x32_f16 v[176:179], v[68:71], v[156:159], v[176:179]\n\t"
  "v_mfma_f32_16x16x32_f16 v[212:215], v[88:91], v[104:107], v[212:215]\n\t"
  "v_mfma_f32_16x16x32_f16 v[176:179], v[92:95], v[104:107], v[176:179]\n\t"
  "v_mfma_f32_16x16x32_f16 v[212:215], v[84:87], v[120:123], v[212:215]\n\ts_waitcnt lgkmcnt(0)\n\tv_readfirstlane_b32 s61, v36\n\tds_read_b32 v25, v27 offset:32\n\t"
  "v_mfma_f32_16x16x32_f16 v[176:179], v[88:91], v[120:123], v[176:179]\n\ts_cmp_lt_u32 s61, 4\n\ts_cbranch_scc1 .Lc1_x4\n.Lc1_x4_b:\n\t"
  "v_mfma_f32_16x16x32_f16 v[176:179], v[84:87], v[124:127], v[176:179]\n\tv_fmac_f32 v48, v40, v26\n\tv_fmac_f32 v49, v41, v26\n\tv_fmac_f32 v50, v42, v26\n\t"
  "v_fmac_f32 v51, v43, v26\n\tv_fmac_f32 v64, v44, v26\n\tv_fmac_f32 v65, v45, v26\n\tv_fmac_f32 v66, v46, v26\n\t"
  "v_fmac_f32 v67, v47, v26\n\tv_fmac_f32 v48, v212, v26\n\tv_fmac_f32 v49, v213, v26\n\tv_fmac_f32 v50, v214, v26\n\t"
  "v_fmac_f32 v51, v215, v26\n\tv_cvt_pk_bf16_f32 v36, v48, v49\n\tv_cvt_pk_bf16_f32 v37, v50, v51\n\tv_fmac_f32 v64, v176, v26\n\t"
  "v_fmac_f32 v65, v177, v26\n\tv_fmac_f32 v66, v178, v26\n\tv_fmac_f32 v67, v179, v26\n\tv_cvt_pk_bf16_f32 v38, v64, v65\n\t"
  "v_cvt_pk_bf16_f32 v39, v66, v67\n\ts_nop 0\n\tv_permlane16_swap_b32 v36, v38\n\tv_permlane16_swap_b32 v37, v39\n\t"
  "s_setprio 3\n\tbuffer_store_dwordx4 v[36:39], v22, s[56:59], 0 offen sc1\n\ts_setprio 0\n\ts_waitcnt lgkmcnt(0)\n\ts_nop 0\n\t"
  ".Lc_end:\n\ts_waitcnt vmcnt(1) lgkmcnt(0)"
  : C1_OPS, "=&{v[28:31]}"(fl), "=&{v[32:35]}"(wfl), "=&{v25}"(g2)
  : "{s[56:59]}"(wrs), "{s60}"((a >> 1) ? 0x03020c0cu : 0x01000c0cu), [a] "s"(a)
  : "memory", "scc", "vcc", "s61", "v26", "v27", "v36", "v37", "v38", "v39", "v40", "v41", "v42", "v43", "v44", "v45", "v46", "v47", "v48", "v49", "v50", "v51", "v52", "v53", "v54", "v55", "v56", "v57", "v58", "v59", "v60", "v61", "v62", "v63", "v64", "v65", "v66", "v67", "v68", "v69", "v70", "v71", "v72", "v73", "v74", "v75", "v76", "v77", "v78", "v79", "v80", "v81", "v82", "v83", "v84", "v85", "v86", "v87", "v88", "v89", "v90", "v91", "v92", "v93", "v94", "v95", "v96", "v97", "v98", "v99", "v100", "v101", "v102", "v103", "v104", "v105", "v106", "v107", "v108", "v109", "v110", "v111", "v112", "v113", "v114", "v115", "v116", "v117", "v118", "v119", "v120", "v121", "v122", "v123", "v124", "v125", "v126", "v127", "v128", "v129", "v130", "v131", "v132", "v133", "v134", "v135", "v136", "v137", "v138", "v139", "v140", "v141", "v142", "v143", "v144", "v145", "v146", "v147", "v148", "v149", "v150", "v151", "v152", "v153", "v154", "v155", "v156", "v157", "v158", "v159", "v160", "v161", "v162", "v163", "v164", "v165", "v166", "v167", "v168", "v169", "v170", "v171", "v172", "v173", "v174", "v175", "v176", "v177", "v178", "v179", "v180", "v181", "v182", "v183", "v184", "v185", "v186", "v187", "v188", "v189", "v190", "v191", "v192", "v193", "v194", "v195", "v196", "v197", "v198", "v199", "v200", "v201", "v202", "v203", "v204", "v205", "v206", "v207", "v208", "v209", "v210", "v211", "v212", "v213", "v214", "v215", "v216", "v217", "v218", "v219", "v220", "v221", "v222", "v223");
  ST(0, 9);
}

struct XTake { u32 p, sw, idx; bf16x8 one; u64 m0, m1, m2; };
typedef __attribute__((ext_vector_type(16))) short bf16x16;
__device__ __forceinline__ void x_pre(XTake& X, int a, int lane) {
  X.p = (u32)(L_TA + a * 8192 + lane * 8);
  X.sw = (u32)(L_SP + ((lane & 15) * 4 + a) * 4 + ((lane >> 4) == ((lane >> 2) & 3) ? 768 : 0));
  X.idx = (lane & 1) ? 0xEEEEu : 0x4444u;
  X.one = __builtin_bit_cast(bf16x8, (u32x4){0x3F803F80u, 0x3F803F80u, 0x3F803F80u, 0x3F803F80u});
  X.m0 = 0xAAAAAAAAAAAAAAAAull; X.m1 = 0xCCCCCCCCCCCCCCCCull; X.m2 = 0xF0000F0000F0000Full;
  asm volatile("" : "+v"(X.p), "+v"(X.sw), "+v"(X.idx), "+v"(X.one));
  asm volatile("" : "+s"(X.m0), "+s"(X.m1), "+s"(X.m2));
}
template <int H, int NY = 0, bool ALL = false>
__device__ __forceinline__ void x_take(const XTake& X, char* smem, rsrc_t wrs, int xin, int rb, int a, int lane, u32 sent = 0xFFFFFFFFu) {
  constexpr int tile = H ? L_TB : L_TA, NT = H ? 3 : 2;
  const LAS char* p = (const LAS char*)(size_t)(H ? opaque(X.p + (L_TB - L_TA)) : X.p);
  const LAS char* ph = (const LAS char*)(size_t)(H ? opaque(X.p + (L_TH - L_TA) - a * 4096) : 0u);
  LAS float* spw = (LAS float*)(size_t)X.sw + H * 384;
  float s = 0.f, dv[NT];
  bf16x16 xf[NT][2];
  f32x4 ss[NT], sg[NT];
  auto ld_ = [&](int o, int j) __attribute__((always_inline)) {
    if (o < 2) xf[o][j] = __builtin_shufflevector(ld_tr8<1024>(p + o * 512 + j * 4096), ld_tr8<1024>(p + o * 512 + j * 4096 + 2048), 0, 1, 2, 3, 4, 5, 6, 7, 8, 9, 10, 11, 12, 13, 14, 15);
    else xf[o][j] = __builtin_shufflevector(ld_tr8<512>(ph + j * 2048), ld_tr8<512>(ph + j * 2048 + 1024), 0, 1, 2, 3, 4, 5, 6, 7, 8, 9, 10, 11, 12, 13, 14, 15);
    SB();
  };
  auto gr_ = [&](int o, int j) __attribute__((always_inline)) {
#define XLO(v_) __builtin_shufflevector(v_, v_, 0, 1, 2, 3, 4, 5, 6, 7)
#define XHI(v_) __builtin_shufflevector(v_, v_, 8, 9, 10, 11, 12, 13, 14, 15)
    if (j == 0) asm volatile("v_mfma_f32_16x16x32_bf16 %0, %1, %1, 0" : "=&v"(sg[o]) : "v"(XLO(xf[o][0])));
    else asm volatile("v_mfma_f32_16x16x32_bf16 %0, %1, %1, %0" : "+v"(sg[o]) : "v"(XLO(xf[o][1])));
    asm volatile("v_mfma_f32_16x16x32_bf16 %0, %1, %1, %0" : "+v"(sg[o]) : "v"(XHI(xf[o][j])));
    SB();
  };
  auto sm_ = [&](int o, int j) __attribute__((always_inline)) {
    asm volatile("v_smfmac_f32_16x16x64_bf16 %0, %1, %2, %3" : "+v"(ss[o]) : "v"(X.one), "v"(xf[o][j]), "v"(X.idx));
    SB();
  };
  auto fin_ = [&](int o, auto V_) __attribute__((always_inline)) {
    float t;
    float& d = dv[o];
    asm volatile("v_cndmask_b32_e64 %0, %3, %4, %9\n\tv_cndmask_b32_e64 %1, %5, %6, %9\n\tv_cndmask_b32_e64 %0, %0, %1, %10\n\t.if %12 >= 0\n\ts_nop %12\n\t.endif\n\t"
                 "v_add_f32 %1, %7, %8\n\tv_cndmask_b32_e64 %0, %1, %0, %11\n\tv_add_f32 %2, %2, %1"
                 : "=&v"(d), "=&v"(t), "+v"(s) : "v"(sg[o][0]), "v"(sg[o][1]), "v"(sg[o][2]), "v"(sg[o][3]), "v"(ss[o][0]), "v"(ss[o][1]),
                   "s"(X.m0), "s"(X.m1), "s"(X.m2), "n"(decltype(V_)::value - 1), "v"(ss[o]));
    SB();
  };
  auto zero_ = [&]() __attribute__((always_inline)) {
#pragma unroll
    for (int o = 0; o < NT; ++o) { ss[o] = (f32x4){0.f, 0.f, 0.f, 0.f}; asm volatile("" : "+v"(ss[o])); }
    asm volatile("" : "+v"(s));
  };
  auto first_ = [&]() __attribute__((always_inline)) { gr_(0, 0); sm_(0, 0); gr_(1, 0); sm_(1, 0); };
  auto second_ = [&]() __attribute__((always_inline)) {
    gr_(0, 1); sm_(0, 1); gr_(1, 1); fin_(0, IC<0>{}); sm_(1, 1);
    if (NT == 2) { fin_(1, IC<5>{}); spw[0] = dv[0]; spw[64] = dv[1]; }
  };
  auto third_ = [&]() __attribute__((always_inline)) {
    gr_(2, 0); fin_(1, IC<0>{}); gr_(2, 1);
    spw[0] = dv[0]; spw[64] = dv[1];
    SB();
    sm_(2, 0); sm_(2, 1); fin_(NT - 1, IC<5>{});
    spw[128] = dv[NT - 1];
  };
  zero_();
  SB();
  if (H) {
    const u32 sa = opaque((u32)(L_TB + a * 8192 + (lane >> 4) * 1024 + (lane & 15) * 32 + ((lane >> 3) & 1) * 512 + ((lane >> 4) & 3) * 8 + 4));
#pragma unroll 1
    for (unsigned spins = 0; spins <= SPIN_CAP; ++spins) {
      x_dma(wrs, xin, rb, tile, a, lane);
      asm volatile("s_waitcnt vmcnt(8)" ::: "memory");
      if (__builtin_amdgcn_ballot_w64(*(const LAS u32*)(size_t)sa == sent) == 0ull) break;
      FAILC(20);
    }
  } else asm volatile("s_waitcnt vmcnt(%0)" :: "n"(4 + NY) : "memory");
  if (H && B6 == 0) { __builtin_amdgcn_s_barrier(); ST(xin != OFF_X1, 14); asm volatile("" ::: "memory"); }
  ld_(0, 0); ld_(1, 0);
  if (!ALL && !(H && B6 == 1)) first_();
  asm volatile("s_waitcnt vmcnt(%0)" :: "n"(4 * (NT - 2) + NY) : "memory");
  if (H && B6) { __builtin_amdgcn_s_barrier(); ST(xin != OFF_X1, 14); asm volatile("" ::: "memory"); }
  ld_(0, 1); ld_(1, 1);
  if (ALL || (H && B6 == 1)) first_();
  second_();
  if (NT == 3) {
    asm volatile("s_waitcnt vmcnt(%0)" :: "n"(NY) : "memory");
    ld_(2, 0); ld_(2, 1);
    third_();
  }
  if (__builtin_expect(__builtin_amdgcn_ballot_w64(s != s) != 0ull, 0)) {
    for (unsigned spins = 0;; ++spins) {
      FAILC((xin == OFF_X1 ? 1 : 9) + H);
      if (H) *(u32*)(smem + L_MI + M_RD) = xin == OFF_X1 ? 1 : 2;
      x_dma(wrs, xin, rb, tile, a, lane);
      asm volatile("s_waitcnt vmcnt(0)" ::: "memory");
      s = 0.f;
#pragma unroll
      for (int o = 0; o < 2 * NT; ++o) ld_(o >> 1, o & 1);
      zero_();
      first_(); second_();
      if (NT == 3) third_();
      if (!__any(s != s)) break;
      bool st = false;
#pragma unroll 1
      for (int e = 0; e < 4 * NT; ++e) { const u32x4 v = *(const u32x4*)(smem + (e < 8 ? tile + a * 8192 : L_TH + a * 4096 - 8192) + lane * 16 + e * 1024); st |= v[1] == sent || v[3] == sent; }
      if (!__any(st) || spins > SPIN_CAP) break;
    }
  }
}
#define PIN(x_) asm volatile("" : "+v"(x_))
struct HaloMsg { u32x4 k, v; };
__device__ __forceinline__ void halo_ld(HaloMsg& h, rsrc_t wrs, int ho) { h.k = ldw(wrs, ho); h.v = ldw(wrs, ho + 1024); }
__device__ __forceinline__ bool halo_ok(const HaloMsg& h, u32 tag) {
  return __builtin_amdgcn_ballot_w64((opaque((h.k[1] ^ tag) | (h.k[3] ^ tag)) | opaque((h.v[1] ^ tag) | (h.v[3] ^ tag))) != 0u) == 0ull;
}
struct AttC { f32x4 mp, mo, mf; u32x2 one; u32 sl, su; };
__device__ __forceinline__ void att_pre(AttC& C, int lane, bool first) {
  const float NINF = -__builtin_inff();
  const u64 pm[4] = {0x0fff00ff000f0000ull, 0x3fff03ff003f0003ull, 0xf000ff00fff0ffffull, 0xc000fc00ffc0fffcull};
#pragma unroll
  for (int e = 0; e < 4; ++e) {
    const u64 par = (e & 1) ? 0xAAAAAAAAAAAAAAAAull : 0x5555555555555555ull;
    C.mp[e] = __builtin_amdgcn_inverse_ballot_w64(pm[e >> 1] & par) ? 0.f : NINF;
    C.mo[e] = __builtin_amdgcn_inverse_ballot_w64(pm[2 + (e >> 1)] & par) ? 0.f : NINF;
    C.mf[e] = first ? NINF : C.mp[e];
  }
  C.one = (u32x2){0x3F803F80u, 0x3F803F80u};
  C.sl = (lane & 1) ? 0x01000c0cu : 0x0c0c0100u; C.su = (lane & 1) ? 0x03020c0cu : 0x0c0c0302u;
  asm volatile("" : "+v"(C.mp), "+v"(C.mo), "+v"(C.mf), "+v"(C.one), "+v"(C.sl), "+v"(C.su));
}
template <class F>
__device__ __forceinline__ void attention(const AttC& C, u32 qp, HaloMsg& hm, int ho, u32 tag, rsrc_t wrs, int oo, int fid, F mid) {
  u32x4 vv, ow;
  u32x2 q0;
  f32x4 l0, o1;
  float inv;
#define SOFT(a_, b_, c_, d_, F1_, F2_, W_, V_, O_)                                                                                                      \
  F1_ "v_permlane16_swap_b32 v232, v233\n\tv_max_f32 v234, v232, v233\n\tv_max_f32 v235, v232, v233\n\t"                                                \
  F2_ "v_permlane32_swap_b32 v234, v235\n\tv_max_f32 v232, v234, v235\n\tv_mul_f32 v232, 0xbeb8aa3b, v232\n\t"                                          \
  "v_fmamk_f32 " a_ ", " a_ ", 0x3eb8aa3b, v232\n\tv_fmamk_f32 " b_ ", " b_ ", 0x3eb8aa3b, v232\n\t"                                                    \
  "v_fmamk_f32 " c_ ", " c_ ", 0x3eb8aa3b, v232\n\tv_fmamk_f32 " d_ ", " d_ ", 0x3eb8aa3b, v232\n\t"                                                    \
  "v_exp_f32 " a_ ", " a_ "\n\tv_exp_f32 " b_ ", " b_ "\n\tv_exp_f32 " c_ ", " c_ "\n\tv_exp_f32 " d_ ", " d_ "\n\t"                                    \
  "v_cvt_pk_bf16_f32 v232, " a_ ", " b_ "\n\tv_cvt_pk_bf16_f32 v233, " c_ ", " d_ "\n\t"                                                                \
  "v_perm_b32 v226, v232, v232, %[sl]\n\tv_perm_b32 v227, v232, v232, %[su]\n\t"                                                                        \
  "v_mfma_f32_16x16x16_bf16 v[236:239], %[one], v[232:233], 0\n\t"                                                                                      \
  "v_perm_b32 v224, v233, v233, %[sl]\n\tv_perm_b32 v225, v233, v233, %[su]\n\t" W_ "\n\ts_nop 0\n\t"                                                   \
  "v_mfma_f32_16x16x32_bf16 " O_ ", " V_ ", v[224:227], 0\n\t"
  asm volatile(
      "ds_read2st64_b32 v[214:215], %[qp] offset0:4 offset1:5\n\tds_read2st64_b32 v[216:217], %[qp] offset0:6 offset1:7\n\t"
      "ds_read2st64_b32 v[212:213], %[qp] offset0:0 offset1:1\n\tds_read2st64_b32 v[218:219], %[qp] offset0:10 offset1:11\n\t"
      "ds_read2st64_b32 v[200:201], %[qp] offset0:2 offset1:3\n\tds_read2st64_b32 v[202:203], %[qp] offset0:8 offset1:9\n\t"
      "s_waitcnt lgkmcnt(4)\n\tv_mfma_f32_16x16x16_bf16 v[220:223], v[216:217], v[214:215], %[mp]\n\t"
      "s_waitcnt lgkmcnt(3)\n\tv_mfma_f32_16x16x16_bf16 v[224:227], v[212:213], v[214:215], %[mo]\n\t"
      "s_waitcnt lgkmcnt(2)\n\tv_mfma_f32_16x16x16_bf16 v[228:231], v[216:217], v[218:219], %[mo]\n\t"
      "v_max_f32 v220, v220, v221\n\tv_max_f32 v221, v222, v223\n\tv_max_f32 v232, v220, v221\n\t"
      "v_max_f32 v222, v224, v225\n\tv_max_f32 v223, v226, v227\n\t"
      "v_max3_f32 v233, v232, v222, v223\n\tv_max3_f32 v232, v232, v222, v223\n\t"
      SOFT("v220", "v221", "v222", "v223", "s_nop 0\n\tv_max_f32 v228, v228, v229\n\t", "v_max_f32 v229, v230, v231\n\tv_max_f32 v230, v228, v229\n\t", "s_waitcnt lgkmcnt(0)", "v[200:203]", "v[244:247]")
      "v_rcp_f32 v248, v236"
      : "=&{v[200:203]}"(vv), "=&{v[218:219]}"(q0), "=&{v[228:231]}"(l0), "=&{v[244:247]}"(o1), "=&{v248}"(inv)
      : [qp] "v"(qp), [mp] "v"(C.mp), [mo] "v"(C.mo), [one] "v"(C.one), [sl] "v"(C.sl), [su] "v"(C.su)
      : "memory", "v212", "v213", "v214", "v215", "v216", "v217", "v220", "v221", "v222", "v223", "v224", "v225", "v226", "v227", "v232", "v233", "v234", "v235", "v236", "v237", "v238", "v239");
  mid();
  {
    u32 n = SPIN_CAP;
    asm volatile("1:\n\tv_bitop3_b32 v232, v209, v211, %[tg] bitop3:0x7e\n\tv_bitop3_b32 v233, v205, v207, %[tg] bitop3:0x7e\n\tv_or_b32 v232, v232, v233\n\tv_cmp_ne_u32 vcc, 0, v232\n\t"
                 "s_cbranch_vccz 2f\n\ts_sleep 2\n\tbuffer_load_dwordx4 v[208:211], %[ho], %[rs], 0 offen sc1\n\tbuffer_load_dwordx4 v[204:207], %[ho], %[rs], 0 offen offset:1024 sc1\n\t"
                 "s_sub_u32 %[n], %[n], 1\n\ts_waitcnt vmcnt(0)\n\ts_cbranch_scc0 1b\n2:"
                 : "+{v[208:211]}"(hm.k), "+{v[204:207]}"(hm.v), [n] "+s"(n) : [tg] "s"(tag), [ho] "v"(ho), [rs] "s"(wrs) : "memory", "vcc", "scc", "v232", "v233");
  }
  asm volatile(
      "v_mov_b32 v209, v210\n\tv_mov_b32 v205, v206\n\ts_nop 0\n\tv_mfma_f32_16x16x16_bf16 v[220:223], v[208:209], v[218:219], %[mf]\n\t"
      "v_mul_f32 v244, v244, v248\n\tv_mul_f32 v245, v245, v248\n\tv_mul_f32 v246, v246, v248\n\tv_mul_f32 v247, v247, v248\n\t"
      "v_cvt_pk_bf16_f32 v242, v244, v245\n\tv_cvt_pk_bf16_f32 v243, v246, v247\n\ts_nop 1\n\t"
      "v_max_f32 v220, v220, v221\n\tv_max_f32 v221, v222, v223\n\t"
      "v_max3_f32 v233, v230, v220, v221\n\tv_max3_f32 v232, v230, v220, v221\n\t"
      SOFT("v220", "v221", "v228", "v229", "s_nop 1\n\t", "s_nop 1\n\t", "s_nop 0", "v[202:205]", "v[244:247]")
      "v_rcp_f32 v248, v236\n\ts_nop 6\n\t"
      "v_mul_f32 v244, v244, v248\n\tv_mul_f32 v245, v245, v248\n\tv_mul_f32 v246, v246, v248\n\tv_mul_f32 v247, v247, v248\n\t"
      "v_cvt_pk_bf16_f32 v240, v244, v245\n\tv_cvt_pk_bf16_f32 v241, v246, v247\n\ts_nop 0\n\t"
      "v_permlane16_swap_b32 v240, v242\n\tv_permlane16_swap_b32 v241, v243"
      : "=&{v[240:243]}"(ow), "+{v[208:211]}"(hm.k), "+{v[204:207]}"(hm.v), "+{v[228:231]}"(l0), "+{v[244:247]}"(o1), "+{v248}"(inv)
      : "{v[200:203]}"(vv), "{v[218:219]}"(q0), [mf] "v"(C.mf), [one] "v"(C.one), [sl] "v"(C.sl), [su] "v"(C.su)
      : "v220", "v221", "v222", "v223", "v224", "v225", "v226", "v227", "v232", "v233", "v234", "v235", "v236", "v237", "v238", "v239");
  stw(wrs, oo, ow);
  asm volatile("s_nop 1" :: "v"(ow) : "memory");
}
__device__ __forceinline__ void o_dma(rsrc_t wrs, int og, int ot, int a, int lane) {
  dma4k(wrs, lane * 16, og + a * 4096, ot + a * 4096);
  dma4k(wrs, lane * 16, og + 16384 + a * 4096, ot + 16384 + a * 4096);
}
__device__ __forceinline__ void o_get(rsrc_t wrs, int vo, int g0, int g1, int ot, u32 sa, u32 mark, int fid) {
#pragma unroll 1
  for (unsigned spins = 0; spins <= SPIN_CAP; ++spins) {
    dma4k(wrs, vo, g0, ot);
    dma4k(wrs, vo, g1, ot + 16384);
    asm volatile("s_waitcnt vmcnt(4)" ::: "memory");
    if (__builtin_amdgcn_ballot_w64(*(const LAS u32*)(size_t)sa == mark) == 0ull) break;
    FAILC(fid);
  }
}
template <bool L1, class F>
__device__ __forceinline__ void wo_mma2(f32x4 (&acc)[2], char* smem, int ot, rsrc_t wrs, int og, int a, int lane, const bf16x8 (&wof)[16], u32 cnt, const f32x4 (&r0)[2], const u32 (&qa)[4], u32 sent, F late) {
  u32x4 of[32];
#define LDO2(s_) of[s_] = *(const LAS u32x4*)((const LAS char*)(size_t)qa[(s_) >> 3] + (ot - L_TA) + ((s_) & 1) * 16384 + (((s_) >> 1) & 3) * 1024)
#define WMM(s_) do { SB(); if ((s_) < 2) asm volatile("v_mfma_f32_16x16x32_bf16 %0, %1, %2, %3" : "=&v"(acc[(s_) & 1]) \
        : "v"(L1 ? __builtin_bit_cast(bf16x8, of[s_]) : wof[0]), "v"(L1 ? wof[0] : __builtin_bit_cast(bf16x8, of[s_])), "v"(r0[(s_) & 1])); \
      else if (L1) asm volatile("v_mfma_f32_16x16x32_bf16 %0, %1, %2, %0" : "+v"(acc[(s_) & 1]) : "v"(of[s_]), "v"(wof[(s_) >> 1])); \
      else asm volatile("v_mfma_f32_16x16x32_bf16 %0, %1, %2, %0" : "+v"(acc[(s_) & 1]) : "v"(wof[(s_) >> 1]), "v"(of[s_])); SB(); } while (0)
  u32 mx = 0, cv;
#pragma unroll
  for (int s = 0; s < 8; ++s) { LDO2(s); SB(); }
#pragma unroll
  for (int s = 0; s < 3; ++s) {
    WMM(s);
#pragma unroll
    for (int e = 3 * s; e < 3 * s + 3 && e < 8; ++e) mx = max(max(mx, of[e][1]), of[e][3]);
    asm volatile("" : "+v"(mx));
  }
  if (__builtin_expect(!__all(mx < 0xFF810000u), 0)) {
    for (unsigned spins = 0; spins <= SPIN_CAP; ++spins) {
      FAILC((L1 ? 5 : 13) + (ot == L_TB));
      __builtin_amdgcn_s_sleep(2);
      o_dma(wrs, og, ot, a, lane);
      asm volatile("s_waitcnt vmcnt(0)" ::: "memory");
      bool st = false;
#pragma unroll
      for (int s = 0; s < 8; ++s) { LDO2(s); st |= of[s][1] == sent || of[s][3] == sent; }
      if (!__any(st)) break;
    }
    WMM(0); WMM(1); WMM(2);
    asm volatile("s_nop 7" : "+v"(acc[0]), "+v"(acc[1]));
  }
  cnt_add(smem, C_OT, lane);
  cv = *(const volatile LAS u32*)(size_t)(u32)(L_MI + C_OT);
  SB();
#pragma unroll
  for (int s = 3; s < WS5; ++s) WMM(s);
  {
    u32 n = 1u << 24;
    asm volatile("1:\n\tv_readfirstlane_b32 vcc_lo, %0\n\ts_sub_u32 %1, %1, 1\n\ts_cbranch_scc1 2f\n\ts_cmp_ge_u32 vcc_lo, %3\n\ts_cbranch_scc1 2f\n\tds_read_b32 %0, %2\n\ts_waitcnt lgkmcnt(0)\n\ts_branch 1b\n2:"
                 : "+v"(cv), "+s"(n) : "v"(L_MI + C_OT), "s"(cnt) : "memory", "vcc", "scc");
  }
#pragma unroll
  for (int s = 8; s < WS5 + 8; ++s) { LDO2(s); SB(); }
#pragma unroll
  for (int s = WS5; s < 32; ++s) {
    WMM(s);
    if (s + 8 < 32) LDO2(s + 8);
    if (s == 24) late();
  }
  asm volatile("s_nop 7" : "+v"(acc[0]), "+v"(acc[1]));
}
template <class F>
__device__ __forceinline__ void wo_last(f32x4 (&acc)[2], char* smem, rsrc_t wrs, int og, int a, int lane, const bf16x8 (&wof)[16], const f32x4 (&r0)[2], const u32 (&qa)[4], u32 sent, F put) {
  u32x4 of[32];
#define LDO3(q_, o_, s_) of[s_] = *(const LAS u32x4*)((const LAS char*)(size_t)q_[((s_) >> 2) & 3] + (o_) + ((s_) >> 4) * 16384 + ((s_) & 3) * 1024)
#define WM3(s_) do { SB(); if (((s_) & 15) == 0) asm volatile("v_mfma_f32_16x16x32_bf16 %0, %1, %2, %3" : "=&v"(acc[(s_) >> 4]) : "v"(wof[0]), "v"(of[s_]), "v"(r0[(s_) >> 4])); \
      else asm volatile("v_mfma_f32_16x16x32_bf16 %0, %1, %2, %0" : "+v"(acc[(s_) >> 4]) : "v"(wof[(s_) & 15]), "v"(of[s_])); SB(); } while (0)
  u32 qb[4];
#pragma unroll
  for (int e = 0; e < 4; ++e) qb[e] = qa[e];
  SB();
  __builtin_amdgcn_s_barrier();
  ST(1, 19);
#pragma unroll 1
  for (u32 pass = 0;; ++pass) {
    asm volatile("" : "+v"(qb[0]), "+v"(qb[1]), "+v"(qb[2]), "+v"(qb[3]) :: "memory");
#pragma unroll
    for (int s = 0; s < WLD; ++s) { LDO3(qb, L_TB - L_TA, s); SB(); }
#pragma unroll
    for (int s = 0; s < 32; ++s) {
      WM3(s);
      if (s + WLD == 16) asm volatile("s_cmp_lg_u32 %0, 0\n\ts_cbranch_scc1 1f\n\ts_barrier\n1:" :: "s"(__builtin_amdgcn_readfirstlane(pass)) : "memory", "scc");
      if (s + WLD < 32) LDO3(qb, L_TB - L_TA, s + WLD);
    }
    asm volatile("s_nop 7" : "+v"(acc[0]), "+v"(acc[1]));
    put();
    SB();
    if (__builtin_expect(__builtin_amdgcn_ballot_w64(__builtin_isunordered(acc[0][0], acc[1][0])) == 0ull, 1)) break;
    asm volatile("s_waitcnt vmcnt(0)" ::: "memory");
    FAILC(14);
    if (pass == 0) {
      lds_barrier();
      const u32 fx = *(const u32*)(smem + L_MI + M_FX);
#pragma unroll
      for (int e = 0; e < 4; ++e) qb[e] -= ((fx >> (8 * ((a + e) & 3))) & 1u) * (L_TB - L_TA);
    } else {
      u32 m = 1;
#pragma unroll 1
      for (int s = 0; s < 32; ++s) {
        const u32x4 v = *(const LAS u32x4*)(size_t)((s & 8 ? (s & 4 ? qb[3] : qb[2]) : (s & 4 ? qb[1] : qb[0])) + (L_TB - L_TA) + (s >> 4) * 16384 + (s & 3) * 1024);
        m = min(min(m, v[1] ^ sent), v[3] ^ sent);
      }
      if (__builtin_amdgcn_ballot_w64(m == 0u) == 0ull || pass > SPIN_CAP) break;
      lds_barrier();
      o_dma(wrs, og, __builtin_amdgcn_readfirstlane(qb[0]) + (L_TB - L_TA) - a * 4096, a, lane);
      asm volatile("s_waitcnt vmcnt(0)" ::: "memory");
      lds_barrier();
    }
  }
}
struct WoPre { f32x4 r[2]; int so; u32 xs, ao; };
template <bool L1>
__device__ __forceinline__ void wo_pre(WoPre& P, const u32x2 (&res)[2], char* ws, int H, int rb, int hb, int a, int lane) {
  const int i16 = lane & 15, kg = lane >> 4, R0 = 1024 * H + 32 * rb;
#pragma unroll
  for (int t = 0; t < 2; ++t) P.r[t] = (f32x4){lo2f(res[t][0]), hi2f(res[t][0]), lo2f(res[t][1]), hi2f(res[t][1])};
  if (L1) {
    P.so = OFF_X2 + ((32 * hb + 8 * a + (i16 >> 1)) * XS_ + R0 + 16 * (lane & 1) + 4 * kg) * 4;
    P.xs = (u32)(L_XS + H * 512 + lane * 8);
    P.ao = (u32)(OFF_ST2 + ((R0 & ~255) + ((lane >> 1) & 3) * 64 + ((R0 >> 2) & 63) + 4 * (lane & 1) + kg) * 16 + (lane & 8));
    asm volatile("" : "+v"(P.r[0]), "+v"(P.r[1]), "+v"(P.so), "+v"(P.xs), "+v"(P.ao));
  } else {
    P.so = ((R0 + 16 * (kg & 1) + i16) * 256 + 32 * hb + 8 * a + 4 * (kg >> 1)) * 4;
    asm volatile("" : "+v"(P.r[0]), "+v"(P.r[1]), "+v"(P.so));
  }
}
template <bool L1>
__device__ __forceinline__ void wo_epi(const f32x4 (&acc)[2], const WoPre& P, char* smem, rsrc_t wrs, char* ws, u32* out, int H, int lane, u32 ONES) {
  if (L1) {
    f32x4 lo_, hi_;
    pair_x(lo_, hi_, acc[0], acc[1]);
    u32x4 v;
#pragma unroll
    for (int e = 0; e < 4; ++e) v[e] = pk2(lo_[e], hi_[e]);
    asm volatile("" : "+v"(v));
    stw(wrs, P.so, v);
    SB();
    float sv, q0, q1, q2, q3;
#define RD_(d_, a_, c_, m_) "v_add_f32_dpp " d_ ", " a_ ", " a_ " " c_ " row_mask:0xf bank_mask:" m_ "\n\t"
    asm volatile("v_dot2_f32_bf16 %5, %13, %17, 0\n\tv_dot2_f32_bf16 %6, %14, %17, 0\n\tv_dot2_f32_bf16 %7, %15, %17, 0\n\tv_dot2_f32_bf16 %8, %16, %17, 0\n\t"
                 "v_dot2_f32_bf16 %9, %13, %13, 0\n\tv_dot2_f32_bf16 %10, %14, %14, 0\n\tv_dot2_f32_bf16 %11, %15, %15, 0\n\tv_dot2_f32_bf16 %12, %16, %16, 0\n\t"
                 RD_("%1", "%5", "row_ror:8", "0x3") RD_("%2", "%6", "row_ror:8", "0x3") RD_("%3", "%7", "row_ror:8", "0x3") RD_("%4", "%8", "row_ror:8", "0x3")
                 RD_("%1", "%9", "row_ror:8", "0xc") RD_("%2", "%10", "row_ror:8", "0xc") RD_("%3", "%11", "row_ror:8", "0xc") RD_("%4", "%12", "row_ror:8", "0xc")
                 RD_("%5", "%1", "row_shl:4", "0x5") RD_("%6", "%2", "row_shl:4", "0x5") RD_("%5", "%3", "row_shr:4", "0xa") RD_("%6", "%4", "row_shr:4", "0xa")
                 "s_nop 0\n\t"
                 RD_("%1", "%5", "quad_perm:[2,3,0,1]", "0xf") RD_("%2", "%6", "quad_perm:[2,3,0,1]", "0xf")
                 "v_cndmask_b32_e64 %0, %1, %2, %18"
                 : "=&v"(sv), "=&v"(q0), "=&v"(q1), "=&v"(q2), "=&v"(q3), "=&v"(lo_[0]), "=&v"(lo_[1]), "=&v"(lo_[2]), "=&v"(lo_[3]), "=&v"(hi_[0]), "=&v"(hi_[1]), "=&v"(hi_[2]), "=&v"(hi_[3])
                 : "v"(v[0]), "v"(v[1]), "v"(v[2]), "v"(v[3]), "v"(ONES), "s"(0xCCCCCCCCCCCCCCCCull));
    const float hf = __builtin_floorf(sv * 0.5f);
    const u32 lo = (u32)(fmaf(sv, 0.5f, -hf) * 4294967296.0f);
    const u64 mine = ((u64)(u32)(int)hf << 32) | (u64)((lo & ~63u) | 1u);
    const u64 old = __hip_atomic_fetch_add((LAS u64*)(size_t)P.xs, mine, __ATOMIC_RELAXED, __HIP_MEMORY_SCOPE_WORKGROUP);
    if (((u32)old & 63u) == 3u) __hip_atomic_fetch_add((GAS u64*)((GAS char*)ws + P.ao), old + mine, __ATOMIC_RELAXED, __HIP_MEMORY_SCOPE_AGENT);
  } else {
    auto r0s = __builtin_amdgcn_permlane16_swap(pk2(acc[0][0], acc[0][1]), pk2(acc[1][0], acc[1][1]), false, false);
    auto r1s = __builtin_amdgcn_permlane16_swap(pk2(acc[0][2], acc[0][3]), pk2(acc[1][2], acc[1][3]), false, false);
    const rsrc_t ors = __builtin_amdgcn_make_buffer_rsrc((void*)out, 0, 2 * MB_, 0x00020000);
    if (H == 0) __builtin_amdgcn_raw_buffer_store_b128((u32x4){r0s[0], r1s[0], r0s[1], r1s[1]}, ors, P.so, 0, OUTA);
    else __builtin_amdgcn_raw_buffer_store_b128((u32x4){r0s[0], r1s[0], r0s[1], r1s[1]}, ors, P.so, 0, OUTB);
  }
}
template <bool L1>
__device__ __forceinline__ void res_ld(u32x2 (&res)[2], const char* smem, int tile, int hb, int a, int lane) {
#pragma unroll
  for (int t = 0; t < 2; ++t) {
    const int o = tile + t * 512 + (4 * hb + a) * 1024;
    if (L1) res[t] = *(const u32x2*)(smem + o + (lane & 15) * 32 + (lane >> 4) * 8);
    else res[t] = __builtin_bit_cast(u32x2, __builtin_amdgcn_ds_read_tr16_b64_v4i16((LAS bf16x4*)(size_t)(u32)(o + lane * 8)));
  }
}
template <bool L1>
__device__ __forceinline__ void a_blayer(char* smem, rsrc_t wrs, char* ws, u32* out, int t, int a, bf16x8 (&wof)[16], u32 tag, u32x4 wfl, ConvK& ck, XTake& xt, AttC& ac) {
  const u32 t9 = L1 ? 0u : KADD(2, now_(), D9);
  if (!L1) { KSLP(13, SLP_B4); SB(); }
  t = (int)opaque((u32)t);
  const int lane = t & 63, i16 = lane & 15, kg = lane >> 4;
  const int XIN = L1 ? OFF_X1 : OFF_X3, OBUF = L1 ? OFF_O1 : OFF_O2;
  const int bid = BIDX;
  const int rb = RB_OF(bid), hb = HB_OF(bid);
  if (!L1) { x_pre(xt, a, lane); att_pre(ac, lane, rb == 0); }
  const int og = OBUF + rb * 32768;
  u32x2 resa[2], resb[2];
  const int wfo = OFF_ST0 + 8192 + (hb * 4 + a) * 128 + (lane & 3) * 16;
#define LDWO(e_) wof[e_] = __builtin_bit_cast(bf16x8, ldws(wrs, ((hb * 64 + a) * 64 + lane) * 16, OFF_WO + ((4 * a + (e_)) & 15) * 4096))
  auto ld_wo = [&](int e0, int e1) __attribute__((always_inline)) {
#pragma unroll
    for (int e = e0; e < e1; ++e) LDWO(e);
    __builtin_amdgcn_sched_barrier(0);
  };
  const u32 mark = x0_mark(tag), sent = L1 ? mark : 0xFFFFFFFFu;
  if (L1) {
    tags_wait(wfl, wrs, wfo, tag, 6); ld_wo(0, WOE);
    SB();
    x_pre(xt, a, lane); SB();
    x_take<0, WOE, true>(xt, smem, wrs, XIN, rb, a, lane, sent);
  }
  ST(!L1, 10);
  SB();
  lds_barrier();
  ST(!L1, 11);
  const u32 t5 = L1 ? 0u : KADD(20, now_(), D5);
  res_ld<L1>(resa, smem, L_TA, hb, a, lane);
  auto pf_x3 = [&]() __attribute__((always_inline)) {
    const u32x4 SENT = {0xFFFFFFFFu, 0xFFFFFFFFu, 0xFFFFFFFFu, 0xFFFFFFFFu};
    const int xo = OFF_X3 + (8 * a + (lane >> 3)) * XBS + ((bid & 7) * 32 + (bid >> 3)) * 128 + (lane & 7) * 16;
    stw(wrs, xo, SENT); stw(wrs, xo + 32 * XBS, SENT);
  };
  const int oo = (int)opaque((u32)(OBUF + (((rb * 2 + (kg & 1)) * 16 + hb * 2 + (a >> 1)) * 64 + (2 * (a & 1) + (kg >> 1)) * 16 + i16) * 16));
  const u32 ONES = opaque(0x3F803F80u);
  if (L1) att_pre(ac, lane, rb == 0);
  if (L1) pf_x3();
  if (L1) SB();
  const int ho = (int)opaque((u32)((rb == 0 ? WSB : KVO(L1, rb, hb * 4 + a)) + lane * 16));
  const u32 qp = opaque((u32)(L_QB + a * 3072 + lane * 4));
  const int tso = (int)opaque((u32)(OFF_TS + rb * 128 + (lane & 7) * 8)), tsw = (int)opaque((u32)(t == hb ? tso : WSB));
  HaloMsg hm;
  SB();
  if (L1) KSLP(1, SLP_XB1); else gate(tmax(t9, t5));
  ST(!L1, 12);
  x_take<1, 0, false>(xt, smem, wrs, XIN, 32 + rb, a, lane, sent);
  ST(!L1, 13);
  if (L1) SB();
  lds_barrier();
  ST(!L1, 15);
  if (L1) KSLP(3, SLP_H1); else KSLP(4, SLP_H2);
  halo_ld(hm, wrs, ho);
  u32 t7 = 0, tq = 0;
  if (!L1) {
    t7 = (u32)__builtin_amdgcn_s_memrealtime();
    tq = KADD(6, now_(), DQ2);
    __builtin_amdgcn_raw_buffer_store_b64((u32x2){t7, tag}, wrs, tsw, 0, 16);
  }
  SB();
  attention(ac, qp, hm, ho, rb == 0 ? 0u : L1 ? mark : tag, wrs, oo, L1 ? 3 : 11, [&]() __attribute__((always_inline)) { if (L1) SB(); });
  const u32 to = L1 ? 0u : KADD(21, now_(), DO2);
  ST(!L1, 16);
  if (L1) __builtin_amdgcn_raw_buffer_store_b32(tag, wrs, (bid | t) == 0 ? OFF_EP : WSB, 0, 16);
  if (L1) ld_wo(WOE, 16);
  res_ld<L1>(resb, smem, L_TB, hb, a, lane);
  cnt_add(smem, C_OB, lane);
  WoPre wp, wq;
  u32 qa[4];
#pragma unroll
  for (int e = 0; e < 4; ++e) qa[e] = opaque((u32)(L_TA + ((a + e) & 3) * 4096 + lane * 16));
  wo_pre<L1>(wp, resa, ws, 0, rb, hb, a, lane);
  wo_pre<L1>(wq, resb, ws, 1, rb, hb, a, lane);
  const u32 sa = opaque((u32)(L_TA + a * 4096 + lane * 64 + (lane & 14) * 4 + 4));
  SB();
  if (L1) KSLP(5, SLP_OA1); else gate(tmax(tq, to));
  ST(!L1, 17);
  u32x2 ts;
  if (!L1) asm volatile("buffer_load_dwordx2 %0, %1, %2, 0 offen sc1" : "=&{v[200:201]}"(ts) : "v"(tso), "s"(wrs) : "memory");
  o_get(wrs, lane * 16, og + a * 4096, og + 16384 + a * 4096, L_TA + a * 4096, sa, mark, L1 ? 21 : 23);
  if (!L1) {
    int d;
    asm volatile("v_subrev_u32 %0, %2, v200\n\tv_cmp_eq_u32 vcc, %3, v201\n\tv_min_i32 %0, 0x96, %0\n\tv_cndmask_b32 %0, 64, %0, vcc\n\t"
                 "s_nop 1\n\tv_max_i32_dpp %0, %0, %0 quad_perm:[1,0,3,2] row_mask:0xf bank_mask:0xf\n\ts_nop 1\n\tv_max_i32_dpp %0, %0, %0 quad_perm:[2,3,0,1] row_mask:0xf bank_mask:0xf\n\t"
                 "s_nop 1\n\tv_max_i32_dpp %0, %0, %0 row_half_mirror row_mask:0xf bank_mask:0xf\n\tv_max_i32 %0, 0, %0\n\tv_mul_u32_u24 %0, 20, %0"
                 : "=&v"(d), "+{v[200:201]}"(ts) : "s"(t7), "s"(tag) : "vcc", "memory");
    *(int*)(smem + L_MI + M_DL) = d;
    cnt_add(smem, C_OV, lane);
  }
  u32 ob = 0;
  int ob0 = og + MB_ + a * 4096, ob1 = ob0 + 16384;
  if (L1) asm volatile("" : "+s"(ob0), "+s"(ob1));
  asm volatile("s_waitcnt vmcnt(0)" ::: "memory");
  f32x4 acc[2];
  wo_mma2<L1>(acc, smem, L_TA, wrs, og, a, lane, wof, L1 ? 4 : 12, wp.r, qa, mark, [&]() __attribute__((always_inline)) { if (L1) ob = *(const volatile LAS u32*)(size_t)(u32)(L_MI + C_OB); });
  ST(!L1, 18);
  wo_epi<L1>(acc, wp, smem, wrs, ws, out, 0, lane, ONES);
  if (L1) {
    cnt_add(smem, C_X2, lane);
    if (__builtin_expect((u32)__builtin_amdgcn_readfirstlane(ob) < 8u, 0)) cnt_spin(smem, C_OB, 8);
    dma4k(wrs, lane * 16, ob0, L_TB + a * 4096);
    dma4k(wrs, lane * 16, ob1, L_TB + 16384 + a * 4096);
    a_conv_pre(ck, a, lane);
    ST(0, 19);
    asm volatile("s_waitcnt vmcnt(0)" ::: "memory");
    wo_mma2<L1>(acc, smem, L_TB, wrs, og + MB_, a, lane, wof, 8, wq.r, qa, mark, []() {});
    wo_epi<L1>(acc, wq, smem, wrs, ws, out, 1, lane, ONES);
  } else wo_last(acc, smem, wrs, og + MB_, a, lane, wof, wq.r, qa, mark, [&]() __attribute__((always_inline)) { wo_epi<L1>(acc, wq, smem, wrs, ws, out, 1, lane, ONES); });
  ST(!L1, 20); if (L1) ST(1, 1);
}

#define WREGS(X) \
  X(0, 76, 77, 78, 79) X(1, 80, 81, 82, 83) X(2, 84, 85, 86, 87) X(3, 88, 89, 90, 91) X(4, 92, 93, 94, 95) X(5, 96, 97, 98, 99) \
  X(6, 100, 101, 102, 103) X(7, 104, 105, 106, 107) X(8, 108, 109, 110, 111) X(9, 112, 113, 114, 115) X(10, 116, 117, 118, 119) X(11, 120, 121, 122, 123) \
  X(12, 124, 125, 126, 127) X(13, 128, 129, 130, 131) X(14, 132, 133, 134, 135) X(15, 136, 137, 138, 139) X(16, 140, 141, 142, 143) X(17, 144, 145, 146, 147) \
  X(18, 148, 149, 150, 151) X(19, 152, 153, 154, 155) X(20, 156, 157, 158, 159) X(21, 160, 161, 162, 163) X(22, 164, 165, 166, 167) X(23, 168, 169, 170, 171) \
  X(24, 172, 173, 174, 175) X(25, 176, 177, 178, 179) X(26, 180, 181, 182, 183) X(27, 184, 185, 186, 187) X(28, 188, 189, 190, 191) X(29, 192, 193, 194, 195) \
  X(30, 196, 197, 198, 199) X(31, 200, 201, 202, 203) X(32, 204, 205, 206, 207) X(33, 208, 209, 210, 211) X(34, 212, 213, 214, 215) X(35, 216, 217, 218, 219) \
  X(36, 220, 221, 222, 223) X(37, 224, 225, 226, 227) X(38, 228, 229, 230, 231) X(39, 232, 233, 234, 235) X(40, 236, 237, 238, 239) X(41, 240, 241, 242, 243) \
  X(42, 244, 245, 246, 247) X(43, 248, 249, 250, 251) X(44, 252, 253, 254, 255) X(45, 64, 65, 66, 67) X(46, 68, 69, 70, 71) X(47, 72, 73, 74, 75)
__device__ __forceinline__ void w_dma(rsrc_t r, int voff, int vof2, int soff, int lds) {
  asm volatile("s_mov_b32 m0, %3\n\ts_nop 0\n\tbuffer_load_dwordx4 %0, %1, %2 offen lds\n\t"
               "s_add_u32 m0, m0, 3072\n\ts_nop 0\n\tbuffer_load_dwordx4 %0, %1, %2 offen offset:1024 lds\n\t"
               "s_add_u32 m0, m0, 3072\n\ts_nop 0\n\tbuffer_load_dwordx4 %4, %1, %2 offen offset:2048 lds"
               :: "v"(voff), "s"(r), "s"(soff), "s"(lds), "v"(vof2) : "memory", "scc");
}
template <int I>
__device__ __forceinline__ void w_ld(bf16x8& r, int vo, rsrc_t rs) {
#define X(n_, a_, b_, c_, d_) if constexpr (I == n_) asm volatile("buffer_load_dwordx4 %0, %1, %2, %3 offen offset:%4 sc1" : "={v[" #a_ ":" #d_ "]}"(r) : "v"(vo), "s"(rs), "s"(OFF_WQ + (I / 3) * 98304), "n"((I % 3) * 1024) : "memory");
  WREGS(X)
#undef X
}
template <int I, int O>
__device__ __forceinline__ void w_get(bf16x8& r, u32 a) {
#define X(n_, a_, b_, c_, d_) if constexpr (I == n_) asm volatile("ds_read_b64_tr_b16 v[" #a_ ":" #b_ "], %1 offset:%2\n\tds_read_b64_tr_b16 v[" #c_ ":" #d_ "], %1 offset:%3" : "=&{v[" #a_ ":" #d_ "]}"(r) : "v"(a), "n"(O), "n"(O + 2048) : "memory");
  WREGS(X)
#undef X
}
template <int I>
__device__ __forceinline__ void w_pin(const bf16x8& r) {
#define X(n_, a_, b_, c_, d_) if constexpr (I == n_) asm volatile("" :: "{v[" #a_ ":" #d_ "]}"(r));
  WREGS(X)
#undef X
}
template <int I>
__device__ __forceinline__ void w_sum(f32x4& d, const bf16x8& one, u32 idx, const bf16x8 (&wf)[48]) {
  w_pin<I>(wf[I]); w_pin<(I + 1) % 48>(wf[(I + 1) % 48]);
  asm volatile("v_smfmac_f32_16x16x64_bf16 %0, %1, v[%3:%3+7], %2" : "+v"(d) : "v"(one), "v"(idx), "n"(I < 45 ? 76 + 4 * I : 64 + 4 * (I - 45)));
  w_pin<I>(wf[I]); w_pin<(I + 1) % 48>(wf[(I + 1) % 48]);
}
struct GPre { u32 ap, l4, fr, fq, sp, wx, mo, oo, tg, ly; };
template <bool L1>
__device__ __forceinline__ void g_pre(GPre& P, int g, u32 tag) {
  const int lane = lane_id(), bid = BIDX;
  const int rb = RB_OF(bid), hb = HB_OF(bid);
  P.mo = (u32)KVO(L1, (rb + 1) & 31, hb * 4 + g);
  P.oo = (u32)((L1 ? OFF_O1 : OFF_O2) + MB_ + ((rb * 32 + hb * 2 + (g >> 1)) * 64 + 32 * (g & 1)) * 16);
  P.tg = L1 ? x0_mark(tag) : tag;
  P.ly = L1 ? 1 : 2;
  asm volatile("" : "+{s60}"(P.mo), "+{s61}"(P.oo), "+{s62}"(P.tg), "+{s63}"(P.ly));
  P.ap = (u32)(L_TA + lane * 8);
  P.l4 = (u32)(lane * 4);
  P.fr = (u32)(L_FS + g * 768 + (lane & 15) * 4);
  P.fq = (u32)(L_FS + g * 768 + (lane >> 4) * 16);
  P.sp = (u32)(L_SP + (lane & 31) * 16);
  P.wx = (u32)(L_WX + g * 3072 + lane * 16);
  asm volatile("" : "+{v0}"(P.ap), "+{v1}"(P.l4), "+{v2}"(P.fr), "+{v3}"(P.fq), "+{v4}"(P.sp), "+{v5}"(P.wx));
}
template <bool L1, class F>
__device__ __forceinline__ void g_blayer(char* smem, rsrc_t wrs, int g, u32 tag, bf16x8 (&wf)[48], GPre& P, F mid) {
  if (!L1) ST(1, 10);
  auto pin = [&](auto I_) __attribute__((always_inline)) { w_pin<decltype(I_)::value>(wf[decltype(I_)::value]); };
  static_for<0, 45>(pin);
#define G_OPS : "+{v0}"(P.ap), "+{v1}"(P.l4), "+{v2}"(P.fr), "+{v3}"(P.fq), "+{v4}"(P.sp), "+{v5}"(P.wx) \
  : "{s[56:59]}"(wrs), "{s60}"(P.mo), "{s61}"(P.oo), "{s62}"(P.tg), "{s63}"(P.ly), "{s64}"(L_QB + g * 3072), "{s65}"(L_FS + g * 768), "{s66}"(L_WX + g * 3072) \
  : "memory", "scc", "vcc", "v6", "v7", "v8", "v9", "v10", "v11", "v12", "v13", "v14", "v15", "v16", "v17", "v18", "v19", "v20", "v21", "v22", "v23", "v24", "v25", "v26", "v27", "v28", "v29", "v30", "v31", "v32", "v33", "v34", "v35", "v36", "v37", "v38", "v39", "v40", "v41", "v42", "v43", "v44", "v45", "v46", "v47", "v48", "v49", "v50", "v51", "v52", "v53", "v54", "v55", "v56", "v57", "v58", "v59", "v60", "v61", "v62", "v63", "v64", "v65", "v66", "v67", "v68", "v69", "v70", "v71", "v72", "v73", "v74", "v75", "s54", "s55", "s67", "s68", "s69", "s70", "s71", "s72", "s73", "s74", "s75", "s76", "s77"
  if constexpr (L1) asm volatile(".Lg_body:\n\t"
  "ds_read_b128 v[56:59], v5 offset:1024\n\tds_read_b128 v[40:43], v5 offset:2048\n\tds_read_b128 v[16:19], v5 offset:0\n\t"
  "s_waitcnt lgkmcnt(3)\n\ts_barrier\n\t" GST(11)
  "ds_read_b64_tr_b16 v[28:29], v0 offset:31232\n\tds_read_b64_tr_b16 v[30:31], v0 offset:32256\n\tds_read_b64_tr_b16 v[48:49], v0 offset:512\n\tds_read_b64_tr_b16 v[50:51], v0 offset:1536\n\t"
  "ds_read_b128 v[64:67], v4\n\tds_read_b128 v[72:75], v4 offset:768\n\tds_read_b64_tr_b16 v[36:37], v0 offset:2560\n\tds_read_b64_tr_b16 v[38:39], v0 offset:3584\n\t"
  "ds_read_b64_tr_b16 v[24:25], v0 offset:4608\n\tds_read_b64_tr_b16 v[26:27], v0 offset:5632\n\ts_mov_b32 s68, 0xaaaaaaaa\n\ts_mov_b32 s69, 0xaaaaaaaa\n\t"
  "s_mov_b32 s70, 0xf0000\n\ts_mov_b32 s71, 0xfff00ff\n\ts_mov_b32 s72, 0x3f0003\n\ts_mov_b32 s73, 0x3fff03ff\n\t"
  "s_mov_b32 s74, 0xfff0ffff\n\ts_mov_b32 s75, 0xf000ff00\n\ts_mov_b32 s76, 0xffc0fffc\n\ts_mov_b32 s77, 0xc000fc00\n\t"
  "s_mov_b32 s67, 0x0c0c0100\n\ts_mov_b32 s55, 0x0c0c0302\n\ts_waitcnt lgkmcnt(8)\n\t"
  "v_mfma_f32_16x16x32_bf16 v[12:15], v[56:59], v[28:31], 0\n\tds_read_b64_tr_b16 v[4:5], v0 offset:6656\n\t"
  "v_mfma_f32_16x16x32_bf16 v[56:59], v[28:31], v[40:43], 0\n\tds_read_b64_tr_b16 v[6:7], v0 offset:7680\n\t"
  "v_mfma_f32_16x16x32_bf16 v[68:71], v[16:19], v[28:31], 0\n\ts_waitcnt lgkmcnt(2)\n\t"
  "v_mfma_f32_16x16x32_bf16 v[12:15], v[80:83], v[48:51], v[12:15]\n\tds_read_b64_tr_b16 v[44:45], v0 offset:8704\n\tv_add_f32 v64, v64, v65\n\t"
  "v_mfma_f32_16x16x32_bf16 v[56:59], v[48:51], v[84:87], v[56:59]\n\tds_read_b64_tr_b16 v[46:47], v0 offset:9728\n\tv_add_f32 v66, v66, v67\n\t"
  "v_mfma_f32_16x16x32_bf16 v[68:71], v[76:79], v[48:51], v[68:71]\n\tv_add_f32 v72, v72, v73\n\tv_add_f32 v64, v64, v66\n\t"
  "v_mfma_f32_16x16x32_bf16 v[12:15], v[92:95], v[36:39], v[12:15]\n\tds_read_b64_tr_b16 v[40:41], v0 offset:10752\n\tv_add_f32 v74, v74, v75\n\t"
  "v_mfma_f32_16x16x32_bf16 v[56:59], v[36:39], v[96:99], v[56:59]\n\tds_read_b64_tr_b16 v[42:43], v0 offset:11776\n\tv_mul_f32 v65, 0x3b000000, v64\n\t"
  "v_mfma_f32_16x16x32_bf16 v[68:71], v[88:91], v[36:39], v[68:71]\n\tv_add_f32 v72, v72, v74\n\tv_mul_f32_e64 v66, -v65, v65\n\t"
  "v_mfma_f32_16x16x32_bf16 v[12:15], v[104:107], v[24:27], v[12:15]\n\tds_read_b64_tr_b16 v[8:9], v0 offset:12800\n\tv_fmac_f32 v66, 0x3b000000, v72\n\t"
  "v_mfma_f32_16x16x32_bf16 v[56:59], v[24:27], v[108:111], v[56:59]\n\tds_read_b64_tr_b16 v[10:11], v0 offset:13824\n\tv_max_f32 v66, 0, v66\n\t"
  "v_mfma_f32_16x16x32_bf16 v[68:71], v[100:103], v[24:27], v[68:71]\n\tv_add_f32 v66, 0x3727c5ac, v66\n\ts_waitcnt lgkmcnt(4)\n\t"
  "v_mfma_f32_16x16x32_bf16 v[12:15], v[116:119], v[4:7], v[12:15]\n\tds_read_b64_tr_b16 v[28:29], v0 offset:14848\n\tv_add_u32 v67, s65, v1\n\t"
  "v_mfma_f32_16x16x32_bf16 v[56:59], v[4:7], v[120:123], v[56:59]\n\tds_read_b64_tr_b16 v[30:31], v0 offset:15872\n\t"
  "v_mfma_f32_16x16x32_bf16 v[68:71], v[112:115], v[4:7], v[68:71]\n\tv_rsq_f32 v66, v66\n\t"
  "v_mfma_f32_16x16x32_bf16 v[12:15], v[128:131], v[44:47], v[12:15]\n\tds_read_b64_tr_b16 v[4:5], v0 offset:16896\n\tds_write2st64_b32 v67, v66, v65 offset1:1\n\t"
  "v_mfma_f32_16x16x32_bf16 v[56:59], v[44:47], v[132:135], v[56:59]\n\tds_read_b64_tr_b16 v[6:7], v0 offset:17920\n\tds_read_b128 v[24:27], v3 offset:576\n\t"
  "v_mfma_f32_16x16x32_bf16 v[68:71], v[124:127], v[44:47], v[68:71]\n\tds_read2_b32 v[44:45], v2 offset0:64 offset1:80\n\ts_waitcnt lgkmcnt(7)\n\t"
  "v_mfma_f32_16x16x32_bf16 v[12:15], v[140:143], v[40:43], v[12:15]\n\tds_read_b64_tr_b16 v[16:17], v0 offset:18944\n\tds_read_b128 v[36:39], v3 offset:256\n\t"
  "v_mfma_f32_16x16x32_bf16 v[56:59], v[40:43], v[144:147], v[56:59]\n\tds_read_b64_tr_b16 v[18:19], v0 offset:19968\n\tds_read_b128 v[32:35], v3 offset:512\n\t"
  "v_mfma_f32_16x16x32_bf16 v[68:71], v[136:139], v[40:43], v[68:71]\n\tds_read_b32 v47, v2 offset:640\n\t"
  "v_mfma_f32_16x16x32_bf16 v[12:15], v[152:155], v[8:11], v[12:15]\n\tds_read_b64_tr_b16 v[20:21], v0 offset:20992\n\t"
  "v_mfma_f32_16x16x32_bf16 v[56:59], v[8:11], v[156:159], v[56:59]\n\tds_read_b64_tr_b16 v[22:23], v0 offset:22016\n\t"
  "v_mfma_f32_16x16x32_bf16 v[68:71], v[148:151], v[8:11], v[68:71]\n\ts_waitcnt lgkmcnt(9)\n\t"
  "v_mfma_f32_16x16x32_bf16 v[12:15], v[164:167], v[28:31], v[12:15]\n\tds_read_b64_tr_b16 v[64:65], v0 offset:23040\n\ts_waitcnt lgkmcnt(8)\n\t"
  "v_mfma_f32_16x16x32_bf16 v[56:59], v[28:31], v[168:171], v[56:59]\n\tds_read_b64_tr_b16 v[66:67], v0 offset:24064\n\tv_mul_f32_e64 v52, -v44, v24\n\t"
  "v_mfma_f32_16x16x32_bf16 v[68:71], v[160:163], v[28:31], v[68:71]\n\tv_mul_f32_e64 v53, -v44, v25\n\tv_mul_f32_e64 v54, -v44, v26\n\t"
  "v_mfma_f32_16x16x32_bf16 v[12:15], v[176:179], v[4:7], v[12:15]\n\tds_read_b64_tr_b16 v[48:49], v0 offset:25088\n\tv_mul_f32_e64 v55, -v44, v27\n\t"
  "v_mfma_f32_16x16x32_bf16 v[56:59], v[4:7], v[180:183], v[56:59]\n\tds_read_b64_tr_b16 v[50:51], v0 offset:26112\n\ts_waitcnt lgkmcnt(5)\n\t"
  "v_mfma_f32_16x16x32_bf16 v[68:71], v[172:175], v[4:7], v[68:71]\n\tv_mul_f32_e64 v72, -v36, v47\n\tv_mul_f32_e64 v73, -v37, v47\n\t"
  "v_mfma_f32_16x16x32_bf16 v[12:15], v[188:191], v[16:19], v[12:15]\n\t"
  GST(20)
  "ds_read_b64_tr_b16 v[8:9], v0 offset:27136\n\tds_read2_b32 v[40:41], v2 offset0:0 offset1:16\n\t"
  "v_mfma_f32_16x16x32_bf16 v[56:59], v[16:19], v[192:195], v[56:59]\n\tds_read_b64_tr_b16 v[10:11], v0 offset:28160\n\tv_mul_f32_e64 v74, -v38, v47\n\t"
  "v_mfma_f32_16x16x32_bf16 v[68:71], v[184:187], v[16:19], v[68:71]\n\tv_mul_f32_e64 v75, -v39, v47\n\ts_waitcnt lgkmcnt(5)\n\t"
  "v_mfma_f32_16x16x32_bf16 v[12:15], v[200:203], v[20:23], v[12:15]\n\tds_read_b64_tr_b16 v[16:17], v0 offset:29184\n\tv_mul_f32_e64 v60, -v44, v32\n\t"
  "v_mfma_f32_16x16x32_bf16 v[56:59], v[20:23], v[204:207], v[56:59]\n\tds_read_b64_tr_b16 v[18:19], v0 offset:30208\n\tv_mul_f32_e64 v61, -v44, v33\n\t"
  "v_mfma_f32_16x16x32_bf16 v[68:71], v[196:199], v[20:23], v[68:71]\n\tv_mul_f32_e64 v62, -v44, v34\n\tv_mul_f32_e64 v63, -v44, v35\n\t"
  "v_mfma_f32_16x16x32_bf16 v[12:15], v[212:215], v[64:67], v[12:15]\n\tds_read_b64_tr_b16 v[36:37], v0 offset:0\n\t"
  "v_mfma_f32_16x16x32_bf16 v[56:59], v[64:67], v[216:219], v[56:59]\n\tds_read_b64_tr_b16 v[38:39], v0 offset:1024\n\t"
  "v_mfma_f32_16x16x32_bf16 v[68:71], v[208:211], v[64:67], v[68:71]\n\ts_waitcnt lgkmcnt(4)\n\t"
  "v_mfma_f32_16x16x32_bf16 v[12:15], v[224:227], v[48:51], v[12:15]\n\tds_read_b64_tr_b16 v[4:5], v0 offset:2048\n\t"
  "v_mfma_f32_16x16x32_bf16 v[56:59], v[48:51], v[228:231], v[56:59]\n\tds_read_b64_tr_b16 v[6:7], v0 offset:3072\n\tv_lshl_add_u32 v43, v1, 2, s60\n\t"
  "v_mfma_f32_16x16x32_bf16 v[68:71], v[220:223], v[48:51], v[68:71]\n\t"
  "v_mfma_f32_16x16x32_bf16 v[12:15], v[236:239], v[8:11], v[12:15]\n\tds_read_b64_tr_b16 v[20:21], v0 offset:4096\n\tds_read_b128 v[48:51], v3 offset:320\n\t"
  "v_mfma_f32_16x16x32_bf16 v[56:59], v[8:11], v[240:243], v[56:59]\n\tds_read_b64_tr_b16 v[22:23], v0 offset:5120\n\t"
  "v_mfma_f32_16x16x32_bf16 v[68:71], v[232:235], v[8:11], v[68:71]\n\tds_read_b128 v[8:11], v3 offset:64\n\ts_waitcnt lgkmcnt(6)\n\t"
  "v_mfma_f32_16x16x32_bf16 v[12:15], v[248:251], v[16:19], v[12:15]\n\tds_read_b64_tr_b16 v[28:29], v0 offset:6144\n\t"
  "v_mfma_f32_16x16x32_bf16 v[56:59], v[16:19], v[252:255], v[56:59]\n\tds_read_b64_tr_b16 v[30:31], v0 offset:7168\n\t"
  "v_mfma_f32_16x16x32_bf16 v[68:71], v[244:247], v[16:19], v[68:71]\n\tv_fma_f32 v12, -v45, v24, v12\n\tv_fma_f32 v13, -v45, v25, v13\n\t"
  "v_mfma_f32_16x16x32_bf16 v[52:55], v[80:83], v[36:39], v[52:55]\n\tds_read_b64_tr_b16 v[16:17], v0 offset:8192\n\tv_fma_f32 v14, -v45, v26, v14\n\t"
  "v_mfma_f32_16x16x32_bf16 v[72:75], v[36:39], v[84:87], v[72:75]\n\tds_read_b64_tr_b16 v[18:19], v0 offset:9216\n\tv_fma_f32 v15, -v45, v27, v15\n\t"
  "v_mfma_f32_16x16x32_bf16 v[60:63], v[76:79], v[36:39], v[60:63]\n\tv_mul_f32 v12, v12, v41\n\ts_waitcnt lgkmcnt(5)\n\t"
  "v_mfma_f32_16x16x32_bf16 v[52:55], v[92:95], v[4:7], v[52:55]\n\tds_read_b64_tr_b16 v[64:65], v0 offset:10240\n\tv_mul_f32 v13, v13, v41\n\t"
  "v_mfma_f32_16x16x32_bf16 v[72:75], v[4:7], v[96:99], v[72:75]\n\tds_read_b64_tr_b16 v[66:67], v0 offset:11264\n\tv_mul_f32 v14, v14, v41\n\t"
  "v_mfma_f32_16x16x32_bf16 v[60:63], v[88:91], v[4:7], v[60:63]\n\t"
  GST(17)
  "v_mul_f32 v15, v15, v41\n\tv_cvt_pk_bf16_f32 v12, v12, v13\n\t"
  "v_mfma_f32_16x16x32_bf16 v[52:55], v[104:107], v[20:23], v[52:55]\n\tds_read_b64_tr_b16 v[4:5], v0 offset:12288\n\tv_cvt_pk_bf16_f32 v14, v14, v15\n\t"
  "v_mfma_f32_16x16x32_bf16 v[72:75], v[20:23], v[108:111], v[72:75]\n\tds_read_b64_tr_b16 v[6:7], v0 offset:13312\n\tv_mov_b32 v13, s62\n\t"
  "v_mfma_f32_16x16x32_bf16 v[60:63], v[100:103], v[20:23], v[60:63]\n\tv_mov_b32 v15, s62\n\ts_waitcnt lgkmcnt(4)\n\t"
  "v_mfma_f32_16x16x32_bf16 v[52:55], v[116:119], v[28:31], v[52:55]\n\tds_read_b64_tr_b16 v[20:21], v0 offset:14336\n\tv_fma_f32 v56, -v48, v47, v56\n\t"
  "v_mfma_f32_16x16x32_bf16 v[72:75], v[28:31], v[120:123], v[72:75]\n\tds_read_b64_tr_b16 v[22:23], v0 offset:15360\n\tv_fma_f32 v57, -v49, v47, v57\n\t"
  "v_mfma_f32_16x16x32_bf16 v[60:63], v[112:115], v[28:31], v[60:63]\n\tv_fma_f32 v58, -v50, v47, v58\n\tv_fma_f32 v59, -v51, v47, v59\n\t"
  "v_mfma_f32_16x16x32_bf16 v[52:55], v[128:131], v[16:19], v[52:55]\n\tds_read_b64_tr_b16 v[36:37], v0 offset:16384\n\tv_mul_f32 v56, v56, v8\n\t"
  "v_mfma_f32_16x16x32_bf16 v[72:75], v[16:19], v[132:135], v[72:75]\n\tds_read_b64_tr_b16 v[38:39], v0 offset:17408\n\tv_mul_f32 v57, v57, v9\n\t"
  "v_mfma_f32_16x16x32_bf16 v[60:63], v[124:127], v[16:19], v[60:63]\n\tv_mul_f32 v58, v58, v10\n\ts_waitcnt lgkmcnt(4)\n\t"
  "v_mfma_f32_16x16x32_bf16 v[52:55], v[140:143], v[64:67], v[52:55]\n\tds_read_b64_tr_b16 v[16:17], v0 offset:18432\n\tv_mul_f32 v59, v59, v11\n\t"
  "v_mfma_f32_16x16x32_bf16 v[72:75], v[64:67], v[144:147], v[72:75]\n\tds_read_b64_tr_b16 v[18:19], v0 offset:19456\n\tv_cvt_pk_bf16_f32 v56, v56, v57\n\t"
  "v_mfma_f32_16x16x32_bf16 v[60:63], v[136:139], v[64:67], v[60:63]\n\tv_cvt_pk_bf16_f32 v58, v58, v59\n\tv_mov_b32 v57, s62\n\t"
  "v_mfma_f32_16x16x32_bf16 v[52:55], v[152:155], v[4:7], v[52:55]\n\tds_read_b64_tr_b16 v[8:9], v0 offset:20480\n\tv_mov_b32 v59, s62\n\t"
  "v_mfma_f32_16x16x32_bf16 v[72:75], v[4:7], v[156:159], v[72:75]\n\tds_read_b64_tr_b16 v[10:11], v0 offset:21504\n\tv_fma_f32 v68, -v45, v32, v68\n\t"
  "v_mfma_f32_16x16x32_bf16 v[60:63], v[148:151], v[4:7], v[60:63]\n\tv_add_u32 v46, s64, v1\n\ts_waitcnt lgkmcnt(4)\n\t"
  "v_mfma_f32_16x16x32_bf16 v[52:55], v[164:167], v[20:23], v[52:55]\n\tds_read_b64_tr_b16 v[48:49], v0 offset:22528\n\tv_fma_f32 v69, -v45, v33, v69\n\t"
  "v_mfma_f32_16x16x32_bf16 v[72:75], v[20:23], v[168:171], v[72:75]\n\tds_read_b64_tr_b16 v[50:51], v0 offset:23552\n\tv_fma_f32 v70, -v45, v34, v70\n\t"
  "v_mfma_f32_16x16x32_bf16 v[60:63], v[160:163], v[20:23], v[60:63]\n\tv_fma_f32 v71, -v45, v35, v71\n\tv_mul_f32 v68, v68, v41\n\t"
  "v_mfma_f32_16x16x32_bf16 v[52:55], v[176:179], v[36:39], v[52:55]\n\tds_read_b64_tr_b16 v[28:29], v0 offset:24576\n\tv_mul_f32 v69, v69, v41\n\t"
  "v_mfma_f32_16x16x32_bf16 v[72:75], v[36:39], v[180:183], v[72:75]\n\tds_read_b64_tr_b16 v[30:31], v0 offset:25600\n\tv_mul_f32 v70, v70, v41\n\t"
  "v_mfma_f32_16x16x32_bf16 v[60:63], v[172:175], v[36:39], v[60:63]\n\tv_mul_f32 v71, v71, v41\n\ts_waitcnt lgkmcnt(4)\n\t"
  "v_mfma_f32_16x16x32_bf16 v[52:55], v[188:191], v[16:19], v[52:55]\n\tds_read_b64_tr_b16 v[20:21], v0 offset:26624\n\tv_cvt_pk_bf16_f32 v68, v68, v69\n\t"
  "v_mfma_f32_16x16x32_bf16 v[72:75], v[16:19], v[192:195], v[72:75]\n\tds_read_b64_tr_b16 v[22:23], v0 offset:27648\n\tv_cvt_pk_bf16_f32 v70, v70, v71\n\t"
  "v_mfma_f32_16x16x32_bf16 v[60:63], v[184:187], v[16:19], v[60:63]\n\t"
  "v_mfma_f32_16x16x32_bf16 v[52:55], v[200:203], v[8:11], v[52:55]\n\t"
  GST(13)
  "ds_read_b64_tr_b16 v[36:37], v0 offset:28672\n\t"
  "v_mfma_f32_16x16x32_bf16 v[72:75], v[8:11], v[204:207], v[72:75]\n\ts_barrier\n\tds_read_b64_tr_b16 v[38:39], v0 offset:29696\n\t"
  "v_mfma_f32_16x16x32_bf16 v[60:63], v[196:199], v[8:11], v[60:63]\n\tbuffer_store_dwordx4 v[12:15], v43, s[56:59], 0 offen offset:0 sc1\n\ts_waitcnt lgkmcnt(4)\n\t"
  "v_mfma_f32_16x16x32_bf16 v[52:55], v[212:215], v[48:51], v[52:55]\n\tds_read_b64_tr_b16 v[64:65], v0 offset:30720\n\tbuffer_store_dwordx4 v[56:59], v43, s[56:59], 0 offen offset:1024 sc1\n\t"
  "v_mfma_f32_16x16x32_bf16 v[72:75], v[48:51], v[216:219], v[72:75]\n\tds_read_b64_tr_b16 v[66:67], v0 offset:31744\n\tv_lshl_add_u32 v4, v1, 2, s66\n\tv_add_u32 v0, 0x8000, v0\n\t"
  "v_mfma_f32_16x16x32_bf16 v[60:63], v[208:211], v[48:51], v[60:63]\n\tds_read_b128 v[42:45], v4 offset:1024\n\tds_read_b128 v[8:11], v4 offset:2048\n\t"
  "v_mfma_f32_16x16x32_bf16 v[52:55], v[224:227], v[28:31], v[52:55]\n\tds_read_b64_tr_b16 v[16:17], v0 offset:0\n\tds_read_b128 v[48:51], v4 offset:0\n\t"
  "v_mfma_f32_16x16x32_bf16 v[72:75], v[28:31], v[228:231], v[72:75]\n\tds_read_b64_tr_b16 v[18:19], v0 offset:1024\n\tv_min_u32 v41, 188, v1\n\t"
  "v_mfma_f32_16x16x32_bf16 v[60:63], v[220:223], v[28:31], v[60:63]\n\tds_read_b128 v[4:7], v3\n\ts_waitcnt lgkmcnt(8)\n\t"
  "v_mfma_f32_16x16x32_bf16 v[52:55], v[236:239], v[20:23], v[52:55]\n\tds_read_b64_tr_b16 v[28:29], v0 offset:2048\n\tv_lshlrev_b32 v41, 2, v41\n\t"
  "v_mfma_f32_16x16x32_bf16 v[72:75], v[20:23], v[240:243], v[72:75]\n\tds_read_b64_tr_b16 v[30:31], v0 offset:3072\n\tv_add_u32 v41, 0x22c00, v41\n\t"
  "v_mfma_f32_16x16x32_bf16 v[60:63], v[232:235], v[20:23], v[60:63]\n\t"
  "v_mfma_f32_16x16x32_bf16 v[52:55], v[248:251], v[36:39], v[52:55]\n\tds_read_b64_tr_b16 v[20:21], v0 offset:8192\n\t"
  "v_mfma_f32_16x16x32_bf16 v[72:75], v[36:39], v[252:255], v[72:75]\n\tds_read_b64_tr_b16 v[22:23], v0 offset:9216\n\t"
  "v_mfma_f32_16x16x32_bf16 v[60:63], v[244:247], v[36:39], v[60:63]\n\ts_waitcnt lgkmcnt(5)\n\t"
  "v_mfma_f32_16x16x32_bf16 v[52:55], v[42:45], v[64:67], v[52:55]\n\tds_read_b64_tr_b16 v[42:43], v0 offset:10240\n\t"
  "v_mfma_f32_16x16x32_bf16 v[72:75], v[64:67], v[8:11], v[72:75]\n\tds_read_b64_tr_b16 v[44:45], v0 offset:11264\n\t"
  "v_mfma_f32_16x16x32_bf16 v[60:63], v[48:51], v[64:67], v[60:63]\n\t"
  GST(12)
  "v_mul_f32 v52, v52, v40\n\tv_mul_f32 v53, v53, v40\n\t"
  "v_mfma_f32_16x16x32_bf16 v[8:11], v[80:83], v[16:19], 0\n\tds_read_b64_tr_b16 v[36:37], v0 offset:16384\n\tv_mul_f32 v54, v54, v40\n\t"
  "v_mfma_f32_16x16x32_bf16 v[48:51], v[16:19], v[84:87], 0\n\tds_read_b64_tr_b16 v[38:39], v0 offset:17408\n\tv_mul_f32 v55, v55, v40\n\t"
  "v_mfma_f32_16x16x32_bf16 v[64:67], v[76:79], v[16:19], 0\n\tv_cvt_pk_bf16_f32 v52, v52, v53\n\ts_waitcnt lgkmcnt(4)\n\t"
  "v_mfma_f32_16x16x32_bf16 v[8:11], v[92:95], v[28:31], v[8:11]\n\tds_read_b64_tr_b16 v[16:17], v0 offset:18432\n\tv_cvt_pk_bf16_f32 v54, v54, v55\n\t"
  "v_mfma_f32_16x16x32_bf16 v[48:51], v[28:31], v[96:99], v[48:51]\n\tds_read_b64_tr_b16 v[18:19], v0 offset:19456\n\tv_mul_f32 v60, v60, v40\n\t"
  "v_mfma_f32_16x16x32_bf16 v[64:67], v[88:91], v[28:31], v[64:67]\n\tv_mul_f32 v61, v61, v40\n\tv_mul_f32 v62, v62, v40\n\t"
  "v_mfma_f32_16x16x32_bf16 v[8:11], v[128:131], v[20:23], v[8:11]\n\tds_read_b64_tr_b16 v[28:29], v0 offset:24576\n\tds_write2st64_b32 v46, v12, v14 offset0:0 offset1:1\n\tds_write2st64_b32 v46, v52, v54 offset0:6 offset1:7\n\t"
  "v_mfma_f32_16x16x32_bf16 v[48:51], v[20:23], v[132:135], v[48:51]\n\tds_read_b64_tr_b16 v[30:31], v0 offset:25600\n\tds_write2st64_b32 v46, v56, v58 offset0:2 offset1:3\n\t"
  "v_mfma_f32_16x16x32_bf16 v[64:67], v[124:127], v[20:23], v[64:67]\n\tds_write2st64_b32 v46, v68, v70 offset0:4 offset1:5\n\tv_mul_f32 v63, v63, v40\n\tv_cvt_pk_bf16_f32 v60, v60, v61\n\t"
  "v_cvt_pk_bf16_f32 v62, v62, v63\n\ts_waitcnt lgkmcnt(8)\n\t"
  "v_mfma_f32_16x16x32_bf16 v[8:11], v[140:143], v[42:45], v[8:11]\n\tds_read_b64_tr_b16 v[12:13], v0 offset:26624\n\tds_write2st64_b32 v46, v60, v62 offset0:10 offset1:11\n\t"
  "v_mfma_f32_16x16x32_bf16 v[48:51], v[42:45], v[144:147], v[48:51]\n\tds_read_b64_tr_b16 v[14:15], v0 offset:27648\n\tv_mul_f32 v72, v72, v4\n\t"
  "v_mfma_f32_16x16x32_bf16 v[64:67], v[136:139], v[42:45], v[64:67]\n\tv_mul_f32 v73, v73, v5\n\tv_mul_f32 v74, v74, v6\n\tv_mul_f32 v75, v75, v7\n\t"
  "v_cvt_pk_bf16_f32 v72, v72, v73\n\tv_cvt_pk_bf16_f32 v74, v74, v75\n\t"
  "v_mfma_f32_16x16x32_bf16 v[8:11], v[176:179], v[36:39], v[8:11]\n\tds_write2st64_b32 v46, v72, v74 offset0:8 offset1:9\n\t"
  "v_mfma_f32_16x16x32_bf16 v[48:51], v[36:39], v[180:183], v[48:51]\n\t"
  "v_mfma_f32_16x16x32_bf16 v[64:67], v[172:175], v[36:39], v[64:67]\n\tv_and_b32 v44, 0x40, v1\n\ts_waitcnt lgkmcnt(4)\n\t"
  "v_mfma_f32_16x16x32_bf16 v[8:11], v[188:191], v[16:19], v[8:11]\n\tv_lshl_add_u32 v44, v44, 8, s61\n\tv_and_b32 v45, 0x3c, v1\n\t"
  "v_mfma_f32_16x16x32_bf16 v[48:51], v[16:19], v[192:195], v[48:51]\n\ts_waitcnt lgkmcnt(0)\n\ts_barrier\n\tds_read_b32 v62, v41 offset:5700\n\t"
  "ds_read_b128 v[72:75], v41\n\tds_read_b128 v[52:55], v41 offset:768\n\t"
  "v_mfma_f32_16x16x32_bf16 v[64:67], v[184:187], v[16:19], v[64:67]\n\tds_read_b64_tr_b16 v[40:41], v0 offset:4096\n\tds_read_b64_tr_b16 v[42:43], v0 offset:5120\n\t"
  "v_mfma_f32_16x16x32_bf16 v[8:11], v[224:227], v[28:31], v[8:11]\n\tds_read_b64_tr_b16 v[20:21], v0 offset:6144\n\tds_read_b64_tr_b16 v[22:23], v0 offset:7168\n\t"
  "v_mfma_f32_16x16x32_bf16 v[48:51], v[28:31], v[228:231], v[48:51]\n\tds_read_b64_tr_b16 v[36:37], v0 offset:12288\n\tv_lshl_add_u32 v44, v45, 2, v44\n\t"
  "v_mfma_f32_16x16x32_bf16 v[64:67], v[220:223], v[28:31], v[64:67]\n\tds_read_b64_tr_b16 v[38:39], v0 offset:13312\n\tv_and_b32 v45, 0x80, v1\n\t"
  "v_mfma_f32_16x16x32_bf16 v[8:11], v[236:239], v[12:15], v[8:11]\n\tds_read_b64_tr_b16 v[28:29], v0 offset:14336\n\tv_lshl_add_u32 v44, v45, 1, v44\n\t"
  "v_mfma_f32_16x16x32_bf16 v[48:51], v[12:15], v[240:243], v[48:51]\n\tds_read_b64_tr_b16 v[30:31], v0 offset:15360\n\t"
  "v_mfma_f32_16x16x32_bf16 v[64:67], v[232:235], v[12:15], v[64:67]\n\ts_waitcnt lgkmcnt(9)\n\ts_waitcnt lgkmcnt(6)\n\t"
  "v_mfma_f32_16x16x32_bf16 v[8:11], v[104:107], v[40:43], v[8:11]\n\tds_read_b64_tr_b16 v[68:69], v0 offset:20480\n\tv_readfirstlane_b32 s54, v62\n\t"
  "v_mfma_f32_16x16x32_bf16 v[48:51], v[40:43], v[108:111], v[48:51]\n\tds_read_b64_tr_b16 v[70:71], v0 offset:21504\n\tv_add_f32 v72, v72, v73\n\t"
  "v_mfma_f32_16x16x32_bf16 v[64:67], v[100:103], v[40:43], v[64:67]\n\ts_cmp_eq_u32 s54, s63\n\ts_cbranch_scc1 .Lg_redo\n.Lg_back:\n\ts_waitcnt lgkmcnt(5)\n\t"
  "v_mfma_f32_16x16x32_bf16 v[8:11], v[116:119], v[20:23], v[8:11]\n\tds_read_b64_tr_b16 v[56:57], v0 offset:22528\n\tv_add_f32 v74, v74, v75\n\t"
  "v_mfma_f32_16x16x32_bf16 v[48:51], v[20:23], v[120:123], v[48:51]\n\tds_read_b64_tr_b16 v[58:59], v0 offset:23552\n\tv_add_f32 v52, v52, v53\n\t"
  "v_mfma_f32_16x16x32_bf16 v[64:67], v[112:115], v[20:23], v[64:67]\n\tv_lshl_add_u32 v45, v1, 2, s66\n\ts_waitcnt lgkmcnt(4)\n\t"
  "v_mfma_f32_16x16x32_bf16 v[8:11], v[152:155], v[36:39], v[8:11]\n\tds_read_b64_tr_b16 v[16:17], v0 offset:30720\n\tv_add_f32 v72, v72, v74\n\t"
  "v_mfma_f32_16x16x32_bf16 v[48:51], v[36:39], v[156:159], v[48:51]\n\tds_read_b64_tr_b16 v[18:19], v0 offset:31744\n\tds_read_b128 v[20:23], v45 offset:1024\n\t"
  "v_mfma_f32_16x16x32_bf16 v[64:67], v[148:151], v[36:39], v[64:67]\n\tv_add_f32 v54, v54, v55\n\tv_mul_f32 v73, 0x3b000000, v72\n\t"
  "v_mfma_f32_16x16x32_bf16 v[8:11], v[164:167], v[28:31], v[8:11]\n\tds_read_b64_tr_b16 v[60:61], v0 offset:28672\n\tv_add_f32 v52, v52, v54\n\t"
  "v_mfma_f32_16x16x32_bf16 v[48:51], v[28:31], v[168:171], v[48:51]\n\tds_read_b64_tr_b16 v[62:63], v0 offset:29696\n\tv_mul_f32_e64 v74, -v73, v73\n\tv_fmac_f32 v74, 0x3b000000, v52\n\t"
  "v_max_f32 v74, 0, v74\n\tv_add_f32 v74, 0x3727c5ac, v74\n\t"
  "v_mfma_f32_16x16x32_bf16 v[64:67], v[160:163], v[28:31], v[64:67]\n\tv_rsq_f32 v74, v74\n\tv_add_u32 v75, s65, v1\n\tds_write2st64_b32 v75, v74, v73 offset1:1\n\t"
  "ds_read_b128 v[4:7], v45 offset:2048\n\ts_waitcnt lgkmcnt(6)\n\t"
  "v_mfma_f32_16x16x32_bf16 v[8:11], v[200:203], v[68:71], v[8:11]\n\tds_read_b64_tr_b16 v[12:13], v0 offset:49152\n\tds_read2_b32 v[72:73], v2 offset0:64 offset1:80\n\tds_read_b128 v[28:31], v45 offset:0\n\t"
  "v_mfma_f32_16x16x32_bf16 v[48:51], v[68:71], v[204:207], v[48:51]\n\tds_read_b64_tr_b16 v[14:15], v0 offset:49664\n\t"
  "v_mfma_f32_16x16x32_bf16 v[64:67], v[196:199], v[68:71], v[64:67]\n\tds_read_b64_tr_b16 v[52:53], v0 offset:50176\n\t"
  "v_mfma_f32_16x16x32_bf16 v[8:11], v[212:215], v[56:59], v[8:11]\n\tds_read_b64_tr_b16 v[54:55], v0 offset:50688\n\t"
  "v_mfma_f32_16x16x32_bf16 v[48:51], v[56:59], v[216:219], v[48:51]\n\tds_read_b64_tr_b16 v[68:69], v0 offset:51200\n\t"
  "v_mfma_f32_16x16x32_bf16 v[64:67], v[208:211], v[56:59], v[64:67]\n\tds_read_b64_tr_b16 v[70:71], v0 offset:51712\n\ts_waitcnt lgkmcnt(10)\n\t"
  "v_mfma_f32_16x16x32_bf16 v[8:11], v[20:23], v[16:19], v[8:11]\n\tds_read_b64_tr_b16 v[40:41], v0 offset:52224\n\ts_waitcnt lgkmcnt(9)\n\t"
  "v_mfma_f32_16x16x32_bf16 v[48:51], v[16:19], v[4:7], v[48:51]\n\tds_read_b64_tr_b16 v[42:43], v0 offset:52736\n\ts_waitcnt lgkmcnt(7)\n\t"
  "v_mfma_f32_16x16x32_bf16 v[64:67], v[28:31], v[16:19], v[64:67]\n\tds_read_b64_tr_b16 v[36:37], v0 offset:53248\n\tds_read_b128 v[16:19], v3 offset:256\n\t"
  "v_mfma_f32_16x16x32_bf16 v[8:11], v[248:251], v[60:63], v[8:11]\n\tds_read_b64_tr_b16 v[38:39], v0 offset:53760\n\t"
  "v_mfma_f32_16x16x32_bf16 v[48:51], v[60:63], v[252:255], v[48:51]\n\tds_read_b64_tr_b16 v[56:57], v0 offset:54272\n\t"
  "v_mfma_f32_16x16x32_bf16 v[64:67], v[244:247], v[60:63], v[64:67]\n\tds_read_b64_tr_b16 v[58:59], v0 offset:54784\n\ts_waitcnt lgkmcnt(8)\n\t"
  "v_mfma_f32_16x16x32_bf16 v[60:63], v[80:83], v[12:15], 0\n\t"
  GST(18)
  "ds_read_b64_tr_b16 v[28:29], v0 offset:55296\n\tv_fma_f32 v8, -v72, v24, v8\n\t"
  "v_mfma_f32_16x16x32_bf16 v[20:23], v[12:15], v[84:87], 0\n\tds_read_b64_tr_b16 v[30:31], v0 offset:55808\n\tv_fma_f32 v9, -v72, v25, v9\n\t"
  "v_mfma_f32_16x16x32_bf16 v[60:63], v[92:95], v[52:55], v[60:63]\n\tds_read_b64_tr_b16 v[4:5], v0 offset:56320\n\tv_fma_f32 v10, -v72, v26, v10\n\t"
  "v_mfma_f32_16x16x32_bf16 v[20:23], v[52:55], v[96:99], v[20:23]\n\tds_read_b64_tr_b16 v[6:7], v0 offset:56832\n\ts_waitcnt lgkmcnt(7)\n\t"
  "v_mfma_f32_16x16x32_bf16 v[60:63], v[104:107], v[68:71], v[60:63]\n\tds_read_b64_tr_b16 v[52:53], v0 offset:57344\n\tv_fma_f32 v11, -v72, v27, v11\n\t"
  "v_mfma_f32_16x16x32_bf16 v[20:23], v[68:71], v[108:111], v[20:23]\n\tds_read_b64_tr_b16 v[54:55], v0 offset:57856\n\tv_cvt_pk_bf16_f32 v74, v8, v9\n\t"
  "v_mfma_f32_16x16x32_bf16 v[60:63], v[116:119], v[40:43], v[60:63]\n\tds_read_b64_tr_b16 v[8:9], v0 offset:58368\n\tv_cvt_pk_bf16_f32 v75, v10, v11\n\t"
  "v_mfma_f32_16x16x32_bf16 v[20:23], v[40:43], v[120:123], v[20:23]\n\tds_read_b64_tr_b16 v[10:11], v0 offset:58880\n\ts_waitcnt lgkmcnt(7)\n\t"
  "v_mfma_f32_16x16x32_bf16 v[60:63], v[128:131], v[36:39], v[60:63]\n\tds_read_b64_tr_b16 v[68:69], v0 offset:59392\n\tv_fma_f32 v64, -v72, v32, v64\n\t"
  "v_mfma_f32_16x16x32_bf16 v[20:23], v[36:39], v[132:135], v[20:23]\n\tds_read_b64_tr_b16 v[70:71], v0 offset:59904\n\tv_fma_f32 v65, -v72, v33, v65\n\t"
  "v_mfma_f32_16x16x32_bf16 v[60:63], v[140:143], v[56:59], v[60:63]\n\tds_read_b64_tr_b16 v[40:41], v0 offset:60416\n\tv_fma_f32 v66, -v72, v34, v66\n\t"
  "v_mfma_f32_16x16x32_bf16 v[20:23], v[56:59], v[144:147], v[20:23]\n\tds_read_b64_tr_b16 v[42:43], v0 offset:60928\n\ts_waitcnt lgkmcnt(7)\n\t"
  "v_mfma_f32_16x16x32_bf16 v[60:63], v[152:155], v[28:31], v[60:63]\n\tds_read_b64_tr_b16 v[36:37], v0 offset:61440\n\tv_fma_f32 v67, -v72, v35, v67\n\t"
  "v_mfma_f32_16x16x32_bf16 v[20:23], v[28:31], v[156:159], v[20:23]\n\tds_read_b64_tr_b16 v[38:39], v0 offset:61952\n\tv_cvt_pk_bf16_f32 v28, v64, v65\n\t"
  "v_mfma_f32_16x16x32_bf16 v[60:63], v[164:167], v[4:7], v[60:63]\n\tds_read_b64_tr_b16 v[64:65], v0 offset:62464\n\tv_cvt_pk_bf16_f32 v29, v66, v67\n\t"
  "v_mfma_f32_16x16x32_bf16 v[20:23], v[4:7], v[168:171], v[20:23]\n\tds_read_b64_tr_b16 v[66:67], v0 offset:62976\n\ts_waitcnt lgkmcnt(7)\n\t"
  "v_mfma_f32_16x16x32_bf16 v[60:63], v[176:179], v[52:55], v[60:63]\n\tds_read_b64_tr_b16 v[56:57], v0 offset:63488\n\tv_fma_f32 v48, -v16, v47, v48\n\t"
  "v_mfma_f32_16x16x32_bf16 v[20:23], v[52:55], v[180:183], v[20:23]\n\tds_read_b64_tr_b16 v[58:59], v0 offset:64000\n\tv_fma_f32 v49, -v17, v47, v49\n\t"
  "v_mfma_f32_16x16x32_bf16 v[60:63], v[188:191], v[8:11], v[60:63]\n\tds_read_b64_tr_b16 v[52:53], v0 offset:64512\n\tv_lshl_add_u32 v31, v1, 2, s66\n\t"
  "v_mfma_f32_16x16x32_bf16 v[20:23], v[8:11], v[192:195], v[20:23]\n\tds_read_b64_tr_b16 v[54:55], v0 offset:65024\n\ts_waitcnt lgkmcnt(7)\n\t"
  "v_mfma_f32_16x16x32_bf16 v[60:63], v[200:203], v[68:71], v[60:63]\n\tds_read_b64_tr_b16 v[8:9], v0 offset:31232\n\tv_fma_f32 v50, -v18, v47, v50\n\t"
  "v_mfma_f32_16x16x32_bf16 v[20:23], v[68:71], v[204:207], v[20:23]\n\tds_read_b64_tr_b16 v[10:11], v0 offset:32256\n\tv_fma_f32 v51, -v19, v47, v51\n\t"
  "v_mfma_f32_16x16x32_bf16 v[60:63], v[212:215], v[40:43], v[60:63]\n\tv_cvt_pk_bf16_f32 v70, v48, v49\n\tv_cvt_pk_bf16_f32 v71, v50, v51\n\tds_read_b128 v[48:51], v31 offset:1024\n\t"
  "v_mfma_f32_16x16x32_bf16 v[20:23], v[40:43], v[216:219], v[20:23]\n\tds_read_b64_tr_b16 v[12:13], v0 offset:512\n\tds_read_b128 v[16:19], v31 offset:2048\n\ts_waitcnt lgkmcnt(8)\n\t"
  "v_mfma_f32_16x16x32_bf16 v[60:63], v[224:227], v[36:39], v[60:63]\n\tds_read_b64_tr_b16 v[14:15], v0 offset:1536\n\tds_read_b32 v68, v2 offset:384\n\tv_mul_f32_e64 v4, -v73, v24\n\t"
  "v_mul_f32_e64 v5, -v73, v25\n\tv_mul_f32_e64 v6, -v73, v26\n\t"
  "v_mfma_f32_16x16x32_bf16 v[20:23], v[36:39], v[228:231], v[20:23]\n\tv_mul_f32_e64 v7, -v73, v27\n\tds_read_b128 v[40:43], v31 offset:0\n\t"
  "v_mfma_f32_16x16x32_bf16 v[60:63], v[236:239], v[64:67], v[60:63]\n\tds_read_b64_tr_b16 v[36:37], v0 offset:2560\n\tv_mul_f32_e64 v30, -v73, v32\n\t"
  "v_mfma_f32_16x16x32_bf16 v[20:23], v[64:67], v[240:243], v[20:23]\n\tds_read_b64_tr_b16 v[38:39], v0 offset:3584\n\ts_waitcnt lgkmcnt(8)\n\t"
  "v_mfma_f32_16x16x32_bf16 v[60:63], v[248:251], v[56:59], v[60:63]\n\tv_mul_f32_e64 v31, -v73, v33\n\tv_mul_f32_e64 v32, -v73, v34\n\tv_mul_f32_e64 v33, -v73, v35\n\t"
  "v_mfma_f32_16x16x32_bf16 v[20:23], v[56:59], v[252:255], v[20:23]\n\tds_read_b64_tr_b16 v[64:65], v0 offset:4608\n\ts_waitcnt lgkmcnt(6)\n\t"
  "v_mfma_f32_16x16x32_bf16 v[60:63], v[48:51], v[52:55], v[60:63]\n\tds_read_b64_tr_b16 v[66:67], v0 offset:5632\n\tds_read2_b32 v[34:35], v2 offset0:0 offset1:16\n\t"
  "v_mfma_f32_16x16x32_bf16 v[20:23], v[52:55], v[16:19], v[20:23]\n\t"
  "v_mfma_f32_16x16x32_bf16 v[4:7], v[48:51], v[8:11], v[4:7]\n\t"
  GST(19)
  "ds_read_b64_tr_b16 v[56:57], v0 offset:6656\n\tv_and_or_b32 v46, v1, 4, v3\n\tds_read2_b32 v[72:73], v46 offset0:32 offset1:34\n\tds_read2_b32 v[52:53], v46 offset0:0 offset1:2\n\t"
  "v_mov_b32 v55, 0xff800000\n\ts_waitcnt lgkmcnt(7)\n\t"
  "v_mfma_f32_16x16x32_bf16 v[30:33], v[40:43], v[8:11], v[30:33]\n\tds_read_b64_tr_b16 v[58:59], v0 offset:7680\n\tv_cndmask_b32_e64 v54, v55, 0, s[70:71]\n\tv_cndmask_b32_e64 v45, v55, 0, s[72:73]\n\t"
  "v_cndmask_b32_e64 v2, v55, 0, s[74:75]\n\tv_cndmask_b32_e64 v55, v55, 0, s[76:77]\n\t"
  "v_mfma_f32_16x16x32_bf16 v[40:43], v[8:11], v[16:19], 0\n\tv_fma_f32 v60, -v68, v24, v60\n\tv_fma_f32 v61, -v68, v25, v61\n\t"
  "v_mfma_f32_16x16x32_bf16 v[4:7], v[80:83], v[12:15], v[4:7]\n\tds_read_b64_tr_b16 v[8:9], v0 offset:8704\n\tv_fma_f32 v62, -v68, v26, v62\n\t"
  "v_mfma_f32_16x16x32_bf16 v[30:33], v[76:79], v[12:15], v[30:33]\n\tds_read_b64_tr_b16 v[10:11], v0 offset:9728\n\tv_mfma_f32_16x16x16_bf16 v[48:51], v[74:75], v[28:29], 0\n\tv_fma_f32 v63, -v68, v27, v63\n\t"
  "v_cvt_pk_bf16_f32 v60, v60, v61\n\t"
  "v_mfma_f32_16x16x32_bf16 v[40:43], v[12:15], v[84:87], v[40:43]\n\tv_cvt_pk_bf16_f32 v61, v62, v63\n\ts_waitcnt lgkmcnt(3)\n\t"
  "v_mfma_f32_16x16x32_bf16 v[4:7], v[92:95], v[36:39], v[4:7]\n\tds_read_b64_tr_b16 v[12:13], v0 offset:10752\n\tv_mfma_f32_16x16x16_bf16 v[24:27], v[60:61], v[28:29], 0\n\tv_cndmask_b32_e64 v29, v48, v49, s[68:69]\n\t"
  "v_fma_f32 v29, v29, v52, v2\n\t"
  "v_mfma_f32_16x16x32_bf16 v[30:33], v[88:91], v[36:39], v[30:33]\n\tds_read_b64_tr_b16 v[14:15], v0 offset:11776\n\tv_cndmask_b32_e64 v28, v50, v51, s[68:69]\n\t"
  "v_mfma_f32_16x16x32_bf16 v[40:43], v[36:39], v[96:99], v[40:43]\n\tds_read_b128 v[16:19], v3 offset:384\n\tv_fma_f32 v28, v28, v53, v55\n\t"
  "v_mfma_f32_16x16x32_bf16 v[4:7], v[104:107], v[64:67], v[4:7]\n\tds_read_b64_tr_b16 v[36:37], v0 offset:12800\n\tv_max_f32 v60, v29, v28\n\t"
  "v_mfma_f32_16x16x32_bf16 v[30:33], v[100:103], v[64:67], v[30:33]\n\tds_read_b64_tr_b16 v[38:39], v0 offset:13824\n\tv_cndmask_b32_e64 v50, v24, v25, s[68:69]\n\t"
  "v_mfma_f32_16x16x32_bf16 v[40:43], v[64:67], v[108:111], v[40:43]\n\tv_fma_f32 v50, v50, v72, v54\n\ts_waitcnt lgkmcnt(5)\n\t"
  "v_mfma_f32_16x16x32_bf16 v[4:7], v[116:119], v[56:59], v[4:7]\n\tds_read_b64_tr_b16 v[64:65], v0 offset:14848\n\tv_cndmask_b32_e64 v48, v26, v27, s[68:69]\n\t"
  "v_mfma_f32_16x16x32_bf16 v[30:33], v[112:115], v[56:59], v[30:33]\n\tds_read_b64_tr_b16 v[66:67], v0 offset:15872\n\tv_fma_f32 v48, v48, v73, v45\n\t"
  "v_mfma_f32_16x16x32_bf16 v[40:43], v[56:59], v[120:123], v[40:43]\n\tv_lshlrev_b32 v51, 2, v1\n\tv_alignbit_b32 v49, s67, s67, v51\n\t"
  "v_mfma_f32_16x16x32_bf16 v[4:7], v[128:131], v[8:11], v[4:7]\n\tds_read_b64_tr_b16 v[24:25], v0 offset:16896\n\tv_alignbit_b32 v51, s55, s55, v51\n\t"
  "v_mfma_f32_16x16x32_bf16 v[30:33], v[124:127], v[8:11], v[30:33]\n\tds_read_b64_tr_b16 v[26:27], v0 offset:17920\n\tv_mov_b32 v58, 0x3f803f80\n\t"
  "v_mfma_f32_16x16x32_bf16 v[40:43], v[8:11], v[132:135], v[40:43]\n\tv_mov_b32 v59, 0x3f803f80\n\ts_waitcnt lgkmcnt(4)\n\t"
  "v_mfma_f32_16x16x32_bf16 v[4:7], v[140:143], v[12:15], v[4:7]\n\tds_read_b64_tr_b16 v[8:9], v0 offset:18944\n\tv_mul_f32 v34, 0x3eb8aa3b, v34\n\t"
  "v_mfma_f32_16x16x32_bf16 v[30:33], v[136:139], v[12:15], v[30:33]\n\tds_read_b64_tr_b16 v[10:11], v0 offset:19968\n\tv_max3_f32 v61, v60, v50, v48\n\t"
  "v_mfma_f32_16x16x32_bf16 v[40:43], v[12:15], v[144:147], v[40:43]\n\tv_mul_f32 v35, 0x3eb8aa3b, v35\n\tv_max3_f32 v60, v60, v50, v48\n\t"
  "v_mfma_f32_16x16x32_bf16 v[4:7], v[152:155], v[36:39], v[4:7]\n\tds_read_b64_tr_b16 v[12:13], v0 offset:20992\n\tv_fma_f32 v20, -v16, v47, v20\n\t"
  "v_mfma_f32_16x16x32_bf16 v[30:33], v[148:151], v[36:39], v[30:33]\n\tds_read_b64_tr_b16 v[14:15], v0 offset:22016\n\tv_fma_f32 v21, -v17, v47, v21\n\t"
  "v_mfma_f32_16x16x32_bf16 v[40:43], v[36:39], v[156:159], v[40:43]\n\tv_fma_f32 v22, -v18, v47, v22\n\ts_waitcnt lgkmcnt(4)\n\t"
  "v_mfma_f32_16x16x32_bf16 v[4:7], v[164:167], v[64:67], v[4:7]\n\tds_read_b64_tr_b16 v[16:17], v0 offset:23040\n\tv_fma_f32 v23, -v19, v47, v23\n\t"
  "v_mfma_f32_16x16x32_bf16 v[30:33], v[160:163], v[64:67], v[30:33]\n\tds_read_b64_tr_b16 v[18:19], v0 offset:24064\n\tv_cvt_pk_bf16_f32 v68, v20, v21\n\t"
  "v_mfma_f32_16x16x32_bf16 v[40:43], v[64:67], v[168:171], v[40:43]\n\tv_permlane16_swap_b32 v60, v61\n\t"
  "v_mfma_f32_16x16x32_bf16 v[4:7], v[176:179], v[24:27], v[4:7]\n\tds_read_b64_tr_b16 v[36:37], v0 offset:25088\n\tv_max_f32 v62, v60, v61\n\t"
  "v_mfma_f32_16x16x32_bf16 v[30:33], v[172:175], v[24:27], v[30:33]\n\tds_read_b64_tr_b16 v[38:39], v0 offset:26112\n\tv_max_f32 v63, v60, v61\n\t"
  "v_mfma_f32_16x16x32_bf16 v[40:43], v[24:27], v[180:183], v[40:43]\n\tds_read_b64_tr_b16 v[24:25], v0 offset:27136\n\ts_waitcnt lgkmcnt(5)\n\t"
  "v_mfma_f32_16x16x32_bf16 v[4:7], v[188:191], v[8:11], v[4:7]\n\tds_read_b64_tr_b16 v[26:27], v0 offset:28160\n\tv_cvt_pk_bf16_f32 v69, v22, v23\n\t"
  "v_mfma_f32_16x16x32_bf16 v[30:33], v[184:187], v[8:11], v[30:33]\n\tds_read_b64_tr_b16 v[20:21], v0 offset:29184\n\tds_read2_b32 v[56:57], v46 offset0:16 offset1:18\n\t"
  "v_mfma_f32_16x16x32_bf16 v[40:43], v[8:11], v[192:195], v[40:43]\n\tds_read_b64_tr_b16 v[22:23], v0 offset:30208\n\tv_permlane32_swap_b32 v62, v63\n\t"
  "v_mfma_f32_16x16x32_bf16 v[4:7], v[200:203], v[12:15], v[4:7]\n\tv_max_f32 v60, v62, v63\n\tv_mul_f32_e64 v60, -v60, v34\n\t"
  "v_mfma_f32_16x16x32_bf16 v[30:33], v[196:199], v[12:15], v[30:33]\n\tv_fma_f32 v50, v50, v34, v60\n\tv_fma_f32 v48, v48, v34, v60\n\t"
  "v_mfma_f32_16x16x32_bf16 v[40:43], v[12:15], v[204:207], v[40:43]\n\tv_fma_f32 v29, v29, v34, v60\n\ts_waitcnt lgkmcnt(5)\n\t"
  "v_mfma_f32_16x16x32_bf16 v[4:7], v[212:215], v[16:19], v[4:7]\n\tv_fma_f32 v28, v28, v34, v60\n\tds_read_b128 v[64:67], v3 offset:320\n\t"
  "v_mfma_f32_16x16x32_bf16 v[30:33], v[208:211], v[16:19], v[30:33]\n\tv_exp_f32 v50, v50\n\t"
  "v_mfma_f32_16x16x32_bf16 v[40:43], v[16:19], v[216:219], v[40:43]\n\tv_exp_f32 v48, v48\n\t"
  "v_mfma_f32_16x16x32_bf16 v[4:7], v[224:227], v[36:39], v[4:7]\n\tv_exp_f32 v29, v29\n\t"
  "v_mfma_f32_16x16x32_bf16 v[30:33], v[220:223], v[36:39], v[30:33]\n\tv_cvt_pk_bf16_f32 v60, v50, v48\n\ts_waitcnt lgkmcnt(1)\n\t"
  "v_mfma_f32_16x16x32_bf16 v[4:7], v[236:239], v[24:27], v[4:7]\n\tv_exp_f32 v28, v28\n\t"
  "v_mfma_f32_16x16x32_bf16 v[30:33], v[232:235], v[24:27], v[30:33]\n\tv_cvt_pk_bf16_f32 v61, v29, v28\n\tv_mul_f32 v50, v50, v72\n\t"
  "v_mfma_f32_16x16x32_bf16 v[4:7], v[248:251], v[20:23], v[4:7]\n\tv_mul_f32 v48, v48, v73\n\tv_mfma_f32_16x16x16_bf16 v[8:11], v[58:59], v[60:61], 0\n\tv_mul_f32 v29, v29, v52\n\t"
  "v_mul_f32 v28, v28, v53\n\t"
  "v_mfma_f32_16x16x32_bf16 v[30:33], v[244:247], v[20:23], v[30:33]\n\tv_cvt_pk_bf16_f32 v4, v4, v5\n\tv_cvt_pk_bf16_f32 v5, v6, v7\n\t"
  "v_mfma_f32_16x16x32_bf16 v[40:43], v[36:39], v[228:231], v[40:43]\n\tv_cvt_pk_bf16_f32 v50, v50, v48\n\tv_cvt_pk_bf16_f32 v29, v29, v28\n\t"
  "v_mfma_f32_16x16x32_bf16 v[40:43], v[24:27], v[240:243], v[40:43]\n\tv_cvt_pk_bf16_f32 v30, v30, v31\n\tv_cvt_pk_bf16_f32 v31, v32, v33\n\t"
  "v_mfma_f32_16x16x32_bf16 v[40:43], v[20:23], v[252:255], v[40:43]\n\t"
  GST(14)
  "v_perm_b32 v32, v50, v50, v49\n\tv_mfma_f32_16x16x16_bf16 v[12:15], v[4:5], v[30:31], 0\n\tv_mfma_f32_16x16x16_bf16 v[4:7], v[74:75], v[30:31], 0\n\ts_waitcnt lgkmcnt(0)\n\t"
  "v_fma_f32 v40, -v64, v47, v40\n\tv_fma_f32 v41, -v65, v47, v41\n\tv_fma_f32 v42, -v66, v47, v42\n\tv_cndmask_b32_e64 v28, v12, v13, s[68:69]\n\t"
  "v_fma_f32 v28, v28, v56, v2\n\tv_cndmask_b32_e64 v31, v14, v15, s[68:69]\n\tv_fma_f32 v31, v31, v57, v55\n\tv_max_f32 v64, v28, v31\n\t"
  "v_cndmask_b32_e64 v46, v4, v5, s[68:69]\n\tv_fma_f32 v46, v46, v52, v54\n\tv_cndmask_b32_e64 v34, v6, v7, s[68:69]\n\tv_fma_f32 v34, v34, v53, v45\n\t"
  "v_max3_f32 v65, v64, v46, v34\n\tv_max3_f32 v64, v64, v46, v34\n\tv_fma_f32 v43, -v67, v47, v43\n\tv_cvt_pk_bf16_f32 v72, v40, v41\n\t"
  "v_permlane16_swap_b32 v64, v65\n\tv_max_f32 v66, v64, v65\n\tv_max_f32 v67, v64, v65\n\tv_cvt_pk_bf16_f32 v73, v42, v43\n\t"
  "v_perm_b32 v33, v50, v50, v51\n\tv_permlane32_swap_b32 v66, v67\n\tv_max_f32 v64, v66, v67\n\tv_mul_f32_e64 v64, -v64, v35\n\t"
  "v_fma_f32 v46, v46, v35, v64\n\tv_fma_f32 v34, v34, v35, v64\n\tv_fma_f32 v28, v28, v35, v64\n\tv_fma_f32 v31, v31, v35, v64\n\t"
  "v_exp_f32 v46, v46\n\tv_exp_f32 v34, v34\n\tv_exp_f32 v28, v28\n\tv_exp_f32 v31, v31\n\t"
  "v_cvt_pk_bf16_f32 v64, v46, v34\n\tv_cvt_pk_bf16_f32 v65, v28, v31\n\tv_mul_f32 v46, v46, v52\n\tv_mul_f32 v34, v34, v53\n\t"
  "v_mfma_f32_16x16x16_bf16 v[40:43], v[58:59], v[64:65], 0\n\tv_mul_f32 v28, v28, v56\n\tv_mul_f32 v31, v31, v57\n\tv_cvt_pk_bf16_f32 v46, v46, v34\n\t"
  "v_cvt_pk_bf16_f32 v28, v28, v31\n\tv_perm_b32 v20, v46, v46, v49\n\tv_perm_b32 v34, v29, v29, v49\n\tv_perm_b32 v21, v46, v46, v51\n\t"
  "v_perm_b32 v35, v29, v29, v51\n\tv_perm_b32 v22, v28, v28, v49\n\tv_perm_b32 v23, v28, v28, v51\n\t"
  "v_mfma_f32_16x16x32_bf16 v[4:7], v[68:71], v[32:35], 0\n\tv_rcp_f32 v8, v8\n\t"
  "v_mfma_f32_16x16x32_bf16 v[24:27], v[70:73], v[20:23], 0\n\tv_rcp_f32 v40, v40\n\ts_nop 1\n\tv_mul_f32 v4, v4, v8\n\t"
  "v_mul_f32 v5, v5, v8\n\tv_mul_f32 v6, v6, v8\n\tv_mul_f32 v7, v7, v8\n\tv_cvt_pk_bf16_f32 v28, v4, v5\n\t"
  "v_mul_f32 v24, v24, v40\n\tv_mul_f32 v25, v25, v40\n\tv_mul_f32 v26, v26, v40\n\tv_mul_f32 v27, v27, v40\n\t"
  "v_cvt_pk_bf16_f32 v30, v24, v25\n\tv_cvt_pk_bf16_f32 v31, v26, v27\n\tv_cvt_pk_bf16_f32 v29, v6, v7\n\tv_permlane16_swap_b32 v28, v30\n\t"
  "s_nop 0\n\tv_permlane16_swap_b32 v29, v31\n\tbuffer_store_dwordx4 v[28:31], v44, s[56:59], 0 offen sc1\n\t"
  "s_cmp_eq_u32 s63, 2\n\ts_cbranch_scc1 .Lg_ret\n\ts_nop 0"
  G_OPS);
  else asm volatile("s_branch .Lg_body\n.Lg_ret:\n\ts_nop 0" G_OPS);
  static_for<0, 45>(pin);
  ST(!L1, 16);
  if (L1) {
    KSLP(7, SLP_OB1);
    cnt_add(smem, C_OB, 0);
    mid();
    cnt_wait(smem, C_X2, 4);
    return;
  }
  u32 t16 = KADD(8, now_(), DB2);
  cnt_add(smem, C_OB, 0);
  const int bid = BIDX, lane = lane_id();
  int og0 = OFF_O2 + MB_ + RB_OF(bid) * 32768 + g * 4096, og1 = og0 + 16384, vo = lane * 16;
  u32 sa = (u32)(L_TB + g * 4096 + lane * 64 + (lane & 14) * 4 + 4);
  asm volatile("" : "+s"(og0), "+s"(og1), "+v"(vo), "+v"(sa));
  u32 dl;
  for (unsigned n = 0; n < (1u << 24); ++n) {
    asm volatile("" ::: "memory");
    const u32 c0 = *(const volatile LAS u32*)(size_t)(u32)(L_MI + C_OV), c1 = *(const volatile LAS u32*)(size_t)(u32)(L_MI + C_OB);
    dl = *(const volatile LAS u32*)(size_t)(u32)(L_MI + M_DL);
    if ((u32)__builtin_amdgcn_readfirstlane(min(c0 + 11u, c1)) >= 16u) break;
  }
  gate(t16 + dl);
  ST(1, 15);
  const u32 mark = x0_mark(tag);
  o_get(wrs, vo, og0, og1, L_TB + g * 4096, sa, mark, 25);
  asm volatile("s_barrier\n\ts_waitcnt vmcnt(0)\n\ts_barrier" ::: "memory");
  if (__any(*(const LAS u32*)(size_t)(sa + 16384) == mark)) {
    FAILC(12);
    for (unsigned spins = 0; spins <= SPIN_CAP; ++spins) {
      dma4k(wrs, vo, og0, L_TA + g * 4096);
      dma4k(wrs, vo, og1, L_TA + 16384 + g * 4096);
      asm volatile("s_waitcnt vmcnt(0)" ::: "memory");
      if (!__any(*(const LAS u32*)(size_t)(sa - (L_TB - L_TA)) == mark || *(const LAS u32*)(size_t)(sa - (L_TB - L_TA) + 16384) == mark)) break;
    }
    smem[L_MI + M_FX + g] = 1;
    asm volatile("s_waitcnt lgkmcnt(0)" ::: "memory");
  }
}

using u32x8 = __attribute__((ext_vector_type(8))) unsigned;
using u32x16 = __attribute__((ext_vector_type(16))) unsigned;
__global__ __launch_bounds__(512, 2) KATTR void k_mega(const u16*, const u16*, const u16*, const u16*, const u16*, const u16*, const u16*, const u16*,
                                                       const u16*, const u16*, const u16*, char*, u32*) {
  extern __shared__ __attribute__((aligned(16))) char smem[];
  u32x16 ka;
  u32x8 kb;
  u32x2 kc;
  asm volatile("s_load_dwordx16 %0, %3, 0x0\n\ts_load_dwordx8 %1, %3, 0x40\n\ts_load_dwordx2 %2, %3, 0x60"
               : "=&s"(ka), "=&s"(kb), "=&s"(kc) : "s"(__builtin_amdgcn_kernarg_segment_ptr()) : "memory");
  const int tid = TIDX, lane = tid & 63;
  const int w = __builtin_amdgcn_readfirstlane(tid >> 6);
  const int bid = BIDX;
  ST_INIT(); ST(0, 0); ST(1, 0);
  if (w == 0) {
    u64 pc;
    int clen;
    asm volatile("s_getpc_b64 %0\n.Lcw_base:\n\ts_mov_b32 %1, .Lcw_end-.Lcw_base-4" : "=s"(pc), "=s"(clen));
    asm volatile("s_mov_b32 m0, %2\n\ts_nop 0\n\tglobal_load_lds_dword %0, %1" :: "v"(min(min(lane, 15) * 128 + (bid >> 3) * 2048, clen)), "s"(pc), "s"(L_FS) : "memory");
  }
  const int kp = (bid & 7) * 32 + (bid >> 3), hb = HB_OF(bid);
  const int c = (w >> 1) & 1, g = w & 3;
  u32 po = (u32)(kp * 512 + (tid & 255) * 2);
  const int pb = (bid & 7) + 8 * (bid >> 6), ps = (bid >> 3) & 7, gr = pb * 8 + ps;
  u32 xo = (u32)((w < 4 ? (pb * 64 + 16 * w) * 1024 + ps * 128 : gr * 8192 + g * 2048) + (lane & 7) * 16 + (lane >> 3) * (w < 4 ? 2048 : 128));
  asm volatile("" : "+v"(po), "+v"(xo) :: "memory");
  asm volatile("s_waitcnt lgkmcnt(0)" : "+s"(ka), "+s"(kb), "+s"(kc) :: "memory");
  ST_FIX();
#define KP(T_, v_, i_) ((T_)(((u64)v_[2 * (i_) + 1] << 32) | v_[2 * (i_)]))
  const u16 *x0 = KP(const u16*, ka, 0), *A_re = KP(const u16*, ka, 1), *A_im = KP(const u16*, ka, 2), *B_re = KP(const u16*, ka, 3), *B_im = KP(const u16*, ka, 4);
  const u16 *C_re = KP(const u16*, ka, 5), *C_im = KP(const u16*, ka, 6), *log_dt = KP(const u16*, ka, 7), *Dskip = KP(const u16*, kb, 0), *wqkv = KP(const u16*, kb, 1), *wo = KP(const u16*, kb, 2);
  char* ws = KP(char*, kb, 3);
  u32* out = KP(u32*, kc, 0);
#define LD_A() asm volatile("global_load_ushort v248, %0, %1\n\tglobal_load_ushort v249, %0, %2" :: "v"(po), "s"(A_re), "s"(A_im) : "memory", "v248", "v249")
  u32 ldtw = 0, dskw = 0;
  if (w < 4) {
    asm volatile("s_load_dword %0, %2, %4\n\ts_load_dword %1, %3, %4" : "=&s"(ldtw), "=&s"(dskw) : "s"(log_dt), "s"(Dskip), "s"(4 * kp) : "memory");
    LD_A();
  }
  asm volatile("global_load_dwordx4 v[240:243], %0, %1\n\tglobal_load_dwordx4 v[244:247], %0, %1 offset:1024\n\tglobal_load_dword v254, %2, %3 sc1"
               :: "v"(xo), "s"(x0), "v"(OFF_EP), "s"(ws) : "memory", "v240", "v254");
  if (w >= 4) {
    asm volatile("global_load_ushort v250, %0, %1\n\tglobal_load_ushort v251, %0, %2\n\tglobal_load_ushort v252, %0, %3\n\tglobal_load_ushort v253, %0, %4"
                 :: "v"(po), "s"(B_re), "s"(B_im), "s"(C_re), "s"(C_im) : "memory", "v250", "v253");
    LD_A();
  }
  __builtin_amdgcn_sched_barrier(0);
  const rsrc_t wrs = __builtin_amdgcn_make_buffer_rsrc((void*)ws, 0, WSB, 0x00020000);
  if (w >= 4) {
    asm volatile("s_mov_b32 m0, %2\n\ts_nop 0\n\tglobal_load_lds_dword %0, %1" :: "v"((bid * 16 + g * 4 + min(lane, 3)) * 128), "s"(wqkv), "s"(L_FS + w * 256) : "memory");
    u32x4 zz = {0, 0, 0, 0};
    asm volatile("" : "+v"(zz));
    const int z0 = (int)opaque((u32)(L_U + (lane >> 5) * 4608 + (lane & 31) * 16)), z1 = (int)opaque((u32)(L_MI + min((tid & 255) * 16 - 1024, 1088)));
    *(u32x4*)(smem + (g == 0 ? z0 : z1)) = zz;
    asm volatile("s_nop 1\n\ts_waitcnt lgkmcnt(0)" :: "v"(zz) : "memory");
  }
  if (w >= 4) asm volatile("s_load_dword %0, %1, %2" : "=s"(ldtw) : "s"(log_dt), "s"(4 * kp) : "memory");
  __builtin_amdgcn_sched_barrier(0);
#define XT_GET(x_) asm volatile("v_mov_b64 %0, v[240:241]\n\tv_mov_b64 %1, v[242:243]\n\tv_mov_b64 %2, v[244:245]\n\tv_mov_b64 %3, v[246:247]" \
                   : "=v"(x_[0].xy), "=v"(x_[0].zw), "=v"(x_[1].xy), "=v"(x_[1].zw) :: "memory")
  const int ftb = c ? L_TA : 0;
#define PUT(e_, r_, i_) *(LAS u32*)(wa + (e_) * FTS) = pkh(r_, i_)
#define CMR(a_, b_) fmaf(a_[0], b_[0], -(a_[1] * b_[1]))
#define CMI(a_, b_) fmaf(a_[0], b_[1], a_[1] * b_[0])
#define CMUL(a_, b_) (f32x2){CMR(a_, b_), CMI(a_, b_)}
  struct P0A { u32 gad[12], sad[8], ua, ur; };
  auto p0_addr = [&](P0A& A, int lane, int ut) __attribute__((always_inline)) {
    const int i = lane & 15, kk = lane >> 4;
    const u32 sb = (u32)((L_KT - KTO + c * TBS - 0x7E00) & 0xFFFF) * 0x10001u;
    const float f0 = __uint_as_float(opaque(__float_as_uint((float)((ut < 2 ? 1023 : 2047) - 512 * (w & 1) - 8 * lane))));
#pragma unroll
    for (int e = 0; e < 6; ++e) { const u32 ga = tb_addr(pk2(f0 - (float)(2 * e), f0 - (float)(2 * e + 1)), sb); A.gad[2 * e] = ga & 0xFFFFu; A.gad[2 * e + 1] = ga >> 16; }
    if (ut < 2) {
      A.ua = (u32)(ftb + i * FTS + kk * 16); A.ur = A.ua + ut * 16 * FTS;
      const int a0 = 128 * (i >> 3) + (i & 7), a1 = i < 8 ? 256 + 128 * (i >> 2) + 2 * (i & 3) : 128 * (i >> 1) + 4 * (i & 1), a2 = min(1024 + 128 * i, 2064);
#pragma unroll
      for (int t = 0; t < 2; ++t) {
        const float st = (t && i >= 8) ? 16.f : 8.f;
        const float fb = (float)(t ? a2 : ut == 0 ? a0 : a1) + st * (float)(4 * kk);
        const u32 s0 = tb_addr(pk2(fb, fb + st), sb), s1 = tb_addr(pk2(fb + 2.f * st, fb + 3.f * st), sb);
        A.sad[4 * t] = s0 & 0xFFFFu; A.sad[4 * t + 1] = s0 >> 16; A.sad[4 * t + 2] = s1 & 0xFFFFu; A.sad[4 * t + 3] = s1 >> 16;
      }
      asm volatile("" : "+v"(A.ua), "+v"(A.ur), "+v"(A.sad[0]), "+v"(A.sad[1]), "+v"(A.sad[2]), "+v"(A.sad[3]), "+v"(A.sad[4]), "+v"(A.sad[5]), "+v"(A.sad[6]), "+v"(A.sad[7]));
    }
    asm volatile("" : "+v"(A.gad[0]), "+v"(A.gad[1]), "+v"(A.gad[2]), "+v"(A.gad[3]), "+v"(A.gad[4]), "+v"(A.gad[5]), "+v"(A.gad[6]), "+v"(A.gad[7]), "+v"(A.gad[8]), "+v"(A.gad[9]), "+v"(A.gad[10]));
    __builtin_amdgcn_sched_barrier(0);
  };
  auto k_unit = [&](const P0A& A, float dk, bool lag0) __attribute__((always_inline)) {
    f16x8 fr[8], fc[8];
    const f32x2 bd = *(const f32x2*)(smem + L_P0 + 512 + c * 8);
    __builtin_amdgcn_sched_barrier(0);
#pragma unroll
    for (int e = 0; e < 8; ++e) {
      fc[e] = *(const LAS f16x8*)(size_t)(A.ua + e * 64 + 41 * FTS);
      fr[e] = *(const LAS f16x8*)(size_t)(A.ur + e * 64);
      __builtin_amdgcn_sched_barrier(0);
    }
    f16x8 f2[8];
    if (!(w & 1)) {
#pragma unroll
      for (int e = 0; e < 8; ++e) { f2[e] = *(const LAS f16x8*)(size_t)(A.ua + e * 64 + 32 * FTS); __builtin_amdgcn_sched_barrier(0); }
    }
    const int ec = bexp(bd[0] + bd[1] + fabsf(dk));
    const float f0 = __uint_as_float((u32)max(127 + bexp(bd[0]) - ec, 0) << 23), f1 = __uint_as_float((u32)max(127 + bexp(bd[1]) - ec, 0) << 23);
    const float d0s = lag0 ? dk * __uint_as_float((u32)(268 - ec) << 23) : 0.f;
    auto unit = [&](const f16x8 (&fr)[8], u32x4 sa, float add) __attribute__((always_inline)) {
      f32x4 k0 = {0.f, 0.f, 0.f, 0.f}, k1 = k0;
#pragma unroll
      for (int e = 0; e < 4; ++e) k0 = __builtin_amdgcn_mfma_f32_16x16x32_f16(fc[e], fr[e], k0, 0, 0, 0);
#pragma unroll
      for (int e = 4; e < 8; ++e) k1 = __builtin_amdgcn_mfma_f32_16x16x32_f16(fc[e], fr[e], k1, 0, 0, 0);
      k0 *= f0; k0[0] += add;
      const u32 h01 = pkh(fmaf(k1[0], f1, k0[0]), fmaf(k1[1], f1, k0[1])), h23 = pkh(fmaf(k1[2], f1, k0[2]), fmaf(k1[3], f1, k0[3]));
      *(LAS u16*)((LAS char*)(size_t)sa[0] + KTO) = (u16)h01; *(LAS u16*)((LAS char*)(size_t)sa[1] + KTO) = (u16)(h01 >> 16);
      *(LAS u16*)((LAS char*)(size_t)sa[2] + KTO) = (u16)h23; *(LAS u16*)((LAS char*)(size_t)sa[3] + KTO) = (u16)(h23 >> 16);
    };
    if (!(w & 1)) {
      unit(fr, (u32x4){A.sad[0], A.sad[1], A.sad[2], A.sad[3]}, d0s);
      unit(f2, (u32x4){A.sad[4], A.sad[5], A.sad[6], A.sad[7]}, 0.f);
    } else {
      unit(fr, (u32x4){A.sad[0], A.sad[1], A.sad[2], A.sad[3]}, 0.f);
      *(float*)(smem + L_MI + 4 * c) = __uint_as_float((u32)(ec - 14) << 23);
    }
  };
  auto k_get = [&](u32 (&kh)[11], const P0A& A) __attribute__((always_inline)) {
#pragma unroll
    for (int e = 0; e < 11; ++e) kh[e] = *(const LAS u16*)((const LAS char*)(size_t)A.gad[e] + KTO);
    __builtin_amdgcn_sched_barrier(0);
  };
  auto k_copies = [&](const u32 (&kh)[11], int lane, bool low) __attribute__((always_inline)) {
    u32 kw[6];
#pragma unroll
    for (int e = 0; e < 5; ++e) kw[e] = __builtin_amdgcn_perm(kh[2 * e + 1], kh[2 * e], 0x05040100u);
    kw[5] = kh[10];
    if (low && (w & 1) && lane == 63) { kw[4] = 0; kw[5] = 0; }
    build_copies(smem, c, (w & 1) * 64 + lane + (low ? 128 : 0), kw);
    __builtin_amdgcn_sched_barrier(0);
  };
  __builtin_amdgcn_sched_barrier(0);
  int w2 = w;
  asm volatile("" : "+s"(w2));
  if (w < 4) {
  P0A pa;
  p0_addr(pa, lane, w & 1);
  LAS char* wa = (LAS char*)(size_t)opaque((u32)(ftb + (tid & 127) * 4));
  u32 tg;
  const bool lo = pb < 16;
  const int xc8 = lane >> 3, xrp = lane & 7;
  const int xts = lo ? 128 : XTS, xt2 = 2 * xts;
  const int xto = (int)opaque((u32)(lo ? OFF_X1 + (pb * 4 + w) * XBS + (ps * 32 + 4 * xc8) * 128 + xrp * 16
                                       : XTO(ps * 32 + 4 * xc8) + ((pb & 15) * 64 + 16 * w + 2 * xrp) * 8));
  auto xpub = [&]() __attribute__((always_inline)) {
    u32x4 q[4];
    u32x4 xt[2];
    __builtin_amdgcn_sched_barrier(0);
    asm volatile("s_waitcnt vmcnt(0)\n\tv_add_u32 %0, 1, v254" : "=v"(tg) :: "memory");
    XT_GET(xt);
    const int sl = ((lane & 7) * 8 + (lane >> 3)) * 4;
#pragma unroll
    for (int e = 0; e < 8; ++e) xt[e & 1][e >> 1] = (u32)__builtin_amdgcn_ds_bpermute(sl, (int)xt[e & 1][e >> 1]);
#pragma unroll
    for (int e = 0; e < 4; ++e) q[e] = (u32x4){xt[0][e], x0_mark(tg), xt[1][e], x0_mark(tg)};
    asm volatile("" : "+v"(q[0]), "+v"(q[1]), "+v"(q[2]), "+v"(q[3]));
#pragma unroll
    for (int e = 0; e < 4; ++e) stw(wrs, xto + (e & 1) * xts + (e >> 1) * xt2, q[e]);
    asm volatile("s_nop 1" :: "v"(q[0]), "v"(q[1]), "v"(q[2]), "v"(q[3]) : "memory");
    ST(0, 3);
    __builtin_amdgcn_sched_barrier(0);
  };
  u32 ha, hz, hs, ht;
  C1Pre cp;
  int xs0, xs1, xl0, xl1, xv;
  u32 za;
  auto setup1 = [&]() __attribute__((always_inline)) {
    SB();
    const int t = (w & 1) * 64 + lane;
    ha = (u32)(L_KT + c * TBS - 0x7E00 + 2 * (0x44FF - t)); hz = ha - (t == 127 ? 2 : 0);
    hs = (lane & 1) ? 0x01000302u : 0x03020302u; ht = (lane & 1) ? 0x01000100u : 0x01000302u;
    asm volatile("" : "+v"(ha), "+v"(hz), "+v"(hs), "+v"(ht));
    SB();
  };
  auto setup2 = [&]() __attribute__((always_inline)) {
    SB();
    c1_pre(cp, kp, w, lane);
    xs0 = OFF_X1 + RB_OF(bid) * XBS + w * 8192; xs1 = xs0 + 4096; xl0 = L_TA + w * 8192; xl1 = xl0 + 4096; xv = x_lane(lane);
    za = (u32)(((lane >> 1) & 7) * CSTR + 4096 + (lane & 1) * 16);
    asm volatile("" : "+s"(xs0), "+s"(xs1), "+s"(xl0), "+s"(xl1), "+v"(xv), "+v"(za));
    SB();
  };
  {
    float ar, ai;
#define B0() do { SB(); __builtin_amdgcn_s_barrier(); SB(); } while (0)
    B0();
    asm volatile("s_waitcnt vmcnt(3)\n\tv_lshlrev_b32 %0, 16, v248\n\tv_lshlrev_b32 %1, 16, v249" : "=v"(ar), "=v"(ai) :: "memory");
    asm volatile("s_waitcnt lgkmcnt(0)" : "+s"(ldtw), "+s"(dskw) :: "memory");
    ST(0, 1);
    const float dt = rbf(__expf(c ? hi2f(ldtw) : lo2f(ldtw)));
    const float zr = dt * ar * 1.4426950408889634f, zi = dt * ai * 0.15915494309189535f;
    float cs = __builtin_amdgcn_exp2f(fmaxf(zr, 0.f) * -1928.f);
    f32x2 P[8], Q1, R[17];
    { float a_, b_; cexp2(zr, zi, a_, b_); P[1] = (f32x2){a_, b_}; cexp2(zr * 128.f, zi * 128.f, a_, b_); Q1 = (f32x2){a_, b_}; }
    asm volatile("" : "+v"(cs), "+v"(P[1]), "+v"(Q1));
    xpub();
    P[2] = CMUL(P[1], P[1]);
    const f32x2 Q2 = CMUL(Q1, Q1);
    R[1] = Q1 * cs; R[2] = Q2 * cs;
    P[3] = CMUL(P[2], P[1]); P[4] = CMUL(P[2], P[2]);
    const f32x2 Q4 = CMUL(Q2, Q2);
    R[3] = CMUL(R[1], Q2); R[4] = Q4 * cs;
    P[5] = CMUL(P[4], P[1]); P[6] = CMUL(P[3], P[3]); P[7] = CMUL(P[4], P[3]);
#pragma unroll
    for (int j = 5; j <= 16; ++j) R[j] = CMUL(R[j - 4], Q4);
    asm volatile("" : "+v"(R[13]), "+v"(R[14]), "+v"(R[15]), "+v"(R[16]), "+v"(P[7]), "+v"(P[6]), "+v"(P[5]));
    PUT(0, cs, 0.f); PUT(8, R[1][0], R[1][1]);
#pragma unroll
    for (int r = 1; r < 8; ++r) { PUT(r, P[r][0] * cs, P[r][1] * cs); PUT(8 + r, CMR(R[1], P[r]), CMI(R[1], P[r])); }
    asm volatile("" ::: "memory");
#pragma unroll
    for (int j = 2; j < 4; ++j) {
      PUT(16 + 4 * (j - 2), R[j][0], R[j][1]);
#pragma unroll
      for (int r = 1; r < 4; ++r) PUT(16 + 4 * (j - 2) + r, CMR(R[j], P[2 * r]), CMI(R[j], P[2 * r]));
    }
#pragma unroll
    for (int j = 4; j < 8; ++j) { PUT(24 + 2 * (j - 4), R[j][0], R[j][1]); PUT(25 + 2 * (j - 4), CMR(R[j], P[4]), CMI(R[j], P[4])); }
#pragma unroll
    for (int j = 8; j <= 16; ++j) PUT(24 + j, R[j][0], R[j][1]);
    __builtin_amdgcn_sched_barrier(0);
  }
  ST(0, 2);
  cnt_add(smem, C_F, lane);
  u32 stag = __builtin_amdgcn_readfirstlane(tg), g2;
  asm volatile("" : "+s"(stag));
  cnt_spin(smem, C_F, 8);
  k_unit(pa, c ? hi2f(dskw) : lo2f(dskw), (tid & 127) == 0);
  cnt_add(smem, C_A, lane);
  setup1();
  setup2();
  cnt_spin(smem, C_A, 4);
  ST(0, 4);
  {
    u32 kl[11];
    k_get(kl, pa);
    const u32 h0 = *(const LAS u16*)(size_t)ha, h1 = *(const LAS u16*)(size_t)(ha + 2), h2 = *(const LAS u16*)(size_t)hz;
    SB();
    u32x4 zz = {0, 0, 0, 0};
    asm volatile("" : "+v"(zz));
    *(LAS u32x4*)(size_t)za = zz;
    k_copies(kl, lane, true);
    asm volatile("" :: "v"(zz));
    cnt_add(smem, C_A, lane);
    ST(0, 5);
    {
      const u32 p = h0 | (h1 << 16), hh = __builtin_amdgcn_perm(p, p, 0x03020302u), ll = __builtin_amdgcn_perm(p, p, 0x01000100u);
      const u32 hx = __builtin_amdgcn_perm(p, p, hs), xl = __builtin_amdgcn_perm(p, p, ht);
      u32x4 c0 = {hh, hx, ll, ll}, c1 = {hh, xl, ll, ll}, c2 = {hx, ll, ll, ll}, c3 = {xl, ll, ll, h0 | (h2 << 16)};
      asm volatile("" : "+v"(c0), "+v"(c1), "+v"(c2), "+v"(c3));
      char* cop = smem + c * 4 * CSTR + ((w & 1) * 64 + lane) * 16;
      *(u32x4*)(cop + 0 * CSTR) = c0; *(u32x4*)(cop + 1 * CSTR) = c1; *(u32x4*)(cop + 2 * CSTR) = c2; *(u32x4*)(cop + 3 * CSTR) = c3;
      asm volatile("s_nop 1" :: "v"(c0), "v"(c1), "v"(c2), "v"(c3) : "memory");
      cnt_add(smem, C_KH, lane);
    }
  }
  cnt_spin(smem, C_A, 8);
  bf16x8 wof[16];
  u32x4 pfl, wfl;
  XTake xt;
  AttC ac;
  a_conv1(wrs, w, cp, pfl, wfl, g2);
  asm volatile("" : "+v"(pfl), "+v"(wfl));
  SB();
  KSLP(15, SLP_XA1);
  tags_wait(pfl, wrs, OFF_ST0 + tid * 16, stag, 19);
  dma4k(wrs, xv, xs0, xl0);
  dma4k(wrs, xv, xs1, xl1);
  KSLP(14, SLP_WE);
  SB();
  ConvK ck;
  a_blayer<true>(smem, wrs, ws, out, tid, w, wof, stag, wfl, ck, xt, ac);
  ST(1, 2);
  a_conv<false>(smem, wrs, RB_OF(bid), w, ck);
  a_blayer<false>(smem, wrs, ws, out, tid, w, wof, stag, wfl, ck, xt, ac);
  ST_OUT();
  }
  if (w2 >= 4) {
    const int lane = lane_id();
    LAS char* wa = (LAS char*)(size_t)opaque((u32)(ftb + (w & 1) * 256 + lane * 4));
    u32 tag;
    u32 st0, st1;
    int pbo;
    auto cols = [&]() __attribute__((always_inline)) {
    {
      float ar, ai, br, bi, cr, ci;
      asm volatile("v_lshlrev_b32 %0, 16, v248\n\tv_lshlrev_b32 %1, 16, v249\n\tv_lshlrev_b32 %2, 16, v250\n\t"
                   "v_lshlrev_b32 %3, 16, v251\n\tv_lshlrev_b32 %4, 16, v252\n\tv_lshlrev_b32 %5, 16, v253"
                   : "=v"(ar), "=v"(ai), "=v"(br), "=v"(bi), "=v"(cr), "=v"(ci) :: "memory");
      asm volatile("s_waitcnt lgkmcnt(0)" : "+s"(ldtw) :: "memory");
      const float dt = rbf(__expf(c ? hi2f(ldtw) : lo2f(ldtw)));
      const float dbr = dt * br, dbi = dt * bi;
      const float zr = dt * ar * 1.4426950408889634f, zi = dt * ai * 0.15915494309189535f;
      const float zp = fmaxf(zr, 0.f), g1 = __builtin_amdgcn_exp2f(zp * 1928.f);
      const float wr = (cr * dbr - ci * dbi) * g1, wi = (cr * dbi + ci * dbr) * g1;
      float bd = (fabsf(wr) + fabsf(wi)) * __builtin_amdgcn_exp2f(zp * 120.f);
      f32x2 S1, C[16];
      { float a_, b_; cexp2(zr * 8.f, zi * 8.f, a_, b_); S1 = (f32x2){a_, b_}; }
      bd = dpp_add<0x111, 0xf>(bd); bd = dpp_add<0x112, 0xf>(bd);
      const f32x2 S2 = CMUL(S1, S1);
      bd = dpp_add<0x114, 0xf>(bd); bd = dpp_add<0x118, 0xf>(bd);
      const f32x2 S4 = CMUL(S2, S2);
      bd = dpp_add<0x142, 0xa>(bd); bd = dpp_add<0x143, 0xc>(bd);
      bd = __int_as_float(__builtin_amdgcn_readlane(__float_as_int(bd), 63));
      *(float*)(smem + L_P0 + 512 + g * 4) = bd;
      const float sc = __uint_as_float((u32)(268 - bexp(bd)) << 23);
      C[0] = (f32x2){wr * sc, wi * sc};
      C[1] = CMUL(C[0], S1); C[2] = CMUL(C[0], S2); C[3] = CMUL(C[1], S2);
#pragma unroll
      for (int e = 4; e < 16; ++e) C[e] = CMUL(C[e - 4], S4);
#pragma unroll
      for (int e = 0; e < 16; ++e) *(LAS u32*)(wa + (41 + e) * FTS) = pkhn(C[e][0], C[e][1]);
    }
      cnt_add(smem, C_F, lane);
      ST(0, 2);
    };
    auto stats = [&]() __attribute__((always_inline)) {
    {
      const u32 ONES = opaque(0x3F803F80u);
      u32x4 xt[2];
      XT_GET(xt);
      float sm[2] = {0.f, 0.f}, sq[2] = {0.f, 0.f};
#pragma unroll
      for (int e = 0; e < 8; ++e) { sm[e & 1] = dot2acc(xt[e & 1][e >> 1], ONES, sm[e & 1]); sq[e & 1] = dot2acc(xt[e & 1][e >> 1], xt[e & 1][e >> 1], sq[e & 1]); }
      const bool b0 = lane & 1, b1 = lane & 2;
      const float v0 = (b0 ? sm[1] : sm[0]) + dpp_mov<DPP_XOR1>(b0 ? sm[0] : sm[1]), v1 = (b0 ? sq[1] : sq[0]) + dpp_mov<DPP_XOR1>(b0 ? sq[0] : sq[1]);
      float v = (b1 ? v1 : v0) + dpp_mov<DPP_XOR2>(b1 ? v0 : v1);
      v += dpp_mov<0x124>(v); v += dpp_mov<DPP_ROR8>(v);
      auto r = __builtin_amdgcn_permlane16_swap(__float_as_uint(v), __float_as_uint(v), false, false);
      v = __uint_as_float(r[0]) + __uint_as_float(r[1]);
      auto q = __builtin_amdgcn_permlane32_swap(__float_as_uint(v), __float_as_uint(v), false, false);
      v = (__uint_as_float(q[0]) + __uint_as_float(q[1])) * (1.f / 512.f);
      st0 = pack_stats(v, fmaxf(dpp_mov<DPP_XOR2>(v) - v * v, 0.f));
      st1 = dpp_u<DPP_XOR1>(st0);
    }
    };
    auto publish = [&]() __attribute__((always_inline)) {
      asm volatile("v_add_u32 %0, 1, v254" : "=v"(tag) :: "memory");
      stw(wrs, pbo, (u32x4){st0, tag, st1, tag});
      __builtin_amdgcn_sched_barrier(0);
    };
#define VMW(n_) asm volatile("s_waitcnt vmcnt(%0)" :: "n"(n_) : "memory")
    constexpr int PTN = 2;
    const int ut = 16 + (bid >> 3), us = ut / 3, um = ut % 3;
    const rsrc_t qrs = __builtin_amdgcn_make_buffer_rsrc((void*)wqkv, 0, 1572864, 0x00020000), ors = __builtin_amdgcn_make_buffer_rsrc((void*)wo, 0, 524288, 0x00020000);
    const int wv0 = (8 * g + (lane >> 3)) * 3072 + (((lane & 7) ^ ((lane >> 3) & 6)) << 4);
#define W_DMA1(r_, v_, s_, l_) asm volatile("s_mov_b32 m0, %3\n\ts_nop 0\n\tbuffer_load_dwordx4 %0, %1, %2 offen lds" :: "v"(v_), "s"(r_), "s"(s_), "s"(l_) : "memory")
    W_DMA1(qrs, wv0, us * 98304 + um * 1024 + (bid & 7) * 128, L_WR + g * 1024);
    const int hs = (3 - (int)(kb[2] >> 7)) & 7, xt_ = bid - 117, xs = (xt_ / 3) * 98304 + (xt_ % 3) * 1024;
    const bool xp = bid >= 128 && bid < 133;
    W_DMA1(xp ? qrs : ors, xp ? wv0 : ((bid >> 3) * 32 + 8 * g + (lane >> 3)) * 1024 + (((lane & 7) ^ (((lane >> 3) & 2) | ((g & 1) << 2))) << 4), xp ? xs + hs * 128 : (bid & 7) * 128, L_WR + 4096 + g * 1024);
    B0();
    const int ln = lane;
    pbo = (int)opaque((u32)(lane == 0 ? OFF_SG + gr * 64 + g * 16 : WSB));
    const int wvo = (int)opaque((u32)(wv0 + hb * 128)), wvx = (int)opaque((u32)(hb == hs ? WSB : wvo));
#define WSO(s_) ((s_) < 4 ? 40960 + (s_) * 12288 : L_WX - L_WS)
    auto wraw = [&](auto S_) __attribute__((always_inline)) { constexpr int s = decltype(S_)::value; w_dma(qrs, s < 4 ? wvo : wvx, s < 3 ? wvo : wvx, s * 98304, L_WS + WSO(s) + g * 1024); };
    __builtin_amdgcn_s_setprio(1);
    VMW(8 + PTN);
    ST(0, 1);
    stats();
    VMW(7 + PTN);
    publish();
    ST(0, 10);
    VMW(2 + PTN);
    cols();
    SB();
    __builtin_amdgcn_s_setprio(0);
    static_for<0, R1D>(wraw);
    VMW(3 * R1D + 1); cnt_add(smem, C_B, lane);
    SB();
    const u32 wga = opaque((u32)(L_WS + (ln >> 2) * 128 + (((2 * g + ((ln & 3) >> 1)) ^ ((ln >> 2) & 6)) << 4) + (ln & 1) * 8)), wgb = opaque(wga + 53248u);
    const u32 to = opaque(xp ? wga + (L_WR - L_WS + 4096) : (u32)(L_WR + 4096 + (8 * (lane >> 4) + ((lane >> 2) & 3)) * 128 + (((2 * g + ((lane & 3) >> 1)) ^ (((lane >> 2) & 2) | ((lane >> 2) & 4))) << 4) + (lane & 1) * 8));
    const u32 tp = opaque(to + (xp ? 2048 : 512));
    const int us0 = (int)opaque((u32)(OFF_WQ + us * 98304 + ((bid & 7) * 4 + g) * 3072 + um * 1024 + lane * 16));
    const int us1 = (int)opaque((u32)((bid < 128 ? OFF_WO + (((bid & 7) * 16 + (bid >> 3)) * 4 + g) * 1024 : xp ? OFF_WQ + xs + (hs * 4 + g) * 3072 : WSB) + lane * 16));
    cnt_spin(smem, C_B, 4);
    {
#define TR4(a_) __builtin_amdgcn_ds_read_tr16_b64_v4i16((LAS bf16x4*)(size_t)(a_))
      const bf16x4 q0 = TR4(wgb + (L_WR - L_WS - 53248)), q1 = TR4(wgb + (L_WR - L_WS - 53248 + 2048)), q2 = TR4(to), q3 = TR4(tp);
      cnt_add(smem, C_B, lane);
      stw(wrs, us0, __builtin_bit_cast(u32x4, __builtin_shufflevector(q0, q1, 0, 1, 2, 3, 4, 5, 6, 7)));
      stw(wrs, us1, __builtin_bit_cast(u32x4, __builtin_shufflevector(q2, q3, 0, 1, 2, 3, 4, 5, 6, 7)));
      SB();
    }
    static_for<R1D, R1F>(wraw);
    SB();
    const u32 stag = __builtin_amdgcn_readfirstlane(tag);
    const int fs0 = OFF_ST0 + 4096 + ((bid & 7) * 4 + g) * 128 + (bid >> 3) * 4, fs1 = bid < 128 ? fs0 + 4096 : bid < 136 ? OFF_ST0 + 8192 + (hs * 4 + g) * 128 + (bid - 112) * 4 : WSB;
    const int fso = (int)opaque((u32)(lane == 0 ? fs0 : lane == 1 ? fs1 : WSB));
    const int wbo = (int)opaque((u32)((hb * 4 + g) * 3072 + ln * 16));
    const int fo = (int)opaque((u32)(OFF_ST0 + 4096 + (hb * 4 + g) * 128 + (hb == hs && (ln & 8) ? 4096 + 64 + (ln & 1) * 16 : (ln & 7) * 16)));
    const int r0 = 256 * g + 2 * ln;
    const int sto = (int)opaque((u32)(OFF_SG + r0 * 8)), xto = (int)opaque((u32)(XTO(kp) + r0 * 8));
    const int xlo = (int)opaque((u32)(OFF_X1 + (16 * g + (ln >> 3)) * XBS + kp * 128 + (ln & 7) * 16));
    const u32 uw = opaque((u32)(L_U + (256 + r0) * 2)), xw = opaque((u32)(L_X + r0 * 4));
    const int mso = (int)opaque((u32)(ln == 0 ? OFF_ST0 + kp * 16 + g * 4 : WSB));
    const int p2o = (int)opaque((u32)(OFF_X2 + (kp * XS_ + 256 * g + 4 * ln) * 4)), pso = (int)opaque((u32)(ln < 2 ? OFF_ST2 + (512 * g + 2 * bid + ln) * 16 : WSB));
    SB();
    VMW(3 * (R1F - R1D));
    __builtin_amdgcn_raw_buffer_store_b32(tag, wrs, fso, 0, 16);
    ST(0, 6);
    SB();
    static_for<R1F, 5>(wraw);
    W_DMA1(qrs, wvx, 5 * 98304, L_SP + g * 1024);
    bf16x8 wf[48];
    GPre gp;
    {
      auto wget = [&](auto S_) __attribute__((always_inline)) {
        constexpr int s = decltype(S_)::value, o = WSO(s) >= 53248 ? WSO(s) - 53248 : WSO(s);
        w_get<3 * s, o>(wf[3 * s], WSO(s) >= 53248 ? wgb : wga);
        w_get<3 * s + 1, o + 4096>(wf[3 * s + 1], WSO(s) >= 53248 ? wgb : wga);
        w_get<3 * s + 2, o + 8192>(wf[3 * s + 2], WSO(s) >= 53248 ? wgb : wga);
      };
      const u32 mk = x0_mark(stag);
      auto wld = [&](auto I_) __attribute__((always_inline)) { w_ld<decltype(I_)::value>(wf[decltype(I_)::value], wbo, wrs); };
      u32x4 gs[4], gx[4];
#define G_LD(e_) do { gs[e_] = ldws(wrs, sto + ((e_) & 1) * 1024, ((e_) >> 1) * 8192); gx[e_] = (e_) < 2 ? ldws(wrs, xlo, (e_) * 8 * XBS) : ldw(wrs, xto + ((e_) - 2) * 1024); } while (0)
#define G_B2(v_, t_) opaque((v_[1] ^ (t_)) | (v_[3] ^ (t_)))
#define G_OK(h_) (__builtin_amdgcn_ballot_w64(((G_B2(gs[2 * (h_)], stag) | G_B2(gs[2 * (h_) + 1], stag)) | (G_B2(gx[2 * (h_)], mk) | G_B2(gx[2 * (h_) + 1], mk))) != 0u) == 0ull)
      auto take = [&](auto H_) __attribute__((always_inline)) {
        constexpr int H = decltype(H_)::value;
        if (__builtin_expect(!G_OK(H), 0)) {
          for (unsigned spins = 0;; ++spins) {
            FAILC(H ? 4 : 0);
            __builtin_amdgcn_s_sleep(SLP_U);
            asm volatile("" ::: "memory");
            G_LD(2 * H); G_LD(2 * H + 1);
            if (G_OK(H) || spins > SPIN_CAP) break;
          }
        }
#pragma unroll
        for (int e = 2 * H; e < 2 * H + 2; ++e) {
          const float rs0 = __builtin_amdgcn_rcpf(hi2f(gs[e][0])), rs1 = __builtin_amdgcn_rcpf(hi2f(gs[e][2])), n0 = -lo2f(gs[e][0]) * rs0, n1 = -lo2f(gs[e][2]) * rs1;
          const int r = (e & 1) * 128 + (e >> 1) * 1024;
          *(LAS u32*)(size_t)(uw + r * 2) = pkh(fmaf(lo2f(gx[e][0]), rs0, n0), fmaf(lo2f(gx[e][2]), rs1, n1));
          *(LAS u32*)(size_t)(uw + r * 2 + 4608) = pkh(fmaf(hi2f(gx[e][0]), rs0, n0), fmaf(hi2f(gx[e][2]), rs1, n1));
          *(LAS u32x2*)(size_t)(xw + r * 4) = (u32x2){gx[e][0], gx[e][2]};
        }
        __builtin_amdgcn_sched_barrier(0);
      };
      KSLP(16, SLP_G);
      asm volatile("" ::: "memory");
#pragma unroll
      for (int e = 0; e < 4; ++e) G_LD(e);
      __builtin_amdgcn_sched_barrier(0);
      ST(0, 5);
      f32x4 cs[3];
      u32x4 SENT = {0xFFFFFFFFu, 0xFFFFFFFFu, 0xFFFFFFFFu, 0xFFFFFFFFu};
      const u32 o1 = ((ln >> 1) & 1) == (ln >> 5) ? 0x3F803F80u : 0u;
      bf16x8 one8 = __builtin_bit_cast(bf16x8, (u32x4){o1, o1, o1, o1});
      u32 sidx = (ln & 1) ? 0xEEEEu : 0x4444u;
      cs[0] = cs[1] = cs[2] = (f32x4){0.f, 0.f, 0.f, 0.f};
      asm volatile("" : "+v"(one8), "+v"(sidx), "+v"(cs[0]), "+v"(cs[1]), "+v"(cs[2]), "+v"(SENT));
      auto wsum = [&](auto I_) __attribute__((always_inline)) { constexpr int i = 2 * decltype(I_)::value + 1; w_sum<i>(cs[i % 3], one8, sidx, wf); };
      cnt_spin(smem, C_B, 8);
      __builtin_amdgcn_s_setprio(1);
      VMW(8);
      cnt_add(smem, C_W, ln);
      u32x4 fl;
      asm volatile("" : "+v"(gs[0]), "+v"(gs[1]), "+v"(gx[0]), "+v"(gx[1]) :: "memory");
      take(IC<0>{});
      cnt_add(smem, C_U, ln);
      __builtin_amdgcn_raw_buffer_store_b32(tag, wrs, mso, 0, 16);
      ST(0, 3);
      stw(wrs, p2o, SENT); __builtin_amdgcn_raw_buffer_store_b128(SENT, wrs, p2o, 4096, 16);
      stw(wrs, pso, __builtin_bit_cast(u32x4, cs[0]));
      asm volatile("s_nop 1" :: "v"(SENT) : "memory");
      SB();
      auto rest = [&]() __attribute__((always_inline)) {
        cnt_spin(smem, C_W, 4);
        static_for<0, 5>(wget);
        w_get<15, L_SP - L_WS - 53248>(wf[15], wgb);
        cnt_add(smem, C_B, ln);
        take(IC<1>{});
        cnt_add(smem, C_P2, ln);
        ST(0, 4);
      };
      rest();
      { KSLP(17, SLP_FL); fl = ldw(wrs, fo); SB(); asm volatile("s_waitcnt lgkmcnt(0)" ::: "memory"); static_for<0, 7>(wsum); g_pre<true>(gp, g, stag); }
      asm volatile("" : "+v"(gs[2]), "+v"(gs[3]), "+v"(gx[2]), "+v"(gx[3]), "+v"(fl) :: "memory");
      tags_wait(fl, wrs, fo, stag, 15);
      ST(0, 7);
      asm volatile("s_cmp_lg_u32 %5, %6\n\ts_cbranch_scc1 1f\n\tbuffer_load_dwordx4 %0, %7, %8, %9 offen offset:2048 sc1\n\t"
                   "buffer_load_dwordx4 %1, %7, %8, %10 offen sc1\n\tbuffer_load_dwordx4 %2, %7, %8, %10 offen offset:1024 sc1\n\tbuffer_load_dwordx4 %3, %7, %8, %10 offen offset:2048 sc1\n\t"
                   "buffer_load_dwordx4 %4, %7, %8, %11 offen sc1\n1:"
                   : "+{v[120:123]}"(wf[11]), "+{v[124:127]}"(wf[12]), "+{v[128:131]}"(wf[13]), "+{v[132:135]}"(wf[14]), "+{v[136:139]}"(wf[15])
                   : "s"(hb), "s"(hs), "v"(wbo), "s"(wrs), "s"(OFF_WQ + 3 * 98304), "s"(OFF_WQ + 4 * 98304), "s"(OFF_WQ + 5 * 98304) : "memory", "scc");
      static_for<45, 48>(wld);
      static_for<16, 16 + H1>(wld);
      VMW(3 + H1);
      cnt_add(smem, C_P3, ln);
      static_for<16 + H1, 45>(wld);
      __builtin_amdgcn_s_setprio(0);
      ST(0, 9);
      cnt_spin(smem, C_B, 12);
      VMW(29);
#pragma unroll
      for (int e = 0; e < 3; ++e) *(LAS bf16x8*)(size_t)(gp.wx + e * 1024) = wf[45 + e];
      static_for<22, 24>(wsum);
      asm volatile("s_cmp_lg_u32 %4, %5\n\ts_cbranch_scc1 1f\n\tv_smfmac_f32_16x16x64_bf16 %0, %2, v[120:127], %3\n\tv_smfmac_f32_16x16x64_bf16 %1, %2, v[128:135], %3\n1:"
                   : "+v"(cs[2]), "+v"(cs[1]) : "v"(one8), "v"(sidx), "s"(hb), "s"(hs), "{v[120:123]}"(wf[11]), "{v[124:127]}"(wf[12]), "{v[128:131]}"(wf[13]), "{v[132:135]}"(wf[14]) : "scc");
      VMW(12); static_for<7, 16>(wsum);
      VMW(6); static_for<16, 19>(wsum);
      VMW(2); static_for<19, 21>(wsum);
      VMW(0); static_for<21, 22>(wsum);
      asm volatile("s_nop 7" : "+v"(cs[0]), "+v"(cs[1]), "+v"(cs[2]));
#pragma unroll
      for (int e = 0; e < 3; ++e) *(LAS float*)(size_t)(gp.fr + 512 + e * 64) = (cs[e][0] + cs[e][1]) + (cs[(e + 2) % 3][2] + cs[(e + 2) % 3][3]);
      asm volatile("" ::: "memory");
      ST(0, 8);
    }
    {
      ConvPre pa2, pb2;
      u32x4 xa, xb;
      int ln2, l2, xo2, xo, so;
      u32 ua, rp0, rp1;
      float isc;
      g_blayer<true>(smem, wrs, g, stag, wf, gp, [&]() __attribute__((always_inline)) {
        ln2 = lane_id();
        l2 = 256 * g + 4 * ln2;
        xo2 = (int)opaque((u32)(OFF_X3 + (8 * g + (ln2 >> 3)) * XBS + kp * 128 + (ln2 & 1) * 64 + (ln2 & 6) * 8));
        xo = (int)opaque((u32)(OFF_X2 + (kp * XS_ + l2) * 4)); so = (int)opaque((u32)(OFF_ST2 + (256 * g + ln2) * 16));
        ua = opaque((u32)(L_U + (256 + l2) * 2));
        rp0 = opaque((u32)(L_REDA + (ln2 & 1) * 8192 + (g * 64 + (ln2 ^ ((ln2 >> 3) & 3))) * 16)); rp1 = opaque(rp0 ^ 16u);
        isc = *(const float*)(smem + L_MI + 4 * (ln2 & 1));
        asm volatile("" : "+v"(isc));
      });
#define VM0() __builtin_amdgcn_s_waitcnt(0x0F70)
      KSLP(9, SLP_C2);
      u32x4 gg[4];
      ST(1, 2);
      xa = ldw(wrs, xo);
      asm volatile("" : "+v"(xa));
      if (__builtin_expect(!x2_ok(xa), 0)) { FAILC(16); __builtin_amdgcn_s_sleep(SLP_XP); xa = x2_ld(wrs, kp, l2); x2_poll(wrs, kp, l2, xa); VM0(); }
      KSLP(10, SLP_S);
      asm volatile("" ::: "memory");
#pragma unroll
      for (int e = 0; e < 4; ++e) gg[e] = ldw(wrs, so + e * 1024);
      SB();
      conv_unp(pa2, xa);
      asm volatile("" : "+v"(gg[0]), "+v"(gg[1]), "+v"(gg[2]), "+v"(gg[3]));
      if (__builtin_expect(!st2_ok(gg), 0)) { FAILC(7); __builtin_amdgcn_s_sleep(SLP_SP); st2_ld(wrs, l2, gg); st2_poll(wrs, l2, gg); VM0(); }
      conv2_in<false>(smem, ua, pa2, xa, gg);
      ST(1, 3);
      lds_barrier();
      ST(1, 7);
      conv_res(pa2);
      const int so2 = (int)opaque((u32)(so + 16384));
      auto rq_b = [&]() __attribute__((always_inline)) {
        KSLP(11, SLP_C3);
        asm volatile("" ::: "memory");
        xb = ldws(wrs, xo, 4096);
        SB();
#pragma unroll
        for (int e = 0; e < 4; ++e) gg[e] = ldw(wrs, so2 + e * 1024);
        SB();
      };
      rq_b();
      __builtin_amdgcn_s_barrier();
      asm volatile("" ::: "memory");
      ST(1, 8);
      stw(wrs, xo2, conv_epi(rp0, rp1, 0, isc, pa2));
      SB();
      asm volatile("" : "+v"(xb), "+v"(gg[0]), "+v"(gg[1]), "+v"(gg[2]), "+v"(gg[3]) :: "memory");
      ST(1, 4);
      if (__builtin_expect(!x2st_ok(xb, gg), 0)) {
        FAILC(18);
        xb = x2_ld(wrs, kp, l2 + 1024);
        st2_ld(wrs, l2 + 1024, gg);
        if (!x2_ok(xb)) {
          x2_poll(wrs, kp, l2 + 1024, xb);
          st2_ld(wrs, l2 + 1024, gg);
        }
        st2_poll(wrs, l2 + 1024, gg);
        VM0();
      }
      ST(1, 1);
      conv2_in<true>(smem, ua + 2048, pb2, xb, gg);
      ST(1, 5);
      lds_barrier();
      ST(1, 6);
      const int rb = RB_OF(bid);
      conv_res(pb2);
      XTake xt;
      x_pre(xt, g, ln2);
      g_pre<false>(gp, g, stag);
      const int xo3 = (int)opaque((u32)(xo2 + 32 * XBS));
      KSLP(12, SLP_XA2);
      x_dma(wrs, OFF_X3, rb, L_TA, g, ln2);
      SB();
      __builtin_amdgcn_s_barrier();
      asm volatile("" ::: "memory");
      ST(1, 9);
      u32x4 ov = conv_epi(rp0, rp1, 16384, isc, pb2);
      stw(wrs, xo3, ov);
      SB();
      x_take<0, 1, true>(xt, smem, wrs, OFF_X3, rb, g, ln2);
      asm volatile("" :: "v"(ov));
    }
    g_blayer<false>(smem, wrs, g, stag, wf, gp, []() {});

    ST_OUT();
  }
}

__device__ __attribute__((used, noinline)) void k_text_end() {
  asm volatile(
  ".Lg_redo:\n\ts_waitcnt lgkmcnt(0)\n\t"
  "ds_read_b64_tr_b16 v[20:21], v0 offset:0\n\tds_read_b64_tr_b16 v[22:23], v0 offset:1024\n\ts_waitcnt lgkmcnt(0)\n\t"
  "v_mfma_f32_16x16x32_bf16 v[8:11], v[80:83], v[20:23], 0\n\tv_mfma_f32_16x16x32_bf16 v[48:51], v[20:23], v[84:87], 0\n\tv_mfma_f32_16x16x32_bf16 v[64:67], v[76:79], v[20:23], 0\n\t"
  "ds_read_b64_tr_b16 v[20:21], v0 offset:2048\n\tds_read_b64_tr_b16 v[22:23], v0 offset:3072\n\ts_waitcnt lgkmcnt(0)\n\t"
  "v_mfma_f32_16x16x32_bf16 v[8:11], v[92:95], v[20:23], v[8:11]\n\tv_mfma_f32_16x16x32_bf16 v[48:51], v[20:23], v[96:99], v[48:51]\n\tv_mfma_f32_16x16x32_bf16 v[64:67], v[88:91], v[20:23], v[64:67]\n\t"
  "ds_read_b64_tr_b16 v[20:21], v0 offset:8192\n\tds_read_b64_tr_b16 v[22:23], v0 offset:9216\n\ts_waitcnt lgkmcnt(0)\n\t"
  "v_mfma_f32_16x16x32_bf16 v[8:11], v[128:131], v[20:23], v[8:11]\n\tv_mfma_f32_16x16x32_bf16 v[48:51], v[20:23], v[132:135], v[48:51]\n\tv_mfma_f32_16x16x32_bf16 v[64:67], v[124:127], v[20:23], v[64:67]\n\t"
  "ds_read_b64_tr_b16 v[20:21], v0 offset:10240\n\tds_read_b64_tr_b16 v[22:23], v0 offset:11264\n\ts_waitcnt lgkmcnt(0)\n\t"
  "v_mfma_f32_16x16x32_bf16 v[8:11], v[140:143], v[20:23], v[8:11]\n\tv_mfma_f32_16x16x32_bf16 v[48:51], v[20:23], v[144:147], v[48:51]\n\tv_mfma_f32_16x16x32_bf16 v[64:67], v[136:139], v[20:23], v[64:67]\n\t"
  "ds_read_b64_tr_b16 v[20:21], v0 offset:16384\n\tds_read_b64_tr_b16 v[22:23], v0 offset:17408\n\ts_waitcnt lgkmcnt(0)\n\t"
  "v_mfma_f32_16x16x32_bf16 v[8:11], v[176:179], v[20:23], v[8:11]\n\tv_mfma_f32_16x16x32_bf16 v[48:51], v[20:23], v[180:183], v[48:51]\n\tv_mfma_f32_16x16x32_bf16 v[64:67], v[172:175], v[20:23], v[64:67]\n\t"
  "ds_read_b64_tr_b16 v[20:21], v0 offset:18432\n\tds_read_b64_tr_b16 v[22:23], v0 offset:19456\n\ts_waitcnt lgkmcnt(0)\n\t"
  "v_mfma_f32_16x16x32_bf16 v[8:11], v[188:191], v[20:23], v[8:11]\n\tv_mfma_f32_16x16x32_bf16 v[48:51], v[20:23], v[192:195], v[48:51]\n\tv_mfma_f32_16x16x32_bf16 v[64:67], v[184:187], v[20:23], v[64:67]\n\t"
  "ds_read_b64_tr_b16 v[20:21], v0 offset:24576\n\tds_read_b64_tr_b16 v[22:23], v0 offset:25600\n\ts_waitcnt lgkmcnt(0)\n\t"
  "v_mfma_f32_16x16x32_bf16 v[8:11], v[224:227], v[20:23], v[8:11]\n\tv_mfma_f32_16x16x32_bf16 v[48:51], v[20:23], v[228:231], v[48:51]\n\tv_mfma_f32_16x16x32_bf16 v[64:67], v[220:223], v[20:23], v[64:67]\n\t"
  "ds_read_b64_tr_b16 v[20:21], v0 offset:26624\n\tds_read_b64_tr_b16 v[22:23], v0 offset:27648\n\ts_waitcnt lgkmcnt(0)\n\t"
  "v_mfma_f32_16x16x32_bf16 v[8:11], v[236:239], v[20:23], v[8:11]\n\tv_mfma_f32_16x16x32_bf16 v[48:51], v[20:23], v[240:243], v[48:51]\n\tv_mfma_f32_16x16x32_bf16 v[64:67], v[232:235], v[20:23], v[64:67]\n\t"
  "ds_read_b64_tr_b16 v[20:21], v0 offset:4096\n\tds_read_b64_tr_b16 v[22:23], v0 offset:5120\n\ts_waitcnt lgkmcnt(0)\n\t"
  "v_mfma_f32_16x16x32_bf16 v[8:11], v[104:107], v[20:23], v[8:11]\n\tv_mfma_f32_16x16x32_bf16 v[48:51], v[20:23], v[108:111], v[48:51]\n\tv_mfma_f32_16x16x32_bf16 v[64:67], v[100:103], v[20:23], v[64:67]\n\t"
  "ds_read_b64_tr_b16 v[20:21], v0 offset:6144\n\tds_read_b64_tr_b16 v[22:23], v0 offset:7168\n\tds_read_b64_tr_b16 v[36:37], v0 offset:12288\n\tds_read_b64_tr_b16 v[38:39], v0 offset:13312\n\tds_read_b64_tr_b16 v[28:29], v0 offset:14336\n\tds_read_b64_tr_b16 v[30:31], v0 offset:15360\n\tds_read_b64_tr_b16 v[68:69], v0 offset:20480\n\tds_read_b64_tr_b16 v[70:71], v0 offset:21504\n\t"
  "s_waitcnt lgkmcnt(0)\n\ts_branch .Lg_back\n\t"
  ".Lc0_kh:\n\ts_mov_b32 vcc_hi, 0x1000000\n\t.Lc0_kh_s:\n\tds_read_b32 v101, v27 offset:48\n\ts_sub_u32 vcc_hi, vcc_hi, 1\n\ts_cselect_b32 vcc_lo, 0, 4\n\ts_waitcnt lgkmcnt(0)\n\tv_readfirstlane_b32 s61, v101\n\ts_nop 1\n\ts_cmp_lt_u32 s61, vcc_lo\n\ts_cbranch_scc1 .Lc0_kh_s\n\t"
  "s_waitcnt lgkmcnt(0)\n\ts_branch .Lc0_kh_b\n\t"
  ".Lc0_x0:\n\ts_mov_b32 vcc_hi, 0x1000000\n\t.Lc0_x0_s:\n\tds_read_b32 v101, v21 offset:8\n\ts_sub_u32 vcc_hi, vcc_hi, 1\n\ts_cselect_b32 vcc_lo, 0, 2\n\ts_waitcnt lgkmcnt(0)\n\tv_readfirstlane_b32 s61, v101\n\ts_nop 1\n\ts_cmp_lt_u32 s61, vcc_lo\n\ts_cbranch_scc1 .Lc0_x0_s\n\t"
  "ds_read_b128 v[144:147], v20 offset:2048\n\tds_read_b128 v[204:207], v20 offset:3072\n\ts_waitcnt lgkmcnt(0)\n\ts_branch .Lc0_x0_b\n\t"
  ".Lc0_ub:\n\ts_mov_b32 vcc_hi, 0x1000000\n\t.Lc0_ub_s:\n\tds_read_b32 v101, v27 offset:52\n\ts_sub_u32 vcc_hi, vcc_hi, 1\n\ts_cselect_b32 vcc_lo, 0, 4\n\ts_waitcnt lgkmcnt(0)\n\tv_readfirstlane_b32 s61, v101\n\ts_nop 1\n\ts_cmp_lt_u32 s61, vcc_lo\n\ts_cbranch_scc1 .Lc0_ub_s\n\t"
  "s_waitcnt lgkmcnt(0)\n\ts_branch .Lc0_ub_b\n\t"
  ".Lc0_p3:\n\ts_mov_b32 vcc_hi, 0x1000000\n\t.Lc0_p3_s:\n\tds_read_b32 v101, v27 offset:64\n\ts_sub_u32 vcc_hi, vcc_hi, 1\n\ts_cselect_b32 vcc_lo, 0, 4\n\ts_waitcnt lgkmcnt(0)\n\tv_readfirstlane_b32 s61, v101\n\ts_nop 1\n\ts_cmp_lt_u32 s61, vcc_lo\n\ts_cbranch_scc1 .Lc0_p3_s\n\t"
  "s_waitcnt lgkmcnt(0)\n\ts_branch .Lc0_p3_b\n\t"
  ".Lc0_x4:\n\ts_mov_b32 vcc_hi, 0x1000000\n\t.Lc0_x4_s:\n\tds_read_b32 v36, v27 offset:16\n\ts_sub_u32 vcc_hi, vcc_hi, 1\n\ts_cselect_b32 vcc_lo, 0, 4\n\ts_waitcnt lgkmcnt(0)\n\tv_readfirstlane_b32 s61, v36\n\ts_nop 1\n\ts_cmp_lt_u32 s61, vcc_lo\n\ts_cbranch_scc1 .Lc0_x4_s\n\t"
  "ds_read_b128 v[40:43], v20 offset:59392\n\tds_read_b128 v[44:47], v20 offset:60416\n\ts_waitcnt lgkmcnt(0)\n\ts_branch .Lc0_x4_b\n\t"
  ".Lc1_kh:\n\ts_mov_b32 vcc_hi, 0x1000000\n\t.Lc1_kh_s:\n\tds_read_b32 v101, v27 offset:48\n\ts_sub_u32 vcc_hi, vcc_hi, 1\n\ts_cselect_b32 vcc_lo, 0, 4\n\ts_waitcnt lgkmcnt(0)\n\tv_readfirstlane_b32 s61, v101\n\ts_nop 1\n\ts_cmp_lt_u32 s61, vcc_lo\n\ts_cbranch_scc1 .Lc1_kh_s\n\t"
  "s_waitcnt lgkmcnt(0)\n\ts_branch .Lc1_kh_b\n\t"
  ".Lc1_x0:\n\ts_mov_b32 vcc_hi, 0x1000000\n\t.Lc1_x0_s:\n\tds_read_b32 v101, v21 offset:8\n\ts_sub_u32 vcc_hi, vcc_hi, 1\n\ts_cselect_b32 vcc_lo, 0, 2\n\ts_waitcnt lgkmcnt(0)\n\tv_readfirstlane_b32 s61, v101\n\ts_nop 1\n\ts_cmp_lt_u32 s61, vcc_lo\n\ts_cbranch_scc1 .Lc1_x0_s\n\t"
  "ds_read_b128 v[148:151], v20 offset:0\n\tds_read_b128 v[204:207], v20 offset:1024\n\ts_waitcnt lgkmcnt(0)\n\ts_branch .Lc1_x0_b\n\t"
  ".Lc1_ub:\n\ts_mov_b32 vcc_hi, 0x1000000\n\t.Lc1_ub_s:\n\tds_read_b32 v101, v27 offset:52\n\ts_sub_u32 vcc_hi, vcc_hi, 1\n\ts_cselect_b32 vcc_lo, 0, 4\n\ts_waitcnt lgkmcnt(0)\n\tv_readfirstlane_b32 s61, v101\n\ts_nop 1\n\ts_cmp_lt_u32 s61, vcc_lo\n\ts_cbranch_scc1 .Lc1_ub_s\n\t"
  "s_waitcnt lgkmcnt(0)\n\ts_branch .Lc1_ub_b\n\t"
  ".Lc1_p3:\n\ts_mov_b32 vcc_hi, 0x1000000\n\t.Lc1_p3_s:\n\tds_read_b32 v101, v27 offset:64\n\ts_sub_u32 vcc_hi, vcc_hi, 1\n\ts_cselect_b32 vcc_lo, 0, 4\n\ts_waitcnt lgkmcnt(0)\n\tv_readfirstlane_b32 s61, v101\n\ts_nop 1\n\ts_cmp_lt_u32 s61, vcc_lo\n\ts_cbranch_scc1 .Lc1_p3_s\n\t"
  "s_waitcnt lgkmcnt(0)\n\ts_branch .Lc1_p3_b\n\t"
  ".Lc1_x4:\n\ts_mov_b32 vcc_hi, 0x1000000\n\t.Lc1_x4_s:\n\tds_read_b32 v36, v27 offset:16\n\ts_sub_u32 vcc_hi, vcc_hi, 1\n\ts_cselect_b32 vcc_lo, 0, 4\n\ts_waitcnt lgkmcnt(0)\n\tv_readfirstlane_b32 s61, v36\n\ts_nop 1\n\ts_cmp_lt_u32 s61, vcc_lo\n\ts_cbranch_scc1 .Lc1_x4_s\n\t"
  "ds_read_b128 v[40:43], v20 offset:57344\n\tds_read_b128 v[44:47], v20 offset:58368\n\ts_waitcnt lgkmcnt(0)\n\ts_branch .Lc1_x4_b\n\t"
  ".Lcw_end:");
}

extern "C" void kernel_launch(void* const* d_in, const int* in_sizes, int n_in, void* d_out, int out_size,
                              void* d_ws, size_t ws_size, hipStream_t stream) {
  (void)hipFuncSetAttribute((const void*)k_mega, hipFuncAttributeMaxDynamicSharedMemorySize, MEGA_LDS);
  k_mega<<<256, 512, MEGA_LDS, stream>>>((const u16*)d_in[0], (const u16*)d_in[1], (const u16*)d_in[2], (const u16*)d_in[3],
                                         (const u16*)d_in[4], (const u16*)d_in[5], (const u16*)d_in[6], (const u16*)d_in[7],
                                         (const u16*)d_in[8], (const u16*)d_in[10], (const u16*)d_in[11], (char*)d_ws,
                                         (u32*)d_out);
}
#if defined(__HIP_DEVICE_COMPILE__)
#pragma clang attribute pop
#endif
```

```cpp
#if defined(__HIP_DEVICE_COMPILE__)
#pragma clang attribute push (__attribute__((target("no-packed-fp32-ops"))), apply_to = function)
#endif
#include <hip/hip_runtime.h>
#include <hip/hip_bf16.h>
#include <stdint.h>
#define TIDX ((int)__builtin_amdgcn_workitem_id_x())
#define BIDX ((int)__builtin_amdgcn_workgroup_id_x())
#define ST(set, k)
#define ST_INIT()
#define ST_OUT()
#define KATTR
#define FAILC(k)

#define ST_FIX()
#define CST(k)
#define GST(k)
typedef unsigned short u16;
typedef unsigned int u32;
typedef unsigned long long u64;
using bf16x8 = __attribute__((ext_vector_type(8))) short;
using bf16x4 = __attribute__((ext_vector_type(4))) short;
using f16x8 = __attribute__((ext_vector_type(8))) _Float16;
using f16x4 = __attribute__((ext_vector_type(4))) _Float16;
using f32x2 = __attribute__((ext_vector_type(2))) float;
using f32x4 = __attribute__((ext_vector_type(4))) float;
using u32x2 = __attribute__((ext_vector_type(2))) unsigned;
using u32x4 = __attribute__((ext_vector_type(4))) unsigned;
#define LAS __attribute__((address_space(3)))
#define GAS __attribute__((address_space(1)))

#define D_ 512
#define NS_ 128
#define XS_ 2080

#define MB_ (1 << 20)
#define XTO(kp_) (OFF_O1 + (((kp_) >> 4) & 1) * MB_ + ((kp_) >> 5) * 131072 + ((kp_) & 15) * 8192)
#define XTS 8192
#define RB_OF(b_) (((b_) & 7) * 4 + ((b_) >> 6))
#define HB_OF(b_) (((b_) >> 3) & 7)
#define OFF_ST0 (5 * MB_)
#define OFF_TS (OFF_ST0 + 16384)
#define OFF_SG (OFF_ST0 + 32768)
#define OFF_ST2 (OFF_ST0 + 65536)
#define OFF_T5 (OFF_ST0 + 262144)
#define OFF_EP (OFF_ST0 + 131072)
#define OFF_WQ (9 * MB_ - 262144)
#define OFF_WO (10 * MB_ + 393216)
#define OFF_X1 (11 * MB_)
#define OFF_O1 (14 * MB_)
#define OFF_X2 (16 * MB_)
#define OFF_X3 (19 * MB_)
#define OFF_O2 (22 * MB_)
#define OFF_KV2 (26 * MB_)
#define KVO(L1_, rb_, h_) (((L1_) ? OFF_O2 : OFF_KV2) + (((h_) >> 3) & 1) * MB_ + (rb_) * 32768 + (((h_) >> 2) & 1) * 16384 + ((h_) >> 4) * 8192 + ((h_) & 3) * 2048)
#define WSB (32 * MB_)
#define XBS 32896
#define SPIN_CAP (1u << 14)
#define XPB 1
#define GPE 1
#define B6 0
#define OUTA 2
#define OUTB 0
#define WLD 8
#define SLP_FL 4
#define SLP_WE 0
#define R1F 3
#define D9 1300
#define WS5 5
#define SLP_B4 4
#define H1 9
#define DB2 450
#define D5 200
#define DQ2 0
#define DO2 900
#define SLP_U 2
#define SLP_F 2
#define SLP_G 0
#define R1D 1
#define WOE 4
#define SLP_H1 0
#define SLP_H2 2
#define WO0 0
#define SLP_XA2 2
#define CPF 5
#define SLP_XP 2
#define SLP_SP 4
#define SLP_S 6
#define SLP_XA1 4
#define SLP_XB1 6
#define SLP_OB1 6
#define DO1 800
#define DO5 4600
#define WX 4
#define WA 0
#define WM 4
#define SLP_C2 3
#define SLP_C3 15

__device__ __forceinline__ float bf2f(u16 v) { return __uint_as_float(((u32)v) << 16); }
typedef __attribute__((ext_vector_type(2))) __bf16 bf16x2v;
__device__ __forceinline__ u32 pk2(float a, float b) {
  f32x2 f = {a, b};
  bf16x2v v = __builtin_convertvector(f, bf16x2v);
  return __builtin_bit_cast(u32, v);
}
__device__ __forceinline__ float rbf(float f) { return __uint_as_float(pk2(f, 0.f) << 16); }
__device__ __forceinline__ float lo2f(u32 w) { return __uint_as_float(w << 16); }
__device__ __forceinline__ float hi2f(u32 w) { return __uint_as_float(w & 0xFFFF0000u); }
template <int CTRL>
__device__ __forceinline__ float dpp_mov(float v) {
  return __int_as_float(__builtin_amdgcn_update_dpp(0, __float_as_int(v), CTRL, 0xf, 0xf, false));
}
#define DPP_XOR1 0xB1
#define DPP_XOR2 0x4E
#define DPP_ROR8 0x128
template <int CTRL, int RMASK>
__device__ __forceinline__ float dpp_add(float v) {
  return v + __int_as_float(__builtin_amdgcn_update_dpp(0, __float_as_int(v), CTRL, RMASK, 0xf, false));
}
__device__ __forceinline__ void wave_sum3(float& a, float& b, float& c) {
#define WS3(CT_, RM_) a = dpp_add<CT_, RM_>(a); b = dpp_add<CT_, RM_>(b); c = dpp_add<CT_, RM_>(c); __builtin_amdgcn_sched_barrier(0);
  WS3(0x111, 0xf) WS3(0x112, 0xf) WS3(0x114, 0xf) WS3(0x118, 0xf) WS3(0x142, 0xa) WS3(0x143, 0xc)
  a = __int_as_float(__builtin_amdgcn_readlane(__float_as_int(a), 63));
  b = __int_as_float(__builtin_amdgcn_readlane(__float_as_int(b), 63));
  c = __int_as_float(__builtin_amdgcn_readlane(__float_as_int(c), 63));
}
__device__ __forceinline__ float rmax(float a, float b) {
  float r;
  asm("v_max_f32 %0, %1, %2" : "=v"(r) : "v"(a), "v"(b));
  return r;
}
__device__ __forceinline__ void lds_barrier() {
  asm volatile("s_waitcnt lgkmcnt(0)" ::: "memory");
  __builtin_amdgcn_s_barrier();
  asm volatile("" ::: "memory");
}
__device__ __forceinline__ void cexp2(float re2, float rev, float& orr, float& oi) {
  float e = __builtin_amdgcn_exp2f(re2);
  rev = __builtin_amdgcn_fractf(rev);
  float s = __builtin_amdgcn_sinf(rev);
  float c = __builtin_amdgcn_cosf(rev);
  orr = e * c;
  oi = e * s;
}
__device__ __forceinline__ void cexp3(float re2, float rev, float& e, float& c, float& s) {
  e = __builtin_amdgcn_exp2f(re2);
  rev = __builtin_amdgcn_fractf(rev);
  s = __builtin_amdgcn_sinf(rev); c = __builtin_amdgcn_cosf(rev);
}
__device__ __forceinline__ float dot2acc(u32 a, u32 b, float c) {
  return __builtin_amdgcn_fdot2_f32_bf16(__builtin_bit_cast(bf16x2v, a), __builtin_bit_cast(bf16x2v, b), c, false);
}
__device__ __forceinline__ u32 opaque(u32 v) { asm volatile("" : "+v"(v)); return v; }
__device__ __forceinline__ int lane_id() { return (int)__builtin_amdgcn_mbcnt_hi(~0u, __builtin_amdgcn_mbcnt_lo(~0u, opaque(0u))); }
__device__ __forceinline__ u32 pack_stats(float mean, float var) {
  float vb = rbf(var);
  float ve = rbf(vb + 1e-5f);
  float sd = __builtin_amdgcn_sqrtf(ve);
  return pk2(mean, sd);
}

#define SB() __builtin_amdgcn_sched_barrier(0)

#define KADD(id_, t_, v_) kadd_<(v_)>(t_)
#define KSLP(id_, v_) do { if ((v_) > 0) __builtin_amdgcn_s_sleep(v_); } while (0)
template <int V>
__device__ __forceinline__ u32 kadd_(u32 t) { u32 r; asm("s_add_i32 %0, %1, %2" : "=s"(r) : "s"(__builtin_amdgcn_readfirstlane(t)), "n"(V) : "scc"); return r; }
template <int V> struct IC { static constexpr int value = V; };
template <int B, int E, class F>
__device__ __forceinline__ void static_for(F& f) { if constexpr (B < E) { f(IC<B>{}); static_for<B + 1, E>(f); } }
typedef __amdgpu_buffer_rsrc_t rsrc_t;
__device__ __forceinline__ u32x4 ldw(rsrc_t r, int off) { return __builtin_amdgcn_raw_buffer_load_b128(r, off, 0, 16); }
__device__ __forceinline__ u32x4 ldws(rsrc_t r, int off, int soff) { return __builtin_amdgcn_raw_buffer_load_b128(r, off, soff, 16); }
__device__ __forceinline__ void stw(rsrc_t r, int off, u32x4 v) { __builtin_amdgcn_raw_buffer_store_b128(v, r, off, 0, 16); }
__device__ __forceinline__ u32 umax4(u32 m, u32x4 v) { return max(max(max(m, v[0]), v[1]), max(v[2], v[3])); }
template <int CTRL>
__device__ __forceinline__ u32 dpp_u(u32 v) { return (u32)__builtin_amdgcn_update_dpp(0, (int)v, CTRL, 0xf, 0xf, false); }

#define CSTR 4160
#define L_U 33280
#define L_TA 42496
#define L_TB (L_TA + 32768)
#define L_QB (L_TB + 32768)
#define L_TH (L_QB + 16384)
#define L_SP (L_QB + 32768)
#define L_FS (L_SP + 4096)
#define L_MI (L_FS + 3072)
#define L_XS (L_MI + 80)
#define L_WX (L_XS + 1024)
#define MEGA_LDS (L_WX + 12288)
#define L_REDA L_QB
#define L_REDB (L_QB + 16384)
#define L_XC L_REDB
#define L_X (L_REDB + 8192)
#define TBS 3136
#define L_WR (L_QB + 24576)
#define L_WS L_TA
#define L_KT L_TB
#define KTO 65024
#define FTS 528
#define L_P0 (L_FS + 2048)
#define STAT_SCALE 33554432.0f
#define C_OB 20
#define C_OT 24
#define M_FX 72
#define C_X2 28
#define C_G2 32
#define C_A 36
#define C_B 40
#define C_F 44
#define C_KH 48
#define C_P2 52
#define C_XC 8
#define C_P3 64
#define M_RD 68
#define M_DL 76
#define C_OV 28
#define C_U 56
#define C_W 60

typedef __attribute__((ext_vector_type(2))) _Float16 f16x2v;
typedef __attribute__((ext_vector_type(2))) short i16x2;
__device__ __forceinline__ u32 pkh(float a, float b) {
  u32 r;
  asm("v_cvt_pk_f16_f32 %0, %1, %2" : "=v"(r) : "v"(a), "v"(b));
  return r;
}
__device__ __forceinline__ u32 pkhn(float a, float b) {
  u32 r;
  asm("v_cvt_pk_f16_f32 %0, %1, -%2" : "=v"(r) : "v"(a), "v"(b));
  return r;
}
__device__ __forceinline__ u32 tb_addr(u32 bits2, u32 base2) {
  i16x2 b = __builtin_elementwise_max(__builtin_bit_cast(i16x2, bits2), (i16x2){0x3F00, 0x3F00});
  u32 r;
  asm("v_pk_mad_u16 %0, %1, 2, %2 op_sel_hi:[1,0,1]" : "=v"(r) : "v"(b), "s"(base2));
  return r;
}
__device__ __forceinline__ int bexp(float b) { return min(max((int)((__float_as_uint(b) >> 23) & 0xFF), 20), 240); }
__device__ __forceinline__ void cnt_add(char* smem, int off, int lane) {
  asm volatile("s_mov_b64 exec, 1\n\tds_add_u32 %0, %1\n\ts_mov_b64 exec, -1" :: "v"(L_MI + off), "v"(1u) : "memory");
}
__device__ __forceinline__ void cnt_wait(char* smem, int off, u32 target) {
#pragma unroll 1
  for (unsigned n = 0; n < (1u << 23) && (u32)__builtin_amdgcn_readfirstlane(__hip_atomic_load((LAS u32*)(smem + L_MI + off), __ATOMIC_RELAXED, __HIP_MEMORY_SCOPE_WORKGROUP)) < target; ++n)
    __builtin_amdgcn_s_sleep(2);
  asm volatile("" ::: "memory");
}
__device__ __forceinline__ void cnt_spin(char* smem, int off, u32 target) {
  for (unsigned n = 0; n < (1u << 24) && (u32)__builtin_amdgcn_readfirstlane(__hip_atomic_load((LAS u32*)(smem + L_MI + off), __ATOMIC_RELAXED, __HIP_MEMORY_SCOPE_WORKGROUP)) < target; ++n) {}
  asm volatile("" ::: "memory");
}
__device__ __forceinline__ void cnt_spin2(int o0, u32 t0, int o1, u32 t1) {
  for (unsigned n = 0; n < (1u << 24); ++n) {
    asm volatile("" ::: "memory");
    const u32 c0 = *(const volatile LAS u32*)(size_t)(u32)(L_MI + o0), c1 = *(const volatile LAS u32*)(size_t)(u32)(L_MI + o1);
    if (__builtin_amdgcn_readfirstlane(min((int)(c0 - t0), (int)(c1 - t1))) >= 0) break;
  }
  asm volatile("" ::: "memory");
}
__device__ __forceinline__ u32 now_() { return (u32)__builtin_amdgcn_s_memtime(); }
__device__ __forceinline__ u32 tmax(u32 a, u32 b) { return (int)(a - b) > 0 ? a : b; }
__device__ __forceinline__ void gate(u32 t) {
  u32 n = 256;
  asm volatile("1:\n\ts_memtime vcc\n\ts_sub_u32 %0, %0, 1\n\ts_waitcnt lgkmcnt(0)\n\ts_cbranch_scc1 2f\n\ts_sub_i32 vcc_lo, %1, vcc_lo\n\ts_cmp_lt_i32 vcc_lo, 1\n\ts_cbranch_scc1 2f\n\t"
               "s_cmp_lt_i32 vcc_lo, 64\n\ts_cbranch_scc1 1b\n\ts_sleep 1\n\ts_cmp_lt_i32 vcc_lo, 0x200\n\ts_cbranch_scc1 1b\n\ts_sleep 6\n\ts_branch 1b\n2:"
               : "+s"(n) : "s"(__builtin_amdgcn_readfirstlane(t)) : "memory", "vcc", "scc");
}
__device__ __forceinline__ u32 x0_mark(u32 tag) { return 0xFF807F80u + (((tag & 31u) + 1u) << 16) + ((tag >> 5) & 31u) + 1u; }
__device__ __forceinline__ bool tags_ok(const u32x4& f, u32 tag) {
  return __builtin_amdgcn_ballot_w64((opaque((f[0] ^ tag) | (f[1] ^ tag)) | opaque((f[2] ^ tag) | (f[3] ^ tag))) != 0u) == 0ull;
}
__device__ __forceinline__ void tags_wait(u32x4& f, rsrc_t wrs, int fo, u32 tag, int fid) {
  if (__builtin_expect(!tags_ok(f, tag), 0)) {
    for (unsigned spins = 0; spins <= SPIN_CAP; ++spins) {
      FAILC(fid);
      __builtin_amdgcn_s_sleep(SLP_F);
      asm volatile("" ::: "memory");
      if (tags_ok(ldw(wrs, fo), tag)) break;
    }
    __builtin_amdgcn_s_waitcnt(0x0F70);
  }
  asm volatile("" ::: "memory");
}
__device__ __forceinline__ void build_copies(char* smem, int cc, int ch, const u32 (&kw)[6]) {
  char* cop = smem + cc * 4 * CSTR + ch * 16;
  u32 sh[5];
#pragma unroll
  for (int k = 0; k < 5; ++k) sh[k] = __builtin_amdgcn_alignbit(kw[k + 1], kw[k], 16);
  u32x4 c0 = {kw[0], kw[1], kw[2], kw[3]}, c1 = {sh[0], sh[1], sh[2], sh[3]}, c2 = {kw[1], kw[2], kw[3], kw[4]}, c3 = {sh[1], sh[2], sh[3], sh[4]};
  asm volatile("" : "+v"(c0), "+v"(c1), "+v"(c2), "+v"(c3));
  *(u32x4*)(cop + 0 * CSTR) = c0; *(u32x4*)(cop + 1 * CSTR) = c1; *(u32x4*)(cop + 2 * CSTR) = c2; *(u32x4*)(cop + 3 * CSTR) = c3;
  asm volatile("s_nop 1" :: "v"(c0), "v"(c1), "v"(c2), "v"(c3) : "memory");
}
struct ConvPre { f32x4 x0, x1; };
__device__ __forceinline__ void conv_unp(ConvPre& P, u32x4 xw) {
#pragma unroll
  for (int e = 0; e < 4; ++e) { P.x0[e] = lo2f(xw[e]); P.x1[e] = hi2f(xw[e]); }
  asm volatile("" : "+v"(P.x0), "+v"(P.x1));
}
__device__ __forceinline__ u32x4 x2_ld(rsrc_t wrs, int kp, int l0) { return ldw(wrs, OFF_X2 + (kp * XS_ + l0) * 4); }
__device__ __forceinline__ bool x2_ok(const u32x4& xw) { return __all(umax4(0u, xw) != 0xFFFFFFFFu); }
__device__ __forceinline__ void x2_poll(rsrc_t wrs, int kp, int l0, u32x4& xw) {
  for (unsigned spins = 0; !x2_ok(xw) && spins <= SPIN_CAP; ++spins) {
    FAILC(17);
    __builtin_amdgcn_s_sleep(SLP_XP);
    asm volatile("" ::: "memory");
    xw = x2_ld(wrs, kp, l0);
  }
}
__device__ __forceinline__ void st2_ld(rsrc_t wrs, int l0, u32x4 (&g)[4]) {
#pragma unroll
  for (int e = 0; e < 4; ++e) g[e] = ldw(wrs, OFF_ST2 + ((l0 & ~255) + ((l0 >> 2) & 63)) * 16 + e * 1024);
}
__device__ __forceinline__ bool st2_ok(const u32x4 (&g)[4]) {
  u32 ok = 32u;
#pragma unroll
  for (int e = 0; e < 4; ++e) ok &= g[e][0] & g[e][2];
  return __all(ok != 0u);
}
__device__ __forceinline__ bool x2st_ok(const u32x4& xw, const u32x4 (&g)[4]) {
  u32 ok = umax4(0u, xw) != 0xFFFFFFFFu ? 32u : 0u;
#pragma unroll
  for (int e = 0; e < 4; ++e) ok &= g[e][0] & g[e][2];
  return __all(ok != 0u);
}
__device__ __forceinline__ void st2_poll(rsrc_t wrs, int l0, u32x4 (&g)[4]) {
  for (unsigned spins = 0; !st2_ok(g) && spins <= SPIN_CAP; ++spins) {
    FAILC(8);
    __builtin_amdgcn_s_sleep(SLP_SP);
    asm volatile("" ::: "memory");
    st2_ld(wrs, l0, g);
  }
}
template <bool UNP>
__device__ __forceinline__ void conv2_in(char* smem, u32 ua, ConvPre& P, u32x4 xw, u32x4 (&g)[4]) {
  f32x4 rs, nm;
#pragma unroll
  for (int e = 0; e < 4; ++e) {
    const float c1 = 4294967296.0f / (64.0f * 512.0f * STAT_SCALE), c2 = 1.0f / 4294967296.0f;
    const float t1 = fmaf((float)g[e][0], c2, (float)(int)g[e][1]), t2 = fmaf((float)g[e][2], c2, (float)(int)g[e][3]);
    const float m1 = -c1 * t1;
    rs[e] = __builtin_amdgcn_rsqf(fmaf(rmax(fmaf(m1, t1, t2), 0.f), c1, 1e-5f));
    nm[e] = m1 * rs[e];
  }
  if (UNP) conv_unp(P, xw);
  u32x2 u0 = {pkh(fmaf(P.x0[0], rs[0], nm[0]), fmaf(P.x0[1], rs[1], nm[1])), pkh(fmaf(P.x0[2], rs[2], nm[2]), fmaf(P.x0[3], rs[3], nm[3]))};
  u32x2 u1 = {pkh(fmaf(P.x1[0], rs[0], nm[0]), fmaf(P.x1[1], rs[1], nm[1])), pkh(fmaf(P.x1[2], rs[2], nm[2]), fmaf(P.x1[3], rs[3], nm[3]))};
  asm volatile("" : "+v"(u0), "+v"(u1));
  *(LAS u32x2*)(size_t)ua = u0;
  *(LAS u32x2*)(size_t)(ua + 4608) = u1;
  asm volatile("s_nop 1" :: "v"(u0), "v"(u1) : "memory");
}
__device__ __forceinline__ void pair_x(f32x4& lo, f32x4& hi, const f32x4& a, const f32x4& b) {
#define PXD " quad_perm:[1,0,3,2] row_mask:0xf bank_mask:0xf\n\t"
  asm volatile("s_mov_b64 vcc, %16\n\ts_nop 0\n\t"
               "v_cndmask_b32_dpp %0, %12, %8, vcc" PXD "v_cndmask_b32_dpp %1, %13, %9, vcc" PXD "v_cndmask_b32_dpp %2, %14, %10, vcc" PXD "v_cndmask_b32_dpp %3, %15, %11, vcc" PXD
               "s_not_b64 vcc, vcc\n\t"
               "v_cndmask_b32_dpp %4, %8, %12, vcc" PXD "v_cndmask_b32_dpp %5, %9, %13, vcc" PXD "v_cndmask_b32_dpp %6, %10, %14, vcc" PXD "v_cndmask_b32_dpp %7, %11, %15, vcc" PXD
               : "=&v"(lo[0]), "=&v"(lo[1]), "=&v"(lo[2]), "=&v"(lo[3]), "=&v"(hi[0]), "=&v"(hi[1]), "=&v"(hi[2]), "=&v"(hi[3])
               : "v"(a[0]), "v"(a[1]), "v"(a[2]), "v"(a[3]), "v"(b[0]), "v"(b[1]), "v"(b[2]), "v"(b[3]), "s"(0x5555555555555555ull) : "vcc", "scc");
}
__device__ __forceinline__ void conv_res(ConvPre& P) {
  f32x4 lo, hi;
  pair_x(lo, hi, P.x0, P.x1);
  P.x0 = lo; P.x1 = hi;
}
__device__ __forceinline__ u32x4 conv_epi(u32 rp0, u32 rp1, int off, float isc, const ConvPre& P) {
  const f32x4 a0 = *(const LAS f32x4*)(size_t)(rp0 + off), a1 = *(const LAS f32x4*)(size_t)(rp0 + off + 4096), b0 = *(const LAS f32x4*)(size_t)(rp1 + off), b1 = *(const LAS f32x4*)(size_t)(rp1 + off + 4096);
  __builtin_amdgcn_sched_barrier(0);
  u32x4 r;
#pragma unroll
  for (int e = 0; e < 2; ++e) {
    r[e] = pk2(fmaf(a1[2 * e], isc, fmaf(a0[2 * e], isc, P.x0[2 * e])), fmaf(a1[2 * e + 1], isc, fmaf(a0[2 * e + 1], isc, P.x0[2 * e + 1])));
    r[2 + e] = pk2(fmaf(b1[2 * e], isc, fmaf(b0[2 * e], isc, P.x1[2 * e])), fmaf(b1[2 * e + 1], isc, fmaf(b0[2 * e + 1], isc, P.x1[2 * e + 1])));
  }
  return r;
}
__device__ __forceinline__ void dma4k(rsrc_t r, int voff, int soff, int lds) {
  asm volatile("s_mov_b32 m0, %3\n\ts_nop 0\n\t"
               "buffer_load_dwordx4 %0, %1, %2 offen sc1 lds\n\t"
               "buffer_load_dwordx4 %0, %1, %2 offen offset:1024 sc1 lds\n\t"
               "buffer_load_dwordx4 %0, %1, %2 offen offset:2048 sc1 lds\n\t"
               "buffer_load_dwordx4 %0, %1, %2 offen offset:3072 sc1 lds"
               :: "v"(voff), "s"(r), "s"(soff), "s"(lds) : "memory");
}
__device__ __forceinline__ void dma1k(rsrc_t r, int voff, int soff, int lds) {
  asm volatile("s_mov_b32 m0, %3\n\ts_nop 0\n\tbuffer_load_dwordx4 %0, %1, %2 offen sc1 lds" :: "v"(voff), "s"(r), "s"(soff), "s"(lds) : "memory");
}
__device__ __forceinline__ int x_lane(int lane) { return ((lane & 31) >> 1) * 64 + (lane >> 5) * 32 + (lane & 1) * 16; }
__device__ __forceinline__ void x_dma(rsrc_t wrs, int xin, int rb, int tile, int a, int lane) {
  const int vo = x_lane(lane);
  dma4k(wrs, vo, xin + rb * XBS + a * 8192, tile + a * 8192);
  dma4k(wrs, vo, xin + rb * XBS + a * 8192 + 4096, tile + a * 8192 + 4096);
  if (tile == L_TB) {
    int so = xin + (rb - 1) * XBS + a * 8192;
    asm volatile("s_mov_b32 m0, %3\n\ts_nop 0\n\tbuffer_load_dwordx4 %1, %2, %0 offen sc1 lds\n\t"
                 "s_sub_u32 m0, m0, 1024\n\ts_nop 0\n\tbuffer_load_dwordx4 %1, %2, %0 offen offset:2048 sc1 lds\n\t"
                 "s_add_u32 m0, m0, 3072\n\ts_add_u32 %0, %0, 4096\n\tbuffer_load_dwordx4 %1, %2, %0 offen sc1 lds\n\t"
                 "s_sub_u32 m0, m0, 1024\n\ts_nop 0\n\tbuffer_load_dwordx4 %1, %2, %0 offen offset:2048 sc1 lds"
                 : "+s"(so) : "v"((lane >> 1) * 64 + 32 + (lane & 1) * 16), "s"(wrs), "s"(L_TH + a * 4096) : "memory", "scc");
  }
}
template <int S>
__device__ __forceinline__ bf16x8 ld_tr8(const LAS char* p) {
  const bf16x4 lo = __builtin_amdgcn_ds_read_tr16_b64_v4i16((LAS bf16x4*)p), hi = __builtin_amdgcn_ds_read_tr16_b64_v4i16((LAS bf16x4*)(p + S));
  return __builtin_shufflevector(lo, hi, 0, 1, 2, 3, 4, 5, 6, 7);
}

struct ConvK { u32 ah, ah2, bp, ra; f16x8 k[16]; };
__device__ __forceinline__ void a_conv_pre(ConvK& C, int a, int lane) {
  lane = (int)opaque((u32)lane);
  const int c = a >> 1, qq = a & 1, j16 = lane & 15, kg = lane >> 4;
  const int y00 = 2031 - j16 + 8 * kg - 128 * qq;
  const int sg = y00 & 3;
  C.ah = opaque((u32)(c * 4 * CSTR + sg * CSTR + 2 * (y00 - sg) - 3776));
  C.ah2 = opaque(C.ah + 8);
  C.bp = opaque((u32)(L_U + c * 4608 + (256 + 16 * j16 - 16 + 8 * kg - 128 * qq) * 2 - 192));
  C.ra = opaque((u32)(L_REDA + (a * 256 + 4 * j16 + (kg ^ ((j16 >> 1) & 3) ^ c)) * 16));
#pragma unroll
  for (int e = 0; e < 16; ++e) {
    const int ao = 3776 - (512 * (e & 3) + 64 * (e >> 2));
    const f16x4 h0 = *(const LAS f16x4*)(size_t)(C.ah + ao), h1 = *(const LAS f16x4*)(size_t)(C.ah2 + ao);
    C.k[e] = __builtin_shufflevector(h0, h1, 0, 1, 2, 3, 4, 5, 6, 7);
    __builtin_amdgcn_sched_barrier(0);
  }
#pragma unroll
  for (int e = 0; e < 16; ++e) asm volatile("" : "+v"(C.k[e]));
}
template <bool L1>
__device__ __forceinline__ void a_conv(char* smem, rsrc_t wrs, int rb, int a, const ConvK& C) {
  const LAS char* ah = (const LAS char*)(size_t)C.ah;
  const LAS char* ah2 = (const LAS char*)(size_t)C.ah2;
  const LAS char* bp = (const LAS char*)(size_t)C.bp;
  LAS f32x4* ra = (LAS f32x4*)(size_t)C.ra;
  f32x4 acc[8];
#pragma unroll
  for (int e = 0; e < 8; ++e) acc[e] = (f32x4){0.f, 0.f, 0.f, 0.f};
  f16x8 bf[4][8], fh[4][8];
#define S_RR(s_) ((s_) < 16 ? (s_) >> 2 : (s_) < 44 ? ((s_) - 16) / 7 : ((s_) - 44) >> 2)
#define S_MU(s_) ((s_) < 16 ? (s_) & 3 : (s_) < 44 ? ((s_) - 16) % 7 + 1 : ((s_) - 44) & 3)
#define S_NEW(s_) ((s_) < 16 || ((s_) < 44 && S_MU(s_) >= 4))
#define LDA(s_) do { const int ao_ = 3776 - (512 * S_MU(s_) + 64 * S_RR(s_));                                        \
      f16x4 h0_ = *(const LAS f16x4*)(ah + ao_), h1_ = *(const LAS f16x4*)(ah2 + ao_);                                \
      fh[S_RR(s_)][S_MU(s_)] = __builtin_shufflevector(h0_, h1_, 0, 1, 2, 3, 4, 5, 6, 7); } while (0)
#define LDB(r_, t_) bf[r_][t_] = *(const LAS f16x8*)(bp + 512 * (t_) + 192 - 64 * (r_))
#define CSTEPS(s0_, s1_)                                                                                               \
  _Pragma("unroll") for (int st = s0_; st < s1_; ++st) {                                                               \
    const int rr = S_RR(st), mu = S_MU(st);                                                                           \
    if (S_NEW(st + CPF) && (L1 || st + CPF >= 16)) LDA(st + CPF);                                                     \
    if (st < 4) { LDB(2 + (st >> 1), 2 * (st & 1)); LDB(2 + (st >> 1), 2 * (st & 1) + 1); }                           \
    if (st >= 44 && st < 48) { LDB(2 + ((st - 44) >> 1), 4 + 2 * (st & 1)); LDB(2 + ((st - 44) >> 1), 5 + 2 * (st & 1)); } \
    __builtin_amdgcn_sched_barrier(0);                                                                                \
    _Pragma("unroll") for (int cb = 0; cb < 8; ++cb) {                                                                \
      const int t = cb - mu;                                                                                          \
      const bool on = st < 16 ? (cb < 4 && t >= 0) : st < 44 ? (cb >= 4 && t >= 0 && t < 4) : (t >= 4);               \
      if (on) acc[cb] = __builtin_amdgcn_mfma_f32_16x16x32_f16(fh[rr][mu], bf[rr][t < 0 ? 0 : t], acc[cb], 0, 0, 0);  \
    }                                                                                                                 \
    __builtin_amdgcn_sched_barrier(0);                                                                                \
    if (st >= 13 && st < 16) ra[(st - 13) * 64] = acc[st - 13];                                                       \
    if (st >= 57) ra[1024 + (st - 57) * 64] = acc[4 + st - 57];                                                       \
  }
#pragma unroll
  for (int e = 0; e < 16; ++e) fh[e >> 2][e & 3] = C.k[e];
  __builtin_amdgcn_s_barrier();
  asm volatile("" ::: "memory");
  ST(!L1, 7);
#pragma unroll
  for (int e = 0; e < CPF; ++e) { if (L1) LDA(e); __builtin_amdgcn_sched_barrier(0); if (e < 2) { LDB(e, 0); __builtin_amdgcn_sched_barrier(0); LDB(e, 1); __builtin_amdgcn_sched_barrier(0); LDB(e, 2); __builtin_amdgcn_sched_barrier(0); LDB(e, 3); __builtin_amdgcn_sched_barrier(0); } }
  __builtin_amdgcn_sched_barrier(0);
  CSTEPS(0, 16)
  CSTEPS(16, 17)
  ra[3 * 64] = acc[3];
  __builtin_amdgcn_sched_barrier(0);
  asm volatile("" ::: "memory");
  CSTEPS(17, 18)
  if (L1) asm volatile("s_waitcnt vmcnt(0)" ::: "memory");
  asm volatile("s_waitcnt lgkmcnt(2)" ::: "memory");
  __builtin_amdgcn_s_barrier();
  asm volatile("" ::: "memory");
  ST(!L1, 8);
  CSTEPS(18, 44)
  if (!L1) ST(1, 5);
  if (!L1) __builtin_amdgcn_s_barrier();
  if (!L1) ST(1, 6);
  asm volatile("" ::: "memory");
#pragma unroll
  for (int e = 0; e < 8; ++e) { LDB(e >> 2, 4 + (e & 3)); __builtin_amdgcn_sched_barrier(0); }
  CSTEPS(44, 60)
  ra[1024 + 3 * 64] = acc[7];
  lds_barrier();
  ST(!L1, 9);
}

struct C1Pre { u32 ah, ah2, bp, xp, xc, cn; int so, fo0, fo1; };
#define C1_OPS "+{v16}"(C.ah), "+{v17}"(C.ah2), "+{v18}"(C.bp), "+{v19}"(C.xp), "+{v20}"(C.xc), "+{v21}"(C.cn), "+{v22}"(C.so), "+{v23}"(C.fo0), "+{v24}"(C.fo1)
__device__ __forceinline__ void c1_pre(C1Pre& C, int kp, int a, int lane) {
  lane = (int)opaque((u32)lane);
  const int c = a >> 1, j16 = lane & 15, kg = lane >> 4, P = a & 1;
  const int y0 = 2031 - j16 + 8 * kg - 128 * P, sg = y0 & 3;
  C.ah = (u32)(c * 4 * CSTR + sg * CSTR + 2 * (y0 - sg) - 3776);
  C.ah2 = C.ah + 8;
  C.bp = (u32)(L_U + c * 4608 + (240 + 16 * j16 + 8 * kg - 128 * P) * 2 - 192);
  C.xp = (u32)(L_X + j16 * 64 + kg * 16);
  C.xc = (u32)(L_TA + 24576 + c * 4096 + lane * 16);
  C.cn = (u32)(L_MI + 4 * c);
  C.so = OFF_X1 + (8 * ((kg & 1) ? 3 - P : P) + (j16 >> 1)) * XBS + kp * 128 + c * 64 + (j16 & 1) * 32 + (kg >> 1) * 16;
  C.fo0 = OFF_ST0 + (a * 64 + lane) * 16;
  C.fo1 = OFF_ST0 + 8192 + (HB_OF(BIDX) * 4 + a) * 128 + (lane & 3) * 16;
  asm volatile("" : C1_OPS);
}
static_assert(L_XC - L_TA - 24576 == 57344 && C_XC == 8 && C_KH == 48 && C_P2 == 52 && C_U == 56 && C_P3 == 64 && C_G2 == 32 && L_MI % 8 == 0, "generated offsets");
__device__ __forceinline__ void a_conv1(rsrc_t wrs, int a, C1Pre& C, u32x4& fl, u32x4& wfl, u32& g2) {
  asm volatile("s_bitcmp1_b32 %[a], 0\n\ts_cbranch_scc1 .Lc_odd\n\t"
  "ds_read_b64 v[36:37], v16 offset:3776\n\tds_read_b64 v[38:39], v17 offset:3776\n\tds_read_b64 v[40:41], v16 offset:3264\n\tds_read_b64 v[42:43], v17 offset:3264\n\t"
  "ds_read_b64 v[44:45], v16 offset:2752\n\tds_read_b64 v[46:47], v17 offset:2752\n\tds_read_b64 v[48:49], v16 offset:2240\n\tds_read_b64 v[50:51], v17 offset:2240\n\t"
  "ds_read_b64 v[52:53], v16 offset:3712\n\tds_read_b64 v[54:55], v17 offset:3712\n\tds_read_b64 v[56:57], v16 offset:3200\n\tds_read_b64 v[58:59], v17 offset:3200\n\t"
  "ds_read_b64 v[60:61], v16 offset:2688\n\tds_read_b64 v[62:63], v17 offset:2688\n\tds_read_b64 v[64:65], v16 offset:2176\n\tds_read_b64 v[66:67], v17 offset:2176\n\t"
  "ds_read_b64 v[68:69], v16 offset:3648\n\tds_read_b64 v[70:71], v17 offset:3648\n\tds_read_b64 v[72:73], v16 offset:3136\n\tds_read_b64 v[74:75], v17 offset:3136\n\t"
  "ds_read_b64 v[76:77], v16 offset:2624\n\tds_read_b64 v[78:79], v17 offset:2624\n\tds_read_b64 v[80:81], v16 offset:2112\n\tds_read_b64 v[82:83], v17 offset:2112\n\t"
  "ds_read_b64 v[84:85], v16 offset:3584\n\tds_read_b64 v[86:87], v17 offset:3584\n\tds_read_b64 v[88:89], v16 offset:3072\n\tds_read_b64 v[90:91], v17 offset:3072\n\t"
  "ds_read_b64 v[92:93], v16 offset:2560\n\tds_read_b64 v[94:95], v17 offset:2560\n\tds_read_b64 v[96:97], v16 offset:2048\n\tds_read_b64 v[98:99], v17 offset:2048\n\t"
  "ds_read_b32 v26, v21\n\tv_and_b32 v27, -8, v21\n\tv_mov_b32 v100, 1\n\t"
  CST(6)
  "s_mov_b32 vcc_hi, 0x1000000\n\t.Lc0_u:\n\tds_read_b32 v101, v27 offset:56\n\ts_sub_u32 vcc_hi, vcc_hi, 1\n\ts_cselect_b32 vcc_lo, 0, 4\n\ts_waitcnt lgkmcnt(0)\n\tv_readfirstlane_b32 s61, v101\n\ts_nop 1\n\ts_cmp_lt_u32 s61, vcc_lo\n\ts_cbranch_scc1 .Lc0_u\n\t"
  CST(7)
  "ds_read_b128 v[102:105], v18 offset:192\n\tds_read_b128 v[106:109], v18 offset:704\n\tds_read_b128 v[110:113], v18 offset:1216\n\tds_read_b128 v[116:119], v18 offset:128\n\t"
  "ds_read_b128 v[120:123], v18 offset:640\n\tds_read_b128 v[124:127], v18 offset:1152\n\tds_read_b128 v[128:131], v18 offset:64\n\ts_waitcnt lgkmcnt(6)\n\t"
  "v_mfma_f32_16x16x32_f16 v[132:135], v[40:43], v[102:105], 0\n\tds_read_b128 v[136:139], v18 offset:576\n\tds_read_b128 v[140:143], v18 offset:1728\n\t"
  "v_mfma_f32_16x16x32_f16 v[144:147], v[44:47], v[102:105], 0\n\tds_read_b128 v[148:151], v18 offset:1088\n\ts_waitcnt lgkmcnt(8)\n\t"
  "v_mfma_f32_16x16x32_f16 v[132:135], v[36:39], v[106:109], v[132:135]\n\tds_read_b128 v[152:155], v18 offset:1664\n\tds_read_b128 v[156:159], v18 offset:1600\n\t"
  "v_mfma_f32_16x16x32_f16 v[144:147], v[40:43], v[106:109], v[144:147]\n\tds_read_b128 v[160:163], v18 offset:0\n\tds_read_b32 v101, v27 offset:48\n\ts_waitcnt lgkmcnt(7)\n\t"
  "v_mfma_f32_16x16x32_f16 v[144:147], v[36:39], v[110:113], v[144:147]\n\tds_read_b128 v[164:167], v18 offset:512\n\t"
  "v_mfma_f32_16x16x32_f16 v[132:135], v[56:59], v[116:119], v[132:135]\n\tds_read_b128 v[168:171], v18 offset:1024\n\t"
  "v_mfma_f32_16x16x32_f16 v[144:147], v[60:63], v[116:119], v[144:147]\n\tds_read_b128 v[172:175], v18 offset:1536\n\t"
  "v_mfma_f32_16x16x32_f16 v[132:135], v[52:55], v[120:123], v[132:135]\n\t"
  "v_mfma_f32_16x16x32_f16 v[144:147], v[56:59], v[120:123], v[144:147]\n\t"
  "v_mfma_f32_16x16x32_f16 v[144:147], v[52:55], v[124:127], v[144:147]\n\t"
  "v_mfma_f32_16x16x32_f16 v[132:135], v[72:75], v[128:131], v[132:135]\n\tds_read_b128 v[176:179], v19 offset:0\n\tds_read_b128 v[180:183], v19 offset:3072\n\t"
  "v_mfma_f32_16x16x32_f16 v[144:147], v[76:79], v[128:131], v[144:147]\n\ts_waitcnt lgkmcnt(5)\n\tv_readfirstlane_b32 s61, v101\n\t"
  "v_mfma_f32_16x16x32_f16 v[132:135], v[68:71], v[136:139], v[132:135]\n\t"
  "v_mfma_f32_16x16x32_f16 v[144:147], v[72:75], v[136:139], v[144:147]\n\ts_cmp_lt_u32 s61, 4\n\ts_cbranch_scc1 .Lc0_kh\n.Lc0_kh_b:\n\t"
  "v_mfma_f32_16x16x32_f16 v[144:147], v[68:71], v[148:151], v[144:147]\n\tds_read_b64 v[184:185], v16 offset:1728\n\tds_read_b64 v[186:187], v17 offset:1728\n\t"
  "v_mfma_f32_16x16x32_f16 v[132:135], v[88:91], v[160:163], v[132:135]\n\tds_read_b64 v[188:189], v16 offset:1216\n\tds_read_b64 v[190:191], v17 offset:1216\n\t"
  "v_mfma_f32_16x16x32_f16 v[144:147], v[92:95], v[160:163], v[144:147]\n\tds_read_b64 v[192:193], v16 offset:704\n\ts_waitcnt lgkmcnt(7)\n\t"
  "v_mfma_f32_16x16x32_f16 v[132:135], v[84:87], v[164:167], v[132:135]\n\tds_read_b64 v[194:195], v17 offset:704\n\tds_read_b64 v[196:197], v16 offset:192\n\tds_read_b64 v[198:199], v17 offset:192\n\t"
  "v_mfma_f32_16x16x32_f16 v[144:147], v[88:91], v[164:167], v[144:147]\n\ts_waitcnt lgkmcnt(8)\n\tv_perm_b32 v176, v176, v176, s60\n\t"
  "v_mfma_f32_16x16x32_f16 v[144:147], v[84:87], v[168:171], v[144:147]\n\tds_write_b128 v20, v[132:135] offset:0\n\tv_perm_b32 v177, v177, v177, s60\n\t"
  "v_mfma_f32_16x16x32_f16 v[200:203], v[36:39], v[102:105], 0\n\tv_perm_b32 v178, v178, v178, s60\n\tv_perm_b32 v179, v179, v179, s60\n\t"
  "v_mfma_f32_16x16x32_f16 v[132:135], v[48:51], v[102:105], 0\n\tds_write_b128 v20, v[144:147] offset:1024\n\tv_perm_b32 v180, v180, v180, s60\n\t"
  "v_mfma_f32_16x16x32_f16 v[132:135], v[44:47], v[106:109], v[132:135]\n\ts_mov_b64 exec, 1\n\tds_add_u32 v21, v100 offset:8\n\ts_mov_b64 exec, -1\n\t"
  "v_mfma_f32_16x16x32_f16 v[132:135], v[40:43], v[110:113], v[132:135]\n\tv_perm_b32 v181, v181, v181, s60\n\tv_perm_b32 v182, v182, v182, s60\n\t"
  "v_mfma_f32_16x16x32_f16 v[132:135], v[36:39], v[140:143], v[132:135]\n\tv_perm_b32 v183, v183, v183, s60\n\t"
  "v_mfma_f32_16x16x32_f16 v[200:203], v[52:55], v[116:119], v[200:203]\n\t"
  "v_mfma_f32_16x16x32_f16 v[132:135], v[64:67], v[116:119], v[132:135]\n\t"
  "v_mfma_f32_16x16x32_f16 v[132:135], v[60:63], v[120:123], v[132:135]\n\t"
  "v_mfma_f32_16x16x32_f16 v[132:135], v[56:59], v[124:127], v[132:135]\n\t"
  "v_mfma_f32_16x16x32_f16 v[132:135], v[52:55], v[152:155], v[132:135]\n\t"
  "v_mfma_f32_16x16x32_f16 v[200:203], v[68:71], v[128:131], v[200:203]\n\t"
  "v_mfma_f32_16x16x32_f16 v[132:135], v[80:83], v[128:131], v[132:135]\n\tds_read_b32 v101, v21 offset:8\n\tds_read_b128 v[144:147], v20 offset:2048\n\tds_read_b128 v[204:207], v20 offset:3072\n\t"
  "v_mfma_f32_16x16x32_f16 v[132:135], v[76:79], v[136:139], v[132:135]\n\t"
  "v_mfma_f32_16x16x32_f16 v[132:135], v[72:75], v[148:151], v[132:135]\n\t"
  "v_mfma_f32_16x16x32_f16 v[132:135], v[68:71], v[156:159], v[132:135]\n\t"
  "v_mfma_f32_16x16x32_f16 v[200:203], v[84:87], v[160:163], v[200:203]\n\t"
  "v_mfma_f32_16x16x32_f16 v[132:135], v[96:99], v[160:163], v[132:135]\n\t"
  "v_mfma_f32_16x16x32_f16 v[132:135], v[92:95], v[164:167], v[132:135]\n\t"
  "v_mfma_f32_16x16x32_f16 v[132:135], v[88:91], v[168:171], v[132:135]\n\t"
  "v_mfma_f32_16x16x32_f16 v[132:135], v[84:87], v[172:175], v[132:135]\n\ts_waitcnt lgkmcnt(0)\n\tv_readfirstlane_b32 s61, v101\n\t"
  "v_mfma_f32_16x16x32_f16 v[208:211], v[40:43], v[140:143], 0\n\t"
  "v_mfma_f32_16x16x32_f16 v[208:211], v[44:47], v[110:113], v[208:211]\n\ts_cmp_lt_u32 s61, 2\n\ts_cbranch_scc1 .Lc0_x0\n.Lc0_x0_b:\n\tv_fmac_f32 v176, v144, v26\n\tv_fmac_f32 v177, v145, v26\n\t"
  "v_fmac_f32 v178, v146, v26\n\t"
  "v_mfma_f32_16x16x32_f16 v[212:215], v[44:47], v[140:143], 0\n\tv_fmac_f32 v179, v147, v26\n\tv_fmac_f32 v180, v204, v26\n\tv_fmac_f32 v181, v205, v26\n\t"
  "v_fmac_f32 v182, v206, v26\n\tv_fmac_f32 v183, v207, v26\n\t"
  "v_mfma_f32_16x16x32_f16 v[208:211], v[48:51], v[106:109], v[208:211]\n\tv_fmac_f32 v176, v200, v26\n\tv_fmac_f32 v177, v201, v26\n\t"
  "v_mfma_f32_16x16x32_f16 v[212:215], v[48:51], v[110:113], v[212:215]\n\tv_fmac_f32 v178, v202, v26\n\tv_fmac_f32 v179, v203, v26\n\tv_cvt_pk_bf16_f32 v144, v176, v177\n\t"
  "v_cvt_pk_bf16_f32 v145, v178, v179\n\tv_fmac_f32 v180, v132, v26\n\t"
  "v_mfma_f32_16x16x32_f16 v[176:179], v[48:51], v[140:143], 0\n\tv_fmac_f32 v181, v133, v26\n\tv_fmac_f32 v182, v134, v26\n\tv_fmac_f32 v183, v135, v26\n\t"
  "v_cvt_pk_bf16_f32 v146, v180, v181\n\tv_cvt_pk_bf16_f32 v147, v182, v183\n\t"
  "v_mfma_f32_16x16x32_f16 v[208:211], v[184:187], v[102:105], v[208:211]\n\tv_permlane16_swap_b32 v144, v146\n\tv_permlane16_swap_b32 v145, v147\n\ts_setprio 3\n\tbuffer_store_dwordx4 v[144:147], v22, s[56:59], 0 offen sc1\n\ts_setprio 0\n\t"
  CST(8)
  "v_mfma_f32_16x16x32_f16 v[212:215], v[184:187], v[106:109], v[212:215]\n\tds_read_b64 v[132:133], v16 offset:1664\n\tds_read_b64 v[134:135], v17 offset:1664\n\t"
  "v_mfma_f32_16x16x32_f16 v[176:179], v[184:187], v[110:113], v[176:179]\n\tds_read_b64 v[144:145], v16 offset:1152\n\tds_read_b64 v[146:147], v17 offset:1152\n\t"
  "v_mfma_f32_16x16x32_f16 v[180:183], v[184:187], v[140:143], 0\n\tds_read_b64 v[140:141], v16 offset:640\n\tds_read_b64 v[142:143], v17 offset:640\n\t"
  "v_mfma_f32_16x16x32_f16 v[212:215], v[188:191], v[102:105], v[212:215]\n\t"
  "v_mfma_f32_16x16x32_f16 v[176:179], v[188:191], v[106:109], v[176:179]\n\tds_read_b64 v[184:185], v16 offset:128\n\tds_read_b64 v[186:187], v17 offset:128\n\t"
  "v_mfma_f32_16x16x32_f16 v[180:183], v[188:191], v[110:113], v[180:183]\n\t"
  "v_mfma_f32_16x16x32_f16 v[176:179], v[192:195], v[102:105], v[176:179]\n\t"
  "v_mfma_f32_16x16x32_f16 v[180:183], v[192:195], v[106:109], v[180:183]\n\t"
  "v_mfma_f32_16x16x32_f16 v[180:183], v[196:199], v[102:105], v[180:183]\n\t"
  "v_mfma_f32_16x16x32_f16 v[208:211], v[56:59], v[152:155], v[208:211]\n\t"
  "v_mfma_f32_16x16x32_f16 v[208:211], v[60:63], v[124:127], v[208:211]\n\t"
  "v_mfma_f32_16x16x32_f16 v[212:215], v[60:63], v[152:155], v[212:215]\n\tds_read_b64 v[102:103], v16 offset:1600\n\tds_read_b64 v[104:105], v17 offset:1600\n\t"
  "v_mfma_f32_16x16x32_f16 v[208:211], v[64:67], v[120:123], v[208:211]\n\t"
  "v_mfma_f32_16x16x32_f16 v[212:215], v[64:67], v[124:127], v[212:215]\n\t"
  "v_mfma_f32_16x16x32_f16 v[176:179], v[64:67], v[152:155], v[176:179]\n\ts_waitcnt lgkmcnt(2)\n\t"
  "v_mfma_f32_16x16x32_f16 v[208:211], v[132:135], v[116:119], v[208:211]\n\tds_read_b64 v[108:109], v16 offset:1088\n\tds_read_b64 v[110:111], v17 offset:1088\n\t"
  "v_mfma_f32_16x16x32_f16 v[212:215], v[132:135], v[120:123], v[212:215]\n\t"
  "v_mfma_f32_16x16x32_f16 v[176:179], v[132:135], v[124:127], v[176:179]\n\t"
  "v_mfma_f32_16x16x32_f16 v[180:183], v[132:135], v[152:155], v[180:183]\n\tds_read_b64 v[112:113], v16 offset:576\n\tds_read_b64 v[114:115], v17 offset:576\n\t"
  "v_mfma_f32_16x16x32_f16 v[212:215], v[144:147], v[116:119], v[212:215]\n\t"
  "v_mfma_f32_16x16x32_f16 v[176:179], v[144:147], v[120:123], v[176:179]\n\tds_read_b64 v[132:133], v16 offset:64\n\tds_read_b64 v[134:135], v17 offset:64\n\t"
  "v_mfma_f32_16x16x32_f16 v[180:183], v[144:147], v[124:127], v[180:183]\n\t"
  "v_mfma_f32_16x16x32_f16 v[176:179], v[140:143], v[116:119], v[176:179]\n\t"
  "v_mfma_f32_16x16x32_f16 v[180:183], v[140:143], v[120:123], v[180:183]\n\tv_add_u32 v22, 0x101000, v22\n\t"
  "v_mfma_f32_16x16x32_f16 v[180:183], v[184:187], v[116:119], v[180:183]\n\t"
  "v_mfma_f32_16x16x32_f16 v[208:211], v[72:75], v[156:159], v[208:211]\n\t"
  "v_mfma_f32_16x16x32_f16 v[208:211], v[76:79], v[148:151], v[208:211]\n\t"
  "v_mfma_f32_16x16x32_f16 v[212:215], v[76:79], v[156:159], v[212:215]\n\tds_read_b64 v[116:117], v16 offset:1536\n\tds_read_b64 v[118:119], v17 offset:1536\n\t"
  "v_mfma_f32_16x16x32_f16 v[208:211], v[80:83], v[136:139], v[208:211]\n\t"
  "v_mfma_f32_16x16x32_f16 v[212:215], v[80:83], v[148:151], v[212:215]\n\t"
  "v_mfma_f32_16x16x32_f16 v[176:179], v[80:83], v[156:159], v[176:179]\n\ts_waitcnt lgkmcnt(2)\n\t"
  "v_mfma_f32_16x16x32_f16 v[208:211], v[102:105], v[128:131], v[208:211]\n\tds_read_b32 v101, v27 offset:52\n\tds_read_b64 v[120:121], v16 offset:1024\n\t"
  "v_mfma_f32_16x16x32_f16 v[212:215], v[102:105], v[136:139], v[212:215]\n\tds_read_b64 v[122:123], v17 offset:1024\n\t"
  "v_mfma_f32_16x16x32_f16 v[176:179], v[102:105], v[148:151], v[176:179]\n\t"
  "v_mfma_f32_16x16x32_f16 v[180:183], v[102:105], v[156:159], v[180:183]\n\tds_read_b64 v[102:103], v16 offset:512\n\tds_read_b64 v[104:105], v17 offset:512\n\t"
  "v_mfma_f32_16x16x32_f16 v[212:215], v[108:111], v[128:131], v[212:215]\n\t"
  "v_mfma_f32_16x16x32_f16 v[176:179], v[108:111], v[136:139], v[176:179]\n\tds_read_b64 v[124:125], v16 offset:0\n\tds_read_b64 v[126:127], v17 offset:0\n\t"
  "v_mfma_f32_16x16x32_f16 v[180:183], v[108:111], v[148:151], v[180:183]\n\t"
  "v_mfma_f32_16x16x32_f16 v[176:179], v[112:115], v[128:131], v[176:179]\n\t"
  "v_mfma_f32_16x16x32_f16 v[180:183], v[112:115], v[136:139], v[180:183]\n\ts_waitcnt lgkmcnt(5)\n\tv_readfirstlane_b32 s61, v101\n\t"
  "v_mfma_f32_16x16x32_f16 v[180:183], v[132:135], v[128:131], v[180:183]\n\t"
  "v_mfma_f32_16x16x32_f16 v[208:211], v[88:91], v[172:175], v[208:211]\n\ts_cmp_lt_u32 s61, 4\n\ts_cbranch_scc1 .Lc0_ub\n.Lc0_ub_b:\n\t"
  "v_mfma_f32_16x16x32_f16 v[208:211], v[92:95], v[168:171], v[208:211]\n\tds_read_b128 v[108:111], v18 offset:2240\n\tds_read_b128 v[112:115], v18 offset:2752\n\t"
  "v_mfma_f32_16x16x32_f16 v[212:215], v[92:95], v[172:175], v[212:215]\n\tds_read_b128 v[128:131], v18 offset:3264\n\tds_read_b128 v[132:135], v18 offset:3776\n\t"
  "v_mfma_f32_16x16x32_f16 v[208:211], v[96:99], v[164:167], v[208:211]\n\t"
  "v_mfma_f32_16x16x32_f16 v[212:215], v[96:99], v[168:171], v[212:215]\n\t"
  "v_mfma_f32_16x16x32_f16 v[176:179], v[96:99], v[172:175], v[176:179]\n\tds_read_b128 v[136:139], v18 offset:2176\n\tds_read_b128 v[140:143], v18 offset:2688\n\t"
  "v_mfma_f32_16x16x32_f16 v[208:211], v[116:119], v[160:163], v[208:211]\n\tds_read_b128 v[144:147], v18 offset:3200\n\tds_read_b128 v[148:151], v18 offset:3712\n\t"
  "v_mfma_f32_16x16x32_f16 v[212:215], v[116:119], v[164:167], v[212:215]\n\t"
  "v_mfma_f32_16x16x32_f16 v[176:179], v[116:119], v[168:171], v[176:179]\n\t"
  "v_mfma_f32_16x16x32_f16 v[180:183], v[116:119], v[172:175], v[180:183]\n\ts_waitcnt lgkmcnt(6)\n\t"
  "v_mfma_f32_16x16x32_f16 v[212:215], v[120:123], v[160:163], v[212:215]\n\tds_read_b128 v[116:119], v18 offset:2112\n\tds_read_b128 v[152:155], v18 offset:2624\n\t"
  "v_mfma_f32_16x16x32_f16 v[176:179], v[120:123], v[164:167], v[176:179]\n\tds_read_b128 v[156:159], v18 offset:3136\n\tds_read_b128 v[172:175], v18 offset:3648\n\t"
  "v_mfma_f32_16x16x32_f16 v[180:183], v[120:123], v[168:171], v[180:183]\n\t"
  "v_mfma_f32_16x16x32_f16 v[176:179], v[102:105], v[160:163], v[176:179]\n\t"
  "v_mfma_f32_16x16x32_f16 v[180:183], v[102:105], v[164:167], v[180:183]\n\t"
  "v_mfma_f32_16x16x32_f16 v[180:183], v[124:127], v[160:163], v[180:183]\n\tds_read_b128 v[104:107], v18 offset:2048\n\tds_read_b128 v[120:123], v18 offset:2560\n\t"
  "v_mfma_f32_16x16x32_f16 v[212:215], v[40:43], v[108:111], v[212:215]\n\tds_read_b128 v[124:127], v18 offset:3072\n\tds_read_b128 v[160:163], v18 offset:3584\n\t"
  "v_mfma_f32_16x16x32_f16 v[176:179], v[44:47], v[108:111], v[176:179]\n\t"
  "v_mfma_f32_16x16x32_f16 v[212:215], v[36:39], v[112:115], v[212:215]\n\t"
  "v_mfma_f32_16x16x32_f16 v[176:179], v[40:43], v[112:115], v[176:179]\n\ts_waitcnt lgkmcnt(4)\n\t"
  "v_mfma_f32_16x16x32_f16 v[176:179], v[36:39], v[128:131], v[176:179]\n\t"
  "v_mfma_f32_16x16x32_f16 v[212:215], v[56:59], v[136:139], v[212:215]\n\t"
  "v_mfma_f32_16x16x32_f16 v[176:179], v[60:63], v[136:139], v[176:179]\n\t"
  "v_mfma_f32_16x16x32_f16 v[212:215], v[52:55], v[140:143], v[212:215]\n\t"
  "v_mfma_f32_16x16x32_f16 v[176:179], v[56:59], v[140:143], v[176:179]\n\t"
  "v_mfma_f32_16x16x32_f16 v[176:179], v[52:55], v[144:147], v[176:179]\n\t"
  "v_mfma_f32_16x16x32_f16 v[212:215], v[72:75], v[116:119], v[212:215]\n\tds_read_b128 v[164:167], v19 offset:4096\n\tds_read_b128 v[168:171], v19 offset:7168\n\t"
  "v_mfma_f32_16x16x32_f16 v[176:179], v[76:79], v[116:119], v[176:179]\n\t"
  "v_mfma_f32_16x16x32_f16 v[212:215], v[68:71], v[152:155], v[212:215]\n\t"
  "v_mfma_f32_16x16x32_f16 v[176:179], v[72:75], v[152:155], v[176:179]\n\t"
  "v_mfma_f32_16x16x32_f16 v[176:179], v[68:71], v[156:159], v[176:179]\n\ts_waitcnt lgkmcnt(2)\n\t"
  "v_mfma_f32_16x16x32_f16 v[212:215], v[88:91], v[104:107], v[212:215]\n\t"
  "v_mfma_f32_16x16x32_f16 v[176:179], v[92:95], v[104:107], v[176:179]\n\t"
  "v_mfma_f32_16x16x32_f16 v[212:215], v[84:87], v[120:123], v[212:215]\n\t"
  "v_mfma_f32_16x16x32_f16 v[176:179], v[88:91], v[120:123], v[176:179]\n\tds_read_b32 v101, v27 offset:64\n\ts_waitcnt lgkmcnt(1)\n\t"
  "v_mfma_f32_16x16x32_f16 v[176:179], v[84:87], v[124:127], v[176:179]\n\tds_write_b128 v20, v[212:215] offset:57344\n\tv_perm_b32 v164, v164, v164, s60\n\t"
  "v_mfma_f32_16x16x32_f16 v[208:211], v[36:39], v[108:111], v[208:211]\n\ts_setprio 3\n\tbuffer_load_dwordx4 v[28:31], v23, s[56:59], 0 offen sc1\n\tbuffer_load_dwordx4 v[32:35], v24, s[56:59], 0 offen sc1\n\ts_setprio 0\n\t"
  "v_mfma_f32_16x16x32_f16 v[180:183], v[48:51], v[108:111], v[180:183]\n\tds_write_b128 v20, v[176:179] offset:58368\n\tv_perm_b32 v165, v165, v165, s60\n\t"
  "v_mfma_f32_16x16x32_f16 v[180:183], v[44:47], v[112:115], v[180:183]\n\ts_mov_b64 exec, 1\n\tds_add_u32 v27, v100 offset:16\n\ts_mov_b64 exec, -1\n\t"
  "v_mfma_f32_16x16x32_f16 v[180:183], v[40:43], v[128:131], v[180:183]\n\tv_perm_b32 v166, v166, v166, s60\n\tv_perm_b32 v167, v167, v167, s60\n\t"
  "v_mfma_f32_16x16x32_f16 v[180:183], v[36:39], v[132:135], v[180:183]\n\tv_perm_b32 v168, v168, v168, s60\n\tv_perm_b32 v169, v169, v169, s60\n\t"
  "v_mfma_f32_16x16x32_f16 v[208:211], v[52:55], v[136:139], v[208:211]\n\tv_perm_b32 v170, v170, v170, s60\n\tv_perm_b32 v171, v171, v171, s60\n\t"
  "v_mfma_f32_16x16x32_f16 v[180:183], v[64:67], v[136:139], v[180:183]\n\ts_waitcnt lgkmcnt(3)\n\tv_readfirstlane_b32 s61, v101\n\t"
  "v_mfma_f32_16x16x32_f16 v[180:183], v[60:63], v[140:143], v[180:183]\n\t"
  "v_mfma_f32_16x16x32_f16 v[180:183], v[56:59], v[144:147], v[180:183]\n\ts_cmp_lt_u32 s61, 4\n\ts_cbranch_scc1 .Lc0_p3\n.Lc0_p3_b:\n\t"
  "v_mfma_f32_16x16x32_f16 v[180:183], v[52:55], v[148:151], v[180:183]\n\tds_read_b32 v36, v27 offset:16\n\tds_read_b128 v[40:43], v20 offset:59392\n\tds_read_b128 v[44:47], v20 offset:60416\n\t"
  "v_mfma_f32_16x16x32_f16 v[208:211], v[68:71], v[116:119], v[208:211]\n\t"
  "v_mfma_f32_16x16x32_f16 v[180:183], v[80:83], v[116:119], v[180:183]\n\t"
  "v_mfma_f32_16x16x32_f16 v[180:183], v[76:79], v[152:155], v[180:183]\n\t"
  "v_mfma_f32_16x16x32_f16 v[180:183], v[72:75], v[156:159], v[180:183]\n\t"
  "v_mfma_f32_16x16x32_f16 v[180:183], v[68:71], v[172:175], v[180:183]\n\t"
  "v_mfma_f32_16x16x32_f16 v[208:211], v[84:87], v[104:107], v[208:211]\n\t"
  "v_mfma_f32_16x16x32_f16 v[180:183], v[96:99], v[104:107], v[180:183]\n\t"
  "v_mfma_f32_16x16x32_f16 v[180:183], v[92:95], v[120:123], v[180:183]\n\ts_waitcnt lgkmcnt(0)\n\tv_readfirstlane_b32 s61, v36\n\tds_read_b32 v25, v27 offset:32\n\t"
  "v_mfma_f32_16x16x32_f16 v[180:183], v[88:91], v[124:127], v[180:183]\n\ts_cmp_lt_u32 s61, 4\n\ts_cbranch_scc1 .Lc0_x4\n.Lc0_x4_b:\n\t"
  "v_mfma_f32_16x16x32_f16 v[180:183], v[84:87], v[160:163], v[180:183]\n\tv_fmac_f32 v164, v40, v26\n\tv_fmac_f32 v165, v41, v26\n\tv_fmac_f32 v166, v42, v26\n\t"
  "v_fmac_f32 v167, v43, v26\n\tv_fmac_f32 v168, v44, v26\n\tv_fmac_f32 v169, v45, v26\n\tv_fmac_f32 v170, v46, v26\n\t"
  "v_fmac_f32 v171, v47, v26\n\tv_fmac_f32 v164, v208, v26\n\tv_fmac_f32 v165, v209, v26\n\tv_fmac_f32 v166, v210, v26\n\t"
  "v_fmac_f32 v167, v211, v26\n\tv_cvt_pk_bf16_f32 v36, v164, v165\n\tv_cvt_pk_bf16_f32 v37, v166, v167\n\tv_fmac_f32 v168, v180, v26\n\t"
  "v_fmac_f32 v169, v181, v26\n\tv_fmac_f32 v170, v182, v26\n\tv_fmac_f32 v171, v183, v26\n\tv_cvt_pk_bf16_f32 v38, v168, v169\n\t"
  "v_cvt_pk_bf16_f32 v39, v170, v171\n\ts_nop 0\n\tv_permlane16_swap_b32 v36, v38\n\tv_permlane16_swap_b32 v37, v39\n\t"
  "s_setprio 3\n\tbuffer_store_dwordx4 v[36:39], v22, s[56:59], 0 offen sc1\n\ts_setprio 0\n\ts_waitcnt lgkmcnt(0)\n\ts_nop 0\n\t"
  "s_branch .Lc_end\n.Lc_odd:\n\t"
  "ds_read_b64 v[36:37], v16 offset:3776\n\tds_read_b64 v[38:39], v17 offset:3776\n\tds_read_b64 v[40:41], v16 offset:3264\n\tds_read_b64 v[42:43], v17 offset:3264\n\t"
  "ds_read_b64 v[44:45], v16 offset:2752\n\tds_read_b64 v[46:47], v17 offset:2752\n\tds_read_b64 v[48:49], v16 offset:2240\n\tds_read_b64 v[50:51], v17 offset:2240\n\t"
  "ds_read_b64 v[52:53], v16 offset:3712\n\tds_read_b64 v[54:55], v17 offset:3712\n\tds_read_b64 v[56:57], v16 offset:3200\n\tds_read_b64 v[58:59], v17 offset:3200\n\t"
  "ds_read_b64 v[60:61], v16 offset:2688\n\tds_read_b64 v[62:63], v17 offset:2688\n\tds_read_b64 v[64:65], v16 offset:2176\n\tds_read_b64 v[66:67], v17 offset:2176\n\t"
  "ds_read_b64 v[68:69], v16 offset:3648\n\tds_read_b64 v[70:71], v17 offset:3648\n\tds_read_b64 v[72:73], v16 offset:3136\n\tds_read_b64 v[74:75], v17 offset:3136\n\t"
  "ds_read_b64 v[76:77], v16 offset:2624\n\tds_read_b64 v[78:79], v17 offset:2624\n\tds_read_b64 v[80:81], v16 offset:2112\n\tds_read_b64 v[82:83], v17 offset:2112\n\t"
  "ds_read_b64 v[84:85], v16 offset:3584\n\tds_read_b64 v[86:87], v17 offset:3584\n\tds_read_b64 v[88:89], v16 offset:3072\n\tds_read_b64 v[90:91], v17 offset:3072\n\t"
  "ds_read_b64 v[92:93], v16 offset:2560\n\tds_read_b64 v[94:95], v17 offset:2560\n\tds_read_b64 v[96:97], v16 offset:2048\n\tds_read_b64 v[98:99], v17 offset:2048\n\t"
  "ds_read_b32 v26, v21\n\tv_and_b32 v27, -8, v21\n\tv_mov_b32 v100, 1\n\t"
  CST(6)
  "s_mov_b32 vcc_hi, 0x1000000\n\t.Lc1_u:\n\tds_read_b32 v101, v27 offset:56\n\ts_sub_u32 vcc_hi, vcc_hi, 1\n\ts_cselect_b32 vcc_lo, 0, 4\n\ts_waitcnt lgkmcnt(0)\n\tv_readfirstlane_b32 s61, v101\n\ts_nop 1\n\ts_cmp_lt_u32 s61, vcc_lo\n\ts_cbranch_scc1 .Lc1_u\n\t"
  CST(7)
  "ds_read_b128 v[102:105], v18 offset:192\n\tds_read_b128 v[106:109], v18 offset:704\n\tds_read_b128 v[110:113], v18 offset:1216\n\tds_read_b128 v[114:117], v18 offset:1728\n\t"
  "ds_read_b128 v[120:123], v18 offset:128\n\tds_read_b128 v[124:127], v18 offset:640\n\tds_read_b128 v[128:131], v18 offset:1152\n\tds_read_b128 v[132:135], v18 offset:1664\n\t"
  "ds_read_b128 v[136:139], v18 offset:64\n\ts_waitcnt lgkmcnt(8)\n\t"
  "v_mfma_f32_16x16x32_f16 v[140:143], v[36:39], v[102:105], 0\n\tds_read_b128 v[144:147], v18 offset:576\n\t"
  "v_mfma_f32_16x16x32_f16 v[148:151], v[48:51], v[102:105], 0\n\tds_read_b128 v[152:155], v18 offset:1088\n\ts_waitcnt lgkmcnt(9)\n\t"
  "v_mfma_f32_16x16x32_f16 v[148:151], v[44:47], v[106:109], v[148:151]\n\tds_read_b128 v[156:159], v18 offset:1600\n\ts_waitcnt lgkmcnt(3)\n\t"
  "v_mfma_f32_16x16x32_f16 v[148:151], v[40:43], v[110:113], v[148:151]\n\tds_read_b128 v[160:163], v18 offset:0\n\tds_read_b32 v101, v27 offset:48\n\t"
  "v_mfma_f32_16x16x32_f16 v[148:151], v[36:39], v[114:117], v[148:151]\n\tds_read_b128 v[164:167], v18 offset:512\n\t"
  "v_mfma_f32_16x16x32_f16 v[140:143], v[52:55], v[120:123], v[140:143]\n\tds_read_b128 v[168:171], v18 offset:1024\n\t"
  "v_mfma_f32_16x16x32_f16 v[148:151], v[64:67], v[120:123], v[148:151]\n\tds_read_b128 v[172:175], v18 offset:1536\n\t"
  "v_mfma_f32_16x16x32_f16 v[148:151], v[60:63], v[124:127], v[148:151]\n\t"
  "v_mfma_f32_16x16x32_f16 v[148:151], v[56:59], v[128:131], v[148:151]\n\t"
  "v_mfma_f32_16x16x32_f16 v[148:151], v[52:55], v[132:135], v[148:151]\n\t"
  "v_mfma_f32_16x16x32_f16 v[140:143], v[68:71], v[136:139], v[140:143]\n\tds_read_b128 v[176:179], v19 offset:1024\n\tds_read_b128 v[180:183], v19 offset:2048\n\t"
  "v_mfma_f32_16x16x32_f16 v[148:151], v[80:83], v[136:139], v[148:151]\n\ts_waitcnt lgkmcnt(5)\n\tv_readfirstlane_b32 s61, v101\n\t"
  "v_mfma_f32_16x16x32_f16 v[148:151], v[76:79], v[144:147], v[148:151]\n\t"
  "v_mfma_f32_16x16x32_f16 v[148:151], v[72:75], v[152:155], v[148:151]\n\ts_cmp_lt_u32 s61, 4\n\ts_cbranch_scc1 .Lc1_kh\n.Lc1_kh_b:\n\t"
  "v_mfma_f32_16x16x32_f16 v[148:151], v[68:71], v[156:159], v[148:151]\n\tds_read_b64 v[184:185], v16 offset:1728\n\tds_read_b64 v[186:187], v17 offset:1728\n\t"
  "v_mfma_f32_16x16x32_f16 v[140:143], v[84:87], v[160:163], v[140:143]\n\tds_read_b64 v[188:189], v16 offset:1216\n\tds_read_b64 v[190:191], v17 offset:1216\n\t"
  "v_mfma_f32_16x16x32_f16 v[148:151], v[96:99], v[160:163], v[148:151]\n\tds_read_b64 v[192:193], v16 offset:704\n\ts_waitcnt lgkmcnt(7)\n\t"
  "v_mfma_f32_16x16x32_f16 v[148:151], v[92:95], v[164:167], v[148:151]\n\tds_read_b64 v[194:195], v17 offset:704\n\tds_read_b64 v[196:197], v16 offset:192\n\tds_read_b64 v[198:199], v17 offset:192\n\t"
  "v_mfma_f32_16x16x32_f16 v[148:151], v[88:91], v[168:171], v[148:151]\n\ts_waitcnt lgkmcnt(8)\n\tv_perm_b32 v176, v176, v176, s60\n\t"
  "v_mfma_f32_16x16x32_f16 v[148:151], v[84:87], v[172:175], v[148:151]\n\tds_write_b128 v20, v[140:143] offset:2048\n\tv_perm_b32 v177, v177, v177, s60\n\t"
  "v_mfma_f32_16x16x32_f16 v[200:203], v[40:43], v[102:105], 0\n\tv_perm_b32 v178, v178, v178, s60\n\tv_perm_b32 v179, v179, v179, s60\n\t"
  "v_mfma_f32_16x16x32_f16 v[140:143], v[44:47], v[102:105], 0\n\tds_write_b128 v20, v[148:151] offset:3072\n\tv_perm_b32 v180, v180, v180, s60\n\t"
  "v_mfma_f32_16x16x32_f16 v[200:203], v[36:39], v[106:109], v[200:203]\n\ts_mov_b64 exec, 1\n\tds_add_u32 v21, v100 offset:8\n\ts_mov_b64 exec, -1\n\t"
  "v_mfma_f32_16x16x32_f16 v[140:143], v[40:43], v[106:109], v[140:143]\n\tv_perm_b32 v181, v181, v181, s60\n\tv_perm_b32 v182, v182, v182, s60\n\t"
  "v_mfma_f32_16x16x32_f16 v[140:143], v[36:39], v[110:113], v[140:143]\n\tv_perm_b32 v183, v183, v183, s60\n\t"
  "v_mfma_f32_16x16x32_f16 v[200:203], v[56:59], v[120:123], v[200:203]\n\t"
  "v_mfma_f32_16x16x32_f16 v[140:143], v[60:63], v[120:123], v[140:143]\n\t"
  "v_mfma_f32_16x16x32_f16 v[200:203], v[52:55], v[124:127], v[200:203]\n\t"
  "v_mfma_f32_16x16x32_f16 v[140:143], v[56:59], v[124:127], v[140:143]\n\t"
  "v_mfma_f32_16x16x32_f16 v[140:143], v[52:55], v[128:131], v[140:143]\n\t"
  "v_mfma_f32_16x16x32_f16 v[200:203], v[72:75], v[136:139], v[200:203]\n\t"
  "v_mfma_f32_16x16x32_f16 v[140:143], v[76:79], v[136:139], v[140:143]\n\tds_read_b32 v101, v21 offset:8\n\tds_read_b128 v[148:151], v20 offset:0\n\tds_read_b128 v[204:207], v20 offset:1024\n\t"
  "v_mfma_f32_16x16x32_f16 v[200:203], v[68:71], v[144:147], v[200:203]\n\t"
  "v_mfma_f32_16x16x32_f16 v[140:143], v[72:75], v[144:147], v[140:143]\n\t"
  "v_mfma_f32_16x16x32_f16 v[140:143], v[68:71], v[152:155], v[140:143]\n\t"
  "v_mfma_f32_16x16x32_f16 v[200:203], v[88:91], v[160:163], v[200:203]\n\t"
  "v_mfma_f32_16x16x32_f16 v[140:143], v[92:95], v[160:163], v[140:143]\n\t"
  "v_mfma_f32_16x16x32_f16 v[200:203], v[84:87], v[164:167], v[200:203]\n\t"
  "v_mfma_f32_16x16x32_f16 v[140:143], v[88:91], v[164:167], v[140:143]\n\t"
  "v_mfma_f32_16x16x32_f16 v[140:143], v[84:87], v[168:171], v[140:143]\n\ts_waitcnt lgkmcnt(0)\n\tv_readfirstlane_b32 s61, v101\n\t"
  "v_mfma_f32_16x16x32_f16 v[208:211], v[40:43], v[114:117], 0\n\t"
  "v_mfma_f32_16x16x32_f16 v[208:211], v[44:47], v[110:113], v[208:211]\n\ts_cmp_lt_u32 s61, 2\n\ts_cbranch_scc1 .Lc1_x0\n.Lc1_x0_b:\n\tv_fmac_f32 v176, v148, v26\n\tv_fmac_f32 v177, v149, v26\n\t"
  "v_fmac_f32 v178, v150, v26\n\t"
  "v_mfma_f32_16x16x32_f16 v[212:215], v[44:47], v[114:117], 0\n\tv_fmac_f32 v179, v151, v26\n\tv_fmac_f32 v180, v204, v26\n\tv_fmac_f32 v181, v205, v26\n\t"
  "v_fmac_f32 v182, v206, v26\n\tv_fmac_f32 v183, v207, v26\n\t"
  "v_mfma_f32_16x16x32_f16 v[208:211], v[48:51], v[106:109], v[208:211]\n\tv_fmac_f32 v176, v200, v26\n\tv_fmac_f32 v177, v201, v26\n\t"
  "v_mfma_f32_16x16x32_f16 v[212:215], v[48:51], v[110:113], v[212:215]\n\tv_fmac_f32 v178, v202, v26\n\tv_fmac_f32 v179, v203, v26\n\tv_cvt_pk_bf16_f32 v148, v176, v177\n\t"
  "v_cvt_pk_bf16_f32 v149, v178, v179\n\tv_fmac_f32 v180, v140, v26\n\t"
  "v_mfma_f32_16x16x32_f16 v[176:179], v[48:51], v[114:117], 0\n\tv_fmac_f32 v181, v141, v26\n\tv_fmac_f32 v182, v142, v26\n\tv_fmac_f32 v183, v143, v26\n\t"
  "v_cvt_pk_bf16_f32 v150, v180, v181\n\tv_cvt_pk_bf16_f32 v151, v182, v183\n\t"
  "v_mfma_f32_16x16x32_f16 v[208:211], v[184:187], v[102:105], v[208:211]\n\tv_permlane16_swap_b32 v148, v150\n\tv_permlane16_swap_b32 v149, v151\n\ts_setprio 3\n\tbuffer_store_dwordx4 v[148:151], v22, s[56:59], 0 offen sc1\n\ts_setprio 0\n\t"
  CST(8)
  "v_mfma_f32_16x16x32_f16 v[212:215], v[184:187], v[106:109], v[212:215]\n\tds_read_b64 v[140:141], v16 offset:1664\n\tds_read_b64 v[142:143], v17 offset:1664\n\t"
  "v_mfma_f32_16x16x32_f16 v[176:179], v[184:187], v[110:113], v[176:179]\n\tds_read_b64 v[148:149], v16 offset:1152\n\tds_read_b64 v[150:151], v17 offset:1152\n\t"
  "v_mfma_f32_16x16x32_f16 v[180:183], v[184:187], v[114:117], 0\n\tds_read_b64 v[116:117], v16 offset:640\n\tds_read_b64 v[118:119], v17 offset:640\n\t"
  "v_mfma_f32_16x16x32_f16 v[212:215], v[188:191], v[102:105], v[212:215]\n\t"
  "v_mfma_f32_16x16x32_f16 v[176:179], v[188:191], v[106:109], v[176:179]\n\tds_read_b64 v[184:185], v16 offset:128\n\tds_read_b64 v[186:187], v17 offset:128\n\t"
  "v_mfma_f32_16x16x32_f16 v[180:183], v[188:191], v[110:113], v[180:183]\n\t"
  "v_mfma_f32_16x16x32_f16 v[176:179], v[192:195], v[102:105], v[176:179]\n\t"
  "v_mfma_f32_16x16x32_f16 v[180:183], v[192:195], v[106:109], v[180:183]\n\t"
  "v_mfma_f32_16x16x32_f16 v[180:183], v[196:199], v[102:105], v[180:183]\n\t"
  "v_mfma_f32_16x16x32_f16 v[208:211], v[56:59], v[132:135], v[208:211]\n\t"
  "v_mfma_f32_16x16x32_f16 v[208:211], v[60:63], v[128:131], v[208:211]\n\t"
  "v_mfma_f32_16x16x32_f16 v[212:215], v[60:63], v[132:135], v[212:215]\n\tds_read_b64 v[102:103], v16 offset:1600\n\tds_read_b64 v[104:105], v17 offset:1600\n\t"
  "v_mfma_f32_16x16x32_f16 v[208:211], v[64:67], v[124:127], v[208:211]\n\t"
  "v_mfma_f32_16x16x32_f16 v[212:215], v[64:67], v[128:131], v[212:215]\n\t"
  "v_mfma_f32_16x16x32_f16 v[176:179], v[64:67], v[132:135], v[176:179]\n\ts_waitcnt lgkmcnt(2)\n\t"
  "v_mfma_f32_16x16x32_f16 v[208:211], v[140:143], v[120:123], v[208:211]\n\tds_read_b64 v[108:109], v16 offset:1088\n\tds_read_b64 v[110:111], v17 offset:1088\n\t"
  "v_mfma_f32_16x16x32_f16 v[212:215], v[140:143], v[124:127], v[212:215]\n\t"
  "v_mfma_f32_16x16x32_f16 v[176:179], v[140:143], v[128:131], v[176:179]\n\t"
  "v_mfma_f32_16x16x32_f16 v[180:183], v[140:143], v[132:135], v[180:183]\n\tds_read_b64 v[112:113], v16 offset:576\n\tds_read_b64 v[114:115], v17 offset:576\n\t"
  "v_mfma_f32_16x16x32_f16 v[212:215], v[148:151], v[120:123], v[212:215]\n\t"
  "v_mfma_f32_16x16x32_f16 v[176:179], v[148:151], v[124:127], v[176:179]\n\tds_read_b64 v[132:133], v16 offset:64\n\tds_read_b64 v[134:135], v17 offset:64\n\t"
  "v_mfma_f32_16x16x32_f16 v[180:183], v[148:151], v[128:131], v[180:183]\n\t"
  "v_mfma_f32_16x16x32_f16 v[176:179], v[116:119], v[120:123], v[176:179]\n\t"
  "v_mfma_f32_16x16x32_f16 v[180:183], v[116:119], v[124:127], v[180:183]\n\tv_add_u32 v22, 0x101000, v22\n\t"
  "v_mfma_f32_16x16x32_f16 v[180:183], v[184:187], v[120:123], v[180:183]\n\t"
  "v_mfma_f32_16x16x32_f16 v[208:211], v[72:75], v[156:159], v[208:211]\n\t"
  "v_mfma_f32_16x16x32_f16 v[208:211], v[76:79], v[152:155], v[208:211]\n\t"
  "v_mfma_f32_16x16x32_f16 v[212:215], v[76:79], v[156:159], v[212:215]\n\tds_read_b64 v[116:117], v16 offset:1536\n\tds_read_b64 v[118:119], v17 offset:1536\n\t"
  "v_mfma_f32_16x16x32_f16 v[208:211], v[80:83], v[144:147], v[208:211]\n\t"
  "v_mfma_f32_16x16x32_f16 v[212:215], v[80:83], v[152:155], v[212:215]\n\t"
  "v_mfma_f32_16x16x32_f16 v[176:179], v[80:83], v[156:159], v[176:179]\n\ts_waitcnt lgkmcnt(2)\n\t"
  "v_mfma_f32_16x16x32_f16 v[208:211], v[102:105], v[136:139], v[208:211]\n\tds_read_b32 v101, v27 offset:52\n\tds_read_b64 v[120:121], v16 offset:1024\n\t"
  "v_mfma_f32_16x16x32_f16 v[212:215], v[102:105], v[144:147], v[212:215]\n\tds_read_b64 v[122:123], v17 offset:1024\n\t"
  "v_mfma_f32_16x16x32_f16 v[176:179], v[102:105], v[152:155], v[176:179]\n\t"
  "v_mfma_f32_16x16x32_f16 v[180:183], v[102:105], v[156:159], v[180:183]\n\tds_read_b64 v[102:103], v16 offset:512\n\tds_read_b64 v[104:105], v17 offset:512\n\t"
  "v_mfma_f32_16x16x32_f16 v[212:215], v[108:111], v[136:139], v[212:215]\n\t"
  "v_mfma_f32_16x16x32_f16 v[176:179], v[108:111], v[144:147], v[176:179]\n\tds_read_b64 v[124:125], v16 offset:0\n\tds_read_b64 v[126:127], v17 offset:0\n\t"
  "v_mfma_f32_16x16x32_f16 v[180:183], v[108:111], v[152:155], v[180:183]\n\t"
  "v_mfma_f32_16x16x32_f16 v[176:179], v[112:115], v[136:139], v[176:179]\n\t"
  "v_mfma_f32_16x16x32_f16 v[180:183], v[112:115], v[144:147], v[180:183]\n\ts_waitcnt lgkmcnt(5)\n\tv_readfirstlane_b32 s61, v101\n\t"
  "v_mfma_f32_16x16x32_f16 v[180:183], v[132:135], v[136:139], v[180:183]\n\t"
  "v_mfma_f32_16x16x32_f16 v[208:211], v[88:91], v[172:175], v[208:211]\n\ts_cmp_lt_u32 s61, 4\n\ts_cbranch_scc1 .Lc1_ub\n.Lc1_ub_b:\n\t"
  "v_mfma_f32_16x16x32_f16 v[208:211], v[92:95], v[168:171], v[208:211]\n\tds_read_b128 v[108:111], v18 offset:2240\n\tds_read_b128 v[112:115], v18 offset:2752\n\t"
  "v_mfma_f32_16x16x32_f16 v[212:215], v[92:95], v[172:175], v[212:215]\n\tds_read_b128 v[128:131], v18 offset:3264\n\tds_read_b128 v[132:135], v18 offset:3776\n\t"
  "v_mfma_f32_16x16x32_f16 v[208:211], v[96:99], v[164:167], v[208:211]\n\t"
  "v_mfma_f32_16x16x32_f16 v[212:215], v[96:99], v[168:171], v[212:215]\n\t"
  "v_mfma_f32_16x16x32_f16 v[176:179], v[96:99], v[172:175], v[176:179]\n\tds_read_b128 v[136:139], v18 offset:2176\n\tds_read_b128 v[140:143], v18 offset:2688\n\t"
  "v_mfma_f32_16x16x32_f16 v[208:211], v[116:119], v[160:163], v[208:211]\n\tds_read_b128 v[144:147], v18 offset:3200\n\tds_read_b128 v[148:151], v18 offset:3712\n\t"
  "v_mfma_f32_16x16x32_f16 v[212:215], v[116:119], v[164:167], v[212:215]\n\t"
  "v_mfma_f32_16x16x32_f16 v[176:179], v[116:119], v[168:171], v[176:179]\n\t"
  "v_mfma_f32_16x16x32_f16 v[180:183], v[116:119], v[172:175], v[180:183]\n\ts_waitcnt lgkmcnt(6)\n\t"
  "v_mfma_f32_16x16x32_f16 v[212:215], v[120:123], v[160:163], v[212:215]\n\tds_read_b128 v[116:119], v18 offset:2112\n\tds_read_b128 v[152:155], v18 offset:2624\n\t"
  "v_mfma_f32_16x16x32_f16 v[176:179], v[120:123], v[164:167], v[176:179]\n\tds_read_b128 v[156:159], v18 offset:3136\n\tds_read_b128 v[172:175], v18 offset:3648\n\t"
  "v_mfma_f32_16x16x32_f16 v[180:183], v[120:123], v[168:171], v[180:183]\n\t"
  "v_mfma_f32_16x16x32_f16 v[176:179], v[102:105], v[160:163], v[176:179]\n\t"
  "v_mfma_f32_16x16x32_f16 v[180:183], v[102:105], v[164:167], v[180:183]\n\t"
  "v_mfma_f32_16x16x32_f16 v[180:183], v[124:127], v[160:163], v[180:183]\n\tds_read_b128 v[104:107], v18 offset:2048\n\tds_read_b128 v[120:123], v18 offset:2560\n\t"
  "v_mfma_f32_16x16x32_f16 v[208:211], v[36:39], v[108:111], v[208:211]\n\tds_read_b128 v[124:127], v18 offset:3072\n\tds_read_b128 v[160:163], v18 offset:3584\n\t"
  "v_mfma_f32_16x16x32_f16 v[180:183], v[48:51], v[108:111], v[180:183]\n\t"
  "v_mfma_f32_16x16x32_f16 v[180:183], v[44:47], v[112:115], v[180:183]\n\ts_waitcnt lgkmcnt(6)\n\t"
  "v_mfma_f32_16x16x32_f16 v[180:183], v[40:43], v[128:131], v[180:183]\n\t"
  "v_mfma_f32_16x16x32_f16 v[180:183], v[36:39], v[132:135], v[180:183]\n\t"
  "v_mfma_f32_16x16x32_f16 v[208:211], v[52:55], v[136:139], v[208:211]\n\t"
  "v_mfma_f32_16x16x32_f16 v[180:183], v[64:67], v[136:139], v[180:183]\n\t"
  "v_mfma_f32_16x16x32_f16 v[180:183], v[60:63], v[140:143], v[180:183]\n\t"
  "v_mfma_f32_16x16x32_f16 v[180:183], v[56:59], v[144:147], v[180:183]\n\t"
  "v_mfma_f32_16x16x32_f16 v[180:183], v[52:55], v[148:151], v[180:183]\n\t"
  "v_mfma_f32_16x16x32_f16 v[208:211], v[68:71], v[116:119], v[208:211]\n\tds_read_b128 v[48:51], v19 offset:5120\n\tds_read_b128 v[64:67], v19 offset:6144\n\t"
  "v_mfma_f32_16x16x32_f16 v[180:183], v[80:83], v[116:119], v[180:183]\n\t"
  "v_mfma_f32_16x16x32_f16 v[180:183], v[76:79], v[152:155], v[180:183]\n\ts_waitcnt lgkmcnt(2)\n\t"
  "v_mfma_f32_16x16x32_f16 v[180:183], v[72:75], v[156:159], v[180:183]\n\t"
  "v_mfma_f32_16x16x32_f16 v[180:183], v[68:71], v[172:175], v[180:183]\n\t"
  "v_mfma_f32_16x16x32_f16 v[208:211], v[84:87], v[104:107], v[208:211]\n\t"
  "v_mfma_f32_16x16x32_f16 v[180:183], v[96:99], v[104:107], v[180:183]\n\t"
  "v_mfma_f32_16x16x32_f16 v[180:183], v[92:95], v[120:123], v[180:183]\n\t"
  "v_mfma_f32_16x16x32_f16 v[180:183], v[88:91], v[124:127], v[180:183]\n\tds_read_b32 v101, v27 offset:64\n\ts_waitcnt lgkmcnt(1)\n\t"
  "v_mfma_f32_16x16x32_f16 v[180:183], v[84:87], v[160:163], v[180:183]\n\tds_write_b128 v20, v[208:211] offset:59392\n\tv_perm_b32 v48, v48, v48, s60\n\t"
  "v_mfma_f32_16x16x32_f16 v[212:215], v[40:43], v[108:111], v[212:215]\n\ts_setprio 3\n\tbuffer_load_dwordx4 v[28:31], v23, s[56:59], 0 offen sc1\n\tbuffer_load_dwordx4 v[32:35], v24, s[56:59], 0 offen sc1\n\ts_setprio 0\n\t"
  "v_mfma_f32_16x16x32_f16 v[176:179], v[44:47], v[108:111], v[176:179]\n\tds_write_b128 v20, v[180:183] offset:60416\n\tv_perm_b32 v49, v49, v49, s60\n\t"
  "v_mfma_f32_16x16x32_f16 v[212:215], v[36:39], v[112:115], v[212:215]\n\ts_mov_b64 exec, 1\n\tds_add_u32 v27, v100 offset:16\n\ts_mov_b64 exec, -1\n\t"
  "v_mfma_f32_16x16x32_f16 v[176:179], v[40:43], v[112:115], v[176:179]\n\tv_perm_b32 v50, v50, v50, s60\n\tv_perm_b32 v51, v51, v51, s60\n\t"
  "v_mfma_f32_16x16x32_f16 v[176:179], v[36:39], v[128:131], v[176:179]\n\tv_perm_b32 v64, v64, v64, s60\n\tv_perm_b32 v65, v65, v65, s60\n\t"
  "v_mfma_f32_16x16x32_f16 v[212:215], v[56:59], v[136:139], v[212:215]\n\tv_perm_b32 v66, v66, v66, s60\n\tv_perm_b32 v67, v67, v67, s60\n\t"
  "v_mfma_f32_16x16x32_f16 v[176:179], v[60:63], v[136:139], v[176:179]\n\ts_waitcnt lgkmcnt(3)\n\tv_readfirstlane_b32 s61, v101\n\t"
  "v_mfma_f32_16x16x32_f16 v[212:215], v[52:55], v[140:143], v[212:215]\n\t"
  "v_mfma_f32_16x16x32_f16 v[176:179], v[56:59], v[140:143], v[176:179]\n\ts_cmp_lt_u32 s61, 4\n\ts_cbranch_scc1 .Lc1_p3\n.Lc1_p3_b:\n\t"
  "v_mfma_f32_16x16x32_f16 v[176:179], v[52:55], v[144:147], v[176:179]\n\tds_read_b32 v36, v27 offset:16\n\tds_read_b128 v[40:43], v20 offset:57344\n\tds_read_b128 v[44:47], v20 offset:58368\n\t"
  "v_mfma_f32_16x16x32_f16 v[212:215], v[72:75], v[116:119], v[212:215]\n\t"
  "v_mfma_f32_16x16x32_f16 v[176:179], v[76:79], v[116:119], v[176:179]\n\t"
  "v_mfma_f32_16x16x32_f16 v[212:215], v[68:71], v[152:155], v[212:215]\n\t"
  "v_mfma_f32_16x16x32_f16 v[176:179], v[72:75], v[152:155], v[176:179]\n\t"
  "v_mfma_f32_16x16x32_f16 v[176:179], v[68:71], v[156:159], v[176:179]\n\t"
  "v_mfma_f32_16x16x32_f16 v[212:215], v[88:91], v[104:107], v[212:215]\n\t"
  "v_mfma_f32_16x16x32_f16 v[176:179], v[92:95], v[104:107], v[176:179]\n\t"
  "v_mfma_f32_16x16x32_f16 v[212:215], v[84:87], v[120:123], v[212:215]\n\ts_waitcnt lgkmcnt(0)\n\tv_readfirstlane_b32 s61, v36\n\tds_read_b32 v25, v27 offset:32\n\t"
  "v_mfma_f32_16x16x32_f16 v[176:179], v[88:91], v[120:123], v[176:179]\n\ts_cmp_lt_u32 s61, 4\n\ts_cbranch_scc1 .Lc1_x4\n.Lc1_x4_b:\n\t"
  "v_mfma_f32_16x16x32_f16 v[176:179], v[84:87], v[124:127], v[176:179]\n\tv_fmac_f32 v48, v40, v26\n\tv_fmac_f32 v49, v41, v26\n\tv_fmac_f32 v50, v42, v26\n\t"
  "v_fmac_f32 v51, v43, v26\n\tv_fmac_f32 v64, v44, v26\n\tv_fmac_f32 v65, v45, v26\n\tv_fmac_f32 v66, v46, v26\n\t"
  "v_fmac_f32 v67, v47, v26\n\tv_fmac_f32 v48, v212, v26\n\tv_fmac_f32 v49, v213, v26\n\tv_fmac_f32 v50, v214, v26\n\t"
  "v_fmac_f32 v51, v215, v26\n\tv_cvt_pk_bf16_f32 v36, v48, v49\n\tv_cvt_pk_bf16_f32 v37, v50, v51\n\tv_fmac_f32 v64, v176, v26\n\t"
  "v_fmac_f32 v65, v177, v26\n\tv_fmac_f32 v66, v178, v26\n\tv_fmac_f32 v67, v179, v26\n\tv_cvt_pk_bf16_f32 v38, v64, v65\n\t"
  "v_cvt_pk_bf16_f32 v39, v66, v67\n\ts_nop 0\n\tv_permlane16_swap_b32 v36, v38\n\tv_permlane16_swap_b32 v37, v39\n\t"
  "s_setprio 3\n\tbuffer_store_dwordx4 v[36:39], v22, s[56:59], 0 offen sc1\n\ts_setprio 0\n\ts_waitcnt lgkmcnt(0)\n\ts_nop 0\n\t"
  ".Lc_end:\n\ts_waitcnt vmcnt(1) lgkmcnt(0)"
  : C1_OPS, "=&{v[28:31]}"(fl), "=&{v[32:35]}"(wfl), "=&{v25}"(g2)
  : "{s[56:59]}"(wrs), "{s60}"((a >> 1) ? 0x03020c0cu : 0x01000c0cu), [a] "s"(a)
  : "memory", "scc", "vcc", "s61", "v26", "v27", "v36", "v37", "v38", "v39", "v40", "v41", "v42", "v43", "v44", "v45", "v46", "v47", "v48", "v49", "v50", "v51", "v52", "v53", "v54", "v55", "v56", "v57", "v58", "v59", "v60", "v61", "v62", "v63", "v64", "v65", "v66", "v67", "v68", "v69", "v70", "v71", "v72", "v73", "v74", "v75", "v76", "v77", "v78", "v79", "v80", "v81", "v82", "v83", "v84", "v85", "v86", "v87", "v88", "v89", "v90", "v91", "v92", "v93", "v94", "v95", "v96", "v97", "v98", "v99", "v100", "v101", "v102", "v103", "v104", "v105", "v106", "v107", "v108", "v109", "v110", "v111", "v112", "v113", "v114", "v115", "v116", "v117", "v118", "v119", "v120", "v121", "v122", "v123", "v124", "v125", "v126", "v127", "v128", "v129", "v130", "v131", "v132", "v133", "v134", "v135", "v136", "v137", "v138", "v139", "v140", "v141", "v142", "v143", "v144", "v145", "v146", "v147", "v148", "v149", "v150", "v151", "v152", "v153", "v154", "v155", "v156", "v157", "v158", "v159", "v160", "v161", "v162", "v163", "v164", "v165", "v166", "v167", "v168", "v169", "v170", "v171", "v172", "v173", "v174", "v175", "v176", "v177", "v178", "v179", "v180", "v181", "v182", "v183", "v184", "v185", "v186", "v187", "v188", "v189", "v190", "v191", "v192", "v193", "v194", "v195", "v196", "v197", "v198", "v199", "v200", "v201", "v202", "v203", "v204", "v205", "v206", "v207", "v208", "v209", "v210", "v211", "v212", "v213", "v214", "v215", "v216", "v217", "v218", "v219", "v220", "v221", "v222", "v223");
  ST(0, 9);
}

struct XTake { u32 p, sw, idx; bf16x8 one; u64 m0, m1, m2; };
typedef __attribute__((ext_vector_type(16))) short bf16x16;
__device__ __forceinline__ void x_pre(XTake& X, int a, int lane) {
  X.p = (u32)(L_TA + a * 8192 + lane * 8);
  X.sw = (u32)(L_SP + ((lane & 15) * 4 + a) * 4 + ((lane >> 4) == ((lane >> 2) & 3) ? 768 : 0));
  X.idx = (lane & 1) ? 0xEEEEu : 0x4444u;
  X.one = __builtin_bit_cast(bf16x8, (u32x4){0x3F803F80u, 0x3F803F80u, 0x3F803F80u, 0x3F803F80u});
  X.m0 = 0xAAAAAAAAAAAAAAAAull; X.m1 = 0xCCCCCCCCCCCCCCCCull; X.m2 = 0xF0000F0000F0000Full;
  asm volatile("" : "+v"(X.p), "+v"(X.sw), "+v"(X.idx), "+v"(X.one));
  asm volatile("" : "+s"(X.m0), "+s"(X.m1), "+s"(X.m2));
}
struct NoMid { __device__ void operator()() const {} };
template <int H, int NY = 0, bool ALL = false, class F = NoMid>
__device__ __forceinline__ void x_take(const XTake& X, char* smem, rsrc_t wrs, int xin, int rb, int a, int lane, u32 sent = 0xFFFFFFFFu, F mid = F()) {
  constexpr int tile = H ? L_TB : L_TA, NT = H ? 3 : 2;
  const LAS char* p = (const LAS char*)(size_t)(H ? opaque(X.p + (L_TB - L_TA)) : X.p);
  const LAS char* ph = (const LAS char*)(size_t)(H ? opaque(X.p + (L_TH - L_TA) - a * 4096) : 0u);
  LAS float* spw = (LAS float*)(size_t)X.sw + H * 384;
  float s = 0.f, dv[NT];
  bf16x16 xf[NT][2];
  f32x4 ss[NT], sg[NT];
  auto ld_ = [&](int o, int j) __attribute__((always_inline)) {
    if (o < 2) xf[o][j] = __builtin_shufflevector(ld_tr8<1024>(p + o * 512 + j * 4096), ld_tr8<1024>(p + o * 512 + j * 4096 + 2048), 0, 1, 2, 3, 4, 5, 6, 7, 8, 9, 10, 11, 12, 13, 14, 15);
    else xf[o][j] = __builtin_shufflevector(ld_tr8<512>(ph + j * 2048), ld_tr8<512>(ph + j * 2048 + 1024), 0, 1, 2, 3, 4, 5, 6, 7, 8, 9, 10, 11, 12, 13, 14, 15);
    SB();
  };
  auto gr_ = [&](int o, int j) __attribute__((always_inline)) {
#define XLO(v_) __builtin_shufflevector(v_, v_, 0, 1, 2, 3, 4, 5, 6, 7)
#define XHI(v_) __builtin_shufflevector(v_, v_, 8, 9, 10, 11, 12, 13, 14, 15)
    if (j == 0) asm volatile("v_mfma_f32_16x16x32_bf16 %0, %1, %1, 0" : "=&v"(sg[o]) : "v"(XLO(xf[o][0])));
    else asm volatile("v_mfma_f32_16x16x32_bf16 %0, %1, %1, %0" : "+v"(sg[o]) : "v"(XLO(xf[o][1])));
    asm volatile("v_mfma_f32_16x16x32_bf16 %0, %1, %1, %0" : "+v"(sg[o]) : "v"(XHI(xf[o][j])));
    SB();
  };
  auto sm_ = [&](int o, int j) __attribute__((always_inline)) {
    asm volatile("v_smfmac_f32_16x16x64_bf16 %0, %1, %2, %3" : "+v"(ss[o]) : "v"(X.one), "v"(xf[o][j]), "v"(X.idx));
    SB();
  };
  auto fin_ = [&](int o, auto V_) __attribute__((always_inline)) {
    float t;
    float& d = dv[o];
    asm volatile("v_cndmask_b32_e64 %0, %3, %4, %9\n\tv_cndmask_b32_e64 %1, %5, %6, %9\n\tv_cndmask_b32_e64 %0, %0, %1, %10\n\t.if %12 >= 0\n\ts_nop %12\n\t.endif\n\t"
                 "v_add_f32 %1, %7, %8\n\tv_cndmask_b32_e64 %0, %1, %0, %11\n\tv_add_f32 %2, %2, %1"
                 : "=&v"(d), "=&v"(t), "+v"(s) : "v"(sg[o][0]), "v"(sg[o][1]), "v"(sg[o][2]), "v"(sg[o][3]), "v"(ss[o][0]), "v"(ss[o][1]),
                   "s"(X.m0), "s"(X.m1), "s"(X.m2), "n"(decltype(V_)::value - 1), "v"(ss[o]));
    SB();
  };
  auto zero_ = [&]() __attribute__((always_inline)) {
#pragma unroll
    for (int o = 0; o < NT; ++o) { ss[o] = (f32x4){0.f, 0.f, 0.f, 0.f}; asm volatile("" : "+v"(ss[o])); }
    asm volatile("" : "+v"(s));
  };
  auto first_ = [&]() __attribute__((always_inline)) { gr_(0, 0); sm_(0, 0); gr_(1, 0); sm_(1, 0); };
  auto second_ = [&]() __attribute__((always_inline)) {
    gr_(0, 1); sm_(0, 1); gr_(1, 1); fin_(0, IC<0>{}); sm_(1, 1);
    if (NT == 2) { fin_(1, IC<5>{}); spw[0] = dv[0]; spw[64] = dv[1]; }
  };
  auto third_ = [&]() __attribute__((always_inline)) {
    gr_(2, 0); fin_(1, IC<0>{}); gr_(2, 1);
    spw[0] = dv[0]; spw[64] = dv[1];
    SB();
    sm_(2, 0); sm_(2, 1); fin_(NT - 1, IC<5>{});
    spw[128] = dv[NT - 1];
  };
  zero_();
  SB();
  if (H) {
    const u32 sa = opaque((u32)(L_TB + a * 8192 + (lane >> 4) * 1024 + (lane & 15) * 32 + ((lane >> 3) & 1) * 512 + ((lane >> 4) & 3) * 8 + 4));
#pragma unroll 1
    for (unsigned spins = 0; spins <= SPIN_CAP; ++spins) {
      x_dma(wrs, xin, rb, tile, a, lane);
      asm volatile("s_waitcnt vmcnt(8)" ::: "memory");
      if (__builtin_amdgcn_ballot_w64(*(const LAS u32*)(size_t)sa == sent) == 0ull) break;
      FAILC(20);
    }
  } else asm volatile("s_waitcnt vmcnt(%0)" :: "n"(4 + NY) : "memory");
  if (H && B6 == 0) { __builtin_amdgcn_s_barrier(); ST(xin != OFF_X1, 14); asm volatile("" ::: "memory"); }
  ld_(0, 0); ld_(1, 0);
  if (!ALL && !(H && B6 == 1)) first_();
  asm volatile("s_waitcnt vmcnt(%0)" :: "n"(4 * (NT - 2) + NY) : "memory");
  if (H && B6) { __builtin_amdgcn_s_barrier(); ST(xin != OFF_X1, 14); asm volatile("" ::: "memory"); }
  ld_(0, 1); ld_(1, 1);
  if (ALL || (H && B6 == 1)) first_();
  second_();
  if (NT == 3) {
    asm volatile("s_waitcnt vmcnt(%0)" :: "n"(NY) : "memory");
    ld_(2, 0); ld_(2, 1);
    mid();
    third_();
  }
  if (__builtin_expect(__builtin_amdgcn_ballot_w64(s != s) != 0ull, 0)) {
    for (unsigned spins = 0;; ++spins) {
      FAILC((xin == OFF_X1 ? 1 : 9) + H);
      if (H) *(u32*)(smem + L_MI + M_RD) = xin == OFF_X1 ? 1 : 2;
      x_dma(wrs, xin, rb, tile, a, lane);
      asm volatile("s_waitcnt vmcnt(0)" ::: "memory");
      s = 0.f;
#pragma unroll
      for (int o = 0; o < 2 * NT; ++o) ld_(o >> 1, o & 1);
      zero_();
      first_(); second_();
      if (NT == 3) third_();
      if (!__any(s != s)) break;
      bool st = false;
#pragma unroll 1
      for (int e = 0; e < 4 * NT; ++e) { const u32x4 v = *(const u32x4*)(smem + (e < 8 ? tile + a * 8192 : L_TH + a * 4096 - 8192) + lane * 16 + e * 1024); st |= v[1] == sent || v[3] == sent; }
      if (!__any(st) || spins > SPIN_CAP) break;
    }
  }
}
#define PIN(x_) asm volatile("" : "+v"(x_))
struct HaloMsg { u32x4 k, v; };
__device__ __forceinline__ void halo_ld(HaloMsg& h, rsrc_t wrs, int ho) { h.k = ldw(wrs, ho); h.v = ldw(wrs, ho + 1024); }
__device__ __forceinline__ bool halo_ok(const HaloMsg& h, u32 tag) {
  return __builtin_amdgcn_ballot_w64((opaque((h.k[1] ^ tag) | (h.k[3] ^ tag)) | opaque((h.v[1] ^ tag) | (h.v[3] ^ tag))) != 0u) == 0ull;
}
struct AttC { f32x4 mp, mo, mf; u32x2 one; u32 sl, su; };
__device__ __forceinline__ void att_pre(AttC& C, int lane, bool first) {
  const float NINF = -__builtin_inff();
  const u64 pm[4] = {0x0fff00ff000f0000ull, 0x3fff03ff003f0003ull, 0xf000ff00fff0ffffull, 0xc000fc00ffc0fffcull};
#pragma unroll
  for (int e = 0; e < 4; ++e) {
    const u64 par = (e & 1) ? 0xAAAAAAAAAAAAAAAAull : 0x5555555555555555ull;
    C.mp[e] = __builtin_amdgcn_inverse_ballot_w64(pm[e >> 1] & par) ? 0.f : NINF;
    C.mo[e] = __builtin_amdgcn_inverse_ballot_w64(pm[2 + (e >> 1)] & par) ? 0.f : NINF;
    C.mf[e] = first ? NINF : C.mp[e];
  }
  C.one = (u32x2){0x3F803F80u, 0x3F803F80u};
  C.sl = (lane & 1) ? 0x01000c0cu : 0x0c0c0100u; C.su = (lane & 1) ? 0x03020c0cu : 0x0c0c0302u;
  asm volatile("" : "+v"(C.mp), "+v"(C.mo), "+v"(C.mf), "+v"(C.one), "+v"(C.sl), "+v"(C.su));
}
template <class F, class F2>
__device__ __forceinline__ void attention(const AttC& C, u32 qp, HaloMsg& hm, int ho, u32 tag, rsrc_t wrs, int oo, int fid, F mid, F2 mid2) {
  u32x4 vv, ow;
  u32x2 q0;
  f32x4 l0, o1;
  float inv;
#define SOFT(a_, b_, c_, d_, F1_, F2_, W_, V_, O_)                                                                                                      \
  F1_ "v_permlane16_swap_b32 v232, v233\n\tv_max_f32 v234, v232, v233\n\tv_max_f32 v235, v232, v233\n\t"                                                \
  F2_ "v_permlane32_swap_b32 v234, v235\n\tv_max_f32 v232, v234, v235\n\tv_mul_f32 v232, 0xbeb8aa3b, v232\n\t"                                          \
  "v_fmamk_f32 " a_ ", " a_ ", 0x3eb8aa3b, v232\n\tv_fmamk_f32 " b_ ", " b_ ", 0x3eb8aa3b, v232\n\t"                                                    \
  "v_fmamk_f32 " c_ ", " c_ ", 0x3eb8aa3b, v232\n\tv_fmamk_f32 " d_ ", " d_ ", 0x3eb8aa3b, v232\n\t"                                                    \
  "v_exp_f32 " a_ ", " a_ "\n\tv_exp_f32 " b_ ", " b_ "\n\tv_exp_f32 " c_ ", " c_ "\n\tv_exp_f32 " d_ ", " d_ "\n\t"                                    \
  "v_cvt_pk_bf16_f32 v232, " a_ ", " b_ "\n\tv_cvt_pk_bf16_f32 v233, " c_ ", " d_ "\n\t"                                                                \
  "v_perm_b32 v226, v232, v232, %[sl]\n\tv_perm_b32 v227, v232, v232, %[su]\n\t"                                                                        \
  "v_mfma_f32_16x16x16_bf16 v[236:239], %[one], v[232:233], 0\n\t"                                                                                      \
  "v_perm_b32 v224, v233, v233, %[sl]\n\tv_perm_b32 v225, v233, v233, %[su]\n\t" W_ "\n\ts_nop 0\n\t"                                                   \
  "v_mfma_f32_16x16x32_bf16 " O_ ", " V_ ", v[224:227], 0\n\t"
  asm volatile(
      "ds_read2st64_b32 v[214:215], %[qp] offset0:4 offset1:5\n\tds_read2st64_b32 v[216:217], %[qp] offset0:6 offset1:7\n\t"
      "ds_read2st64_b32 v[212:213], %[qp] offset0:0 offset1:1\n\tds_read2st64_b32 v[218:219], %[qp] offset0:10 offset1:11\n\t"
      "ds_read2st64_b32 v[200:201], %[qp] offset0:2 offset1:3\n\tds_read2st64_b32 v[202:203], %[qp] offset0:8 offset1:9\n\t"
      "s_waitcnt lgkmcnt(4)\n\tv_mfma_f32_16x16x16_bf16 v[220:223], v[216:217], v[214:215], %[mp]\n\t"
      "s_waitcnt lgkmcnt(3)\n\tv_mfma_f32_16x16x16_bf16 v[224:227], v[212:213], v[214:215], %[mo]\n\t"
      "s_waitcnt lgkmcnt(2)\n\tv_mfma_f32_16x16x16_bf16 v[228:231], v[216:217], v[218:219], %[mo]\n\t"
      "v_max_f32 v220, v220, v221\n\tv_max_f32 v221, v222, v223\n\tv_max_f32 v232, v220, v221\n\t"
      "v_max_f32 v222, v224, v225\n\tv_max_f32 v223, v226, v227\n\t"
      "v_max3_f32 v233, v232, v222, v223\n\tv_max3_f32 v232, v232, v222, v223\n\t"
      SOFT("v220", "v221", "v222", "v223", "s_nop 0\n\tv_max_f32 v228, v228, v229\n\t", "v_max_f32 v229, v230, v231\n\tv_max_f32 v230, v228, v229\n\t", "s_waitcnt lgkmcnt(0)", "v[200:203]", "v[244:247]")
      "v_rcp_f32 v248, v236"
      : "=&{v[200:203]}"(vv), "=&{v[218:219]}"(q0), "=&{v[228:231]}"(l0), "=&{v[244:247]}"(o1), "=&{v248}"(inv)
      : [qp] "v"(qp), [mp] "v"(C.mp), [mo] "v"(C.mo), [one] "v"(C.one), [sl] "v"(C.sl), [su] "v"(C.su)
      : "memory", "v212", "v213", "v214", "v215", "v216", "v217", "v220", "v221", "v222", "v223", "v224", "v225", "v226", "v227", "v232", "v233", "v234", "v235", "v236", "v237", "v238", "v239");
  mid();
  {
    u32 n = SPIN_CAP;
    asm volatile("1:\n\tv_bitop3_b32 v232, v209, v211, %[tg] bitop3:0x7e\n\tv_bitop3_b32 v233, v205, v207, %[tg] bitop3:0x7e\n\tv_or_b32 v232, v232, v233\n\tv_cmp_ne_u32 vcc, 0, v232\n\t"
                 "s_cbranch_vccz 2f\n\ts_sleep 2\n\tbuffer_load_dwordx4 v[208:211], %[ho], %[rs], 0 offen sc1\n\tbuffer_load_dwordx4 v[204:207], %[ho], %[rs], 0 offen offset:1024 sc1\n\t"
                 "s_sub_u32 %[n], %[n], 1\n\ts_waitcnt vmcnt(0)\n\ts_cbranch_scc0 1b\n2:"
                 : "+{v[208:211]}"(hm.k), "+{v[204:207]}"(hm.v), [n] "+s"(n) : [tg] "s"(tag), [ho] "v"(ho), [rs] "s"(wrs) : "memory", "vcc", "scc", "v232", "v233");
  }
  mid2();
  asm volatile(
      "v_mov_b32 v209, v210\n\tv_mov_b32 v205, v206\n\ts_nop 0\n\tv_mfma_f32_16x16x16_bf16 v[220:223], v[208:209], v[218:219], %[mf]\n\t"
      "v_mul_f32 v244, v244, v248\n\tv_mul_f32 v245, v245, v248\n\tv_mul_f32 v246, v246, v248\n\tv_mul_f32 v247, v247, v248\n\t"
      "v_cvt_pk_bf16_f32 v242, v244, v245\n\tv_cvt_pk_bf16_f32 v243, v246, v247\n\ts_nop 1\n\t"
      "v_max_f32 v220, v220, v221\n\tv_max_f32 v221, v222, v223\n\t"
      "v_max3_f32 v233, v230, v220, v221\n\tv_max3_f32 v232, v230, v220, v221\n\t"
      SOFT("v220", "v221", "v228", "v229", "s_nop 1\n\t", "s_nop 1\n\t", "s_nop 0", "v[202:205]", "v[244:247]")
      "v_rcp_f32 v248, v236\n\ts_nop 6\n\t"
      "v_mul_f32 v244, v244, v248\n\tv_mul_f32 v245, v245, v248\n\tv_mul_f32 v246, v246, v248\n\tv_mul_f32 v247, v247, v248\n\t"
      "v_cvt_pk_bf16_f32 v240, v244, v245\n\tv_cvt_pk_bf16_f32 v241, v246, v247\n\ts_nop 0\n\t"
      "v_permlane16_swap_b32 v240, v242\n\tv_permlane16_swap_b32 v241, v243"
      : "=&{v[240:243]}"(ow), "+{v[208:211]}"(hm.k), "+{v[204:207]}"(hm.v), "+{v[228:231]}"(l0), "+{v[244:247]}"(o1), "+{v248}"(inv)
      : "{v[200:203]}"(vv), "{v[218:219]}"(q0), [mf] "v"(C.mf), [one] "v"(C.one), [sl] "v"(C.sl), [su] "v"(C.su)
      : "v220", "v221", "v222", "v223", "v224", "v225", "v226", "v227", "v232", "v233", "v234", "v235", "v236", "v237", "v238", "v239");
  stw(wrs, oo, ow);
  asm volatile("s_nop 1" :: "v"(ow) : "memory");
}
__device__ __forceinline__ void o_dma(rsrc_t wrs, int og, int ot, int a, int lane) {
  dma4k(wrs, lane * 16, og + a * 4096, ot + a * 4096);
  dma4k(wrs, lane * 16, og + 16384 + a * 4096, ot + 16384 + a * 4096);
}
__device__ __forceinline__ void o_get(rsrc_t wrs, int vo, int g0, int g1, int ot, u32 sa, u32 mark, int fid) {
#pragma unroll 1
  for (unsigned spins = 0; spins <= SPIN_CAP; ++spins) {
    dma4k(wrs, vo, g0, ot);
    dma4k(wrs, vo, g1, ot + 16384);
    asm volatile("s_waitcnt vmcnt(4)" ::: "memory");
    if (__builtin_amdgcn_ballot_w64(*(const LAS u32*)(size_t)sa == mark) == 0ull) break;
    FAILC(fid);
  }
}
template <bool L1, class F>
__device__ __forceinline__ void wo_mma2(f32x4 (&acc)[2], char* smem, int ot, rsrc_t wrs, int og, int a, int lane, const bf16x8 (&wof)[16], u32 cnt, const f32x4 (&r0)[2], const u32 (&qa)[4], u32 sent, F late) {
  u32x4 of[32];
#define LDO2(s_) of[s_] = *(const LAS u32x4*)((const LAS char*)(size_t)qa[(s_) >> 3] + (ot - L_TA) + ((s_) & 1) * 16384 + (((s_) >> 1) & 3) * 1024)
#define WMM(s_) do { SB(); if ((s_) < 2) asm volatile("v_mfma_f32_16x16x32_bf16 %0, %1, %2, %3" : "=&v"(acc[(s_) & 1]) \
        : "v"(L1 ? __builtin_bit_cast(bf16x8, of[s_]) : wof[0]), "v"(L1 ? wof[0] : __builtin_bit_cast(bf16x8, of[s_])), "v"(r0[(s_) & 1])); \
      else if (L1) asm volatile("v_mfma_f32_16x16x32_bf16 %0, %1, %2, %0" : "+v"(acc[(s_) & 1]) : "v"(of[s_]), "v"(wof[(s_) >> 1])); \
      else asm volatile("v_mfma_f32_16x16x32_bf16 %0, %1, %2, %0" : "+v"(acc[(s_) & 1]) : "v"(wof[(s_) >> 1]), "v"(of[s_])); SB(); } while (0)
  u32 mx = 0, cv;
#pragma unroll
  for (int s = 0; s < 8; ++s) { LDO2(s); SB(); }
#pragma unroll
  for (int s = 0; s < 3; ++s) {
    WMM(s);
#pragma unroll
    for (int e = 3 * s; e < 3 * s + 3 && e < 8; ++e) mx = max(max(mx, of[e][1]), of[e][3]);
    asm volatile("" : "+v"(mx));
  }
  if (__builtin_expect(!__all(mx < 0xFF810000u), 0)) {
    for (unsigned spins = 0; spins <= SPIN_CAP; ++spins) {
      FAILC((L1 ? 5 : 13) + (ot == L_TB));
      __builtin_amdgcn_s_sleep(2);
      o_dma(wrs, og, ot, a, lane);
      asm volatile("s_waitcnt vmcnt(0)" ::: "memory");
      bool st = false;
#pragma unroll
      for (int s = 0; s < 8; ++s) { LDO2(s); st |= of[s][1] == sent || of[s][3] == sent; }
      if (!__any(st)) break;
    }
    WMM(0); WMM(1); WMM(2);
    asm volatile("s_nop 7" : "+v"(acc[0]), "+v"(acc[1]));
  }
  cnt_add(smem, C_OT, lane);
  cv = *(const volatile LAS u32*)(size_t)(u32)(L_MI + C_OT);
  SB();
#pragma unroll
  for (int s = 3; s < WS5; ++s) WMM(s);
  {
    u32 n = 1u << 24;
    asm volatile("1:\n\tv_readfirstlane_b32 vcc_lo, %0\n\ts_sub_u32 %1, %1, 1\n\ts_cbranch_scc1 2f\n\ts_cmp_ge_u32 vcc_lo, %3\n\ts_cbranch_scc1 2f\n\tds_read_b32 %0, %2\n\ts_waitcnt lgkmcnt(0)\n\ts_branch 1b\n2:"
                 : "+v"(cv), "+s"(n) : "v"(L_MI + C_OT), "s"(cnt) : "memory", "vcc", "scc");
  }
#pragma unroll
  for (int s = 8; s < WS5 + 8; ++s) { LDO2(s); SB(); }
#pragma unroll
  for (int s = WS5; s < 32; ++s) {
    WMM(s);
    if (s + 8 < 32) LDO2(s + 8);
    if (s == 24) late();
  }
  asm volatile("s_nop 7" : "+v"(acc[0]), "+v"(acc[1]));
}
template <class F>
__device__ __forceinline__ void wo_last(f32x4 (&acc)[2], char* smem, rsrc_t wrs, int og, int a, int lane, const bf16x8 (&wof)[16], const f32x4 (&r0)[2], const u32 (&qa)[4], u32 sent, F put) {
  u32x4 of[32];
#define LDO3(q_, o_, s_) of[s_] = *(const LAS u32x4*)((const LAS char*)(size_t)q_[((s_) >> 2) & 3] + (o_) + ((s_) >> 4) * 16384 + ((s_) & 3) * 1024)
#define WM3(s_) do { SB(); if (((s_) & 15) == 0) asm volatile("v_mfma_f32_16x16x32_bf16 %0, %1, %2, %3" : "=&v"(acc[(s_) >> 4]) : "v"(wof[0]), "v"(of[s_]), "v"(r0[(s_) >> 4])); \
      else asm volatile("v_mfma_f32_16x16x32_bf16 %0, %1, %2, %0" : "+v"(acc[(s_) >> 4]) : "v"(wof[(s_) & 15]), "v"(of[s_])); SB(); } while (0)
  u32 qb[4];
#pragma unroll
  for (int e = 0; e < 4; ++e) qb[e] = qa[e];
  SB();
  __builtin_amdgcn_s_barrier();
  ST(1, 19);
#pragma unroll 1
  for (u32 pass = 0;; ++pass) {
    asm volatile("" : "+v"(qb[0]), "+v"(qb[1]), "+v"(qb[2]), "+v"(qb[3]) :: "memory");
#pragma unroll
    for (int s = 0; s < WLD; ++s) { LDO3(qb, L_TB - L_TA, s); SB(); }
#pragma unroll
    for (int s = 0; s < 32; ++s) {
      WM3(s);
      if (s + WLD == 16) asm volatile("s_cmp_lg_u32 %0, 0\n\ts_cbranch_scc1 1f\n\ts_barrier\n1:" :: "s"(__builtin_amdgcn_readfirstlane(pass)) : "memory", "scc");
      if (s + WLD < 32) LDO3(qb, L_TB - L_TA, s + WLD);
    }
    asm volatile("s_nop 7" : "+v"(acc[0]), "+v"(acc[1]));
    put();
    SB();
    if (__builtin_expect(__builtin_amdgcn_ballot_w64(__builtin_isunordered(acc[0][0], acc[1][0])) == 0ull, 1)) break;
    asm volatile("s_waitcnt vmcnt(0)" ::: "memory");
    FAILC(14);
    if (pass == 0) {
      lds_barrier();
      const u32 fx = *(const u32*)(smem + L_MI + M_FX);
#pragma unroll
      for (int e = 0; e < 4; ++e) qb[e] -= ((fx >> (8 * ((a + e) & 3))) & 1u) * (L_TB - L_TA);
    } else {
      u32 m = 1;
#pragma unroll 1
      for (int s = 0; s < 32; ++s) {
        const u32x4 v = *(const LAS u32x4*)(size_t)((s & 8 ? (s & 4 ? qb[3] : qb[2]) : (s & 4 ? qb[1] : qb[0])) + (L_TB - L_TA) + (s >> 4) * 16384 + (s & 3) * 1024);
        m = min(min(m, v[1] ^ sent), v[3] ^ sent);
      }
      if (__builtin_amdgcn_ballot_w64(m == 0u) == 0ull || pass > SPIN_CAP) break;
      lds_barrier();
      o_dma(wrs, og, __builtin_amdgcn_readfirstlane(qb[0]) + (L_TB - L_TA) - a * 4096, a, lane);
      asm volatile("s_waitcnt vmcnt(0)" ::: "memory");
      lds_barrier();
    }
  }
}
struct WoPre { f32x4 r[2]; int so; u32 xs, ao; };
template <bool L1>
__device__ __forceinline__ void wo_pre(WoPre& P, const u32x2 (&res)[2], char* ws, int H, int rb, int hb, int a, int lane) {
  const int i16 = lane & 15, kg = lane >> 4, R0 = 1024 * H + 32 * rb;
#pragma unroll
  for (int t = 0; t < 2; ++t) P.r[t] = (f32x4){lo2f(res[t][0]), hi2f(res[t][0]), lo2f(res[t][1]), hi2f(res[t][1])};
  if (L1) {
    P.so = OFF_X2 + ((32 * hb + 8 * a + (i16 >> 1)) * XS_ + R0 + 16 * (lane & 1) + 4 * kg) * 4;
    P.xs = (u32)(L_XS + H * 512 + lane * 8);
    P.ao = (u32)(OFF_ST2 + ((R0 & ~255) + ((lane >> 1) & 3) * 64 + ((R0 >> 2) & 63) + 4 * (lane & 1) + kg) * 16 + (lane & 8));
    asm volatile("" : "+v"(P.r[0]), "+v"(P.r[1]), "+v"(P.so), "+v"(P.xs), "+v"(P.ao));
  } else {
    P.so = ((R0 + 16 * (kg & 1) + i16) * 256 + 32 * hb + 8 * a + 4 * (kg >> 1)) * 4;
    asm volatile("" : "+v"(P.r[0]), "+v"(P.r[1]), "+v"(P.so));
  }
}
template <bool L1>
__device__ __forceinline__ void wo_epi(const f32x4 (&acc)[2], const WoPre& P, char* smem, rsrc_t wrs, char* ws, u32* out, int H, int lane, u32 ONES) {
  if (L1) {
    f32x4 lo_, hi_;
    pair_x(lo_, hi_, acc[0], acc[1]);
    u32x4 v;
#pragma unroll
    for (int e = 0; e < 4; ++e) v[e] = pk2(lo_[e], hi_[e]);
    asm volatile("" : "+v"(v));
    stw(wrs, P.so, v);
    SB();
    float sv, q0, q1, q2, q3;
#define RD_(d_, a_, c_, m_) "v_add_f32_dpp " d_ ", " a_ ", " a_ " " c_ " row_mask:0xf bank_mask:" m_ "\n\t"
    asm volatile("v_dot2_f32_bf16 %5, %13, %17, 0\n\tv_dot2_f32_bf16 %6, %14, %17, 0\n\tv_dot2_f32_bf16 %7, %15, %17, 0\n\tv_dot2_f32_bf16 %8, %16, %17, 0\n\t"
                 "v_dot2_f32_bf16 %9, %13, %13, 0\n\tv_dot2_f32_bf16 %10, %14, %14, 0\n\tv_dot2_f32_bf16 %11, %15, %15, 0\n\tv_dot2_f32_bf16 %12, %16, %16, 0\n\t"
                 RD_("%1", "%5", "row_ror:8", "0x3") RD_("%2", "%6", "row_ror:8", "0x3") RD_("%3", "%7", "row_ror:8", "0x3") RD_("%4", "%8", "row_ror:8", "0x3")
                 RD_("%1", "%9", "row_ror:8", "0xc") RD_("%2", "%10", "row_ror:8", "0xc") RD_("%3", "%11", "row_ror:8", "0xc") RD_("%4", "%12", "row_ror:8", "0xc")
                 RD_("%5", "%1", "row_shl:4", "0x5") RD_("%6", "%2", "row_shl:4", "0x5") RD_("%5", "%3", "row_shr:4", "0xa") RD_("%6", "%4", "row_shr:4", "0xa")
                 "s_nop 0\n\t"
                 RD_("%1", "%5", "quad_perm:[2,3,0,1]", "0xf") RD_("%2", "%6", "quad_perm:[2,3,0,1]", "0xf")
                 "v_cndmask_b32_e64 %0, %1, %2, %18"
                 : "=&v"(sv), "=&v"(q0), "=&v"(q1), "=&v"(q2), "=&v"(q3), "=&v"(lo_[0]), "=&v"(lo_[1]), "=&v"(lo_[2]), "=&v"(lo_[3]), "=&v"(hi_[0]), "=&v"(hi_[1]), "=&v"(hi_[2]), "=&v"(hi_[3])
                 : "v"(v[0]), "v"(v[1]), "v"(v[2]), "v"(v[3]), "v"(ONES), "s"(0xCCCCCCCCCCCCCCCCull));
    const float hf = __builtin_floorf(sv * 0.5f);
    const u32 lo = (u32)(fmaf(sv, 0.5f, -hf) * 4294967296.0f);
    const u64 mine = ((u64)(u32)(int)hf << 32) | (u64)((lo & ~63u) | 1u);
    const u64 old = __hip_atomic_fetch_add((LAS u64*)(size_t)P.xs, mine, __ATOMIC_RELAXED, __HIP_MEMORY_SCOPE_WORKGROUP);
    if (((u32)old & 63u) == 3u) __hip_atomic_fetch_add((GAS u64*)((GAS char*)ws + P.ao), old + mine, __ATOMIC_RELAXED, __HIP_MEMORY_SCOPE_AGENT);
  } else {
    auto r0s = __builtin_amdgcn_permlane16_swap(pk2(acc[0][0], acc[0][1]), pk2(acc[1][0], acc[1][1]), false, false);
    auto r1s = __builtin_amdgcn_permlane16_swap(pk2(acc[0][2], acc[0][3]), pk2(acc[1][2], acc[1][3]), false, false);
    const rsrc_t ors = __builtin_amdgcn_make_buffer_rsrc((void*)out, 0, 2 * MB_, 0x00020000);
    if (H == 0) __builtin_amdgcn_raw_buffer_store_b128((u32x4){r0s[0], r1s[0], r0s[1], r1s[1]}, ors, P.so, 0, OUTA);
    else __builtin_amdgcn_raw_buffer_store_b128((u32x4){r0s[0], r1s[0], r0s[1], r1s[1]}, ors, P.so, 0, OUTB);
  }
}
template <bool L1>
__device__ __forceinline__ void res_ld(u32x2 (&res)[2], const char* smem, int tile, int hb, int a, int lane) {
#pragma unroll
  for (int t = 0; t < 2; ++t) {
    const int o = tile + t * 512 + (4 * hb + a) * 1024;
    if (L1) res[t] = *(const u32x2*)(smem + o + (lane & 15) * 32 + (lane >> 4) * 8);
    else res[t] = __builtin_bit_cast(u32x2, __builtin_amdgcn_ds_read_tr16_b64_v4i16((LAS bf16x4*)(size_t)(u32)(o + lane * 8)));
  }
}
template <bool L1>
__device__ __forceinline__ void a_blayer(char* smem, rsrc_t wrs, char* ws, u32* out, int t, int a, bf16x8 (&wof)[16], u32 tag, u32x4 wfl, ConvK& ck, XTake& xt, AttC& ac) {
  const u32 t9 = L1 ? 0u : KADD(2, now_(), D9);
  t = (int)opaque((u32)t);
  const int lane = t & 63, i16 = lane & 15, kg = lane >> 4;
  const int XIN = L1 ? OFF_X1 : OFF_X3, OBUF = L1 ? OFF_O1 : OFF_O2;
  const int bid = BIDX;
  const int rb = RB_OF(bid), hb = HB_OF(bid);
  if (!L1) { x_pre(xt, a, lane); SB(); x_take<0, 0, true>(xt, smem, wrs, XIN, rb, a, lane); SB(); }
  const int og = OBUF + rb * 32768;
  u32x2 resa[2], resb[2];
  const int wfo = OFF_ST0 + 8192 + (hb * 4 + a) * 128 + (lane & 3) * 16;
#define LDWO(e_) wof[e_] = __builtin_bit_cast(bf16x8, ldws(wrs, ((hb * 64 + a) * 64 + lane) * 16, OFF_WO + ((4 * a + (e_)) & 15) * 4096))
  auto ld_wo = [&](int e0, int e1) __attribute__((always_inline)) {
#pragma unroll
    for (int e = e0; e < e1; ++e) LDWO(e);
    __builtin_amdgcn_sched_barrier(0);
  };
  const u32 mark = x0_mark(tag), sent = L1 ? mark : 0xFFFFFFFFu;
  if (L1) {
    tags_wait(wfl, wrs, wfo, tag, 6); ld_wo(0, WOE);
    SB();
    x_pre(xt, a, lane); SB();
    x_take<0, WOE, true>(xt, smem, wrs, XIN, rb, a, lane, sent);
  }
  ST(!L1, 10);
  SB();
  lds_barrier();
  ST(!L1, 11);
  const u32 t5 = L1 ? (u32)__builtin_amdgcn_s_memrealtime() : KADD(20, now_(), D5), c5 = L1 ? opaque(KADD(18, now_(), DO5)) : 0u;
  res_ld<L1>(resa, smem, L_TA, hb, a, lane);
  auto pf_x3 = [&]() __attribute__((always_inline)) {
    const u32x4 SENT = {0xFFFFFFFFu, 0xFFFFFFFFu, 0xFFFFFFFFu, 0xFFFFFFFFu};
    const int xo = OFF_X3 + (8 * a + (lane >> 3)) * XBS + ((bid & 7) * 32 + (bid >> 3)) * 128 + (lane & 7) * 16;
    stw(wrs, xo, SENT); stw(wrs, xo + 32 * XBS, SENT);
  };
  const int oo = (int)opaque((u32)(OBUF + (((rb * 2 + (kg & 1)) * 16 + hb * 2 + (a >> 1)) * 64 + (2 * (a & 1) + (kg >> 1)) * 16 + i16) * 16));
  const u32 ONES = opaque(0x3F803F80u);
  att_pre(ac, lane, rb == 0);
  if (L1) pf_x3();
  if (L1) SB();
  const int ho = (int)opaque((u32)((rb == 0 ? WSB : KVO(L1, rb, hb * 4 + a)) + lane * 16));
  const u32 qp = opaque((u32)(L_QB + a * 3072 + lane * 4));
  const int tso = (int)opaque((u32)(L1 ? OFF_T5 + rb * 1024 + (lane & 7) * 128 : OFF_TS + rb * 128 + (lane & 7) * 8)), tsw = (int)opaque((u32)(t == hb ? tso : WSB));
  if (L1) __builtin_amdgcn_raw_buffer_store_b64((u32x2){t5, tag}, wrs, tsw, 0, 16);
  HaloMsg hm;
  SB();
  if (L1) KSLP(1, SLP_XB1); else gate(tmax(t9, t5));
  ST(!L1, 12);
  x_take<1, 0, false>(xt, smem, wrs, XIN, 32 + rb, a, lane, sent, [&]() __attribute__((always_inline)) { if (L1) ld_wo(WOE, WOE + WX); });
  ST(!L1, 13);
  if (L1) SB();
  lds_barrier();
  ST(!L1, 15);
  if (L1) KSLP(3, SLP_H1); else KSLP(4, SLP_H2);
  halo_ld(hm, wrs, ho);
  if (L1) { SB(); ld_wo(WOE + WX, WOE + WX + WA); }
  u32x2 ts;
  u32 t7 = L1 ? t5 : 0u, tq = 0;
  if (!L1) {
    t7 = (u32)__builtin_amdgcn_s_memrealtime();
    tq = KADD(6, now_(), DQ2);
    __builtin_amdgcn_raw_buffer_store_b64((u32x2){t7, tag}, wrs, tsw, 0, 16);
  }
  SB();
  attention(ac, qp, hm, ho, rb == 0 ? 0u : L1 ? mark : tag, wrs, oo, L1 ? 3 : 11, [&]() __attribute__((always_inline)) { if (L1) SB(); }, [&]() __attribute__((always_inline)) {
    if (L1) {
      asm volatile("buffer_load_dwordx2 %0, %1, %2, 0 offen sc1" : "=&{v[250:251]}"(ts) : "v"(tso), "s"(wrs) : "memory");
      ld_wo(WOE + WX + WA, WOE + WX + WA + WM);
    }
  });
  const u32 to = L1 ? KADD(5, now_(), DO1) : KADD(21, now_(), DO2);
  ST(!L1, 16);
  if (L1) __builtin_amdgcn_raw_buffer_store_b32(tag, wrs, (bid | t) == 0 ? OFF_EP : WSB, 0, 16);
  if (L1) ld_wo(WOE + WX + WA + WM, 16);
  res_ld<L1>(resb, smem, L_TB, hb, a, lane);
  cnt_add(smem, C_OB, lane);
  WoPre wp, wq;
  u32 qa[4];
#pragma unroll
  for (int e = 0; e < 4; ++e) qa[e] = opaque((u32)(L_TA + ((a + e) & 3) * 4096 + lane * 16));
  wo_pre<L1>(wp, resa, ws, 0, rb, hb, a, lane);
  wo_pre<L1>(wq, resb, ws, 1, rb, hb, a, lane);
  const u32 sa = opaque((u32)(L_TA + a * 4096 + lane * 64 + (lane & 14) * 4 + 4));
  SB();
  int d;
  auto lead = [&]() __attribute__((always_inline)) {
    asm volatile("v_subrev_u32 %0, %2, v250\n\tv_cmp_eq_u32 vcc, %3, v251\n\tv_min_i32 %0, 0x96, %0\n\tv_cndmask_b32 %0, 64, %0, vcc\n\t"
                 "s_nop 1\n\tv_max_i32_dpp %0, %0, %0 quad_perm:[1,0,3,2] row_mask:0xf bank_mask:0xf\n\ts_nop 1\n\tv_max_i32_dpp %0, %0, %0 quad_perm:[2,3,0,1] row_mask:0xf bank_mask:0xf\n\t"
                 "s_nop 1\n\tv_max_i32_dpp %0, %0, %0 row_half_mirror row_mask:0xf bank_mask:0xf\n\tv_max_i32 %0, 0, %0\n\tv_mul_u32_u24 %0, 20, %0"
                 : "=&v"(d), "+{v[250:251]}"(ts) : "s"(t7), "s"(tag) : "vcc", "memory");
  };
  if (L1) {
    asm volatile("s_waitcnt vmcnt(%0)" :: "n"(18 - WOE - WX - WA) : "memory");
    lead();
    gate(tmax(to, __builtin_amdgcn_readfirstlane(c5 + d)));
  } else gate(tmax(tq, to));
  ST(!L1, 17);
  if (!L1) asm volatile("buffer_load_dwordx2 %0, %1, %2, 0 offen sc1" : "=&{v[250:251]}"(ts) : "v"(tso), "s"(wrs) : "memory");
  o_get(wrs, lane * 16, og + a * 4096, og + 16384 + a * 4096, L_TA + a * 4096, sa, mark, L1 ? 21 : 23);
  if (!L1) {
    lead();
    *(int*)(smem + L_MI + M_DL) = d;
    cnt_add(smem, C_OV, lane);
  }
  u32 ob = 0;
  int ob0 = og + MB_ + a * 4096, ob1 = ob0 + 16384;
  if (L1) asm volatile("" : "+s"(ob0), "+s"(ob1));
  asm volatile("s_waitcnt vmcnt(0)" ::: "memory");
  f32x4 acc[2];
  wo_mma2<L1>(acc, smem, L_TA, wrs, og, a, lane, wof, L1 ? 4 : 12, wp.r, qa, mark, [&]() __attribute__((always_inline)) { if (L1) ob = *(const volatile LAS u32*)(size_t)(u32)(L_MI + C_OB); });
  ST(!L1, 18);
  wo_epi<L1>(acc, wp, smem, wrs, ws, out, 0, lane, ONES);
  if (L1) {
    cnt_add(smem, C_X2, lane);
    if (__builtin_expect((u32)__builtin_amdgcn_readfirstlane(ob) < 8u, 0)) cnt_spin(smem, C_OB, 8);
    dma4k(wrs, lane * 16, ob0, L_TB + a * 4096);
    dma4k(wrs, lane * 16, ob1, L_TB + 16384 + a * 4096);
    a_conv_pre(ck, a, lane);
    ST(0, 19);
    asm volatile("s_waitcnt vmcnt(0)" ::: "memory");
    wo_mma2<L1>(acc, smem, L_TB, wrs, og + MB_, a, lane, wof, 8, wq.r, qa, mark, []() {});
    wo_epi<L1>(acc, wq, smem, wrs, ws, out, 1, lane, ONES);
  } else wo_last(acc, smem, wrs, og + MB_, a, lane, wof, wq.r, qa, mark, [&]() __attribute__((always_inline)) { wo_epi<L1>(acc, wq, smem, wrs, ws, out, 1, lane, ONES); });
  ST(!L1, 20); if (L1) ST(1, 1);
}

#define WREGS(X) \
  X(0, 76, 77, 78, 79) X(1, 80, 81, 82, 83) X(2, 84, 85, 86, 87) X(3, 88, 89, 90, 91) X(4, 92, 93, 94, 95) X(5, 96, 97, 98, 99) \
  X(6, 100, 101, 102, 103) X(7, 104, 105, 106, 107) X(8, 108, 109, 110, 111) X(9, 112, 113, 114, 115) X(10, 116, 117, 118, 119) X(11, 120, 121, 122, 123) \
  X(12, 124, 125, 126, 127) X(13, 128, 129, 130, 131) X(14, 132, 133, 134, 135) X(15, 136, 137, 138, 139) X(16, 140, 141, 142, 143) X(17, 144, 145, 146, 147) \
  X(18, 148, 149, 150, 151) X(19, 152, 153, 154, 155) X(20, 156, 157, 158, 159) X(21, 160, 161, 162, 163) X(22, 164, 165, 166, 167) X(23, 168, 169, 170, 171) \
  X(24, 172, 173, 174, 175) X(25, 176, 177, 178, 179) X(26, 180, 181, 182, 183) X(27, 184, 185, 186, 187) X(28, 188, 189, 190, 191) X(29, 192, 193, 194, 195) \
  X(30, 196, 197, 198, 199) X(31, 200, 201, 202, 203) X(32, 204, 205, 206, 207) X(33, 208, 209, 210, 211) X(34, 212, 213, 214, 215) X(35, 216, 217, 218, 219) \
  X(36, 220, 221, 222, 223) X(37, 224, 225, 226, 227) X(38, 228, 229, 230, 231) X(39, 232, 233, 234, 235) X(40, 236, 237, 238, 239) X(41, 240, 241, 242, 243) \
  X(42, 244, 245, 246, 247) X(43, 248, 249, 250, 251) X(44, 252, 253, 254, 255) X(45, 64, 65, 66, 67) X(46, 68, 69, 70, 71) X(47, 72, 73, 74, 75)
__device__ __forceinline__ void w_dma(rsrc_t r, int voff, int vof2, int soff, int lds) {
  asm volatile("s_mov_b32 m0, %3\n\ts_nop 0\n\tbuffer_load_dwordx4 %0, %1, %2 offen lds\n\t"
               "s_add_u32 m0, m0, 3072\n\ts_nop 0\n\tbuffer_load_dwordx4 %0, %1, %2 offen offset:1024 lds\n\t"
               "s_add_u32 m0, m0, 3072\n\ts_nop 0\n\tbuffer_load_dwordx4 %4, %1, %2 offen offset:2048 lds"
               :: "v"(voff), "s"(r), "s"(soff), "s"(lds), "v"(vof2) : "memory", "scc");
}
template <int I>
__device__ __forceinline__ void w_ld(bf16x8& r, int vo, rsrc_t rs) {
#define X(n_, a_, b_, c_, d_) if constexpr (I == n_) asm volatile("buffer_load_dwordx4 %0, %1, %2, %3 offen offset:%4 sc1" : "={v[" #a_ ":" #d_ "]}"(r) : "v"(vo), "s"(rs), "s"(OFF_WQ + (I / 3) * 98304), "n"((I % 3) * 1024) : "memory");
  WREGS(X)
#undef X
}
template <int I, int O>
__device__ __forceinline__ void w_get(bf16x8& r, u32 a) {
#define X(n_, a_, b_, c_, d_) if constexpr (I == n_) asm volatile("ds_read_b64_tr_b16 v[" #a_ ":" #b_ "], %1 offset:%2\n\tds_read_b64_tr_b16 v[" #c_ ":" #d_ "], %1 offset:%3" : "=&{v[" #a_ ":" #d_ "]}"(r) : "v"(a), "n"(O), "n"(O + 2048) : "memory");
  WREGS(X)
#undef X
}
template <int I>
__device__ __forceinline__ void w_pin(const bf16x8& r) {
#define X(n_, a_, b_, c_, d_) if constexpr (I == n_) asm volatile("" :: "{v[" #a_ ":" #d_ "]}"(r));
  WREGS(X)
#undef X
}
template <int I>
__device__ __forceinline__ void w_sum(f32x4& d, const bf16x8& one, u32 idx, const bf16x8 (&wf)[48]) {
  w_pin<I>(wf[I]); w_pin<(I + 1) % 48>(wf[(I + 1) % 48]);
  asm volatile("v_smfmac_f32_16x16x64_bf16 %0, %1, v[%3:%3+7], %2" : "+v"(d) : "v"(one), "v"(idx), "n"(I < 45 ? 76 + 4 * I : 64 + 4 * (I - 45)));
  w_pin<I>(wf[I]); w_pin<(I + 1) % 48>(wf[(I + 1) % 48]);
}
struct GPre { u32 ap, l4, fr, fq, sp, wx, mo, oo, tg, ly; };
template <bool L1>
__device__ __forceinline__ void g_pre(GPre& P, int g, u32 tag) {
  const int lane = lane_id(), bid = BIDX;
  const int rb = RB_OF(bid), hb = HB_OF(bid);
  P.mo = (u32)KVO(L1, (rb + 1) & 31, hb * 4 + g);
  P.oo = (u32)((L1 ? OFF_O1 : OFF_O2) + MB_ + ((rb * 32 + hb * 2 + (g >> 1)) * 64 + 32 * (g & 1)) * 16);
  P.tg = L1 ? x0_mark(tag) : tag;
  P.ly = L1 ? 1 : 2;
  asm volatile("" : "+{s60}"(P.mo), "+{s61}"(P.oo), "+{s62}"(P.tg), "+{s63}"(P.ly));
  P.ap = (u32)(L_TA + lane * 8);
  P.l4 = (u32)(lane * 4);
  P.fr = (u32)(L_FS + g * 768 + (lane & 15) * 4);
  P.fq = (u32)(L_FS + g * 768 + (lane >> 4) * 16);
  P.sp = (u32)(L_SP + (lane & 31) * 16);
  P.wx = (u32)(L_WX + g * 3072 + lane * 16);
  asm volatile("" : "+{v0}"(P.ap), "+{v1}"(P.l4), "+{v2}"(P.fr), "+{v3}"(P.fq), "+{v4}"(P.sp), "+{v5}"(P.wx));
}
template <bool L1, class F>
__device__ __forceinline__ void g_blayer(char* smem, rsrc_t wrs, int g, u32 tag, bf16x8 (&wf)[48], GPre& P, F mid) {
  if (!L1) ST(1, 10);
  auto pin = [&](auto I_) __attribute__((always_inline)) { w_pin<decltype(I_)::value>(wf[decltype(I_)::value]); };
  static_for<0, 45>(pin);
#define G_OPS : "+{v0}"(P.ap), "+{v1}"(P.l4), "+{v2}"(P.fr), "+{v3}"(P.fq), "+{v4}"(P.sp), "+{v5}"(P.wx) \
  : "{s[56:59]}"(wrs), "{s60}"(P.mo), "{s61}"(P.oo), "{s62}"(P.tg), "{s63}"(P.ly), "{s64}"(L_QB + g * 3072), "{s65}"(L_FS + g * 768), "{s66}"(L_WX + g * 3072) \
  : "memory", "scc", "vcc", "v6", "v7", "v8", "v9", "v10", "v11", "v12", "v13", "v14", "v15", "v16", "v17", "v18", "v19", "v20", "v21", "v22", "v23", "v24", "v25", "v26", "v27", "v28", "v29", "v30", "v31", "v32", "v33", "v34", "v35", "v36", "v37", "v38", "v39", "v40", "v41", "v42", "v43", "v44", "v45", "v46", "v47", "v48", "v49", "v50", "v51", "v52", "v53", "v54", "v55", "v56", "v57", "v58", "v59", "v60", "v61", "v62", "v63", "v64", "v65", "v66", "v67", "v68", "v69", "v70", "v71", "v72", "v73", "v74", "v75", "s54", "s55", "s67", "s68", "s69", "s70", "s71", "s72", "s73", "s74", "s75", "s76", "s77"
  if constexpr (L1) asm volatile(".Lg_body:\n\t"
  "ds_read_b128 v[56:59], v5 offset:1024\n\tds_read_b128 v[40:43], v5 offset:2048\n\tds_read_b128 v[16:19], v5 offset:0\n\t"
  "s_waitcnt lgkmcnt(3)\n\ts_barrier\n\t" GST(11)
  "ds_read_b64_tr_b16 v[28:29], v0 offset:31232\n\tds_read_b64_tr_b16 v[30:31], v0 offset:32256\n\tds_read_b64_tr_b16 v[48:49], v0 offset:512\n\tds_read_b64_tr_b16 v[50:51], v0 offset:1536\n\t"
  "ds_read_b128 v[64:67], v4\n\tds_read_b128 v[72:75], v4 offset:768\n\tds_read_b64_tr_b16 v[36:37], v0 offset:2560\n\tds_read_b64_tr_b16 v[38:39], v0 offset:3584\n\t"
  "ds_read_b64_tr_b16 v[24:25], v0 offset:4608\n\tds_read_b64_tr_b16 v[26:27], v0 offset:5632\n\ts_mov_b32 s68, 0xaaaaaaaa\n\ts_mov_b32 s69, 0xaaaaaaaa\n\t"
  "s_mov_b32 s70, 0xf0000\n\ts_mov_b32 s71, 0xfff00ff\n\ts_mov_b32 s72, 0x3f0003\n\ts_mov_b32 s73, 0x3fff03ff\n\t"
  "s_mov_b32 s74, 0xfff0ffff\n\ts_mov_b32 s75, 0xf000ff00\n\ts_mov_b32 s76, 0xffc0fffc\n\ts_mov_b32 s77, 0xc000fc00\n\t"
  "s_mov_b32 s67, 0x0c0c0100\n\ts_mov_b32 s55, 0x0c0c0302\n\ts_waitcnt lgkmcnt(8)\n\t"
  "v_mfma_f32_16x16x32_bf16 v[12:15], v[56:59], v[28:31], 0\n\tds_read_b64_tr_b16 v[4:5], v0 offset:6656\n\t"
  "v_mfma_f32_16x16x32_bf16 v[56:59], v[28:31], v[40:43], 0\n\tds_read_b64_tr_b16 v[6:7], v0 offset:7680\n\t"
  "v_mfma_f32_16x16x32_bf16 v[68:71], v[16:19], v[28:31], 0\n\ts_waitcnt lgkmcnt(2)\n\t"
  "v_mfma_f32_16x16x32_bf16 v[12:15], v[80:83], v[48:51], v[12:15]\n\tds_read_b64_tr_b16 v[44:45], v0 offset:8704\n\tv_add_f32 v64, v64, v65\n\t"
  "v_mfma_f32_16x16x32_bf16 v[56:59], v[48:51], v[84:87], v[56:59]\n\tds_read_b64_tr_b16 v[46:47], v0 offset:9728\n\tv_add_f32 v66, v66, v67\n\t"
  "v_mfma_f32_16x16x32_bf16 v[68:71], v[76:79], v[48:51], v[68:71]\n\tv_add_f32 v72, v72, v73\n\tv_add_f32 v64, v64, v66\n\t"
  "v_mfma_f32_16x16x32_bf16 v[12:15], v[92:95], v[36:39], v[12:15]\n\tds_read_b64_tr_b16 v[40:41], v0 offset:10752\n\tv_add_f32 v74, v74, v75\n\t"
  "v_mfma_f32_16x16x32_bf16 v[56:59], v[36:39], v[96:99], v[56:59]\n\tds_read_b64_tr_b16 v[42:43], v0 offset:11776\n\tv_mul_f32 v65, 0x3b000000, v64\n\t"
  "v_mfma_f32_16x16x32_bf16 v[68:71], v[88:91], v[36:39], v[68:71]\n\tv_add_f32 v72, v72, v74\n\tv_mul_f32_e64 v66, -v65, v65\n\t"
  "v_mfma_f32_16x16x32_bf16 v[12:15], v[104:107], v[24:27], v[12:15]\n\tds_read_b64_tr_b16 v[8:9], v0 offset:12800\n\tv_fmac_f32 v66, 0x3b000000, v72\n\t"
  "v_mfma_f32_16x16x32_bf16 v[56:59], v[24:27], v[108:111], v[56:59]\n\tds_read_b64_tr_b16 v[10:11], v0 offset:13824\n\tv_max_f32 v66, 0, v66\n\t"
  "v_mfma_f32_16x16x32_bf16 v[68:71], v[100:103], v[24:27], v[68:71]\n\tv_add_f32 v66, 0x3727c5ac, v66\n\ts_waitcnt lgkmcnt(4)\n\t"
  "v_mfma_f32_16x16x32_bf16 v[12:15], v[116:119], v[4:7], v[12:15]\n\tds_read_b64_tr_b16 v[28:29], v0 offset:14848\n\tv_add_u32 v67, s65, v1\n\t"
  "v_mfma_f32_16x16x32_bf16 v[56:59], v[4:7], v[120:123], v[56:59]\n\tds_read_b64_tr_b16 v[30:31], v0 offset:15872\n\t"
  "v_mfma_f32_16x16x32_bf16 v[68:71], v[112:115], v[4:7], v[68:71]\n\tv_rsq_f32 v66, v66\n\t"
  "v_mfma_f32_16x16x32_bf16 v[12:15], v[128:131], v[44:47], v[12:15]\n\tds_read_b64_tr_b16 v[4:5], v0 offset:16896\n\tds_write2st64_b32 v67, v66, v65 offset1:1\n\t"
  "v_mfma_f32_16x16x32_bf16 v[56:59], v[44:47], v[132:135], v[56:59]\n\tds_read_b64_tr_b16 v[6:7], v0 offset:17920\n\tds_read_b128 v[24:27], v3 offset:576\n\t"
  "v_mfma_f32_16x16x32_bf16 v[68:71], v[124:127], v[44:47], v[68:71]\n\tds_read2_b32 v[44:45], v2 offset0:64 offset1:80\n\ts_waitcnt lgkmcnt(7)\n\t"
  "v_mfma_f32_16x16x32_bf16 v[12:15], v[140:143], v[40:43], v[12:15]\n\tds_read_b64_tr_b16 v[16:17], v0 offset:18944\n\tds_read_b128 v[36:39], v3 offset:256\n\t"
  "v_mfma_f32_16x16x32_bf16 v[56:59], v[40:43], v[144:147], v[56:59]\n\tds_read_b64_tr_b16 v[18:19], v0 offset:19968\n\tds_read_b128 v[32:35], v3 offset:512\n\t"
  "v_mfma_f32_16x16x32_bf16 v[68:71], v[136:139], v[40:43], v[68:71]\n\tds_read_b32 v47, v2 offset:640\n\t"
  "v_mfma_f32_16x16x32_bf16 v[12:15], v[152:155], v[8:11], v[12:15]\n\tds_read_b64_tr_b16 v[20:21], v0 offset:20992\n\t"
  "v_mfma_f32_16x16x32_bf16 v[56:59], v[8:11], v[156:159], v[56:59]\n\tds_read_b64_tr_b16 v[22:23], v0 offset:22016\n\t"
  "v_mfma_f32_16x16x32_bf16 v[68:71], v[148:151], v[8:11], v[68:71]\n\ts_waitcnt lgkmcnt(9)\n\t"
  "v_mfma_f32_16x16x32_bf16 v[12:15], v[164:167], v[28:31], v[12:15]\n\tds_read_b64_tr_b16 v[64:65], v0 offset:23040\n\ts_waitcnt lgkmcnt(8)\n\t"
  "v_mfma_f32_16x16x32_bf16 v[56:59], v[28:31], v[168:171], v[56:59]\n\tds_read_b64_tr_b16 v[66:67], v0 offset:24064\n\tv_mul_f32_e64 v52, -v44, v24\n\t"
  "v_mfma_f32_16x16x32_bf16 v[68:71], v[160:163], v[28:31], v[68:71]\n\tv_mul_f32_e64 v53, -v44, v25\n\tv_mul_f32_e64 v54, -v44, v26\n\t"
  "v_mfma_f32_16x16x32_bf16 v[12:15], v[176:179], v[4:7], v[12:15]\n\tds_read_b64_tr_b16 v[48:49], v0 offset:25088\n\tv_mul_f32_e64 v55, -v44, v27\n\t"
  "v_mfma_f32_16x16x32_bf16 v[56:59], v[4:7], v[180:183], v[56:59]\n\tds_read_b64_tr_b16 v[50:51], v0 offset:26112\n\ts_waitcnt lgkmcnt(5)\n\t"
  "v_mfma_f32_16x16x32_bf16 v[68:71], v[172:175], v[4:7], v[68:71]\n\tv_mul_f32_e64 v72, -v36, v47\n\tv_mul_f32_e64 v73, -v37, v47\n\t"
  "v_mfma_f32_16x16x32_bf16 v[12:15], v[188:191], v[16:19], v[12:15]\n\t"
  GST(20)
  "ds_read_b64_tr_b16 v[8:9], v0 offset:27136\n\tds_read2_b32 v[40:41], v2 offset0:0 offset1:16\n\t"
  "v_mfma_f32_16x16x32_bf16 v[56:59], v[16:19], v[192:195], v[56:59]\n\tds_read_b64_tr_b16 v[10:11], v0 offset:28160\n\tv_mul_f32_e64 v74, -v38, v47\n\t"
  "v_mfma_f32_16x16x32_bf16 v[68:71], v[184:187], v[16:19], v[68:71]\n\tv_mul_f32_e64 v75, -v39, v47\n\ts_waitcnt lgkmcnt(5)\n\t"
  "v_mfma_f32_16x16x32_bf16 v[12:15], v[200:203], v[20:23], v[12:15]\n\tds_read_b64_tr_b16 v[16:17], v0 offset:29184\n\tv_mul_f32_e64 v60, -v44, v32\n\t"
  "v_mfma_f32_16x16x32_bf16 v[56:59], v[20:23], v[204:207], v[56:59]\n\tds_read_b64_tr_b16 v[18:19], v0 offset:30208\n\tv_mul_f32_e64 v61, -v44, v33\n\t"
  "v_mfma_f32_16x16x32_bf16 v[68:71], v[196:199], v[20:23], v[68:71]\n\tv_mul_f32_e64 v62, -v44, v34\n\tv_mul_f32_e64 v63, -v44, v35\n\t"
  "v_mfma_f32_16x16x32_bf16 v[12:15], v[212:215], v[64:67], v[12:15]\n\tds_read_b64_tr_b16 v[36:37], v0 offset:0\n\t"
  "v_mfma_f32_16x16x32_bf16 v[56:59], v[64:67], v[216:219], v[56:59]\n\tds_read_b64_tr_b16 v[38:39], v0 offset:1024\n\t"
  "v_mfma_f32_16x16x32_bf16 v[68:71], v[208:211], v[64:67], v[68:71]\n\ts_waitcnt lgkmcnt(4)\n\t"
  "v_mfma_f32_16x16x32_bf16 v[12:15], v[224:227], v[48:51], v[12:15]\n\tds_read_b64_tr_b16 v[4:5], v0 offset:2048\n\t"
  "v_mfma_f32_16x16x32_bf16 v[56:59], v[48:51], v[228:231], v[56:59]\n\tds_read_b64_tr_b16 v[6:7], v0 offset:3072\n\tv_lshl_add_u32 v43, v1, 2, s60\n\t"
  "v_mfma_f32_16x16x32_bf16 v[68:71], v[220:223], v[48:51], v[68:71]\n\t"
  "v_mfma_f32_16x16x32_bf16 v[12:15], v[236:239], v[8:11], v[12:15]\n\tds_read_b64_tr_b16 v[20:21], v0 offset:4096\n\tds_read_b128 v[48:51], v3 offset:320\n\t"
  "v_mfma_f32_16x16x32_bf16 v[56:59], v[8:11], v[240:243], v[56:59]\n\tds_read_b64_tr_b16 v[22:23], v0 offset:5120\n\t"
  "v_mfma_f32_16x16x32_bf16 v[68:71], v[232:235], v[8:11], v[68:71]\n\tds_read_b128 v[8:11], v3 offset:64\n\ts_waitcnt lgkmcnt(6)\n\t"
  "v_mfma_f32_16x16x32_bf16 v[12:15], v[248:251], v[16:19], v[12:15]\n\tds_read_b64_tr_b16 v[28:29], v0 offset:6144\n\t"
  "v_mfma_f32_16x16x32_bf16 v[56:59], v[16:19], v[252:255], v[56:59]\n\tds_read_b64_tr_b16 v[30:31], v0 offset:7168\n\t"
  "v_mfma_f32_16x16x32_bf16 v[68:71], v[244:247], v[16:19], v[68:71]\n\tv_fma_f32 v12, -v45, v24, v12\n\tv_fma_f32 v13, -v45, v25, v13\n\t"
  "v_mfma_f32_16x16x32_bf16 v[52:55], v[80:83], v[36:39], v[52:55]\n\tds_read_b64_tr_b16 v[16:17], v0 offset:8192\n\tv_fma_f32 v14, -v45, v26, v14\n\t"
  "v_mfma_f32_16x16x32_bf16 v[72:75], v[36:39], v[84:87], v[72:75]\n\tds_read_b64_tr_b16 v[18:19], v0 offset:9216\n\tv_fma_f32 v15, -v45, v27, v15\n\t"
  "v_mfma_f32_16x16x32_bf16 v[60:63], v[76:79], v[36:39], v[60:63]\n\tv_mul_f32 v12, v12, v41\n\ts_waitcnt lgkmcnt(5)\n\t"
  "v_mfma_f32_16x16x32_bf16 v[52:55], v[92:95], v[4:7], v[52:55]\n\tds_read_b64_tr_b16 v[64:65], v0 offset:10240\n\tv_mul_f32 v13, v13, v41\n\t"
  "v_mfma_f32_16x16x32_bf16 v[72:75], v[4:7], v[96:99], v[72:75]\n\tds_read_b64_tr_b16 v[66:67], v0 offset:11264\n\tv_mul_f32 v14, v14, v41\n\t"
  "v_mfma_f32_16x16x32_bf16 v[60:63], v[88:91], v[4:7], v[60:63]\n\t"
  GST(17)
  "v_mul_f32 v15, v15, v41\n\tv_cvt_pk_bf16_f32 v12, v12, v13\n\t"
  "v_mfma_f32_16x16x32_bf16 v[52:55], v[104:107], v[20:23], v[52:55]\n\tds_read_b64_tr_b16 v[4:5], v0 offset:12288\n\tv_cvt_pk_bf16_f32 v14, v14, v15\n\t"
  "v_mfma_f32_16x16x32_bf16 v[72:75], v[20:23], v[108:111], v[72:75]\n\tds_read_b64_tr_b16 v[6:7], v0 offset:13312\n\tv_mov_b32 v13, s62\n\t"
  "v_mfma_f32_16x16x32_bf16 v[60:63], v[100:103], v[20:23], v[60:63]\n\tv_mov_b32 v15, s62\n\ts_waitcnt lgkmcnt(4)\n\t"
  "v_mfma_f32_16x16x32_bf16 v[52:55], v[116:119], v[28:31], v[52:55]\n\tds_read_b64_tr_b16 v[20:21], v0 offset:14336\n\tv_fma_f32 v56, -v48, v47, v56\n\t"
  "v_mfma_f32_16x16x32_bf16 v[72:75], v[28:31], v[120:123], v[72:75]\n\tds_read_b64_tr_b16 v[22:23], v0 offset:15360\n\tv_fma_f32 v57, -v49, v47, v57\n\t"
  "v_mfma_f32_16x16x32_bf16 v[60:63], v[112:115], v[28:31], v[60:63]\n\tv_fma_f32 v58, -v50, v47, v58\n\tv_fma_f32 v59, -v51, v47, v59\n\t"
  "v_mfma_f32_16x16x32_bf16 v[52:55], v[128:131], v[16:19], v[52:55]\n\tds_read_b64_tr_b16 v[36:37], v0 offset:16384\n\tv_mul_f32 v56, v56, v8\n\t"
  "v_mfma_f32_16x16x32_bf16 v[72:75], v[16:19], v[132:135], v[72:75]\n\tds_read_b64_tr_b16 v[38:39], v0 offset:17408\n\tv_mul_f32 v57, v57, v9\n\t"
  "v_mfma_f32_16x16x32_bf16 v[60:63], v[124:127], v[16:19], v[60:63]\n\tv_mul_f32 v58, v58, v10\n\ts_waitcnt lgkmcnt(4)\n\t"
  "v_mfma_f32_16x16x32_bf16 v[52:55], v[140:143], v[64:67], v[52:55]\n\tds_read_b64_tr_b16 v[16:17], v0 offset:18432\n\tv_mul_f32 v59, v59, v11\n\t"
  "v_mfma_f32_16x16x32_bf16 v[72:75], v[64:67], v[144:147], v[72:75]\n\tds_read_b64_tr_b16 v[18:19], v0 offset:19456\n\tv_cvt_pk_bf16_f32 v56, v56, v57\n\t"
  "v_mfma_f32_16x16x32_bf16 v[60:63], v[136:139], v[64:67], v[60:63]\n\tv_cvt_pk_bf16_f32 v58, v58, v59\n\tv_mov_b32 v57, s62\n\t"
  "v_mfma_f32_16x16x32_bf16 v[52:55], v[152:155], v[4:7], v[52:55]\n\tds_read_b64_tr_b16 v[8:9], v0 offset:20480\n\tv_mov_b32 v59, s62\n\t"
  "v_mfma_f32_16x16x32_bf16 v[72:75], v[4:7], v[156:159], v[72:75]\n\tds_read_b64_tr_b16 v[10:11], v0 offset:21504\n\tv_fma_f32 v68, -v45, v32, v68\n\t"
  "v_mfma_f32_16x16x32_bf16 v[60:63], v[148:151], v[4:7], v[60:63]\n\tv_add_u32 v46, s64, v1\n\ts_waitcnt lgkmcnt(4)\n\t"
  "v_mfma_f32_16x16x32_bf16 v[52:55], v[164:167], v[20:23], v[52:55]\n\tds_read_b64_tr_b16 v[48:49], v0 offset:22528\n\tv_fma_f32 v69, -v45, v33, v69\n\t"
  "v_mfma_f32_16x16x32_bf16 v[72:75], v[20:23], v[168:171], v[72:75]\n\tds_read_b64_tr_b16 v[50:51], v0 offset:23552\n\tv_fma_f32 v70, -v45, v34, v70\n\t"
  "v_mfma_f32_16x16x32_bf16 v[60:63], v[160:163], v[20:23], v[60:63]\n\tv_fma_f32 v71, -v45, v35, v71\n\tv_mul_f32 v68, v68, v41\n\t"
  "v_mfma_f32_16x16x32_bf16 v[52:55], v[176:179], v[36:39], v[52:55]\n\tds_read_b64_tr_b16 v[28:29], v0 offset:24576\n\tv_mul_f32 v69, v69, v41\n\t"
  "v_mfma_f32_16x16x32_bf16 v[72:75], v[36:39], v[180:183], v[72:75]\n\tds_read_b64_tr_b16 v[30:31], v0 offset:25600\n\tv_mul_f32 v70, v70, v41\n\t"
  "v_mfma_f32_16x16x32_bf16 v[60:63], v[172:175], v[36:39], v[60:63]\n\tv_mul_f32 v71, v71, v41\n\ts_waitcnt lgkmcnt(4)\n\t"
  "v_mfma_f32_16x16x32_bf16 v[52:55], v[188:191], v[16:19], v[52:55]\n\tds_read_b64_tr_b16 v[20:21], v0 offset:26624\n\tv_cvt_pk_bf16_f32 v68, v68, v69\n\t"
  "v_mfma_f32_16x16x32_bf16 v[72:75], v[16:19], v[192:195], v[72:75]\n\tds_read_b64_tr_b16 v[22:23], v0 offset:27648\n\tv_cvt_pk_bf16_f32 v70, v70, v71\n\t"
  "v_mfma_f32_16x16x32_bf16 v[60:63], v[184:187], v[16:19], v[60:63]\n\t"
  "v_mfma_f32_16x16x32_bf16 v[52:55], v[200:203], v[8:11], v[52:55]\n\t"
  GST(13)
  "ds_read_b64_tr_b16 v[36:37], v0 offset:28672\n\t"
  "v_mfma_f32_16x16x32_bf16 v[72:75], v[8:11], v[204:207], v[72:75]\n\ts_barrier\n\tds_read_b64_tr_b16 v[38:39], v0 offset:29696\n\t"
  "v_mfma_f32_16x16x32_bf16 v[60:63], v[196:199], v[8:11], v[60:63]\n\tbuffer_store_dwordx4 v[12:15], v43, s[56:59], 0 offen offset:0 sc1\n\ts_waitcnt lgkmcnt(4)\n\t"
  "v_mfma_f32_16x16x32_bf16 v[52:55], v[212:215], v[48:51], v[52:55]\n\tds_read_b64_tr_b16 v[64:65], v0 offset:30720\n\tbuffer_store_dwordx4 v[56:59], v43, s[56:59], 0 offen offset:1024 sc1\n\t"
  "v_mfma_f32_16x16x32_bf16 v[72:75], v[48:51], v[216:219], v[72:75]\n\tds_read_b64_tr_b16 v[66:67], v0 offset:31744\n\tv_lshl_add_u32 v4, v1, 2, s66\n\tv_add_u32 v0, 0x8000, v0\n\t"
  "v_mfma_f32_16x16x32_bf16 v[60:63], v[208:211], v[48:51], v[60:63]\n\tds_read_b128 v[42:45], v4 offset:1024\n\tds_read_b128 v[8:11], v4 offset:2048\n\t"
  "v_mfma_f32_16x16x32_bf16 v[52:55], v[224:227], v[28:31], v[52:55]\n\tds_read_b64_tr_b16 v[16:17], v0 offset:0\n\tds_read_b128 v[48:51], v4 offset:0\n\t"
  "v_mfma_f32_16x16x32_bf16 v[72:75], v[28:31], v[228:231], v[72:75]\n\tds_read_b64_tr_b16 v[18:19], v0 offset:1024\n\tv_min_u32 v41, 188, v1\n\t"
  "v_mfma_f32_16x16x32_bf16 v[60:63], v[220:223], v[28:31], v[60:63]\n\tds_read_b128 v[4:7], v3\n\ts_waitcnt lgkmcnt(8)\n\t"
  "v_mfma_f32_16x16x32_bf16 v[52:55], v[236:239], v[20:23], v[52:55]\n\tds_read_b64_tr_b16 v[28:29], v0 offset:2048\n\tv_lshlrev_b32 v41, 2, v41\n\t"
  "v_mfma_f32_16x16x32_bf16 v[72:75], v[20:23], v[240:243], v[72:75]\n\tds_read_b64_tr_b16 v[30:31], v0 offset:3072\n\tv_add_u32 v41, 0x22c00, v41\n\t"
  "v_mfma_f32_16x16x32_bf16 v[60:63], v[232:235], v[20:23], v[60:63]\n\t"
  "v_mfma_f32_16x16x32_bf16 v[52:55], v[248:251], v[36:39], v[52:55]\n\tds_read_b64_tr_b16 v[20:21], v0 offset:8192\n\t"
  "v_mfma_f32_16x16x32_bf16 v[72:75], v[36:39], v[252:255], v[72:75]\n\tds_read_b64_tr_b16 v[22:23], v0 offset:9216\n\t"
  "v_mfma_f32_16x16x32_bf16 v[60:63], v[244:247], v[36:39], v[60:63]\n\ts_waitcnt lgkmcnt(5)\n\t"
  "v_mfma_f32_16x16x32_bf16 v[52:55], v[42:45], v[64:67], v[52:55]\n\tds_read_b64_tr_b16 v[42:43], v0 offset:10240\n\t"
  "v_mfma_f32_16x16x32_bf16 v[72:75], v[64:67], v[8:11], v[72:75]\n\tds_read_b64_tr_b16 v[44:45], v0 offset:11264\n\t"
  "v_mfma_f32_16x16x32_bf16 v[60:63], v[48:51], v[64:67], v[60:63]\n\t"
  GST(12)
  "v_mul_f32 v52, v52, v40\n\tv_mul_f32 v53, v53, v40\n\t"
  "v_mfma_f32_16x16x32_bf16 v[8:11], v[80:83], v[16:19], 0\n\tds_read_b64_tr_b16 v[36:37], v0 offset:16384\n\tv_mul_f32 v54, v54, v40\n\t"
  "v_mfma_f32_16x16x32_bf16 v[48:51], v[16:19], v[84:87], 0\n\tds_read_b64_tr_b16 v[38:39], v0 offset:17408\n\tv_mul_f32 v55, v55, v40\n\t"
  "v_mfma_f32_16x16x32_bf16 v[64:67], v[76:79], v[16:19], 0\n\tv_cvt_pk_bf16_f32 v52, v52, v53\n\ts_waitcnt lgkmcnt(4)\n\t"
  "v_mfma_f32_16x16x32_bf16 v[8:11], v[92:95], v[28:31], v[8:11]\n\tds_read_b64_tr_b16 v[16:17], v0 offset:18432\n\tv_cvt_pk_bf16_f32 v54, v54, v55\n\t"
  "v_mfma_f32_16x16x32_bf16 v[48:51], v[28:31], v[96:99], v[48:51]\n\tds_read_b64_tr_b16 v[18:19], v0 offset:19456\n\tv_mul_f32 v60, v60, v40\n\t"
  "v_mfma_f32_16x16x32_bf16 v[64:67], v[88:91], v[28:31], v[64:67]\n\tv_mul_f32 v61, v61, v40\n\tv_mul_f32 v62, v62, v40\n\t"
  "v_mfma_f32_16x16x32_bf16 v[8:11], v[128:131], v[20:23], v[8:11]\n\tds_read_b64_tr_b16 v[28:29], v0 offset:24576\n\tds_write2st64_b32 v46, v12, v14 offset0:0 offset1:1\n\tds_write2st64_b32 v46, v52, v54 offset0:6 offset1:7\n\t"
  "v_mfma_f32_16x16x32_bf16 v[48:51], v[20:23], v[132:135], v[48:51]\n\tds_read_b64_tr_b16 v[30:31], v0 offset:25600\n\tds_write2st64_b32 v46, v56, v58 offset0:2 offset1:3\n\t"
  "v_mfma_f32_16x16x32_bf16 v[64:67], v[124:127], v[20:23], v[64:67]\n\tds_write2st64_b32 v46, v68, v70 offset0:4 offset1:5\n\tv_mul_f32 v63, v63, v40\n\tv_cvt_pk_bf16_f32 v60, v60, v61\n\t"
  "v_cvt_pk_bf16_f32 v62, v62, v63\n\ts_waitcnt lgkmcnt(8)\n\t"
  "v_mfma_f32_16x16x32_bf16 v[8:11], v[140:143], v[42:45], v[8:11]\n\tds_read_b64_tr_b16 v[12:13], v0 offset:26624\n\tds_write2st64_b32 v46, v60, v62 offset0:10 offset1:11\n\t"
  "v_mfma_f32_16x16x32_bf16 v[48:51], v[42:45], v[144:147], v[48:51]\n\tds_read_b64_tr_b16 v[14:15], v0 offset:27648\n\tv_mul_f32 v72, v72, v4\n\t"
  "v_mfma_f32_16x16x32_bf16 v[64:67], v[136:139], v[42:45], v[64:67]\n\tv_mul_f32 v73, v73, v5\n\tv_mul_f32 v74, v74, v6\n\tv_mul_f32 v75, v75, v7\n\t"
  "v_cvt_pk_bf16_f32 v72, v72, v73\n\tv_cvt_pk_bf16_f32 v74, v74, v75\n\t"
  "v_mfma_f32_16x16x32_bf16 v[8:11], v[176:179], v[36:39], v[8:11]\n\tds_write2st64_b32 v46, v72, v74 offset0:8 offset1:9\n\t"
  "v_mfma_f32_16x16x32_bf16 v[48:51], v[36:39], v[180:183], v[48:51]\n\t"
  "v_mfma_f32_16x16x32_bf16 v[64:67], v[172:175], v[36:39], v[64:67]\n\tv_and_b32 v44, 0x40, v1\n\ts_waitcnt lgkmcnt(4)\n\t"
  "v_mfma_f32_16x16x32_bf16 v[8:11], v[188:191], v[16:19], v[8:11]\n\tv_lshl_add_u32 v44, v44, 8, s61\n\tv_and_b32 v45, 0x3c, v1\n\t"
  "v_mfma_f32_16x16x32_bf16 v[48:51], v[16:19], v[192:195], v[48:51]\n\ts_waitcnt lgkmcnt(0)\n\ts_barrier\n\tds_read_b32 v62, v41 offset:5700\n\t"
  "ds_read_b128 v[72:75], v41\n\tds_read_b128 v[52:55], v41 offset:768\n\t"
  "v_mfma_f32_16x16x32_bf16 v[64:67], v[184:187], v[16:19], v[64:67]\n\tds_read_b64_tr_b16 v[40:41], v0 offset:4096\n\tds_read_b64_tr_b16 v[42:43], v0 offset:5120\n\t"
  "v_mfma_f32_16x16x32_bf16 v[8:11], v[224:227], v[28:31], v[8:11]\n\tds_read_b64_tr_b16 v[20:21], v0 offset:6144\n\tds_read_b64_tr_b16 v[22:23], v0 offset:7168\n\t"
  "v_mfma_f32_16x16x32_bf16 v[48:51], v[28:31], v[228:231], v[48:51]\n\tds_read_b64_tr_b16 v[36:37], v0 offset:12288\n\tv_lshl_add_u32 v44, v45, 2, v44\n\t"
  "v_mfma_f32_16x16x32_bf16 v[64:67], v[220:223], v[28:31], v[64:67]\n\tds_read_b64_tr_b16 v[38:39], v0 offset:13312\n\tv_and_b32 v45, 0x80, v1\n\t"
  "v_mfma_f32_16x16x32_bf16 v[8:11], v[236:239], v[12:15], v[8:11]\n\tds_read_b64_tr_b16 v[28:29], v0 offset:14336\n\tv_lshl_add_u32 v44, v45, 1, v44\n\t"
  "v_mfma_f32_16x16x32_bf16 v[48:51], v[12:15], v[240:243], v[48:51]\n\tds_read_b64_tr_b16 v[30:31], v0 offset:15360\n\t"
  "v_mfma_f32_16x16x32_bf16 v[64:67], v[232:235], v[12:15], v[64:67]\n\ts_waitcnt lgkmcnt(9)\n\ts_waitcnt lgkmcnt(6)\n\t"
  "v_mfma_f32_16x16x32_bf16 v[8:11], v[104:107], v[40:43], v[8:11]\n\tds_read_b64_tr_b16 v[68:69], v0 offset:20480\n\tv_readfirstlane_b32 s54, v62\n\t"
  "v_mfma_f32_16x16x32_bf16 v[48:51], v[40:43], v[108:111], v[48:51]\n\tds_read_b64_tr_b16 v[70:71], v0 offset:21504\n\tv_add_f32 v72, v72, v73\n\t"
  "v_mfma_f32_16x16x32_bf16 v[64:67], v[100:103], v[40:43], v[64:67]\n\ts_cmp_eq_u32 s54, s63\n\ts_cbranch_scc1 .Lg_redo\n.Lg_back:\n\ts_waitcnt lgkmcnt(5)\n\t"
  "v_mfma_f32_16x16x32_bf16 v[8:11], v[116:119], v[20:23], v[8:11]\n\tds_read_b64_tr_b16 v[56:57], v0 offset:22528\n\tv_add_f32 v74, v74, v75\n\t"
  "v_mfma_f32_16x16x32_bf16 v[48:51], v[20:23], v[120:123], v[48:51]\n\tds_read_b64_tr_b16 v[58:59], v0 offset:23552\n\tv_add_f32 v52, v52, v53\n\t"
  "v_mfma_f32_16x16x32_bf16 v[64:67], v[112:115], v[20:23], v[64:67]\n\tv_lshl_add_u32 v45, v1, 2, s66\n\ts_waitcnt lgkmcnt(4)\n\t"
  "v_mfma_f32_16x16x32_bf16 v[8:11], v[152:155], v[36:39], v[8:11]\n\tds_read_b64_tr_b16 v[16:17], v0 offset:30720\n\tv_add_f32 v72, v72, v74\n\t"
  "v_mfma_f32_16x16x32_bf16 v[48:51], v[36:39], v[156:159], v[48:51]\n\tds_read_b64_tr_b16 v[18:19], v0 offset:31744\n\tds_read_b128 v[20:23], v45 offset:1024\n\t"
  "v_mfma_f32_16x16x32_bf16 v[64:67], v[148:151], v[36:39], v[64:67]\n\tv_add_f32 v54, v54, v55\n\tv_mul_f32 v73, 0x3b000000, v72\n\t"
  "v_mfma_f32_16x16x32_bf16 v[8:11], v[164:167], v[28:31], v[8:11]\n\tds_read_b64_tr_b16 v[60:61], v0 offset:28672\n\tv_add_f32 v52, v52, v54\n\t"
  "v_mfma_f32_16x16x32_bf16 v[48:51], v[28:31], v[168:171], v[48:51]\n\tds_read_b64_tr_b16 v[62:63], v0 offset:29696\n\tv_mul_f32_e64 v74, -v73, v73\n\tv_fmac_f32 v74, 0x3b000000, v52\n\t"
  "v_max_f32 v74, 0, v74\n\tv_add_f32 v74, 0x3727c5ac, v74\n\t"
  "v_mfma_f32_16x16x32_bf16 v[64:67], v[160:163], v[28:31], v[64:67]\n\tv_rsq_f32 v74, v74\n\tv_add_u32 v75, s65, v1\n\tds_write2st64_b32 v75, v74, v73 offset1:1\n\t"
  "ds_read_b128 v[4:7], v45 offset:2048\n\ts_waitcnt lgkmcnt(6)\n\t"
  "v_mfma_f32_16x16x32_bf16 v[8:11], v[200:203], v[68:71], v[8:11]\n\tds_read_b64_tr_b16 v[12:13], v0 offset:49152\n\tds_read2_b32 v[72:73], v2 offset0:64 offset1:80\n\tds_read_b128 v[28:31], v45 offset:0\n\t"
  "v_mfma_f32_16x16x32_bf16 v[48:51], v[68:71], v[204:207], v[48:51]\n\tds_read_b64_tr_b16 v[14:15], v0 offset:49664\n\t"
  "v_mfma_f32_16x16x32_bf16 v[64:67], v[196:199], v[68:71], v[64:67]\n\tds_read_b64_tr_b16 v[52:53], v0 offset:50176\n\t"
  "v_mfma_f32_16x16x32_bf16 v[8:11], v[212:215], v[56:59], v[8:11]\n\tds_read_b64_tr_b16 v[54:55], v0 offset:50688\n\t"
  "v_mfma_f32_16x16x32_bf16 v[48:51], v[56:59], v[216:219], v[48:51]\n\tds_read_b64_tr_b16 v[68:69], v0 offset:51200\n\t"
  "v_mfma_f32_16x16x32_bf16 v[64:67], v[208:211], v[56:59], v[64:67]\n\tds_read_b64_tr_b16 v[70:71], v0 offset:51712\n\ts_waitcnt lgkmcnt(10)\n\t"
  "v_mfma_f32_16x16x32_bf16 v[8:11], v[20:23], v[16:19], v[8:11]\n\tds_read_b64_tr_b16 v[40:41], v0 offset:52224\n\ts_waitcnt lgkmcnt(9)\n\t"
  "v_mfma_f32_16x16x32_bf16 v[48:51], v[16:19], v[4:7], v[48:51]\n\tds_read_b64_tr_b16 v[42:43], v0 offset:52736\n\ts_waitcnt lgkmcnt(7)\n\t"
  "v_mfma_f32_16x16x32_bf16 v[64:67], v[28:31], v[16:19], v[64:67]\n\tds_read_b64_tr_b16 v[36:37], v0 offset:53248\n\tds_read_b128 v[16:19], v3 offset:256\n\t"
  "v_mfma_f32_16x16x32_bf16 v[8:11], v[248:251], v[60:63], v[8:11]\n\tds_read_b64_tr_b16 v[38:39], v0 offset:53760\n\t"
  "v_mfma_f32_16x16x32_bf16 v[48:51], v[60:63], v[252:255], v[48:51]\n\tds_read_b64_tr_b16 v[56:57], v0 offset:54272\n\t"
  "v_mfma_f32_16x16x32_bf16 v[64:67], v[244:247], v[60:63], v[64:67]\n\tds_read_b64_tr_b16 v[58:59], v0 offset:54784\n\ts_waitcnt lgkmcnt(8)\n\t"
  "v_mfma_f32_16x16x32_bf16 v[60:63], v[80:83], v[12:15], 0\n\t"
  GST(18)
  "ds_read_b64_tr_b16 v[28:29], v0 offset:55296\n\tv_fma_f32 v8, -v72, v24, v8\n\t"
  "v_mfma_f32_16x16x32_bf16 v[20:23], v[12:15], v[84:87], 0\n\tds_read_b64_tr_b16 v[30:31], v0 offset:55808\n\tv_fma_f32 v9, -v72, v25, v9\n\t"
  "v_mfma_f32_16x16x32_bf16 v[60:63], v[92:95], v[52:55], v[60:63]\n\tds_read_b64_tr_b16 v[4:5], v0 offset:56320\n\tv_fma_f32 v10, -v72, v26, v10\n\t"
  "v_mfma_f32_16x16x32_bf16 v[20:23], v[52:55], v[96:99], v[20:23]\n\tds_read_b64_tr_b16 v[6:7], v0 offset:56832\n\ts_waitcnt lgkmcnt(7)\n\t"
  "v_mfma_f32_16x16x32_bf16 v[60:63], v[104:107], v[68:71], v[60:63]\n\tds_read_b64_tr_b16 v[52:53], v0 offset:57344\n\tv_fma_f32 v11, -v72, v27, v11\n\t"
  "v_mfma_f32_16x16x32_bf16 v[20:23], v[68:71], v[108:111], v[20:23]\n\tds_read_b64_tr_b16 v[54:55], v0 offset:57856\n\tv_cvt_pk_bf16_f32 v74, v8, v9\n\t"
  "v_mfma_f32_16x16x32_bf16 v[60:63], v[116:119], v[40:43], v[60:63]\n\tds_read_b64_tr_b16 v[8:9], v0 offset:58368\n\tv_cvt_pk_bf16_f32 v75, v10, v11\n\t"
  "v_mfma_f32_16x16x32_bf16 v[20:23], v[40:43], v[120:123], v[20:23]\n\tds_read_b64_tr_b16 v[10:11], v0 offset:58880\n\ts_waitcnt lgkmcnt(7)\n\t"
  "v_mfma_f32_16x16x32_bf16 v[60:63], v[128:131], v[36:39], v[60:63]\n\tds_read_b64_tr_b16 v[68:69], v0 offset:59392\n\tv_fma_f32 v64, -v72, v32, v64\n\t"
  "v_mfma_f32_16x16x32_bf16 v[20:23], v[36:39], v[132:135], v[20:23]\n\tds_read_b64_tr_b16 v[70:71], v0 offset:59904\n\tv_fma_f32 v65, -v72, v33, v65\n\t"
  "v_mfma_f32_16x16x32_bf16 v[60:63], v[140:143], v[56:59], v[60:63]\n\tds_read_b64_tr_b16 v[40:41], v0 offset:60416\n\tv_fma_f32 v66, -v72, v34, v66\n\t"
  "v_mfma_f32_16x16x32_bf16 v[20:23], v[56:59], v[144:147], v[20:23]\n\tds_read_b64_tr_b16 v[42:43], v0 offset:60928\n\ts_waitcnt lgkmcnt(7)\n\t"
  "v_mfma_f32_16x16x32_bf16 v[60:63], v[152:155], v[28:31], v[60:63]\n\tds_read_b64_tr_b16 v[36:37], v0 offset:61440\n\tv_fma_f32 v67, -v72, v35, v67\n\t"
  "v_mfma_f32_16x16x32_bf16 v[20:23], v[28:31], v[156:159], v[20:23]\n\tds_read_b64_tr_b16 v[38:39], v0 offset:61952\n\tv_cvt_pk_bf16_f32 v28, v64, v65\n\t"
  "v_mfma_f32_16x16x32_bf16 v[60:63], v[164:167], v[4:7], v[60:63]\n\tds_read_b64_tr_b16 v[64:65], v0 offset:62464\n\tv_cvt_pk_bf16_f32 v29, v66, v67\n\t"
  "v_mfma_f32_16x16x32_bf16 v[20:23], v[4:7], v[168:171], v[20:23]\n\tds_read_b64_tr_b16 v[66:67], v0 offset:62976\n\ts_waitcnt lgkmcnt(7)\n\t"
  "v_mfma_f32_16x16x32_bf16 v[60:63], v[176:179], v[52:55], v[60:63]\n\tds_read_b64_tr_b16 v[56:57], v0 offset:63488\n\tv_fma_f32 v48, -v16, v47, v48\n\t"
  "v_mfma_f32_16x16x32_bf16 v[20:23], v[52:55], v[180:183], v[20:23]\n\tds_read_b64_tr_b16 v[58:59], v0 offset:64000\n\tv_fma_f32 v49, -v17, v47, v49\n\t"
  "v_mfma_f32_16x16x32_bf16 v[60:63], v[188:191], v[8:11], v[60:63]\n\tds_read_b64_tr_b16 v[52:53], v0 offset:64512\n\tv_lshl_add_u32 v31, v1, 2, s66\n\t"
  "v_mfma_f32_16x16x32_bf16 v[20:23], v[8:11], v[192:195], v[20:23]\n\tds_read_b64_tr_b16 v[54:55], v0 offset:65024\n\ts_waitcnt lgkmcnt(7)\n\t"
  "v_mfma_f32_16x16x32_bf16 v[60:63], v[200:203], v[68:71], v[60:63]\n\tds_read_b64_tr_b16 v[8:9], v0 offset:31232\n\tv_fma_f32 v50, -v18, v47, v50\n\t"
  "v_mfma_f32_16x16x32_bf16 v[20:23], v[68:71], v[204:207], v[20:23]\n\tds_read_b64_tr_b16 v[10:11], v0 offset:32256\n\tv_fma_f32 v51, -v19, v47, v51\n\t"
  "v_mfma_f32_16x16x32_bf16 v[60:63], v[212:215], v[40:43], v[60:63]\n\tv_cvt_pk_bf16_f32 v70, v48, v49\n\tv_cvt_pk_bf16_f32 v71, v50, v51\n\tds_read_b128 v[48:51], v31 offset:1024\n\t"
  "v_mfma_f32_16x16x32_bf16 v[20:23], v[40:43], v[216:219], v[20:23]\n\tds_read_b64_tr_b16 v[12:13], v0 offset:512\n\tds_read_b128 v[16:19], v31 offset:2048\n\ts_waitcnt lgkmcnt(8)\n\t"
  "v_mfma_f32_16x16x32_bf16 v[60:63], v[224:227], v[36:39], v[60:63]\n\tds_read_b64_tr_b16 v[14:15], v0 offset:1536\n\tds_read_b32 v68, v2 offset:384\n\tv_mul_f32_e64 v4, -v73, v24\n\t"
  "v_mul_f32_e64 v5, -v73, v25\n\tv_mul_f32_e64 v6, -v73, v26\n\t"
  "v_mfma_f32_16x16x32_bf16 v[20:23], v[36:39], v[228:231], v[20:23]\n\tv_mul_f32_e64 v7, -v73, v27\n\tds_read_b128 v[40:43], v31 offset:0\n\t"
  "v_mfma_f32_16x16x32_bf16 v[60:63], v[236:239], v[64:67], v[60:63]\n\tds_read_b64_tr_b16 v[36:37], v0 offset:2560\n\tv_mul_f32_e64 v30, -v73, v32\n\t"
  "v_mfma_f32_16x16x32_bf16 v[20:23], v[64:67], v[240:243], v[20:23]\n\tds_read_b64_tr_b16 v[38:39], v0 offset:3584\n\ts_waitcnt lgkmcnt(8)\n\t"
  "v_mfma_f32_16x16x32_bf16 v[60:63], v[248:251], v[56:59], v[60:63]\n\tv_mul_f32_e64 v31, -v73, v33\n\tv_mul_f32_e64 v32, -v73, v34\n\tv_mul_f32_e64 v33, -v73, v35\n\t"
  "v_mfma_f32_16x16x32_bf16 v[20:23], v[56:59], v[252:255], v[20:23]\n\tds_read_b64_tr_b16 v[64:65], v0 offset:4608\n\ts_waitcnt lgkmcnt(6)\n\t"
  "v_mfma_f32_16x16x32_bf16 v[60:63], v[48:51], v[52:55], v[60:63]\n\tds_read_b64_tr_b16 v[66:67], v0 offset:5632\n\tds_read2_b32 v[34:35], v2 offset0:0 offset1:16\n\t"
  "v_mfma_f32_16x16x32_bf16 v[20:23], v[52:55], v[16:19], v[20:23]\n\t"
  "v_mfma_f32_16x16x32_bf16 v[4:7], v[48:51], v[8:11], v[4:7]\n\t"
  GST(19)
  "ds_read_b64_tr_b16 v[56:57], v0 offset:6656\n\tv_and_or_b32 v46, v1, 4, v3\n\tds_read2_b32 v[72:73], v46 offset0:32 offset1:34\n\tds_read2_b32 v[52:53], v46 offset0:0 offset1:2\n\t"
  "v_mov_b32 v55, 0xff800000\n\ts_waitcnt lgkmcnt(7)\n\t"
  "v_mfma_f32_16x16x32_bf16 v[30:33], v[40:43], v[8:11], v[30:33]\n\tds_read_b64_tr_b16 v[58:59], v0 offset:7680\n\tv_cndmask_b32_e64 v54, v55, 0, s[70:71]\n\tv_cndmask_b32_e64 v45, v55, 0, s[72:73]\n\t"
  "v_cndmask_b32_e64 v2, v55, 0, s[74:75]\n\tv_cndmask_b32_e64 v55, v55, 0, s[76:77]\n\t"
  "v_mfma_f32_16x16x32_bf16 v[40:43], v[8:11], v[16:19], 0\n\tv_fma_f32 v60, -v68, v24, v60\n\tv_fma_f32 v61, -v68, v25, v61\n\t"
  "v_mfma_f32_16x16x32_bf16 v[4:7], v[80:83], v[12:15], v[4:7]\n\tds_read_b64_tr_b16 v[8:9], v0 offset:8704\n\tv_fma_f32 v62, -v68, v26, v62\n\t"
  "v_mfma_f32_16x16x32_bf16 v[30:33], v[76:79], v[12:15], v[30:33]\n\tds_read_b64_tr_b16 v[10:11], v0 offset:9728\n\tv_mfma_f32_16x16x16_bf16 v[48:51], v[74:75], v[28:29], 0\n\tv_fma_f32 v63, -v68, v27, v63\n\t"
  "v_cvt_pk_bf16_f32 v60, v60, v61\n\t"
  "v_mfma_f32_16x16x32_bf16 v[40:43], v[12:15], v[84:87], v[40:43]\n\tv_cvt_pk_bf16_f32 v61, v62, v63\n\ts_waitcnt lgkmcnt(3)\n\t"
  "v_mfma_f32_16x16x32_bf16 v[4:7], v[92:95], v[36:39], v[4:7]\n\tds_read_b64_tr_b16 v[12:13], v0 offset:10752\n\tv_mfma_f32_16x16x16_bf16 v[24:27], v[60:61], v[28:29], 0\n\tv_cndmask_b32_e64 v29, v48, v49, s[68:69]\n\t"
  "v_fma_f32 v29, v29, v52, v2\n\t"
  "v_mfma_f32_16x16x32_bf16 v[30:33], v[88:91], v[36:39], v[30:33]\n\tds_read_b64_tr_b16 v[14:15], v0 offset:11776\n\tv_cndmask_b32_e64 v28, v50, v51, s[68:69]\n\t"
  "v_mfma_f32_16x16x32_bf16 v[40:43], v[36:39], v[96:99], v[40:43]\n\tds_read_b128 v[16:19], v3 offset:384\n\tv_fma_f32 v28, v28, v53, v55\n\t"
  "v_mfma_f32_16x16x32_bf16 v[4:7], v[104:107], v[64:67], v[4:7]\n\tds_read_b64_tr_b16 v[36:37], v0 offset:12800\n\tv_max_f32 v60, v29, v28\n\t"
  "v_mfma_f32_16x16x32_bf16 v[30:33], v[100:103], v[64:67], v[30:33]\n\tds_read_b64_tr_b16 v[38:39], v0 offset:13824\n\tv_cndmask_b32_e64 v50, v24, v25, s[68:69]\n\t"
  "v_mfma_f32_16x16x32_bf16 v[40:43], v[64:67], v[108:111], v[40:43]\n\tv_fma_f32 v50, v50, v72, v54\n\ts_waitcnt lgkmcnt(5)\n\t"
  "v_mfma_f32_16x16x32_bf16 v[4:7], v[116:119], v[56:59], v[4:7]\n\tds_read_b64_tr_b16 v[64:65], v0 offset:14848\n\tv_cndmask_b32_e64 v48, v26, v27, s[68:69]\n\t"
  "v_mfma_f32_16x16x32_bf16 v[30:33], v[112:115], v[56:59], v[30:33]\n\tds_read_b64_tr_b16 v[66:67], v0 offset:15872\n\tv_fma_f32 v48, v48, v73, v45\n\t"
  "v_mfma_f32_16x16x32_bf16 v[40:43], v[56:59], v[120:123], v[40:43]\n\tv_lshlrev_b32 v51, 2, v1\n\tv_alignbit_b32 v49, s67, s67, v51\n\t"
  "v_mfma_f32_16x16x32_bf16 v[4:7], v[128:131], v[8:11], v[4:7]\n\tds_read_b64_tr_b16 v[24:25], v0 offset:16896\n\tv_alignbit_b32 v51, s55, s55, v51\n\t"
  "v_mfma_f32_16x16x32_bf16 v[30:33], v[124:127], v[8:11], v[30:33]\n\tds_read_b64_tr_b16 v[26:27], v0 offset:17920\n\tv_mov_b32 v58, 0x3f803f80\n\t"
  "v_mfma_f32_16x16x32_bf16 v[40:43], v[8:11], v[132:135], v[40:43]\n\tv_mov_b32 v59, 0x3f803f80\n\ts_waitcnt lgkmcnt(4)\n\t"
  "v_mfma_f32_16x16x32_bf16 v[4:7], v[140:143], v[12:15], v[4:7]\n\tds_read_b64_tr_b16 v[8:9], v0 offset:18944\n\tv_mul_f32 v34, 0x3eb8aa3b, v34\n\t"
  "v_mfma_f32_16x16x32_bf16 v[30:33], v[136:139], v[12:15], v[30:33]\n\tds_read_b64_tr_b16 v[10:11], v0 offset:19968\n\tv_max3_f32 v61, v60, v50, v48\n\t"
  "v_mfma_f32_16x16x32_bf16 v[40:43], v[12:15], v[144:147], v[40:43]\n\tv_mul_f32 v35, 0x3eb8aa3b, v35\n\tv_max3_f32 v60, v60, v50, v48\n\t"
  "v_mfma_f32_16x16x32_bf16 v[4:7], v[152:155], v[36:39], v[4:7]\n\tds_read_b64_tr_b16 v[12:13], v0 offset:20992\n\tv_fma_f32 v20, -v16, v47, v20\n\t"
  "v_mfma_f32_16x16x32_bf16 v[30:33], v[148:151], v[36:39], v[30:33]\n\tds_read_b64_tr_b16 v[14:15], v0 offset:22016\n\tv_fma_f32 v21, -v17, v47, v21\n\t"
  "v_mfma_f32_16x16x32_bf16 v[40:43], v[36:39], v[156:159], v[40:43]\n\tv_fma_f32 v22, -v18, v47, v22\n\ts_waitcnt lgkmcnt(4)\n\t"
  "v_mfma_f32_16x16x32_bf16 v[4:7], v[164:167], v[64:67], v[4:7]\n\tds_read_b64_tr_b16 v[16:17], v0 offset:23040\n\tv_fma_f32 v23, -v19, v47, v23\n\t"
  "v_mfma_f32_16x16x32_bf16 v[30:33], v[160:163], v[64:67], v[30:33]\n\tds_read_b64_tr_b16 v[18:19], v0 offset:24064\n\tv_cvt_pk_bf16_f32 v68, v20, v21\n\t"
  "v_mfma_f32_16x16x32_bf16 v[40:43], v[64:67], v[168:171], v[40:43]\n\tv_permlane16_swap_b32 v60, v61\n\t"
  "v_mfma_f32_16x16x32_bf16 v[4:7], v[176:179], v[24:27], v[4:7]\n\tds_read_b64_tr_b16 v[36:37], v0 offset:25088\n\tv_max_f32 v62, v60, v61\n\t"
  "v_mfma_f32_16x16x32_bf16 v[30:33], v[172:175], v[24:27], v[30:33]\n\tds_read_b64_tr_b16 v[38:39], v0 offset:26112\n\tv_max_f32 v63, v60, v61\n\t"
  "v_mfma_f32_16x16x32_bf16 v[40:43], v[24:27], v[180:183], v[40:43]\n\tds_read_b64_tr_b16 v[24:25], v0 offset:27136\n\ts_waitcnt lgkmcnt(5)\n\t"
  "v_mfma_f32_16x16x32_bf16 v[4:7], v[188:191], v[8:11], v[4:7]\n\tds_read_b64_tr_b16 v[26:27], v0 offset:28160\n\tv_cvt_pk_bf16_f32 v69, v22, v23\n\t"
  "v_mfma_f32_16x16x32_bf16 v[30:33], v[184:187], v[8:11], v[30:33]\n\tds_read_b64_tr_b16 v[20:21], v0 offset:29184\n\tds_read2_b32 v[56:57], v46 offset0:16 offset1:18\n\t"
  "v_mfma_f32_16x16x32_bf16 v[40:43], v[8:11], v[192:195], v[40:43]\n\tds_read_b64_tr_b16 v[22:23], v0 offset:30208\n\tv_permlane32_swap_b32 v62, v63\n\t"
  "v_mfma_f32_16x16x32_bf16 v[4:7], v[200:203], v[12:15], v[4:7]\n\tv_max_f32 v60, v62, v63\n\tv_mul_f32_e64 v60, -v60, v34\n\t"
  "v_mfma_f32_16x16x32_bf16 v[30:33], v[196:199], v[12:15], v[30:33]\n\tv_fma_f32 v50, v50, v34, v60\n\tv_fma_f32 v48, v48, v34, v60\n\t"
  "v_mfma_f32_16x16x32_bf16 v[40:43], v[12:15], v[204:207], v[40:43]\n\tv_fma_f32 v29, v29, v34, v60\n\ts_waitcnt lgkmcnt(5)\n\t"
  "v_mfma_f32_16x16x32_bf16 v[4:7], v[212:215], v[16:19], v[4:7]\n\tv_fma_f32 v28, v28, v34, v60\n\tds_read_b128 v[64:67], v3 offset:320\n\t"
  "v_mfma_f32_16x16x32_bf16 v[30:33], v[208:211], v[16:19], v[30:33]\n\tv_exp_f32 v50, v50\n\t"
  "v_mfma_f32_16x16x32_bf16 v[40:43], v[16:19], v[216:219], v[40:43]\n\tv_exp_f32 v48, v48\n\t"
  "v_mfma_f32_16x16x32_bf16 v[4:7], v[224:227], v[36:39], v[4:7]\n\tv_exp_f32 v29, v29\n\t"
  "v_mfma_f32_16x16x32_bf16 v[30:33], v[220:223], v[36:39], v[30:33]\n\tv_cvt_pk_bf16_f32 v60, v50, v48\n\ts_waitcnt lgkmcnt(1)\n\t"
  "v_mfma_f32_16x16x32_bf16 v[4:7], v[236:239], v[24:27], v[4:7]\n\tv_exp_f32 v28, v28\n\t"
  "v_mfma_f32_16x16x32_bf16 v[30:33], v[232:235], v[24:27], v[30:33]\n\tv_cvt_pk_bf16_f32 v61, v29, v28\n\tv_mul_f32 v50, v50, v72\n\t"
  "v_mfma_f32_16x16x32_bf16 v[4:7], v[248:251], v[20:23], v[4:7]\n\tv_mul_f32 v48, v48, v73\n\tv_mfma_f32_16x16x16_bf16 v[8:11], v[58:59], v[60:61], 0\n\tv_mul_f32 v29, v29, v52\n\t"
  "v_mul_f32 v28, v28, v53\n\t"
  "v_mfma_f32_16x16x32_bf16 v[30:33], v[244:247], v[20:23], v[30:33]\n\tv_cvt_pk_bf16_f32 v4, v4, v5\n\tv_cvt_pk_bf16_f32 v5, v6, v7\n\t"
  "v_mfma_f32_16x16x32_bf16 v[40:43], v[36:39], v[228:231], v[40:43]\n\tv_cvt_pk_bf16_f32 v50, v50, v48\n\tv_cvt_pk_bf16_f32 v29, v29, v28\n\t"
  "v_mfma_f32_16x16x32_bf16 v[40:43], v[24:27], v[240:243], v[40:43]\n\tv_cvt_pk_bf16_f32 v30, v30, v31\n\tv_cvt_pk_bf16_f32 v31, v32, v33\n\t"
  "v_mfma_f32_16x16x32_bf16 v[40:43], v[20:23], v[252:255], v[40:43]\n\t"
  GST(14)
  "v_perm_b32 v32, v50, v50, v49\n\tv_mfma_f32_16x16x16_bf16 v[12:15], v[4:5], v[30:31], 0\n\tv_mfma_f32_16x16x16_bf16 v[4:7], v[74:75], v[30:31], 0\n\ts_waitcnt lgkmcnt(0)\n\t"
  "v_fma_f32 v40, -v64, v47, v40\n\tv_fma_f32 v41, -v65, v47, v41\n\tv_fma_f32 v42, -v66, v47, v42\n\tv_cndmask_b32_e64 v28, v12, v13, s[68:69]\n\t"
  "v_fma_f32 v28, v28, v56, v2\n\tv_cndmask_b32_e64 v31, v14, v15, s[68:69]\n\tv_fma_f32 v31, v31, v57, v55\n\tv_max_f32 v64, v28, v31\n\t"
  "v_cndmask_b32_e64 v46, v4, v5, s[68:69]\n\tv_fma_f32 v46, v46, v52, v54\n\tv_cndmask_b32_e64 v34, v6, v7, s[68:69]\n\tv_fma_f32 v34, v34, v53, v45\n\t"
  "v_max3_f32 v65, v64, v46, v34\n\tv_max3_f32 v64, v64, v46, v34\n\tv_fma_f32 v43, -v67, v47, v43\n\tv_cvt_pk_bf16_f32 v72, v40, v41\n\t"
  "v_permlane16_swap_b32 v64, v65\n\tv_max_f32 v66, v64, v65\n\tv_max_f32 v67, v64, v65\n\tv_cvt_pk_bf16_f32 v73, v42, v43\n\t"
  "v_perm_b32 v33, v50, v50, v51\n\tv_permlane32_swap_b32 v66, v67\n\tv_max_f32 v64, v66, v67\n\tv_mul_f32_e64 v64, -v64, v35\n\t"
  "v_fma_f32 v46, v46, v35, v64\n\tv_fma_f32 v34, v34, v35, v64\n\tv_fma_f32 v28, v28, v35, v64\n\tv_fma_f32 v31, v31, v35, v64\n\t"
  "v_exp_f32 v46, v46\n\tv_exp_f32 v34, v34\n\tv_exp_f32 v28, v28\n\tv_exp_f32 v31, v31\n\t"
  "v_cvt_pk_bf16_f32 v64, v46, v34\n\tv_cvt_pk_bf16_f32 v65, v28, v31\n\tv_mul_f32 v46, v46, v52\n\tv_mul_f32 v34, v34, v53\n\t"
  "v_mfma_f32_16x16x16_bf16 v[40:43], v[58:59], v[64:65], 0\n\tv_mul_f32 v28, v28, v56\n\tv_mul_f32 v31, v31, v57\n\tv_cvt_pk_bf16_f32 v46, v46, v34\n\t"
  "v_cvt_pk_bf16_f32 v28, v28, v31\n\tv_perm_b32 v20, v46, v46, v49\n\tv_perm_b32 v34, v29, v29, v49\n\tv_perm_b32 v21, v46, v46, v51\n\t"
  "v_perm_b32 v35, v29, v29, v51\n\tv_perm_b32 v22, v28, v28, v49\n\tv_perm_b32 v23, v28, v28, v51\n\t"
  "v_mfma_f32_16x16x32_bf16 v[4:7], v[68:71], v[32:35], 0\n\tv_rcp_f32 v8, v8\n\t"
  "v_mfma_f32_16x16x32_bf16 v[24:27], v[70:73], v[20:23], 0\n\tv_rcp_f32 v40, v40\n\ts_nop 1\n\tv_mul_f32 v4, v4, v8\n\t"
  "v_mul_f32 v5, v5, v8\n\tv_mul_f32 v6, v6, v8\n\tv_mul_f32 v7, v7, v8\n\tv_cvt_pk_bf16_f32 v28, v4, v5\n\t"
  "v_mul_f32 v24, v24, v40\n\tv_mul_f32 v25, v25, v40\n\tv_mul_f32 v26, v26, v40\n\tv_mul_f32 v27, v27, v40\n\t"
  "v_cvt_pk_bf16_f32 v30, v24, v25\n\tv_cvt_pk_bf16_f32 v31, v26, v27\n\tv_cvt_pk_bf16_f32 v29, v6, v7\n\tv_permlane16_swap_b32 v28, v30\n\t"
  "s_nop 0\n\tv_permlane16_swap_b32 v29, v31\n\tbuffer_store_dwordx4 v[28:31], v44, s[56:59], 0 offen sc1\n\t"
  "s_cmp_eq_u32 s63, 2\n\ts_cbranch_scc1 .Lg_ret\n\ts_nop 0"
  G_OPS);
  else asm volatile("s_branch .Lg_body\n.Lg_ret:\n\ts_nop 0" G_OPS);
  static_for<0, 45>(pin);
  ST(!L1, 16);
  if (L1) {
    KSLP(7, SLP_OB1);
    cnt_add(smem, C_OB, 0);
    mid();
    cnt_wait(smem, C_X2, 4);
    return;
  }
  u32 t16 = KADD(8, now_(), DB2);
  cnt_add(smem, C_OB, 0);
  const int bid = BIDX, lane = lane_id();
  int og0 = OFF_O2 + MB_ + RB_OF(bid) * 32768 + g * 4096, og1 = og0 + 16384, vo = lane * 16;
  u32 sa = (u32)(L_TB + g * 4096 + lane * 64 + (lane & 14) * 4 + 4);
  asm volatile("" : "+s"(og0), "+s"(og1), "+v"(vo), "+v"(sa));
  u32 dl;
  for (unsigned n = 0; n < (1u << 24); ++n) {
    asm volatile("" ::: "memory");
    const u32 c0 = *(const volatile LAS u32*)(size_t)(u32)(L_MI + C_OV), c1 = *(const volatile LAS u32*)(size_t)(u32)(L_MI + C_OB);
    dl = *(const volatile LAS u32*)(size_t)(u32)(L_MI + M_DL);
    if ((u32)__builtin_amdgcn_readfirstlane(min(c0 + 11u, c1)) >= 16u) break;
  }
  gate(t16 + dl);
  ST(1, 15);
  const u32 mark = x0_mark(tag);
  o_get(wrs, vo, og0, og1, L_TB + g * 4096, sa, mark, 25);
  asm volatile("s_barrier\n\ts_waitcnt vmcnt(0)\n\ts_barrier" ::: "memory");
  if (__any(*(const LAS u32*)(size_t)(sa + 16384) == mark)) {
    FAILC(12);
    for (unsigned spins = 0; spins <= SPIN_CAP; ++spins) {
      dma4k(wrs, vo, og0, L_TA + g * 4096);
      dma4k(wrs, vo, og1, L_TA + 16384 + g * 4096);
      asm volatile("s_waitcnt vmcnt(0)" ::: "memory");
      if (!__any(*(const LAS u32*)(size_t)(sa - (L_TB - L_TA)) == mark || *(const LAS u32*)(size_t)(sa - (L_TB - L_TA) + 16384) == mark)) break;
    }
    smem[L_MI + M_FX + g] = 1;
    asm volatile("s_waitcnt lgkmcnt(0)" ::: "memory");
  }
}

using u32x8 = __attribute__((ext_vector_type(8))) unsigned;
using u32x16 = __attribute__((ext_vector_type(16))) unsigned;
__global__ __launch_bounds__(512, 2) KATTR void k_mega(const u16*, const u16*, const u16*, const u16*, const u16*, const u16*, const u16*, const u16*,
                                                       const u16*, const u16*, const u16*, char*, u32*) {
  extern __shared__ __attribute__((aligned(16))) char smem[];
  u32x16 ka;
  u32x8 kb;
  u32x2 kc;
  asm volatile("s_load_dwordx16 %0, %3, 0x0\n\ts_load_dwordx8 %1, %3, 0x40\n\ts_load_dwordx2 %2, %3, 0x60"
               : "=&s"(ka), "=&s"(kb), "=&s"(kc) : "s"(__builtin_amdgcn_kernarg_segment_ptr()) : "memory");
  const int tid = TIDX, lane = tid & 63;
  const int w = __builtin_amdgcn_readfirstlane(tid >> 6);
  const int bid = BIDX;
  ST_INIT(); ST(0, 0); ST(1, 0);
  if (w == 0) {
    u64 pc;
    int clen;
    asm volatile("s_getpc_b64 %0\n.Lcw_base:\n\ts_mov_b32 %1, .Lcw_end-.Lcw_base-4" : "=s"(pc), "=s"(clen));
    asm volatile("s_mov_b32 m0, %2\n\ts_nop 0\n\tglobal_load_lds_dword %0, %1" :: "v"(min(min(lane, 15) * 128 + (bid >> 3) * 2048, clen)), "s"(pc), "s"(L_FS) : "memory");
  }
  const int kp = (bid & 7) * 32 + (bid >> 3), hb = HB_OF(bid);
  const int c = (w >> 1) & 1, g = w & 3;
  u32 po = (u32)(kp * 512 + (tid & 255) * 2);
  const int pb = (bid & 7) + 8 * (bid >> 6), ps = (bid >> 3) & 7, gr = pb * 8 + ps;
  u32 xo = (u32)((w < 4 ? (pb * 64 + 16 * w) * 1024 + ps * 128 : gr * 8192 + g * 2048) + (lane & 7) * 16 + (lane >> 3) * (w < 4 ? 2048 : 128));
  asm volatile("" : "+v"(po), "+v"(xo) :: "memory");
  asm volatile("s_waitcnt lgkmcnt(0)" : "+s"(ka), "+s"(kb), "+s"(kc) :: "memory");
  ST_FIX();
#define KP(T_, v_, i_) ((T_)(((u64)v_[2 * (i_) + 1] << 32) | v_[2 * (i_)]))
  const u16 *x0 = KP(const u16*, ka, 0), *A_re = KP(const u16*, ka, 1), *A_im = KP(const u16*, ka, 2), *B_re = KP(const u16*, ka, 3), *B_im = KP(const u16*, ka, 4);
  const u16 *C_re = KP(const u16*, ka, 5), *C_im = KP(const u16*, ka, 6), *log_dt = KP(const u16*, ka, 7), *Dskip = KP(const u16*, kb, 0), *wqkv = KP(const u16*, kb, 1), *wo = KP(const u16*, kb, 2);
  char* ws = KP(char*, kb, 3);
  u32* out = KP(u32*, kc, 0);
#define LD_A() asm volatile("global_load_ushort v248, %0, %1\n\tglobal_load_ushort v249, %0, %2" :: "v"(po), "s"(A_re), "s"(A_im) : "memory", "v248", "v249")
  u32 ldtw = 0, dskw = 0;
  if (w < 4) {
    asm volatile("s_load_dword %0, %2, %4\n\ts_load_dword %1, %3, %4" : "=&s"(ldtw), "=&s"(dskw) : "s"(log_dt), "s"(Dskip), "s"(4 * kp) : "memory");
    LD_A();
  }
  asm volatile("global_load_dwordx4 v[240:243], %0, %1\n\tglobal_load_dwordx4 v[244:247], %0, %1 offset:1024\n\tglobal_load_dword v254, %2, %3 sc1"
               :: "v"(xo), "s"(x0), "v"(OFF_EP), "s"(ws) : "memory", "v240", "v254");
  if (w >= 4) {
    asm volatile("global_load_ushort v250, %0, %1\n\tglobal_load_ushort v251, %0, %2\n\tglobal_load_ushort v252, %0, %3\n\tglobal_load_ushort v253, %0, %4"
                 :: "v"(po), "s"(B_re), "s"(B_im), "s"(C_re), "s"(C_im) : "memory", "v250", "v253");
    LD_A();
  }
  __builtin_amdgcn_sched_barrier(0);
  const rsrc_t wrs = __builtin_amdgcn_make_buffer_rsrc((void*)ws, 0, WSB, 0x00020000);
  if (w >= 4) {
    asm volatile("s_mov_b32 m0, %2\n\ts_nop 0\n\tglobal_load_lds_dword %0, %1" :: "v"((bid * 16 + g * 4 + min(lane, 3)) * 128), "s"(wqkv), "s"(L_FS + w * 256) : "memory");
    u32x4 zz = {0, 0, 0, 0};
    asm volatile("" : "+v"(zz));
    const int z0 = (int)opaque((u32)(L_U + (lane >> 5) * 4608 + (lane & 31) * 16)), z1 = (int)opaque((u32)(L_MI + min((tid & 255) * 16 - 1024, 1088)));
    *(u32x4*)(smem + (g == 0 ? z0 : z1)) = zz;
    asm volatile("s_nop 1\n\ts_waitcnt lgkmcnt(0)" :: "v"(zz) : "memory");
  }
  if (w >= 4) asm volatile("s_load_dword %0, %1, %2" : "=s"(ldtw) : "s"(log_dt), "s"(4 * kp) : "memory");
  __builtin_amdgcn_sched_barrier(0);
#define XT_GET(x_) asm volatile("v_mov_b64 %0, v[240:241]\n\tv_mov_b64 %1, v[242:243]\n\tv_mov_b64 %2, v[244:245]\n\tv_mov_b64 %3, v[246:247]" \
                   : "=v"(x_[0].xy), "=v"(x_[0].zw), "=v"(x_[1].xy), "=v"(x_[1].zw) :: "memory")
  const int ftb = c ? L_TA : 0;
#define PUT(e_, r_, i_) *(LAS u32*)(wa + (e_) * FTS) = pkh(r_, i_)
#define CMR(a_, b_) fmaf(a_[0], b_[0], -(a_[1] * b_[1]))
#define CMI(a_, b_) fmaf(a_[0], b_[1], a_[1] * b_[0])
#define CMUL(a_, b_) (f32x2){CMR(a_, b_), CMI(a_, b_)}
  struct P0A { u32 gad[12], sad[8], ua, ur; };
  auto p0_addr = [&](P0A& A, int lane, int ut) __attribute__((always_inline)) {
    const int i = lane & 15, kk = lane >> 4;
    const u32 sb = (u32)((L_KT - KTO + c * TBS - 0x7E00) & 0xFFFF) * 0x10001u;
    const float f0 = __uint_as_float(opaque(__float_as_uint((float)((ut < 2 ? 1023 : 2047) - 512 * (w & 1) - 8 * lane))));
#pragma unroll
    for (int e = 0; e < 6; ++e) { const u32 ga = tb_addr(pk2(f0 - (float)(2 * e), f0 - (float)(2 * e + 1)), sb); A.gad[2 * e] = ga & 0xFFFFu; A.gad[2 * e + 1] = ga >> 16; }
    if (ut < 2) {
      A.ua = (u32)(ftb + i * FTS + kk * 16); A.ur = A.ua + ut * 16 * FTS;
      const int a0 = 128 * (i >> 3) + (i & 7), a1 = i < 8 ? 256 + 128 * (i >> 2) + 2 * (i & 3) : 128 * (i >> 1) + 4 * (i & 1), a2 = min(1024 + 128 * i, 2064);
#pragma unroll
      for (int t = 0; t < 2; ++t) {
        const float st = (t && i >= 8) ? 16.f : 8.f;
        const float fb = (float)(t ? a2 : ut == 0 ? a0 : a1) + st * (float)(4 * kk);
        const u32 s0 = tb_addr(pk2(fb, fb + st), sb), s1 = tb_addr(pk2(fb + 2.f * st, fb + 3.f * st), sb);
        A.sad[4 * t] = s0 & 0xFFFFu; A.sad[4 * t + 1] = s0 >> 16; A.sad[4 * t + 2] = s1 & 0xFFFFu; A.sad[4 * t + 3] = s1 >> 16;
      }
      asm volatile("" : "+v"(A.ua), "+v"(A.ur), "+v"(A.sad[0]), "+v"(A.sad[1]), "+v"(A.sad[2]), "+v"(A.sad[3]), "+v"(A.sad[4]), "+v"(A.sad[5]), "+v"(A.sad[6]), "+v"(A.sad[7]));
    }
    asm volatile("" : "+v"(A.gad[0]), "+v"(A.gad[1]), "+v"(A.gad[2]), "+v"(A.gad[3]), "+v"(A.gad[4]), "+v"(A.gad[5]), "+v"(A.gad[6]), "+v"(A.gad[7]), "+v"(A.gad[8]), "+v"(A.gad[9]), "+v"(A.gad[10]));
    __builtin_amdgcn_sched_barrier(0);
  };
  auto k_unit = [&](const P0A& A, float dk, bool lag0) __attribute__((always_inline)) {
    f16x8 fr[8], fc[8];
    const f32x2 bd = *(const f32x2*)(smem + L_P0 + 512 + c * 8);
    __builtin_amdgcn_sched_barrier(0);
#pragma unroll
    for (int e = 0; e < 8; ++e) {
      fc[e] = *(const LAS f16x8*)(size_t)(A.ua + e * 64 + 41 * FTS);
      fr[e] = *(const LAS f16x8*)(size_t)(A.ur + e * 64);
      __builtin_amdgcn_sched_barrier(0);
    }
    f16x8 f2[8];
    if (!(w & 1)) {
#pragma unroll
      for (int e = 0; e < 8; ++e) { f2[e] = *(const LAS f16x8*)(size_t)(A.ua + e * 64 + 32 * FTS); __builtin_amdgcn_sched_barrier(0); }
    }
    const int ec = bexp(bd[0] + bd[1] + fabsf(dk));
    const float f0 = __uint_as_float((u32)max(127 + bexp(bd[0]) - ec, 0) << 23), f1 = __uint_as_float((u32)max(127 + bexp(bd[1]) - ec, 0) << 23);
    const float d0s = lag0 ? dk * __uint_as_float((u32)(268 - ec) << 23) : 0.f;
    auto unit = [&](const f16x8 (&fr)[8], u32x4 sa, float add) __attribute__((always_inline)) {
      f32x4 k0 = {0.f, 0.f, 0.f, 0.f}, k1 = k0;
#pragma unroll
      for (int e = 0; e < 4; ++e) k0 = __builtin_amdgcn_mfma_f32_16x16x32_f16(fc[e], fr[e], k0, 0, 0, 0);
#pragma unroll
      for (int e = 4; e < 8; ++e) k1 = __builtin_amdgcn_mfma_f32_16x16x32_f16(fc[e], fr[e], k1, 0, 0, 0);
      k0 *= f0; k0[0] += add;
      const u32 h01 = pkh(fmaf(k1[0], f1, k0[0]), fmaf(k1[1], f1, k0[1])), h23 = pkh(fmaf(k1[2], f1, k0[2]), fmaf(k1[3], f1, k0[3]));
      *(LAS u16*)((LAS char*)(size_t)sa[0] + KTO) = (u16)h01; *(LAS u16*)((LAS char*)(size_t)sa[1] + KTO) = (u16)(h01 >> 16);
      *(LAS u16*)((LAS char*)(size_t)sa[2] + KTO) = (u16)h23; *(LAS u16*)((LAS char*)(size_t)sa[3] + KTO) = (u16)(h23 >> 16);
    };
    if (!(w & 1)) {
      unit(fr, (u32x4){A.sad[0], A.sad[1], A.sad[2], A.sad[3]}, d0s);
      unit(f2, (u32x4){A.sad[4], A.sad[5], A.sad[6], A.sad[7]}, 0.f);
    } else {
      unit(fr, (u32x4){A.sad[0], A.sad[1], A.sad[2], A.sad[3]}, 0.f);
      *(float*)(smem + L_MI + 4 * c) = __uint_as_float((u32)(ec - 14) << 23);
    }
  };
  auto k_get = [&](u32 (&kh)[11], const P0A& A) __attribute__((always_inline)) {
#pragma unroll
    for (int e = 0; e < 11; ++e) kh[e] = *(const LAS u16*)((const LAS char*)(size_t)A.gad[e] + KTO);
    __builtin_amdgcn_sched_barrier(0);
  };
  auto k_copies = [&](const u32 (&kh)[11], int lane, bool low) __attribute__((always_inline)) {
    u32 kw[6];
#pragma unroll
    for (int e = 0; e < 5; ++e) kw[e] = __builtin_amdgcn_perm(kh[2 * e + 1], kh[2 * e], 0x05040100u);
    kw[5] = kh[10];
    if (low && (w & 1) && lane == 63) { kw[4] = 0; kw[5] = 0; }
    build_copies(smem, c, (w & 1) * 64 + lane + (low ? 128 : 0), kw);
    __builtin_amdgcn_sched_barrier(0);
  };
  __builtin_amdgcn_sched_barrier(0);
  int w2 = w;
  asm volatile("" : "+s"(w2));
  if (w < 4) {
  P0A pa;
  p0_addr(pa, lane, w & 1);
  LAS char* wa = (LAS char*)(size_t)opaque((u32)(ftb + (tid & 127) * 4));
  u32 tg;
  const bool lo = pb < 16;
  const int xc8 = lane >> 3, xrp = lane & 7;
  const int xts = lo ? 128 : XTS, xt2 = 2 * xts;
  const int xto = (int)opaque((u32)(lo ? OFF_X1 + (pb * 4 + w) * XBS + (ps * 32 + 4 * xc8) * 128 + xrp * 16
                                       : XTO(ps * 32 + 4 * xc8) + ((pb & 15) * 64 + 16 * w + 2 * xrp) * 8));
  auto xpub = [&]() __attribute__((always_inline)) {
    u32x4 q[4];
    u32x4 xt[2];
    __builtin_amdgcn_sched_barrier(0);
    asm volatile("s_waitcnt vmcnt(0)\n\tv_add_u32 %0, 1, v254" : "=v"(tg) :: "memory");
    XT_GET(xt);
    const int sl = ((lane & 7) * 8 + (lane >> 3)) * 4;
#pragma unroll
    for (int e = 0; e < 8; ++e) xt[e & 1][e >> 1] = (u32)__builtin_amdgcn_ds_bpermute(sl, (int)xt[e & 1][e >> 1]);
#pragma unroll
    for (int e = 0; e < 4; ++e) q[e] = (u32x4){xt[0][e], x0_mark(tg), xt[1][e], x0_mark(tg)};
    asm volatile("" : "+v"(q[0]), "+v"(q[1]), "+v"(q[2]), "+v"(q[3]));
#pragma unroll
    for (int e = 0; e < 4; ++e) stw(wrs, xto + (e & 1) * xts + (e >> 1) * xt2, q[e]);
    asm volatile("s_nop 1" :: "v"(q[0]), "v"(q[1]), "v"(q[2]), "v"(q[3]) : "memory");
    ST(0, 3);
    __builtin_amdgcn_sched_barrier(0);
  };
  u32 ha, hz, hs, ht;
  C1Pre cp;
  int xs0, xs1, xl0, xl1, xv;
  u32 za;
  auto setup1 = [&]() __attribute__((always_inline)) {
    SB();
    const int t = (w & 1) * 64 + lane;
    ha = (u32)(L_KT + c * TBS - 0x7E00 + 2 * (0x44FF - t)); hz = ha - (t == 127 ? 2 : 0);
    hs = (lane & 1) ? 0x01000302u : 0x03020302u; ht = (lane & 1) ? 0x01000100u : 0x01000302u;
    asm volatile("" : "+v"(ha), "+v"(hz), "+v"(hs), "+v"(ht));
    SB();
  };
  auto setup2 = [&]() __attribute__((always_inline)) {
    SB();
    c1_pre(cp, kp, w, lane);
    xs0 = OFF_X1 + RB_OF(bid) * XBS + w * 8192; xs1 = xs0 + 4096; xl0 = L_TA + w * 8192; xl1 = xl0 + 4096; xv = x_lane(lane);
    za = (u32)(((lane >> 1) & 7) * CSTR + 4096 + (lane & 1) * 16);
    asm volatile("" : "+s"(xs0), "+s"(xs1), "+s"(xl0), "+s"(xl1), "+v"(xv), "+v"(za));
    SB();
  };
  {
    float ar, ai;
#define B0() do { SB(); __builtin_amdgcn_s_barrier(); SB(); } while (0)
    B0();
    asm volatile("s_waitcnt vmcnt(3)\n\tv_lshlrev_b32 %0, 16, v248\n\tv_lshlrev_b32 %1, 16, v249" : "=v"(ar), "=v"(ai) :: "memory");
    asm volatile("s_waitcnt lgkmcnt(0)" : "+s"(ldtw), "+s"(dskw) :: "memory");
    ST(0, 1);
    const float dt = rbf(__expf(c ? hi2f(ldtw) : lo2f(ldtw)));
    const float zr = dt * ar * 1.4426950408889634f, zi = dt * ai * 0.15915494309189535f;
    float cs = __builtin_amdgcn_exp2f(fmaxf(zr, 0.f) * -1928.f);
    f32x2 P[8], Q1, R[17];
    { float a_, b_; cexp2(zr, zi, a_, b_); P[1] = (f32x2){a_, b_}; cexp2(zr * 128.f, zi * 128.f, a_, b_); Q1 = (f32x2){a_, b_}; }
    asm volatile("" : "+v"(cs), "+v"(P[1]), "+v"(Q1));
    xpub();
    P[2] = CMUL(P[1], P[1]);
    const f32x2 Q2 = CMUL(Q1, Q1);
    R[1] = Q1 * cs; R[2] = Q2 * cs;
    P[3] = CMUL(P[2], P[1]); P[4] = CMUL(P[2], P[2]);
    const f32x2 Q4 = CMUL(Q2, Q2);
    R[3] = CMUL(R[1], Q2); R[4] = Q4 * cs;
    P[5] = CMUL(P[4], P[1]); P[6] = CMUL(P[3], P[3]); P[7] = CMUL(P[4], P[3]);
#pragma unroll
    for (int j = 5; j <= 16; ++j) R[j] = CMUL(R[j - 4], Q4);
    asm volatile("" : "+v"(R[13]), "+v"(R[14]), "+v"(R[15]), "+v"(R[16]), "+v"(P[7]), "+v"(P[6]), "+v"(P[5]));
    PUT(0, cs, 0.f); PUT(8, R[1][0], R[1][1]);
#pragma unroll
    for (int r = 1; r < 8; ++r) { PUT(r, P[r][0] * cs, P[r][1] * cs); PUT(8 + r, CMR(R[1], P[r]), CMI(R[1], P[r])); }
    asm volatile("" ::: "memory");
#pragma unroll
    for (int j = 2; j < 4; ++j) {
      PUT(16 + 4 * (j - 2), R[j][0], R[j][1]);
#pragma unroll
      for (int r = 1; r < 4; ++r) PUT(16 + 4 * (j - 2) + r, CMR(R[j], P[2 * r]), CMI(R[j], P[2 * r]));
    }
#pragma unroll
    for (int j = 4; j < 8; ++j) { PUT(24 + 2 * (j - 4), R[j][0], R[j][1]); PUT(25 + 2 * (j - 4), CMR(R[j], P[4]), CMI(R[j], P[4])); }
#pragma unroll
    for (int j = 8; j <= 16; ++j) PUT(24 + j, R[j][0], R[j][1]);
    __builtin_amdgcn_sched_barrier(0);
  }
  ST(0, 2);
  cnt_add(smem, C_F, lane);
  u32 stag = __builtin_amdgcn_readfirstlane(tg), g2;
  asm volatile("" : "+s"(stag));
  cnt_spin(smem, C_F, 8);
  k_unit(pa, c ? hi2f(dskw) : lo2f(dskw), (tid & 127) == 0);
  cnt_add(smem, C_A, lane);
  setup1();
  setup2();
  cnt_spin(smem, C_A, 4);
  ST(0, 4);
  {
    u32 kl[11];
    k_get(kl, pa);
    const u32 h0 = *(const LAS u16*)(size_t)ha, h1 = *(const LAS u16*)(size_t)(ha + 2), h2 = *(const LAS u16*)(size_t)hz;
    SB();
    u32x4 zz = {0, 0, 0, 0};
    asm volatile("" : "+v"(zz));
    *(LAS u32x4*)(size_t)za = zz;
    k_copies(kl, lane, true);
    asm volatile("" :: "v"(zz));
    cnt_add(smem, C_A, lane);
    ST(0, 5);
    {
      const u32 p = h0 | (h1 << 16), hh = __builtin_amdgcn_perm(p, p, 0x03020302u), ll = __builtin_amdgcn_perm(p, p, 0x01000100u);
      const u32 hx = __builtin_amdgcn_perm(p, p, hs), xl = __builtin_amdgcn_perm(p, p, ht);
      u32x4 c0 = {hh, hx, ll, ll}, c1 = {hh, xl, ll, ll}, c2 = {hx, ll, ll, ll}, c3 = {xl, ll, ll, h0 | (h2 << 16)};
      asm volatile("" : "+v"(c0), "+v"(c1), "+v"(c2), "+v"(c3));
      char* cop = smem + c * 4 * CSTR + ((w & 1) * 64 + lane) * 16;
      *(u32x4*)(cop + 0 * CSTR) = c0; *(u32x4*)(cop + 1 * CSTR) = c1; *(u32x4*)(cop + 2 * CSTR) = c2; *(u32x4*)(cop + 3 * CSTR) = c3;
      asm volatile("s_nop 1" :: "v"(c0), "v"(c1), "v"(c2), "v"(c3) : "memory");
      cnt_add(smem, C_KH, lane);
    }
  }
  cnt_spin(smem, C_A, 8);
  bf16x8 wof[16];
  u32x4 pfl, wfl;
  XTake xt;
  AttC ac;
  a_conv1(wrs, w, cp, pfl, wfl, g2);
  asm volatile("" : "+v"(pfl), "+v"(wfl));
  SB();
  KSLP(15, SLP_XA1);
  tags_wait(pfl, wrs, OFF_ST0 + tid * 16, stag, 19);
  dma4k(wrs, xv, xs0, xl0);
  dma4k(wrs, xv, xs1, xl1);
  KSLP(14, SLP_WE);
  SB();
  ConvK ck;
  a_blayer<true>(smem, wrs, ws, out, tid, w, wof, stag, wfl, ck, xt, ac);
  ST(1, 2);
  a_conv<false>(smem, wrs, RB_OF(bid), w, ck);
  a_blayer<false>(smem, wrs, ws, out, tid, w, wof, stag, wfl, ck, xt, ac);
  ST_OUT();
  }
  if (w2 >= 4) {
    const int lane = lane_id();
    LAS char* wa = (LAS char*)(size_t)opaque((u32)(ftb + (w & 1) * 256 + lane * 4));
    u32 tag;
    u32 st0, st1;
    int pbo;
    auto cols = [&]() __attribute__((always_inline)) {
    {
      float ar, ai, br, bi, cr, ci;
      asm volatile("v_lshlrev_b32 %0, 16, v248\n\tv_lshlrev_b32 %1, 16, v249\n\tv_lshlrev_b32 %2, 16, v250\n\t"
                   "v_lshlrev_b32 %3, 16, v251\n\tv_lshlrev_b32 %4, 16, v252\n\tv_lshlrev_b32 %5, 16, v253"
                   : "=v"(ar), "=v"(ai), "=v"(br), "=v"(bi), "=v"(cr), "=v"(ci) :: "memory");
      asm volatile("s_waitcnt lgkmcnt(0)" : "+s"(ldtw) :: "memory");
      const float dt = rbf(__expf(c ? hi2f(ldtw) : lo2f(ldtw)));
      const float dbr = dt * br, dbi = dt * bi;
      const float zr = dt * ar * 1.4426950408889634f, zi = dt * ai * 0.15915494309189535f;
      const float zp = fmaxf(zr, 0.f), g1 = __builtin_amdgcn_exp2f(zp * 1928.f);
      const float wr = (cr * dbr - ci * dbi) * g1, wi = (cr * dbi + ci * dbr) * g1;
      float bd = (fabsf(wr) + fabsf(wi)) * __builtin_amdgcn_exp2f(zp * 120.f);
      f32x2 S1, C[16];
      { float a_, b_; cexp2(zr * 8.f, zi * 8.f, a_, b_); S1 = (f32x2){a_, b_}; }
      bd = dpp_add<0x111, 0xf>(bd); bd = dpp_add<0x112, 0xf>(bd);
      const f32x2 S2 = CMUL(S1, S1);
      bd = dpp_add<0x114, 0xf>(bd); bd = dpp_add<0x118, 0xf>(bd);
      const f32x2 S4 = CMUL(S2, S2);
      bd = dpp_add<0x142, 0xa>(bd); bd = dpp_add<0x143, 0xc>(bd);
      bd = __int_as_float(__builtin_amdgcn_readlane(__float_as_int(bd), 63));
      *(float*)(smem + L_P0 + 512 + g * 4) = bd;
      const float sc = __uint_as_float((u32)(268 - bexp(bd)) << 23);
      C[0] = (f32x2){wr * sc, wi * sc};
      C[1] = CMUL(C[0], S1); C[2] = CMUL(C[0], S2); C[3] = CMUL(C[1], S2);
#pragma unroll
      for (int e = 4; e < 16; ++e) C[e] = CMUL(C[e - 4], S4);
#pragma unroll
      for (int e = 0; e < 16; ++e) *(LAS u32*)(wa + (41 + e) * FTS) = pkhn(C[e][0], C[e][1]);
    }
      cnt_add(smem, C_F, lane);
      ST(0, 2);
    };
    auto stats = [&]() __attribute__((always_inline)) {
    {
      const u32 ONES = opaque(0x3F803F80u);
      u32x4 xt[2];
      XT_GET(xt);
      float sm[2] = {0.f, 0.f}, sq[2] = {0.f, 0.f};
#pragma unroll
      for (int e = 0; e < 8; ++e) { sm[e & 1] = dot2acc(xt[e & 1][e >> 1], ONES, sm[e & 1]); sq[e & 1] = dot2acc(xt[e & 1][e >> 1], xt[e & 1][e >> 1], sq[e & 1]); }
      const bool b0 = lane & 1, b1 = lane & 2;
      const float v0 = (b0 ? sm[1] : sm[0]) + dpp_mov<DPP_XOR1>(b0 ? sm[0] : sm[1]), v1 = (b0 ? sq[1] : sq[0]) + dpp_mov<DPP_XOR1>(b0 ? sq[0] : sq[1]);
      float v = (b1 ? v1 : v0) + dpp_mov<DPP_XOR2>(b1 ? v0 : v1);
      v += dpp_mov<0x124>(v); v += dpp_mov<DPP_ROR8>(v);
      auto r = __builtin_amdgcn_permlane16_swap(__float_as_uint(v), __float_as_uint(v), false, false);
      v = __uint_as_float(r[0]) + __uint_as_float(r[1]);
      auto q = __builtin_amdgcn_permlane32_swap(__float_as_uint(v), __float_as_uint(v), false, false);
      v = (__uint_as_float(q[0]) + __uint_as_float(q[1])) * (1.f / 512.f);
      st0 = pack_stats(v, fmaxf(dpp_mov<DPP_XOR2>(v) - v * v, 0.f));
      st1 = dpp_u<DPP_XOR1>(st0);
    }
    };
    auto publish = [&]() __attribute__((always_inline)) {
      asm volatile("v_add_u32 %0, 1, v254" : "=v"(tag) :: "memory");
      stw(wrs, pbo, (u32x4){st0, tag, st1, tag});
      __builtin_amdgcn_sched_barrier(0);
    };
#define VMW(n_) asm volatile("s_waitcnt vmcnt(%0)" :: "n"(n_) : "memory")
    constexpr int PTN = 2;
    const int ut = 16 + (bid >> 3), us = ut / 3, um = ut % 3;
    const rsrc_t qrs = __builtin_amdgcn_make_buffer_rsrc((void*)wqkv, 0, 1572864, 0x00020000), ors = __builtin_amdgcn_make_buffer_rsrc((void*)wo, 0, 524288, 0x00020000);
    const int wv0 = (8 * g + (lane >> 3)) * 3072 + (((lane & 7) ^ ((lane >> 3) & 6)) << 4);
#define W_DMA1(r_, v_, s_, l_) asm volatile("s_mov_b32 m0, %3\n\ts_nop 0\n\tbuffer_load_dwordx4 %0, %1, %2 offen lds" :: "v"(v_), "s"(r_), "s"(s_), "s"(l_) : "memory")
    W_DMA1(qrs, wv0, us * 98304 + um * 1024 + (bid & 7) * 128, L_WR + g * 1024);
    const int hs = (3 - (int)(kb[2] >> 7)) & 7, xt_ = bid - 117, xs = (xt_ / 3) * 98304 + (xt_ % 3) * 1024;
    const bool xp = bid >= 128 && bid < 133;
    W_DMA1(xp ? qrs : ors, xp ? wv0 : ((bid >> 3) * 32 + 8 * g + (lane >> 3)) * 1024 + (((lane & 7) ^ (((lane >> 3) & 2) | ((g & 1) << 2))) << 4), xp ? xs + hs * 128 : (bid & 7) * 128, L_WR + 4096 + g * 1024);
    B0();
    const int ln = lane;
    pbo = (int)opaque((u32)(lane == 0 ? OFF_SG + gr * 64 + g * 16 : WSB));
    const int wvo = (int)opaque((u32)(wv0 + hb * 128)), wvx = (int)opaque((u32)(hb == hs ? WSB : wvo));
#define WSO(s_) ((s_) < 4 ? 40960 + (s_) * 12288 : L_WX - L_WS)
    auto wraw = [&](auto S_) __attribute__((always_inline)) { constexpr int s = decltype(S_)::value; w_dma(qrs, s < 4 ? wvo : wvx, s < 3 ? wvo : wvx, s * 98304, L_WS + WSO(s) + g * 1024); };
    __builtin_amdgcn_s_setprio(1);
    VMW(8 + PTN);
    ST(0, 1);
    stats();
    VMW(7 + PTN);
    publish();
    ST(0, 10);
    VMW(2 + PTN);
    cols();
    SB();
    __builtin_amdgcn_s_setprio(0);
    static_for<0, R1D>(wraw);
    VMW(3 * R1D + 1); cnt_add(smem, C_B, lane);
    SB();
    const u32 wga = opaque((u32)(L_WS + (ln >> 2) * 128 + (((2 * g + ((ln & 3) >> 1)) ^ ((ln >> 2) & 6)) << 4) + (ln & 1) * 8)), wgb = opaque(wga + 53248u);
    const u32 to = opaque(xp ? wga + (L_WR - L_WS + 4096) : (u32)(L_WR + 4096 + (8 * (lane >> 4) + ((lane >> 2) & 3)) * 128 + (((2 * g + ((lane & 3) >> 1)) ^ (((lane >> 2) & 2) | ((lane >> 2) & 4))) << 4) + (lane & 1) * 8));
    const u32 tp = opaque(to + (xp ? 2048 : 512));
    const int us0 = (int)opaque((u32)(OFF_WQ + us * 98304 + ((bid & 7) * 4 + g) * 3072 + um * 1024 + lane * 16));
    const int us1 = (int)opaque((u32)((bid < 128 ? OFF_WO + (((bid & 7) * 16 + (bid >> 3)) * 4 + g) * 1024 : xp ? OFF_WQ + xs + (hs * 4 + g) * 3072 : WSB) + lane * 16));
    cnt_spin(smem, C_B, 4);
    {
#define TR4(a_) __builtin_amdgcn_ds_read_tr16_b64_v4i16((LAS bf16x4*)(size_t)(a_))
      const bf16x4 q0 = TR4(wgb + (L_WR - L_WS - 53248)), q1 = TR4(wgb + (L_WR - L_WS - 53248 + 2048)), q2 = TR4(to), q3 = TR4(tp);
      cnt_add(smem, C_B, lane);
      stw(wrs, us0, __builtin_bit_cast(u32x4, __builtin_shufflevector(q0, q1, 0, 1, 2, 3, 4, 5, 6, 7)));
      stw(wrs, us1, __builtin_bit_cast(u32x4, __builtin_shufflevector(q2, q3, 0, 1, 2, 3, 4, 5, 6, 7)));
      SB();
    }
    static_for<R1D, R1F>(wraw);
    SB();
    const u32 stag = __builtin_amdgcn_readfirstlane(tag);
    const int fs0 = OFF_ST0 + 4096 + ((bid & 7) * 4 + g) * 128 + (bid >> 3) * 4, fs1 = bid < 128 ? fs0 + 4096 : bid < 136 ? OFF_ST0 + 8192 + (hs * 4 + g) * 128 + (bid - 112) * 4 : WSB;
    const int fso = (int)opaque((u32)(lane == 0 ? fs0 : lane == 1 ? fs1 : WSB));
    const int wbo = (int)opaque((u32)((hb * 4 + g) * 3072 + ln * 16));
    const int fo = (int)opaque((u32)(OFF_ST0 + 4096 + (hb * 4 + g) * 128 + (hb == hs && (ln & 8) ? 4096 + 64 + (ln & 1) * 16 : (ln & 7) * 16)));
    const int r0 = 256 * g + 2 * ln;
    const int sto = (int)opaque((u32)(OFF_SG + r0 * 8)), xto = (int)opaque((u32)(XTO(kp) + r0 * 8));
    const int xlo = (int)opaque((u32)(OFF_X1 + (16 * g + (ln >> 3)) * XBS + kp * 128 + (ln & 7) * 16));
    const u32 uw = opaque((u32)(L_U + (256 + r0) * 2)), xw = opaque((u32)(L_X + r0 * 4));
    const int mso = (int)opaque((u32)(ln == 0 ? OFF_ST0 + kp * 16 + g * 4 : WSB));
    const int p2o = (int)opaque((u32)(OFF_X2 + (kp * XS_ + 256 * g + 4 * ln) * 4)), pso = (int)opaque((u32)(ln < 2 ? OFF_ST2 + (512 * g + 2 * bid + ln) * 16 : WSB));
    SB();
    VMW(3 * (R1F - R1D));
    __builtin_amdgcn_raw_buffer_store_b32(tag, wrs, fso, 0, 16);
    ST(0, 6);
    SB();
    static_for<R1F, 5>(wraw);
    W_DMA1(qrs, wvx, 5 * 98304, L_SP + g * 1024);
    bf16x8 wf[48];
    GPre gp;
    {
      auto wget = [&](auto S_) __attribute__((always_inline)) {
        constexpr int s = decltype(S_)::value, o = WSO(s) >= 53248 ? WSO(s) - 53248 : WSO(s);
        w_get<3 * s, o>(wf[3 * s], WSO(s) >= 53248 ? wgb : wga);
        w_get<3 * s + 1, o + 4096>(wf[3 * s + 1], WSO(s) >= 53248 ? wgb : wga);
        w_get<3 * s + 2, o + 8192>(wf[3 * s + 2], WSO(s) >= 53248 ? wgb : wga);
      };
      const u32 mk = x0_mark(stag);
      auto wld = [&](auto I_) __attribute__((always_inline)) { w_ld<decltype(I_)::value>(wf[decltype(I_)::value], wbo, wrs); };
      u32x4 gs[4], gx[4];
#define G_LD(e_) do { gs[e_] = ldws(wrs, sto + ((e_) & 1) * 1024, ((e_) >> 1) * 8192); gx[e_] = (e_) < 2 ? ldws(wrs, xlo, (e_) * 8 * XBS) : ldw(wrs, xto + ((e_) - 2) * 1024); } while (0)
#define G_B2(v_, t_) opaque((v_[1] ^ (t_)) | (v_[3] ^ (t_)))
#define G_OK(h_) (__builtin_amdgcn_ballot_w64(((G_B2(gs[2 * (h_)], stag) | G_B2(gs[2 * (h_) + 1], stag)) | (G_B2(gx[2 * (h_)], mk) | G_B2(gx[2 * (h_) + 1], mk))) != 0u) == 0ull)
      auto take = [&](auto H_) __attribute__((always_inline)) {
        constexpr int H = decltype(H_)::value;
        if (__builtin_expect(!G_OK(H), 0)) {
          for (unsigned spins = 0;; ++spins) {
            FAILC(H ? 4 : 0);
            __builtin_amdgcn_s_sleep(SLP_U);
            asm volatile("" ::: "memory");
            G_LD(2 * H); G_LD(2 * H + 1);
            if (G_OK(H) || spins > SPIN_CAP) break;
          }
        }
#pragma unroll
        for (int e = 2 * H; e < 2 * H + 2; ++e) {
          const float rs0 = __builtin_amdgcn_rcpf(hi2f(gs[e][0])), rs1 = __builtin_amdgcn_rcpf(hi2f(gs[e][2])), n0 = -lo2f(gs[e][0]) * rs0, n1 = -lo2f(gs[e][2]) * rs1;
          const int r = (e & 1) * 128 + (e >> 1) * 1024;
          *(LAS u32*)(size_t)(uw + r * 2) = pkh(fmaf(lo2f(gx[e][0]), rs0, n0), fmaf(lo2f(gx[e][2]), rs1, n1));
          *(LAS u32*)(size_t)(uw + r * 2 + 4608) = pkh(fmaf(hi2f(gx[e][0]), rs0, n0), fmaf(hi2f(gx[e][2]), rs1, n1));
          *(LAS u32x2*)(size_t)(xw + r * 4) = (u32x2){gx[e][0], gx[e][2]};
        }
        __builtin_amdgcn_sched_barrier(0);
      };
      KSLP(16, SLP_G);
      asm volatile("" ::: "memory");
#pragma unroll
      for (int e = 0; e < 4; ++e) G_LD(e);
      __builtin_amdgcn_sched_barrier(0);
      ST(0, 5);
      f32x4 cs[3];
      u32x4 SENT = {0xFFFFFFFFu, 0xFFFFFFFFu, 0xFFFFFFFFu, 0xFFFFFFFFu};
      const u32 o1 = ((ln >> 1) & 1) == (ln >> 5) ? 0x3F803F80u : 0u;
      bf16x8 one8 = __builtin_bit_cast(bf16x8, (u32x4){o1, o1, o1, o1});
      u32 sidx = (ln & 1) ? 0xEEEEu : 0x4444u;
      cs[0] = cs[1] = cs[2] = (f32x4){0.f, 0.f, 0.f, 0.f};
      asm volatile("" : "+v"(one8), "+v"(sidx), "+v"(cs[0]), "+v"(cs[1]), "+v"(cs[2]), "+v"(SENT));
      auto wsum = [&](auto I_) __attribute__((always_inline)) { constexpr int i = 2 * decltype(I_)::value + 1; w_sum<i>(cs[i % 3], one8, sidx, wf); };
      cnt_spin(smem, C_B, 8);
      __builtin_amdgcn_s_setprio(1);
      VMW(8);
      cnt_add(smem, C_W, ln);
      u32x4 fl;
      asm volatile("" : "+v"(gs[0]), "+v"(gs[1]), "+v"(gx[0]), "+v"(gx[1]) :: "memory");
      take(IC<0>{});
      cnt_add(smem, C_U, ln);
      __builtin_amdgcn_raw_buffer_store_b32(tag, wrs, mso, 0, 16);
      ST(0, 3);
      stw(wrs, p2o, SENT); __builtin_amdgcn_raw_buffer_store_b128(SENT, wrs, p2o, 4096, 16);
      stw(wrs, pso, __builtin_bit_cast(u32x4, cs[0]));
      asm volatile("s_nop 1" :: "v"(SENT) : "memory");
      SB();
      auto rest = [&]() __attribute__((always_inline)) {
        cnt_spin(smem, C_W, 4);
        static_for<0, 5>(wget);
        w_get<15, L_SP - L_WS - 53248>(wf[15], wgb);
        cnt_add(smem, C_B, ln);
        take(IC<1>{});
        cnt_add(smem, C_P2, ln);
        ST(0, 4);
      };
      rest();
      { KSLP(17, SLP_FL); fl = ldw(wrs, fo); SB(); asm volatile("s_waitcnt lgkmcnt(0)" ::: "memory"); static_for<0, 7>(wsum); g_pre<true>(gp, g, stag); }
      asm volatile("" : "+v"(gs[2]), "+v"(gs[3]), "+v"(gx[2]), "+v"(gx[3]), "+v"(fl) :: "memory");
      tags_wait(fl, wrs, fo, stag, 15);
      ST(0, 7);
      asm volatile("s_cmp_lg_u32 %5, %6\n\ts_cbranch_scc1 1f\n\tbuffer_load_dwordx4 %0, %7, %8, %9 offen offset:2048 sc1\n\t"
                   "buffer_load_dwordx4 %1, %7, %8, %10 offen sc1\n\tbuffer_load_dwordx4 %2, %7, %8, %10 offen offset:1024 sc1\n\tbuffer_load_dwordx4 %3, %7, %8, %10 offen offset:2048 sc1\n\t"
                   "buffer_load_dwordx4 %4, %7, %8, %11 offen sc1\n1:"
                   : "+{v[120:123]}"(wf[11]), "+{v[124:127]}"(wf[12]), "+{v[128:131]}"(wf[13]), "+{v[132:135]}"(wf[14]), "+{v[136:139]}"(wf[15])
                   : "s"(hb), "s"(hs), "v"(wbo), "s"(wrs), "s"(OFF_WQ + 3 * 98304), "s"(OFF_WQ + 4 * 98304), "s"(OFF_WQ + 5 * 98304) : "memory", "scc");
      static_for<45, 48>(wld);
      static_for<16, 16 + H1>(wld);
      VMW(3 + H1);
      cnt_add(smem, C_P3, ln);
      static_for<16 + H1, 45>(wld);
      __builtin_amdgcn_s_setprio(0);
      ST(0, 9);
      cnt_spin(smem, C_B, 12);
      VMW(29);
#pragma unroll
      for (int e = 0; e < 3; ++e) *(LAS bf16x8*)(size_t)(gp.wx + e * 1024) = wf[45 + e];
      static_for<22, 24>(wsum);
      asm volatile("s_cmp_lg_u32 %4, %5\n\ts_cbranch_scc1 1f\n\tv_smfmac_f32_16x16x64_bf16 %0, %2, v[120:127], %3\n\tv_smfmac_f32_16x16x64_bf16 %1, %2, v[128:135], %3\n1:"
                   : "+v"(cs[2]), "+v"(cs[1]) : "v"(one8), "v"(sidx), "s"(hb), "s"(hs), "{v[120:123]}"(wf[11]), "{v[124:127]}"(wf[12]), "{v[128:131]}"(wf[13]), "{v[132:135]}"(wf[14]) : "scc");
      VMW(12); static_for<7, 16>(wsum);
      VMW(6); static_for<16, 19>(wsum);
      VMW(2); static_for<19, 21>(wsum);
      VMW(0); static_for<21, 22>(wsum);
      asm volatile("s_nop 7" : "+v"(cs[0]), "+v"(cs[1]), "+v"(cs[2]));
#pragma unroll
      for (int e = 0; e < 3; ++e) *(LAS float*)(size_t)(gp.fr + 512 + e * 64) = (cs[e][0] + cs[e][1]) + (cs[(e + 2) % 3][2] + cs[(e + 2) % 3][3]);
      asm volatile("" ::: "memory");
      ST(0, 8);
    }
    {
      ConvPre pa2, pb2;
      u32x4 xa, xb;
      int ln2, l2, xo2, xo, so;
      u32 ua, rp0, rp1;
      float isc;
      g_blayer<true>(smem, wrs, g, stag, wf, gp, [&]() __attribute__((always_inline)) {
        ln2 = lane_id();
        l2 = 256 * g + 4 * ln2;
        xo2 = (int)opaque((u32)(OFF_X3 + (8 * g + (ln2 >> 3)) * XBS + kp * 128 + (ln2 & 1) * 64 + (ln2 & 6) * 8));
        xo = (int)opaque((u32)(OFF_X2 + (kp * XS_ + l2) * 4)); so = (int)opaque((u32)(OFF_ST2 + (256 * g + ln2) * 16));
        ua = opaque((u32)(L_U + (256 + l2) * 2));
        rp0 = opaque((u32)(L_REDA + (ln2 & 1) * 8192 + (g * 64 + (ln2 ^ ((ln2 >> 3) & 3))) * 16)); rp1 = opaque(rp0 ^ 16u);
        isc = *(const float*)(smem + L_MI + 4 * (ln2 & 1));
        asm volatile("" : "+v"(isc));
      });
#define VM0() __builtin_amdgcn_s_waitcnt(0x0F70)
      KSLP(9, SLP_C2);
      u32x4 gg[4];
      ST(1, 2);
      xa = ldw(wrs, xo);
      asm volatile("" : "+v"(xa));
      if (__builtin_expect(!x2_ok(xa), 0)) { FAILC(16); __builtin_amdgcn_s_sleep(SLP_XP); xa = x2_ld(wrs, kp, l2); x2_poll(wrs, kp, l2, xa); VM0(); }
      KSLP(10, SLP_S);
      asm volatile("" ::: "memory");
#pragma unroll
      for (int e = 0; e < 4; ++e) gg[e] = ldw(wrs, so + e * 1024);
      SB();
      conv_unp(pa2, xa);
      asm volatile("" : "+v"(gg[0]), "+v"(gg[1]), "+v"(gg[2]), "+v"(gg[3]));
      if (__builtin_expect(!st2_ok(gg), 0)) { FAILC(7); __builtin_amdgcn_s_sleep(SLP_SP); st2_ld(wrs, l2, gg); st2_poll(wrs, l2, gg); VM0(); }
      conv2_in<false>(smem, ua, pa2, xa, gg);
      ST(1, 3);
      lds_barrier();
      ST(1, 7);
      conv_res(pa2);
      const int so2 = (int)opaque((u32)(so + 16384));
      auto rq_b = [&]() __attribute__((always_inline)) {
        KSLP(11, SLP_C3);
        asm volatile("" ::: "memory");
        xb = ldws(wrs, xo, 4096);
        SB();
#pragma unroll
        for (int e = 0; e < 4; ++e) gg[e] = ldw(wrs, so2 + e * 1024);
        SB();
      };
      rq_b();
      __builtin_amdgcn_s_barrier();
      asm volatile("" ::: "memory");
      ST(1, 8);
      stw(wrs, xo2, conv_epi(rp0, rp1, 0, isc, pa2));
      SB();
      asm volatile("" : "+v"(xb), "+v"(gg[0]), "+v"(gg[1]), "+v"(gg[2]), "+v"(gg[3]) :: "memory");
      ST(1, 4);
      if (__builtin_expect(!x2st_ok(xb, gg), 0)) {
        FAILC(18);
        xb = x2_ld(wrs, kp, l2 + 1024);
        st2_ld(wrs, l2 + 1024, gg);
        if (!x2_ok(xb)) {
          x2_poll(wrs, kp, l2 + 1024, xb);
          st2_ld(wrs, l2 + 1024, gg);
        }
        st2_poll(wrs, l2 + 1024, gg);
        VM0();
      }
      ST(1, 1);
      const int rb = RB_OF(bid);
      if (XPB == 2) { x_dma(wrs, OFF_X3, rb, L_TA, g, ln2); SB(); }
      conv2_in<true>(smem, ua + 2048, pb2, xb, gg);
      ST(1, 5);
      lds_barrier();
      ST(1, 6);
      if (XPB == 1) { KSLP(12, SLP_XA2); x_dma(wrs, OFF_X3, rb, L_TA, g, ln2); SB(); }
      conv_res(pb2);
      if (XPB == 0) { KSLP(12, SLP_XA2); x_dma(wrs, OFF_X3, rb, L_TA, g, ln2); }
      SB();
      g_pre<false>(gp, g, stag);
      const int xo3 = (int)opaque((u32)(xo2 + 32 * XBS));
      if (GPE) __builtin_amdgcn_s_setprio(GPE);
      asm volatile("s_waitcnt vmcnt(0)\n\ts_barrier" ::: "memory");
      ST(1, 9);
      u32x4 ov = conv_epi(rp0, rp1, 16384, isc, pb2);
      stw(wrs, xo3, ov);
      if (GPE) __builtin_amdgcn_s_setprio(0);
      asm volatile("s_nop 1" :: "v"(ov));
    }
    g_blayer<false>(smem, wrs, g, stag, wf, gp, []() {});

    ST_OUT();
  }
}

__device__ __attribute__((used, noinline)) void k_text_end() {
  asm volatile(
  ".Lg_redo:\n\ts_waitcnt lgkmcnt(0)\n\t"
  "ds_read_b64_tr_b16 v[20:21], v0 offset:0\n\tds_read_b64_tr_b16 v[22:23], v0 offset:1024\n\ts_waitcnt lgkmcnt(0)\n\t"
  "v_mfma_f32_16x16x32_bf16 v[8:11], v[80:83], v[20:23], 0\n\tv_mfma_f32_16x16x32_bf16 v[48:51], v[20:23], v[84:87], 0\n\tv_mfma_f32_16x16x32_bf16 v[64:67], v[76:79], v[20:23], 0\n\t"
  "ds_read_b64_tr_b16 v[20:21], v0 offset:2048\n\tds_read_b64_tr_b16 v[22:23], v0 offset:3072\n\ts_waitcnt lgkmcnt(0)\n\t"
  "v_mfma_f32_16x16x32_bf16 v[8:11], v[92:95], v[20:23], v[8:11]\n\tv_mfma_f32_16x16x32_bf16 v[48:51], v[20:23], v[96:99], v[48:51]\n\tv_mfma_f32_16x16x32_bf16 v[64:67], v[88:91], v[20:23], v[64:67]\n\t"
  "ds_read_b64_tr_b16 v[20:21], v0 offset:8192\n\tds_read_b64_tr_b16 v[22:23], v0 offset:9216\n\ts_waitcnt lgkmcnt(0)\n\t"
  "v_mfma_f32_16x16x32_bf16 v[8:11], v[128:131], v[20:23], v[8:11]\n\tv_mfma_f32_16x16x32_bf16 v[48:51], v[20:23], v[132:135], v[48:51]\n\tv_mfma_f32_16x16x32_bf16 v[64:67], v[124:127], v[20:23], v[64:67]\n\t"
  "ds_read_b64_tr_b16 v[20:21], v0 offset:10240\n\tds_read_b64_tr_b16 v[22:23], v0 offset:11264\n\ts_waitcnt lgkmcnt(0)\n\t"
  "v_mfma_f32_16x16x32_bf16 v[8:11], v[140:143], v[20:23], v[8:11]\n\tv_mfma_f32_16x16x32_bf16 v[48:51], v[20:23], v[144:147], v[48:51]\n\tv_mfma_f32_16x16x32_bf16 v[64:67], v[136:139], v[20:23], v[64:67]\n\t"
  "ds_read_b64_tr_b16 v[20:21], v0 offset:16384\n\tds_read_b64_tr_b16 v[22:23], v0 offset:17408\n\ts_waitcnt lgkmcnt(0)\n\t"
  "v_mfma_f32_16x16x32_bf16 v[8:11], v[176:179], v[20:23], v[8:11]\n\tv_mfma_f32_16x16x32_bf16 v[48:51], v[20:23], v[180:183], v[48:51]\n\tv_mfma_f32_16x16x32_bf16 v[64:67], v[172:175], v[20:23], v[64:67]\n\t"
  "ds_read_b64_tr_b16 v[20:21], v0 offset:18432\n\tds_read_b64_tr_b16 v[22:23], v0 offset:19456\n\ts_waitcnt lgkmcnt(0)\n\t"
  "v_mfma_f32_16x16x32_bf16 v[8:11], v[188:191], v[20:23], v[8:11]\n\tv_mfma_f32_16x16x32_bf16 v[48:51], v[20:23], v[192:195], v[48:51]\n\tv_mfma_f32_16x16x32_bf16 v[64:67], v[184:187], v[20:23], v[64:67]\n\t"
  "ds_read_b64_tr_b16 v[20:21], v0 offset:24576\n\tds_read_b64_tr_b16 v[22:23], v0 offset:25600\n\ts_waitcnt lgkmcnt(0)\n\t"
  "v_mfma_f32_16x16x32_bf16 v[8:11], v[224:227], v[20:23], v[8:11]\n\tv_mfma_f32_16x16x32_bf16 v[48:51], v[20:23], v[228:231], v[48:51]\n\tv_mfma_f32_16x16x32_bf16 v[64:67], v[220:223], v[20:23], v[64:67]\n\t"
  "ds_read_b64_tr_b16 v[20:21], v0 offset:26624\n\tds_read_b64_tr_b16 v[22:23], v0 offset:27648\n\ts_waitcnt lgkmcnt(0)\n\t"
  "v_mfma_f32_16x16x32_bf16 v[8:11], v[236:239], v[20:23], v[8:11]\n\tv_mfma_f32_16x16x32_bf16 v[48:51], v[20:23], v[240:243], v[48:51]\n\tv_mfma_f32_16x16x32_bf16 v[64:67], v[232:235], v[20:23], v[64:67]\n\t"
  "ds_read_b64_tr_b16 v[20:21], v0 offset:4096\n\tds_read_b64_tr_b16 v[22:23], v0 offset:5120\n\ts_waitcnt lgkmcnt(0)\n\t"
  "v_mfma_f32_16x16x32_bf16 v[8:11], v[104:107], v[20:23], v[8:11]\n\tv_mfma_f32_16x16x32_bf16 v[48:51], v[20:23], v[108:111], v[48:51]\n\tv_mfma_f32_16x16x32_bf16 v[64:67], v[100:103], v[20:23], v[64:67]\n\t"
  "ds_read_b64_tr_b16 v[20:21], v0 offset:6144\n\tds_read_b64_tr_b16 v[22:23], v0 offset:7168\n\tds_read_b64_tr_b16 v[36:37], v0 offset:12288\n\tds_read_b64_tr_b16 v[38:39], v0 offset:13312\n\tds_read_b64_tr_b16 v[28:29], v0 offset:14336\n\tds_read_b64_tr_b16 v[30:31], v0 offset:15360\n\tds_read_b64_tr_b16 v[68:69], v0 offset:20480\n\tds_read_b64_tr_b16 v[70:71], v0 offset:21504\n\t"
  "s_waitcnt lgkmcnt(0)\n\ts_branch .Lg_back\n\t"
  ".Lc0_kh:\n\ts_mov_b32 vcc_hi, 0x1000000\n\t.Lc0_kh_s:\n\tds_read_b32 v101, v27 offset:48\n\ts_sub_u32 vcc_hi, vcc_hi, 1\n\ts_cselect_b32 vcc_lo, 0, 4\n\ts_waitcnt lgkmcnt(0)\n\tv_readfirstlane_b32 s61, v101\n\ts_nop 1\n\ts_cmp_lt_u32 s61, vcc_lo\n\ts_cbranch_scc1 .Lc0_kh_s\n\t"
  "s_waitcnt lgkmcnt(0)\n\ts_branch .Lc0_kh_b\n\t"
  ".Lc0_x0:\n\ts_mov_b32 vcc_hi, 0x1000000\n\t.Lc0_x0_s:\n\tds_read_b32 v101, v21 offset:8\n\ts_sub_u32 vcc_hi, vcc_hi, 1\n\ts_cselect_b32 vcc_lo, 0, 2\n\ts_waitcnt lgkmcnt(0)\n\tv_readfirstlane_b32 s61, v101\n\ts_nop 1\n\ts_cmp_lt_u32 s61, vcc_lo\n\ts_cbranch_scc1 .Lc0_x0_s\n\t"
  "ds_read_b128 v[144:147], v20 offset:2048\n\tds_read_b128 v[204:207], v20 offset:3072\n\ts_waitcnt lgkmcnt(0)\n\ts_branch .Lc0_x0_b\n\t"
  ".Lc0_ub:\n\ts_mov_b32 vcc_hi, 0x1000000\n\t.Lc0_ub_s:\n\tds_read_b32 v101, v27 offset:52\n\ts_sub_u32 vcc_hi, vcc_hi, 1\n\ts_cselect_b32 vcc_lo, 0, 4\n\ts_waitcnt lgkmcnt(0)\n\tv_readfirstlane_b32 s61, v101\n\ts_nop 1\n\ts_cmp_lt_u32 s61, vcc_lo\n\ts_cbranch_scc1 .Lc0_ub_s\n\t"
  "s_waitcnt lgkmcnt(0)\n\ts_branch .Lc0_ub_b\n\t"
  ".Lc0_p3:\n\ts_mov_b32 vcc_hi, 0x1000000\n\t.Lc0_p3_s:\n\tds_read_b32 v101, v27 offset:64\n\ts_sub_u32 vcc_hi, vcc_hi, 1\n\ts_cselect_b32 vcc_lo, 0, 4\n\ts_waitcnt lgkmcnt(0)\n\tv_readfirstlane_b32 s61, v101\n\ts_nop 1\n\ts_cmp_lt_u32 s61, vcc_lo\n\ts_cbranch_scc1 .Lc0_p3_s\n\t"
  "s_waitcnt lgkmcnt(0)\n\ts_branch .Lc0_p3_b\n\t"
  ".Lc0_x4:\n\ts_mov_b32 vcc_hi, 0x1000000\n\t.Lc0_x4_s:\n\tds_read_b32 v36, v27 offset:16\n\ts_sub_u32 vcc_hi, vcc_hi, 1\n\ts_cselect_b32 vcc_lo, 0, 4\n\ts_waitcnt lgkmcnt(0)\n\tv_readfirstlane_b32 s61, v36\n\ts_nop 1\n\ts_cmp_lt_u32 s61, vcc_lo\n\ts_cbranch_scc1 .Lc0_x4_s\n\t"
  "ds_read_b128 v[40:43], v20 offset:59392\n\tds_read_b128 v[44:47], v20 offset:60416\n\ts_waitcnt lgkmcnt(0)\n\ts_branch .Lc0_x4_b\n\t"
  ".Lc1_kh:\n\ts_mov_b32 vcc_hi, 0x1000000\n\t.Lc1_kh_s:\n\tds_read_b32 v101, v27 offset:48\n\ts_sub_u32 vcc_hi, vcc_hi, 1\n\ts_cselect_b32 vcc_lo, 0, 4\n\ts_waitcnt lgkmcnt(0)\n\tv_readfirstlane_b32 s61, v101\n\ts_nop 1\n\ts_cmp_lt_u32 s61, vcc_lo\n\ts_cbranch_scc1 .Lc1_kh_s\n\t"
  "s_waitcnt lgkmcnt(0)\n\ts_branch .Lc1_kh_b\n\t"
  ".Lc1_x0:\n\ts_mov_b32 vcc_hi, 0x1000000\n\t.Lc1_x0_s:\n\tds_read_b32 v101, v21 offset:8\n\ts_sub_u32 vcc_hi, vcc_hi, 1\n\ts_cselect_b32 vcc_lo, 0, 2\n\ts_waitcnt lgkmcnt(0)\n\tv_readfirstlane_b32 s61, v101\n\ts_nop 1\n\ts_cmp_lt_u32 s61, vcc_lo\n\ts_cbranch_scc1 .Lc1_x0_s\n\t"
  "ds_read_b128 v[148:151], v20 offset:0\n\tds_read_b128 v[204:207], v20 offset:1024\n\ts_waitcnt lgkmcnt(0)\n\ts_branch .Lc1_x0_b\n\t"
  ".Lc1_ub:\n\ts_mov_b32 vcc_hi, 0x1000000\n\t.Lc1_ub_s:\n\tds_read_b32 v101, v27 offset:52\n\ts_sub_u32 vcc_hi, vcc_hi, 1\n\ts_cselect_b32 vcc_lo, 0, 4\n\ts_waitcnt lgkmcnt(0)\n\tv_readfirstlane_b32 s61, v101\n\ts_nop 1\n\ts_cmp_lt_u32 s61, vcc_lo\n\ts_cbranch_scc1 .Lc1_ub_s\n\t"
  "s_waitcnt lgkmcnt(0)\n\ts_branch .Lc1_ub_b\n\t"
  ".Lc1_p3:\n\ts_mov_b32 vcc_hi, 0x1000000\n\t.Lc1_p3_s:\n\tds_read_b32 v101, v27 offset:64\n\ts_sub_u32 vcc_hi, vcc_hi, 1\n\ts_cselect_b32 vcc_lo, 0, 4\n\ts_waitcnt lgkmcnt(0)\n\tv_readfirstlane_b32 s61, v101\n\ts_nop 1\n\ts_cmp_lt_u32 s61, vcc_lo\n\ts_cbranch_scc1 .Lc1_p3_s\n\t"
  "s_waitcnt lgkmcnt(0)\n\ts_branch .Lc1_p3_b\n\t"
  ".Lc1_x4:\n\ts_mov_b32 vcc_hi, 0x1000000\n\t.Lc1_x4_s:\n\tds_read_b32 v36, v27 offset:16\n\ts_sub_u32 vcc_hi, vcc_hi, 1\n\ts_cselect_b32 vcc_lo, 0, 4\n\ts_waitcnt lgkmcnt(0)\n\tv_readfirstlane_b32 s61, v36\n\ts_nop 1\n\ts_cmp_lt_u32 s61, vcc_lo\n\ts_cbranch_scc1 .Lc1_x4_s\n\t"
  "ds_read_b128 v[40:43], v20 offset:57344\n\tds_read_b128 v[44:47], v20 offset:58368\n\ts_waitcnt lgkmcnt(0)\n\ts_branch .Lc1_x4_b\n\t"
  ".Lcw_end:");
}

extern "C" void kernel_launch(void* const* d_in, const int* in_sizes, int n_in, void* d_out, int out_size,
                              void* d_ws, size_t ws_size, hipStream_t stream) {
  (void)hipFuncSetAttribute((const void*)k_mega, hipFuncAttributeMaxDynamicSharedMemorySize, MEGA_LDS);
  k_mega<<<256, 512, MEGA_LDS, stream>>>((const u16*)d_in[0], (const u16*)d_in[1], (const u16*)d_in[2], (const u16*)d_in[3],
                                         (const u16*)d_in[4], (const u16*)d_in[5], (const u16*)d_in[6], (const u16*)d_in[7],
                                         (const u16*)d_in[8], (const u16*)d_in[10], (const u16*)d_in[11], (char*)d_ws,
                                         (u32*)d_out);
}
#if defined(__HIP_DEVICE_COMPILE__)
#pragma clang attribute pop
#endif
```

```cpp
#if defined(__HIP_DEVICE_COMPILE__)
#pragma clang attribute push (__attribute__((target("no-packed-fp32-ops"))), apply_to = function)
#endif
#include <hip/hip_runtime.h>
#include <hip/hip_bf16.h>
#include <stdint.h>
#define TIDX ((int)__builtin_amdgcn_workitem_id_x())
#define BIDX ((int)__builtin_amdgcn_workgroup_id_x())
#define ST(set, k)
#define ST_INIT()
#define ST_OUT()
#define KATTR
#define FAILC(k)

#define ST_FIX()
#define CST(k)
#define GST(k)
typedef unsigned short u16;
typedef unsigned int u32;
typedef unsigned long long u64;
using bf16x8 = __attribute__((ext_vector_type(8))) short;
using bf16x4 = __attribute__((ext_vector_type(4))) short;
using f16x8 = __attribute__((ext_vector_type(8))) _Float16;
using f16x4 = __attribute__((ext_vector_type(4))) _Float16;
using f32x2 = __attribute__((ext_vector_type(2))) float;
using f32x4 = __attribute__((ext_vector_type(4))) float;
using u32x2 = __attribute__((ext_vector_type(2))) unsigned;
using u32x4 = __attribute__((ext_vector_type(4))) unsigned;
#define LAS __attribute__((address_space(3)))
#define GAS __attribute__((address_space(1)))

#define D_ 512
#define NS_ 128
#define XS_ 2080

#define MB_ (1 << 20)
#define XTO(kp_) (OFF_O1 + (((kp_) >> 4) & 1) * MB_ + ((kp_) >> 5) * 131072 + ((kp_) & 15) * 8192)
#define XTS 8192
#define RB_OF(b_) (((b_) & 7) * 4 + ((b_) >> 6))
#define HB_OF(b_) (((b_) >> 3) & 7)
#define OFF_ST0 (5 * MB_)
#define OFF_TS (OFF_ST0 + 16384)
#define OFF_SG (OFF_ST0 + 32768)
#define OFF_ST2 (OFF_ST0 + 65536)
#define OFF_T5 (OFF_ST0 + 262144)
#define OFF_EP (OFF_ST0 + 131072)
#define OFF_WQ (9 * MB_ - 262144)
#define OFF_WO (10 * MB_ + 393216)
#define OFF_X1 (11 * MB_)
#define OFF_O1 (14 * MB_)
#define OFF_X2 (16 * MB_)
#define OFF_X3 (19 * MB_)
#define OFF_O2 (22 * MB_)
#define OFF_KV2 (26 * MB_)
#define KVO(L1_, rb_, h_) (((L1_) ? OFF_O2 : OFF_KV2) + (((h_) >> 3) & 1) * MB_ + (rb_) * 32768 + (((h_) >> 2) & 1) * 16384 + ((h_) >> 4) * 8192 + ((h_) & 3) * 2048)
#define WSB (32 * MB_)
#define XBS 32896
#define SPIN_CAP (1u << 14)
#define XPB 1
#define GPE 1
#define B6 0
#define OUTA 2
#define OUTB 0
#define WLD 8
#define SLP_FL 4
#define SLP_WE 0
#define R1F 3
#define D9 1300
#define WS5 5
#define SLP_B4 4
#define H1 9
#define DB2 450
#define D5 200
#define DQ2 0
#define DO2 800
#define SLP_U 2
#define SLP_F 2
#define SLP_G 0
#define R1D 1
#define WOE 4
#define SLP_H1 0
#define SLP_H2 2
#define WO0 0
#define SLP_XA2 2
#define CPF 5
#define SLP_XP 2
#define SLP_SP 4
#define SLP_S 6
#define SLP_XA1 4
#define SLP_XB1 6
#define SLP_OB1 6
#define DO1 800
#define DO5 4600
#define WX 4
#define WA 0
#define WM 4
#define SLP_C2 3
#define SLP_C3 15

__device__ __forceinline__ float bf2f(u16 v) { return __uint_as_float(((u32)v) << 16); }
typedef __attribute__((ext_vector_type(2))) __bf16 bf16x2v;
__device__ __forceinline__ u32 pk2(float a, float b) {
  f32x2 f = {a, b};
  bf16x2v v = __builtin_convertvector(f, bf16x2v);
  return __builtin_bit_cast(u32, v);
}
__device__ __forceinline__ float rbf(float f) { return __uint_as_float(pk2(f, 0.f) << 16); }
__device__ __forceinline__ float lo2f(u32 w) { return __uint_as_float(w << 16); }
__device__ __forceinline__ float hi2f(u32 w) { return __uint_as_float(w & 0xFFFF0000u); }
template <int CTRL>
__device__ __forceinline__ float dpp_mov(float v) {
  return __int_as_float(__builtin_amdgcn_update_dpp(0, __float_as_int(v), CTRL, 0xf, 0xf, false));
}
#define DPP_XOR1 0xB1
#define DPP_XOR2 0x4E
#define DPP_ROR8 0x128
template <int CTRL, int RMASK>
__device__ __forceinline__ float dpp_add(float v) {
  return v + __int_as_float(__builtin_amdgcn_update_dpp(0, __float_as_int(v), CTRL, RMASK, 0xf, false));
}
__device__ __forceinline__ void wave_sum3(float& a, float& b, float& c) {
#define WS3(CT_, RM_) a = dpp_add<CT_, RM_>(a); b = dpp_add<CT_, RM_>(b); c = dpp_add<CT_, RM_>(c); __builtin_amdgcn_sched_barrier(0);
  WS3(0x111, 0xf) WS3(0x112, 0xf) WS3(0x114, 0xf) WS3(0x118, 0xf) WS3(0x142, 0xa) WS3(0x143, 0xc)
  a = __int_as_float(__builtin_amdgcn_readlane(__float_as_int(a), 63));
  b = __int_as_float(__builtin_amdgcn_readlane(__float_as_int(b), 63));
  c = __int_as_float(__builtin_amdgcn_readlane(__float_as_int(c), 63));
}
__device__ __forceinline__ float rmax(float a, float b) {
  float r;
  asm("v_max_f32 %0, %1, %2" : "=v"(r) : "v"(a), "v"(b));
  return r;
}
__device__ __forceinline__ void lds_barrier() {
  asm volatile("s_waitcnt lgkmcnt(0)" ::: "memory");
  __builtin_amdgcn_s_barrier();
  asm volatile("" ::: "memory");
}
__device__ __forceinline__ void cexp2(float re2, float rev, float& orr, float& oi) {
  float e = __builtin_amdgcn_exp2f(re2);
  rev = __builtin_amdgcn_fractf(rev);
  float s = __builtin_amdgcn_sinf(rev);
  float c = __builtin_amdgcn_cosf(rev);
  orr = e * c;
  oi = e * s;
}
__device__ __forceinline__ void cexp3(float re2, float rev, float& e, float& c, float& s) {
  e = __builtin_amdgcn_exp2f(re2);
  rev = __builtin_amdgcn_fractf(rev);
  s = __builtin_amdgcn_sinf(rev); c = __builtin_amdgcn_cosf(rev);
}
__device__ __forceinline__ float dot2acc(u32 a, u32 b, float c) {
  return __builtin_amdgcn_fdot2_f32_bf16(__builtin_bit_cast(bf16x2v, a), __builtin_bit_cast(bf16x2v, b), c, false);
}
__device__ __forceinline__ u32 opaque(u32 v) { asm volatile("" : "+v"(v)); return v; }
__device__ __forceinline__ int lane_id() { return (int)__builtin_amdgcn_mbcnt_hi(~0u, __builtin_amdgcn_mbcnt_lo(~0u, opaque(0u))); }
__device__ __forceinline__ u32 pack_stats(float mean, float var) {
  float vb = rbf(var);
  float ve = rbf(vb + 1e-5f);
  float sd = __builtin_amdgcn_sqrtf(ve);
  return pk2(mean, sd);
}

#define SB() __builtin_amdgcn_sched_barrier(0)

#define KADD(id_, t_, v_) kadd_<(v_)>(t_)
#define KSLP(id_, v_) do { if ((v_) > 0) __builtin_amdgcn_s_sleep(v_); } while (0)
template <int V>
__device__ __forceinline__ u32 kadd_(u32 t) { u32 r; asm("s_add_i32 %0, %1, %2" : "=s"(r) : "s"(__builtin_amdgcn_readfirstlane(t)), "n"(V) : "scc"); return r; }
template <int V> struct IC { static constexpr int value = V; };
template <int B, int E, class F>
__device__ __forceinline__ void static_for(F& f) { if constexpr (B < E) { f(IC<B>{}); static_for<B + 1, E>(f); } }
typedef __amdgpu_buffer_rsrc_t rsrc_t;
__device__ __forceinline__ u32x4 ldw(rsrc_t r, int off) { return __builtin_amdgcn_raw_buffer_load_b128(r, off, 0, 16); }
__device__ __forceinline__ u32x4 ldws(rsrc_t r, int off, int soff) { return __builtin_amdgcn_raw_buffer_load_b128(r, off, soff, 16); }
__device__ __forceinline__ void stw(rsrc_t r, int off, u32x4 v) { __builtin_amdgcn_raw_buffer_store_b128(v, r, off, 0, 16); }
__device__ __forceinline__ u32 umax4(u32 m, u32x4 v) { return max(max(max(m, v[0]), v[1]), max(v[2], v[3])); }
template <int CTRL>
__device__ __forceinline__ u32 dpp_u(u32 v) { return (u32)__builtin_amdgcn_update_dpp(0, (int)v, CTRL, 0xf, 0xf, false); }

#define CSTR 4160
#define L_U 33280
#define L_TA 42496
#define L_TB (L_TA + 32768)
#define L_QB (L_TB + 32768)
#define L_TH (L_QB + 16384)
#define L_SP (L_QB + 32768)
#define L_FS (L_SP + 4096)
#define L_MI (L_FS + 3072)
#define L_XS (L_MI + 80)
#define L_WX (L_XS + 1024)
#define MEGA_LDS (L_WX + 12288)
#define L_REDA L_QB
#define L_REDB (L_QB + 16384)
#define L_XC L_REDB
#define L_X (L_REDB + 8192)
#define TBS 3136
#define L_WR (L_QB + 24576)
#define L_WS L_TA
#define L_KT L_TB
#define KTO 65024
#define FTS 528
#define L_P0 (L_FS + 2048)
#define STAT_SCALE 33554432.0f
#define C_OB 20
#define C_OT 24
#define M_FX 72
#define C_X2 28
#define C_G2 32
#define C_A 36
#define C_B 40
#define C_F 44
#define C_KH 48
#define C_P2 52
#define C_XC 8
#define C_P3 64
#define M_RD 68
#define M_DL 76
#define C_OV 28
#define C_U 56
#define C_W 60

typedef __attribute__((ext_vector_type(2))) _Float16 f16x2v;
typedef __attribute__((ext_vector_type(2))) short i16x2;
__device__ __forceinline__ u32 pkh(float a, float b) {
  u32 r;
  asm("v_cvt_pk_f16_f32 %0, %1, %2" : "=v"(r) : "v"(a), "v"(b));
  return r;
}
__device__ __forceinline__ u32 pkhn(float a, float b) {
  u32 r;
  asm("v_cvt_pk_f16_f32 %0, %1, -%2" : "=v"(r) : "v"(a), "v"(b));
  return r;
}
__device__ __forceinline__ u32 tb_addr(u32 bits2, u32 base2) {
  i16x2 b = __builtin_elementwise_max(__builtin_bit_cast(i16x2, bits2), (i16x2){0x3F00, 0x3F00});
  u32 r;
  asm("v_pk_mad_u16 %0, %1, 2, %2 op_sel_hi:[1,0,1]" : "=v"(r) : "v"(b), "s"(base2));
  return r;
}
__device__ __forceinline__ int bexp(float b) { return min(max((int)((__float_as_uint(b) >> 23) & 0xFF), 20), 240); }
__device__ __forceinline__ void cnt_add(char* smem, int off, int lane) {
  asm volatile("s_mov_b64 exec, 1\n\tds_add_u32 %0, %1\n\ts_mov_b64 exec, -1" :: "v"(L_MI + off), "v"(1u) : "memory");
}
__device__ __forceinline__ void cnt_wait(char* smem, int off, u32 target) {
#pragma unroll 1
  for (unsigned n = 0; n < (1u << 23) && (u32)__builtin_amdgcn_readfirstlane(__hip_atomic_load((LAS u32*)(smem + L_MI + off), __ATOMIC_RELAXED, __HIP_MEMORY_SCOPE_WORKGROUP)) < target; ++n)
    __builtin_amdgcn_s_sleep(2);
  asm volatile("" ::: "memory");
}
__device__ __forceinline__ void cnt_spin(char* smem, int off, u32 target) {
  for (unsigned n = 0; n < (1u << 24) && (u32)__builtin_amdgcn_readfirstlane(__hip_atomic_load((LAS u32*)(smem + L_MI + off), __ATOMIC_RELAXED, __HIP_MEMORY_SCOPE_WORKGROUP)) < target; ++n) {}
  asm volatile("" ::: "memory");
}
__device__ __forceinline__ void cnt_spin2(int o0, u32 t0, int o1, u32 t1) {
  for (unsigned n = 0; n < (1u << 24); ++n) {
    asm volatile("" ::: "memory");
    const u32 c0 = *(const volatile LAS u32*)(size_t)(u32)(L_MI + o0), c1 = *(const volatile LAS u32*)(size_t)(u32)(L_MI + o1);
    if (__builtin_amdgcn_readfirstlane(min((int)(c0 - t0), (int)(c1 - t1))) >= 0) break;
  }
  asm volatile("" ::: "memory");
}
__device__ __forceinline__ u32 now_() { return (u32)__builtin_amdgcn_s_memtime(); }
__device__ __forceinline__ u32 tmax(u32 a, u32 b) { return (int)(a - b) > 0 ? a : b; }
__device__ __forceinline__ void gate(u32 t) {
  u32 n = 256;
  asm volatile("1:\n\ts_memtime vcc\n\ts_sub_u32 %0, %0, 1\n\ts_waitcnt lgkmcnt(0)\n\ts_cbranch_scc1 2f\n\ts_sub_i32 vcc_lo, %1, vcc_lo\n\ts_cmp_lt_i32 vcc_lo, 1\n\ts_cbranch_scc1 2f\n\t"
               "s_cmp_lt_i32 vcc_lo, 64\n\ts_cbranch_scc1 1b\n\ts_sleep 1\n\ts_cmp_lt_i32 vcc_lo, 0x200\n\ts_cbranch_scc1 1b\n\ts_sleep 6\n\ts_branch 1b\n2:"
               : "+s"(n) : "s"(__builtin_amdgcn_readfirstlane(t)) : "memory", "vcc", "scc");
}
__device__ __forceinline__ u32 x0_mark(u32 tag) { return 0xFF807F80u + (((tag & 31u) + 1u) << 16) + ((tag >> 5) & 31u) + 1u; }
__device__ __forceinline__ bool tags_ok(const u32x4& f, u32 tag) {
  return __builtin_amdgcn_ballot_w64((opaque((f[0] ^ tag) | (f[1] ^ tag)) | opaque((f[2] ^ tag) | (f[3] ^ tag))) != 0u) == 0ull;
}
__device__ __forceinline__ void tags_wait(u32x4& f, rsrc_t wrs, int fo, u32 tag, int fid) {
  if (__builtin_expect(!tags_ok(f, tag), 0)) {
    for (unsigned spins = 0; spins <= SPIN_CAP; ++spins) {
      FAILC(fid);
      __builtin_amdgcn_s_sleep(SLP_F);
      asm volatile("" ::: "memory");
      if (tags_ok(ldw(wrs, fo), tag)) break;
    }
    __builtin_amdgcn_s_waitcnt(0x0F70);
  }
  asm volatile("" ::: "memory");
}
__device__ __forceinline__ void build_copies(char* smem, int cc, int ch, const u32 (&kw)[6]) {
  char* cop = smem + cc * 4 * CSTR + ch * 16;
  u32 sh[5];
#pragma unroll
  for (int k = 0; k < 5; ++k) sh[k] = __builtin_amdgcn_alignbit(kw[k + 1], kw[k], 16);
  u32x4 c0 = {kw[0], kw[1], kw[2], kw[3]}, c1 = {sh[0], sh[1], sh[2], sh[3]}, c2 = {kw[1], kw[2], kw[3], kw[4]}, c3 = {sh[1], sh[2], sh[3], sh[4]};
  asm volatile("" : "+v"(c0), "+v"(c1), "+v"(c2), "+v"(c3));
  *(u32x4*)(cop + 0 * CSTR) = c0; *(u32x4*)(cop + 1 * CSTR) = c1; *(u32x4*)(cop + 2 * CSTR) = c2; *(u32x4*)(cop + 3 * CSTR) = c3;
  asm volatile("s_nop 1" :: "v"(c0), "v"(c1), "v"(c2), "v"(c3) : "memory");
}
struct ConvPre { f32x4 x0, x1; };
__device__ __forceinline__ void conv_unp(ConvPre& P, u32x4 xw) {
#pragma unroll
  for (int e = 0; e < 4; ++e) { P.x0[e] = lo2f(xw[e]); P.x1[e] = hi2f(xw[e]); }
  asm volatile("" : "+v"(P.x0), "+v"(P.x1));
}
__device__ __forceinline__ u32x4 x2_ld(rsrc_t wrs, int kp, int l0) { return ldw(wrs, OFF_X2 + (kp * XS_ + l0) * 4); }
__device__ __forceinline__ bool x2_ok(const u32x4& xw) { return __all(umax4(0u, xw) != 0xFFFFFFFFu); }
__device__ __forceinline__ void x2_poll(rsrc_t wrs, int kp, int l0, u32x4& xw) {
  for (unsigned spins = 0; !x2_ok(xw) && spins <= SPIN_CAP; ++spins) {
    FAILC(17);
    __builtin_amdgcn_s_sleep(SLP_XP);
    asm volatile("" ::: "memory");
    xw = x2_ld(wrs, kp, l0);
  }
}
__device__ __forceinline__ void st2_ld(rsrc_t wrs, int l0, u32x4 (&g)[4]) {
#pragma unroll
  for (int e = 0; e < 4; ++e) g[e] = ldw(wrs, OFF_ST2 + ((l0 & ~255) + ((l0 >> 2) & 63)) * 16 + e * 1024);
}
__device__ __forceinline__ bool st2_ok(const u32x4 (&g)[4]) {
  u32 ok = 32u;
#pragma unroll
  for (int e = 0; e < 4; ++e) ok &= g[e][0] & g[e][2];
  return __all(ok != 0u);
}
__device__ __forceinline__ bool x2st_ok(const u32x4& xw, const u32x4 (&g)[4]) {
  u32 ok = umax4(0u, xw) != 0xFFFFFFFFu ? 32u : 0u;
#pragma unroll
  for (int e = 0; e < 4; ++e) ok &= g[e][0] & g[e][2];
  return __all(ok != 0u);
}
__device__ __forceinline__ void st2_poll(rsrc_t wrs, int l0, u32x4 (&g)[4]) {
  for (unsigned spins = 0; !st2_ok(g) && spins <= SPIN_CAP; ++spins) {
    FAILC(8);
    __builtin_amdgcn_s_sleep(SLP_SP);
    asm volatile("" ::: "memory");
    st2_ld(wrs, l0, g);
  }
}
template <bool UNP>
__device__ __forceinline__ void conv2_in(char* smem, u32 ua, ConvPre& P, u32x4 xw, u32x4 (&g)[4]) {
  f32x4 rs, nm;
#pragma unroll
  for (int e = 0; e < 4; ++e) {
    const float c1 = 4294967296.0f / (64.0f * 512.0f * STAT_SCALE), c2 = 1.0f / 4294967296.0f;
    const float t1 = fmaf((float)g[e][0], c2, (float)(int)g[e][1]), t2 = fmaf((float)g[e][2], c2, (float)(int)g[e][3]);
    const float m1 = -c1 * t1;
    rs[e] = __builtin_amdgcn_rsqf(fmaf(rmax(fmaf(m1, t1, t2), 0.f), c1, 1e-5f));
    nm[e] = m1 * rs[e];
  }
  if (UNP) conv_unp(P, xw);
  u32x2 u0 = {pkh(fmaf(P.x0[0], rs[0], nm[0]), fmaf(P.x0[1], rs[1], nm[1])), pkh(fmaf(P.x0[2], rs[2], nm[2]), fmaf(P.x0[3], rs[3], nm[3]))};
  u32x2 u1 = {pkh(fmaf(P.x1[0], rs[0], nm[0]), fmaf(P.x1[1], rs[1], nm[1])), pkh(fmaf(P.x1[2], rs[2], nm[2]), fmaf(P.x1[3], rs[3], nm[3]))};
  asm volatile("" : "+v"(u0), "+v"(u1));
  *(LAS u32x2*)(size_t)ua = u0;
  *(LAS u32x2*)(size_t)(ua + 4608) = u1;
  asm volatile("s_nop 1" :: "v"(u0), "v"(u1) : "memory");
}
__device__ __forceinline__ void pair_x(f32x4& lo, f32x4& hi, const f32x4& a, const f32x4& b) {
#define PXD " quad_perm:[1,0,3,2] row_mask:0xf bank_mask:0xf\n\t"
  asm volatile("s_mov_b64 vcc, %16\n\ts_nop 0\n\t"
               "v_cndmask_b32_dpp %0, %12, %8, vcc" PXD "v_cndmask_b32_dpp %1, %13, %9, vcc" PXD "v_cndmask_b32_dpp %2, %14, %10, vcc" PXD "v_cndmask_b32_dpp %3, %15, %11, vcc" PXD
               "s_not_b64 vcc, vcc\n\t"
               "v_cndmask_b32_dpp %4, %8, %12, vcc" PXD "v_cndmask_b32_dpp %5, %9, %13, vcc" PXD "v_cndmask_b32_dpp %6, %10, %14, vcc" PXD "v_cndmask_b32_dpp %7, %11, %15, vcc" PXD
               : "=&v"(lo[0]), "=&v"(lo[1]), "=&v"(lo[2]), "=&v"(lo[3]), "=&v"(hi[0]), "=&v"(hi[1]), "=&v"(hi[2]), "=&v"(hi[3])
               : "v"(a[0]), "v"(a[1]), "v"(a[2]), "v"(a[3]), "v"(b[0]), "v"(b[1]), "v"(b[2]), "v"(b[3]), "s"(0x5555555555555555ull) : "vcc", "scc");
}
__device__ __forceinline__ void conv_res(ConvPre& P) {
  f32x4 lo, hi;
  pair_x(lo, hi, P.x0, P.x1);
  P.x0 = lo; P.x1 = hi;
}
__device__ __forceinline__ u32x4 conv_epi(u32 rp0, u32 rp1, int off, float isc, const ConvPre& P) {
  const f32x4 a0 = *(const LAS f32x4*)(size_t)(rp0 + off), a1 = *(const LAS f32x4*)(size_t)(rp0 + off + 4096), b0 = *(const LAS f32x4*)(size_t)(rp1 + off), b1 = *(const LAS f32x4*)(size_t)(rp1 + off + 4096);
  __builtin_amdgcn_sched_barrier(0);
  u32x4 r;
#pragma unroll
  for (int e = 0; e < 2; ++e) {
    r[e] = pk2(fmaf(a1[2 * e], isc, fmaf(a0[2 * e], isc, P.x0[2 * e])), fmaf(a1[2 * e + 1], isc, fmaf(a0[2 * e + 1], isc, P.x0[2 * e + 1])));
    r[2 + e] = pk2(fmaf(b1[2 * e], isc, fmaf(b0[2 * e], isc, P.x1[2 * e])), fmaf(b1[2 * e + 1], isc, fmaf(b0[2 * e + 1], isc, P.x1[2 * e + 1])));
  }
  return r;
}
__device__ __forceinline__ void dma4k(rsrc_t r, int voff, int soff, int lds) {
  asm volatile("s_mov_b32 m0, %3\n\ts_nop 0\n\t"
               "buffer_load_dwordx4 %0, %1, %2 offen sc1 lds\n\t"
               "buffer_load_dwordx4 %0, %1, %2 offen offset:1024 sc1 lds\n\t"
               "buffer_load_dwordx4 %0, %1, %2 offen offset:2048 sc1 lds\n\t"
               "buffer_load_dwordx4 %0, %1, %2 offen offset:3072 sc1 lds"
               :: "v"(voff), "s"(r), "s"(soff), "s"(lds) : "memory");
}
__device__ __forceinline__ void dma1k(rsrc_t r, int voff, int soff, int lds) {
  asm volatile("s_mov_b32 m0, %3\n\ts_nop 0\n\tbuffer_load_dwordx4 %0, %1, %2 offen sc1 lds" :: "v"(voff), "s"(r), "s"(soff), "s"(lds) : "memory");
}
__device__ __forceinline__ int x_lane(int lane) { return ((lane & 31) >> 1) * 64 + (lane >> 5) * 32 + (lane & 1) * 16; }
__device__ __forceinline__ void x_dma(rsrc_t wrs, int xin, int rb, int tile, int a, int lane) {
  const int vo = x_lane(lane);
  dma4k(wrs, vo, xin + rb * XBS + a * 8192, tile + a * 8192);
  dma4k(wrs, vo, xin + rb * XBS + a * 8192 + 4096, tile + a * 8192 + 4096);
  if (tile == L_TB) {
    int so = xin + (rb - 1) * XBS + a * 8192;
    asm volatile("s_mov_b32 m0, %3\n\ts_nop 0\n\tbuffer_load_dwordx4 %1, %2, %0 offen sc1 lds\n\t"
                 "s_sub_u32 m0, m0, 1024\n\ts_nop 0\n\tbuffer_load_dwordx4 %1, %2, %0 offen offset:2048 sc1 lds\n\t"
                 "s_add_u32 m0, m0, 3072\n\ts_add_u32 %0, %0, 4096\n\tbuffer_load_dwordx4 %1, %2, %0 offen sc1 lds\n\t"
                 "s_sub_u32 m0, m0, 1024\n\ts_nop 0\n\tbuffer_load_dwordx4 %1, %2, %0 offen offset:2048 sc1 lds"
                 : "+s"(so) : "v"((lane >> 1) * 64 + 32 + (lane & 1) * 16), "s"(wrs), "s"(L_TH + a * 4096) : "memory", "scc");
  }
}
template <int S>
__device__ __forceinline__ bf16x8 ld_tr8(const LAS char* p) {
  const bf16x4 lo = __builtin_amdgcn_ds_read_tr16_b64_v4i16((LAS bf16x4*)p), hi = __builtin_amdgcn_ds_read_tr16_b64_v4i16((LAS bf16x4*)(p + S));
  return __builtin_shufflevector(lo, hi, 0, 1, 2, 3, 4, 5, 6, 7);
}

struct ConvK { u32 ah, ah2, bp, ra; f16x8 k[16]; };
__device__ __forceinline__ void a_conv_pre(ConvK& C, int a, int lane) {
  lane = (int)opaque((u32)lane);
  const int c = a >> 1, qq = a & 1, j16 = lane & 15, kg = lane >> 4;
  const int y00 = 2031 - j16 + 8 * kg - 128 * qq;
  const int sg = y00 & 3;
  C.ah = opaque((u32)(c * 4 * CSTR + sg * CSTR + 2 * (y00 - sg) - 3776));
  C.ah2 = opaque(C.ah + 8);
  C.bp = opaque((u32)(L_U + c * 4608 + (256 + 16 * j16 - 16 + 8 * kg - 128 * qq) * 2 - 192));
  C.ra = opaque((u32)(L_REDA + (a * 256 + 4 * j16 + (kg ^ ((j16 >> 1) & 3) ^ c)) * 16));
#pragma unroll
  for (int e = 0; e < 16; ++e) {
    const int ao = 3776 - (512 * (e & 3) + 64 * (e >> 2));
    const f16x4 h0 = *(const LAS f16x4*)(size_t)(C.ah + ao), h1 = *(const LAS f16x4*)(size_t)(C.ah2 + ao);
    C.k[e] = __builtin_shufflevector(h0, h1, 0, 1, 2, 3, 4, 5, 6, 7);
    __builtin_amdgcn_sched_barrier(0);
  }
#pragma unroll
  for (int e = 0; e < 16; ++e) asm volatile("" : "+v"(C.k[e]));
}
template <bool L1>
__device__ __forceinline__ void a_conv(char* smem, rsrc_t wrs, int rb, int a, const ConvK& C) {
  const LAS char* ah = (const LAS char*)(size_t)C.ah;
  const LAS char* ah2 = (const LAS char*)(size_t)C.ah2;
  const LAS char* bp = (const LAS char*)(size_t)C.bp;
  LAS f32x4* ra = (LAS f32x4*)(size_t)C.ra;
  f32x4 acc[8];
#pragma unroll
  for (int e = 0; e < 8; ++e) acc[e] = (f32x4){0.f, 0.f, 0.f, 0.f};
  f16x8 bf[4][8], fh[4][8];
#define S_RR(s_) ((s_) < 16 ? (s_) >> 2 : (s_) < 44 ? ((s_) - 16) / 7 : ((s_) - 44) >> 2)
#define S_MU(s_) ((s_) < 16 ? (s_) & 3 : (s_) < 44 ? ((s_) - 16) % 7 + 1 : ((s_) - 44) & 3)
#define S_NEW(s_) ((s_) < 16 || ((s_) < 44 && S_MU(s_) >= 4))
#define LDA(s_) do { const int ao_ = 3776 - (512 * S_MU(s_) + 64 * S_RR(s_));                                        \
      f16x4 h0_ = *(const LAS f16x4*)(ah + ao_), h1_ = *(const LAS f16x4*)(ah2 + ao_);                                \
      fh[S_RR(s_)][S_MU(s_)] = __builtin_shufflevector(h0_, h1_, 0, 1, 2, 3, 4, 5, 6, 7); } while (0)
#define LDB(r_, t_) bf[r_][t_] = *(const LAS f16x8*)(bp + 512 * (t_) + 192 - 64 * (r_))
#define CSTEPS(s0_, s1_)                                                                                               \
  _Pragma("unroll") for (int st = s0_; st < s1_; ++st) {                                                               \
    const int rr = S_RR(st), mu = S_MU(st);                                                                           \
    if (S_NEW(st + CPF) && (L1 || st + CPF >= 16)) LDA(st + CPF);                                                     \
    if (st < 4) { LDB(2 + (st >> 1), 2 * (st & 1)); LDB(2 + (st >> 1), 2 * (st & 1) + 1); }                           \
    if (st >= 44 && st < 48) { LDB(2 + ((st - 44) >> 1), 4 + 2 * (st & 1)); LDB(2 + ((st - 44) >> 1), 5 + 2 * (st & 1)); } \
    __builtin_amdgcn_sched_barrier(0);                                                                                \
    _Pragma("unroll") for (int cb = 0; cb < 8; ++cb) {                                                                \
      const int t = cb - mu;                                                                                          \
      const bool on = st < 16 ? (cb < 4 && t >= 0) : st < 44 ? (cb >= 4 && t >= 0 && t < 4) : (t >= 4);               \
      if (on) acc[cb] = __builtin_amdgcn_mfma_f32_16x16x32_f16(fh[rr][mu], bf[rr][t < 0 ? 0 : t], acc[cb], 0, 0, 0);  \
    }                                                                                                                 \
    __builtin_amdgcn_sched_barrier(0);                                                                                \
    if (st >= 13 && st < 16) ra[(st - 13) * 64] = acc[st - 13];                                                       \
    if (st >= 57) ra[1024 + (st - 57) * 64] = acc[4 + st - 57];                                                       \
  }
#pragma unroll
  for (int e = 0; e < 16; ++e) fh[e >> 2][e & 3] = C.k[e];
  __builtin_amdgcn_s_barrier();
  asm volatile("" ::: "memory");
  ST(!L1, 7);
#pragma unroll
  for (int e = 0; e < CPF; ++e) { if (L1) LDA(e); __builtin_amdgcn_sched_barrier(0); if (e < 2) { LDB(e, 0); __builtin_amdgcn_sched_barrier(0); LDB(e, 1); __builtin_amdgcn_sched_barrier(0); LDB(e, 2); __builtin_amdgcn_sched_barrier(0); LDB(e, 3); __builtin_amdgcn_sched_barrier(0); } }
  __builtin_amdgcn_sched_barrier(0);
  CSTEPS(0, 16)
  CSTEPS(16, 17)
  ra[3 * 64] = acc[3];
  __builtin_amdgcn_sched_barrier(0);
  asm volatile("" ::: "memory");
  CSTEPS(17, 18)
  if (L1) asm volatile("s_waitcnt vmcnt(0)" ::: "memory");
  asm volatile("s_waitcnt lgkmcnt(2)" ::: "memory");
  __builtin_amdgcn_s_barrier();
  asm volatile("" ::: "memory");
  ST(!L1, 8);
  CSTEPS(18, 44)
  if (!L1) ST(1, 5);
  if (!L1) __builtin_amdgcn_s_barrier();
  if (!L1) ST(1, 6);
  asm volatile("" ::: "memory");
#pragma unroll
  for (int e = 0; e < 8; ++e) { LDB(e >> 2, 4 + (e & 3)); __builtin_amdgcn_sched_barrier(0); }
  CSTEPS(44, 60)
  ra[1024 + 3 * 64] = acc[7];
  lds_barrier();
  ST(!L1, 9);
}

struct C1Pre { u32 ah, ah2, bp, xp, xc, cn; int so, fo0, fo1; };
#define C1_OPS "+{v16}"(C.ah), "+{v17}"(C.ah2), "+{v18}"(C.bp), "+{v19}"(C.xp), "+{v20}"(C.xc), "+{v21}"(C.cn), "+{v22}"(C.so), "+{v23}"(C.fo0), "+{v24}"(C.fo1)
__device__ __forceinline__ void c1_pre(C1Pre& C, int kp, int a, int lane) {
  lane = (int)opaque((u32)lane);
  const int c = a >> 1, j16 = lane & 15, kg = lane >> 4, P = a & 1;
  const int y0 = 2031 - j16 + 8 * kg - 128 * P, sg = y0 & 3;
  C.ah = (u32)(c * 4 * CSTR + sg * CSTR + 2 * (y0 - sg) - 3776);
  C.ah2 = C.ah + 8;
  C.bp = (u32)(L_U + c * 4608 + (240 + 16 * j16 + 8 * kg - 128 * P) * 2 - 192);
  C.xp = (u32)(L_X + j16 * 64 + kg * 16);
  C.xc = (u32)(L_TA + 24576 + c * 4096 + lane * 16);
  C.cn = (u32)(L_MI + 4 * c);
  C.so = OFF_X1 + (8 * ((kg & 1) ? 3 - P : P) + (j16 >> 1)) * XBS + kp * 128 + c * 64 + (j16 & 1) * 32 + (kg >> 1) * 16;
  C.fo0 = OFF_ST0 + (a * 64 + lane) * 16;
  C.fo1 = OFF_ST0 + 8192 + (HB_OF(BIDX) * 4 + a) * 128 + (lane & 3) * 16;
  asm volatile("" : C1_OPS);
}
static_assert(L_XC - L_TA - 24576 == 57344 && C_XC == 8 && C_KH == 48 && C_P2 == 52 && C_U == 56 && C_P3 == 64 && C_G2 == 32 && L_MI % 8 == 0, "generated offsets");
__device__ __forceinline__ void a_conv1(rsrc_t wrs, int a, C1Pre& C, u32x4& fl, u32x4& wfl, u32& g2) {
  asm volatile("s_bitcmp1_b32 %[a], 0\n\ts_cbranch_scc1 .Lc_odd\n\t"
  "ds_read_b64 v[36:37], v16 offset:3776\n\tds_read_b64 v[38:39], v17 offset:3776\n\tds_read_b64 v[40:41], v16 offset:3264\n\tds_read_b64 v[42:43], v17 offset:3264\n\t"
  "ds_read_b64 v[44:45], v16 offset:2752\n\tds_read_b64 v[46:47], v17 offset:2752\n\tds_read_b64 v[48:49], v16 offset:2240\n\tds_read_b64 v[50:51], v17 offset:2240\n\t"
  "ds_read_b64 v[52:53], v16 offset:3712\n\tds_read_b64 v[54:55], v17 offset:3712\n\tds_read_b64 v[56:57], v16 offset:3200\n\tds_read_b64 v[58:59], v17 offset:3200\n\t"
  "ds_read_b64 v[60:61], v16 offset:2688\n\tds_read_b64 v[62:63], v17 offset:2688\n\tds_read_b64 v[64:65], v16 offset:2176\n\tds_read_b64 v[66:67], v17 offset:2176\n\t"
  "ds_read_b64 v[68:69], v16 offset:3648\n\tds_read_b64 v[70:71], v17 offset:3648\n\tds_read_b64 v[72:73], v16 offset:3136\n\tds_read_b64 v[74:75], v17 offset:3136\n\t"
  "ds_read_b64 v[76:77], v16 offset:2624\n\tds_read_b64 v[78:79], v17 offset:2624\n\tds_read_b64 v[80:81], v16 offset:2112\n\tds_read_b64 v[82:83], v17 offset:2112\n\t"
  "ds_read_b64 v[84:85], v16 offset:3584\n\tds_read_b64 v[86:87], v17 offset:3584\n\tds_read_b64 v[88:89], v16 offset:3072\n\tds_read_b64 v[90:91], v17 offset:3072\n\t"
  "ds_read_b64 v[92:93], v16 offset:2560\n\tds_read_b64 v[94:95], v17 offset:2560\n\tds_read_b64 v[96:97], v16 offset:2048\n\tds_read_b64 v[98:99], v17 offset:2048\n\t"
  "ds_read_b32 v26, v21\n\tv_and_b32 v27, -8, v21\n\tv_mov_b32 v100, 1\n\t"
  CST(6)
  "s_mov_b32 vcc_hi, 0x1000000\n\t.Lc0_u:\n\tds_read_b32 v101, v27 offset:56\n\ts_sub_u32 vcc_hi, vcc_hi, 1\n\ts_cselect_b32 vcc_lo, 0, 4\n\ts_waitcnt lgkmcnt(0)\n\tv_readfirstlane_b32 s61, v101\n\ts_nop 1\n\ts_cmp_lt_u32 s61, vcc_lo\n\ts_cbranch_scc1 .Lc0_u\n\t"
  CST(7)
  "ds_read_b128 v[102:105], v18 offset:192\n\tds_read_b128 v[106:109], v18 offset:704\n\tds_read_b128 v[110:113], v18 offset:1216\n\tds_read_b128 v[116:119], v18 offset:128\n\t"
  "ds_read_b128 v[120:123], v18 offset:640\n\tds_read_b128 v[124:127], v18 offset:1152\n\tds_read_b128 v[128:131], v18 offset:64\n\ts_waitcnt lgkmcnt(6)\n\t"
  "v_mfma_f32_16x16x32_f16 v[132:135], v[40:43], v[102:105], 0\n\tds_read_b128 v[136:139], v18 offset:576\n\tds_read_b128 v[140:143], v18 offset:1728\n\t"
  "v_mfma_f32_16x16x32_f16 v[144:147], v[44:47], v[102:105], 0\n\tds_read_b128 v[148:151], v18 offset:1088\n\ts_waitcnt lgkmcnt(8)\n\t"
  "v_mfma_f32_16x16x32_f16 v[132:135], v[36:39], v[106:109], v[132:135]\n\tds_read_b128 v[152:155], v18 offset:1664\n\tds_read_b128 v[156:159], v18 offset:1600\n\t"
  "v_mfma_f32_16x16x32_f16 v[144:147], v[40:43], v[106:109], v[144:147]\n\tds_read_b128 v[160:163], v18 offset:0\n\tds_read_b32 v101, v27 offset:48\n\ts_waitcnt lgkmcnt(7)\n\t"
  "v_mfma_f32_16x16x32_f16 v[144:147], v[36:39], v[110:113], v[144:147]\n\tds_read_b128 v[164:167], v18 offset:512\n\t"
  "v_mfma_f32_16x16x32_f16 v[132:135], v[56:59], v[116:119], v[132:135]\n\tds_read_b128 v[168:171], v18 offset:1024\n\t"
  "v_mfma_f32_16x16x32_f16 v[144:147], v[60:63], v[116:119], v[144:147]\n\tds_read_b128 v[172:175], v18 offset:1536\n\t"
  "v_mfma_f32_16x16x32_f16 v[132:135], v[52:55], v[120:123], v[132:135]\n\t"
  "v_mfma_f32_16x16x32_f16 v[144:147], v[56:59], v[120:123], v[144:147]\n\t"
  "v_mfma_f32_16x16x32_f16 v[144:147], v[52:55], v[124:127], v[144:147]\n\t"
  "v_mfma_f32_16x16x32_f16 v[132:135], v[72:75], v[128:131], v[132:135]\n\tds_read_b128 v[176:179], v19 offset:0\n\tds_read_b128 v[180:183], v19 offset:3072\n\t"
  "v_mfma_f32_16x16x32_f16 v[144:147], v[76:79], v[128:131], v[144:147]\n\ts_waitcnt lgkmcnt(5)\n\tv_readfirstlane_b32 s61, v101\n\t"
  "v_mfma_f32_16x16x32_f16 v[132:135], v[68:71], v[136:139], v[132:135]\n\t"
  "v_mfma_f32_16x16x32_f16 v[144:147], v[72:75], v[136:139], v[144:147]\n\ts_cmp_lt_u32 s61, 4\n\ts_cbranch_scc1 .Lc0_kh\n.Lc0_kh_b:\n\t"
  "v_mfma_f32_16x16x32_f16 v[144:147], v[68:71], v[148:151], v[144:147]\n\tds_read_b64 v[184:185], v16 offset:1728\n\tds_read_b64 v[186:187], v17 offset:1728\n\t"
  "v_mfma_f32_16x16x32_f16 v[132:135], v[88:91], v[160:163], v[132:135]\n\tds_read_b64 v[188:189], v16 offset:1216\n\tds_read_b64 v[190:191], v17 offset:1216\n\t"
  "v_mfma_f32_16x16x32_f16 v[144:147], v[92:95], v[160:163], v[144:147]\n\tds_read_b64 v[192:193], v16 offset:704\n\ts_waitcnt lgkmcnt(7)\n\t"
  "v_mfma_f32_16x16x32_f16 v[132:135], v[84:87], v[164:167], v[132:135]\n\tds_read_b64 v[194:195], v17 offset:704\n\tds_read_b64 v[196:197], v16 offset:192\n\tds_read_b64 v[198:199], v17 offset:192\n\t"
  "v_mfma_f32_16x16x32_f16 v[144:147], v[88:91], v[164:167], v[144:147]\n\ts_waitcnt lgkmcnt(8)\n\tv_perm_b32 v176, v176, v176, s60\n\t"
  "v_mfma_f32_16x16x32_f16 v[144:147], v[84:87], v[168:171], v[144:147]\n\tds_write_b128 v20, v[132:135] offset:0\n\tv_perm_b32 v177, v177, v177, s60\n\t"
  "v_mfma_f32_16x16x32_f16 v[200:203], v[36:39], v[102:105], 0\n\tv_perm_b32 v178, v178, v178, s60\n\tv_perm_b32 v179, v179, v179, s60\n\t"
  "v_mfma_f32_16x16x32_f16 v[132:135], v[48:51], v[102:105], 0\n\tds_write_b128 v20, v[144:147] offset:1024\n\tv_perm_b32 v180, v180, v180, s60\n\t"
  "v_mfma_f32_16x16x32_f16 v[132:135], v[44:47], v[106:109], v[132:135]\n\ts_mov_b64 exec, 1\n\tds_add_u32 v21, v100 offset:8\n\ts_mov_b64 exec, -1\n\t"
  "v_mfma_f32_16x16x32_f16 v[132:135], v[40:43], v[110:113], v[132:135]\n\tv_perm_b32 v181, v181, v181, s60\n\tv_perm_b32 v182, v182, v182, s60\n\t"
  "v_mfma_f32_16x16x32_f16 v[132:135], v[36:39], v[140:143], v[132:135]\n\tv_perm_b32 v183, v183, v183, s60\n\t"
  "v_mfma_f32_16x16x32_f16 v[200:203], v[52:55], v[116:119], v[200:203]\n\t"
  "v_mfma_f32_16x16x32_f16 v[132:135], v[64:67], v[116:119], v[132:135]\n\t"
  "v_mfma_f32_16x16x32_f16 v[132:135], v[60:63], v[120:123], v[132:135]\n\t"
  "v_mfma_f32_16x16x32_f16 v[132:135], v[56:59], v[124:127], v[132:135]\n\t"
  "v_mfma_f32_16x16x32_f16 v[132:135], v[52:55], v[152:155], v[132:135]\n\t"
  "v_mfma_f32_16x16x32_f16 v[200:203], v[68:71], v[128:131], v[200:203]\n\t"
  "v_mfma_f32_16x16x32_f16 v[132:135], v[80:83], v[128:131], v[132:135]\n\tds_read_b32 v101, v21 offset:8\n\tds_read_b128 v[144:147], v20 offset:2048\n\tds_read_b128 v[204:207], v20 offset:3072\n\t"
  "v_mfma_f32_16x16x32_f16 v[132:135], v[76:79], v[136:139], v[132:135]\n\t"
  "v_mfma_f32_16x16x32_f16 v[132:135], v[72:75], v[148:151], v[132:135]\n\t"
  "v_mfma_f32_16x16x32_f16 v[132:135], v[68:71], v[156:159], v[132:135]\n\t"
  "v_mfma_f32_16x16x32_f16 v[200:203], v[84:87], v[160:163], v[200:203]\n\t"
  "v_mfma_f32_16x16x32_f16 v[132:135], v[96:99], v[160:163], v[132:135]\n\t"
  "v_mfma_f32_16x16x32_f16 v[132:135], v[92:95], v[164:167], v[132:135]\n\t"
  "v_mfma_f32_16x16x32_f16 v[132:135], v[88:91], v[168:171], v[132:135]\n\t"
  "v_mfma_f32_16x16x32_f16 v[132:135], v[84:87], v[172:175], v[132:135]\n\ts_waitcnt lgkmcnt(0)\n\tv_readfirstlane_b32 s61, v101\n\t"
  "v_mfma_f32_16x16x32_f16 v[208:211], v[40:43], v[140:143], 0\n\t"
  "v_mfma_f32_16x16x32_f16 v[208:211], v[44:47], v[110:113], v[208:211]\n\ts_cmp_lt_u32 s61, 2\n\ts_cbranch_scc1 .Lc0_x0\n.Lc0_x0_b:\n\tv_fmac_f32 v176, v144, v26\n\tv_fmac_f32 v177, v145, v26\n\t"
  "v_fmac_f32 v178, v146, v26\n\t"
  "v_mfma_f32_16x16x32_f16 v[212:215], v[44:47], v[140:143], 0\n\tv_fmac_f32 v179, v147, v26\n\tv_fmac_f32 v180, v204, v26\n\tv_fmac_f32 v181, v205, v26\n\t"
  "v_fmac_f32 v182, v206, v26\n\tv_fmac_f32 v183, v207, v26\n\t"
  "v_mfma_f32_16x16x32_f16 v[208:211], v[48:51], v[106:109], v[208:211]\n\tv_fmac_f32 v176, v200, v26\n\tv_fmac_f32 v177, v201, v26\n\t"
  "v_mfma_f32_16x16x32_f16 v[212:215], v[48:51], v[110:113], v[212:215]\n\tv_fmac_f32 v178, v202, v26\n\tv_fmac_f32 v179, v203, v26\n\tv_cvt_pk_bf16_f32 v144, v176, v177\n\t"
  "v_cvt_pk_bf16_f32 v145, v178, v179\n\tv_fmac_f32 v180, v132, v26\n\t"
  "v_mfma_f32_16x16x32_f16 v[176:179], v[48:51], v[140:143], 0\n\tv_fmac_f32 v181, v133, v26\n\tv_fmac_f32 v182, v134, v26\n\tv_fmac_f32 v183, v135, v26\n\t"
  "v_cvt_pk_bf16_f32 v146, v180, v181\n\tv_cvt_pk_bf16_f32 v147, v182, v183\n\t"
  "v_mfma_f32_16x16x32_f16 v[208:211], v[184:187], v[102:105], v[208:211]\n\tv_permlane16_swap_b32 v144, v146\n\tv_permlane16_swap_b32 v145, v147\n\ts_setprio 3\n\tbuffer_store_dwordx4 v[144:147], v22, s[56:59], 0 offen sc1\n\ts_setprio 0\n\t"
  CST(8)
  "v_mfma_f32_16x16x32_f16 v[212:215], v[184:187], v[106:109], v[212:215]\n\tds_read_b64 v[132:133], v16 offset:1664\n\tds_read_b64 v[134:135], v17 offset:1664\n\t"
  "v_mfma_f32_16x16x32_f16 v[176:179], v[184:187], v[110:113], v[176:179]\n\tds_read_b64 v[144:145], v16 offset:1152\n\tds_read_b64 v[146:147], v17 offset:1152\n\t"
  "v_mfma_f32_16x16x32_f16 v[180:183], v[184:187], v[140:143], 0\n\tds_read_b64 v[140:141], v16 offset:640\n\tds_read_b64 v[142:143], v17 offset:640\n\t"
  "v_mfma_f32_16x16x32_f16 v[212:215], v[188:191], v[102:105], v[212:215]\n\t"
  "v_mfma_f32_16x16x32_f16 v[176:179], v[188:191], v[106:109], v[176:179]\n\tds_read_b64 v[184:185], v16 offset:128\n\tds_read_b64 v[186:187], v17 offset:128\n\t"
  "v_mfma_f32_16x16x32_f16 v[180:183], v[188:191], v[110:113], v[180:183]\n\t"
  "v_mfma_f32_16x16x32_f16 v[176:179], v[192:195], v[102:105], v[176:179]\n\t"
  "v_mfma_f32_16x16x32_f16 v[180:183], v[192:195], v[106:109], v[180:183]\n\t"
  "v_mfma_f32_16x16x32_f16 v[180:183], v[196:199], v[102:105], v[180:183]\n\t"
  "v_mfma_f32_16x16x32_f16 v[208:211], v[56:59], v[152:155], v[208:211]\n\t"
  "v_mfma_f32_16x16x32_f16 v[208:211], v[60:63], v[124:127], v[208:211]\n\t"
  "v_mfma_f32_16x16x32_f16 v[212:215], v[60:63], v[152:155], v[212:215]\n\tds_read_b64 v[102:103], v16 offset:1600\n\tds_read_b64 v[104:105], v17 offset:1600\n\t"
  "v_mfma_f32_16x16x32_f16 v[208:211], v[64:67], v[120:123], v[208:211]\n\t"
  "v_mfma_f32_16x16x32_f16 v[212:215], v[64:67], v[124:127], v[212:215]\n\t"
  "v_mfma_f32_16x16x32_f16 v[176:179], v[64:67], v[152:155], v[176:179]\n\ts_waitcnt lgkmcnt(2)\n\t"
  "v_mfma_f32_16x16x32_f16 v[208:211], v[132:135], v[116:119], v[208:211]\n\tds_read_b64 v[108:109], v16 offset:1088\n\tds_read_b64 v[110:111], v17 offset:1088\n\t"
  "v_mfma_f32_16x16x32_f16 v[212:215], v[132:135], v[120:123], v[212:215]\n\t"
  "v_mfma_f32_16x16x32_f16 v[176:179], v[132:135], v[124:127], v[176:179]\n\t"
  "v_mfma_f32_16x16x32_f16 v[180:183], v[132:135], v[152:155], v[180:183]\n\tds_read_b64 v[112:113], v16 offset:576\n\tds_read_b64 v[114:115], v17 offset:576\n\t"
  "v_mfma_f32_16x16x32_f16 v[212:215], v[144:147], v[116:119], v[212:215]\n\t"
  "v_mfma_f32_16x16x32_f16 v[176:179], v[144:147], v[120:123], v[176:179]\n\tds_read_b64 v[132:133], v16 offset:64\n\tds_read_b64 v[134:135], v17 offset:64\n\t"
  "v_mfma_f32_16x16x32_f16 v[180:183], v[144:147], v[124:127], v[180:183]\n\t"
  "v_mfma_f32_16x16x32_f16 v[176:179], v[140:143], v[116:119], v[176:179]\n\t"
  "v_mfma_f32_16x16x32_f16 v[180:183], v[140:143], v[120:123], v[180:183]\n\tv_add_u32 v22, 0x101000, v22\n\t"
  "v_mfma_f32_16x16x32_f16 v[180:183], v[184:187], v[116:119], v[180:183]\n\t"
  "v_mfma_f32_16x16x32_f16 v[208:211], v[72:75], v[156:159], v[208:211]\n\t"
  "v_mfma_f32_16x16x32_f16 v[208:211], v[76:79], v[148:151], v[208:211]\n\t"
  "v_mfma_f32_16x16x32_f16 v[212:215], v[76:79], v[156:159], v[212:215]\n\tds_read_b64 v[116:117], v16 offset:1536\n\tds_read_b64 v[118:119], v17 offset:1536\n\t"
  "v_mfma_f32_16x16x32_f16 v[208:211], v[80:83], v[136:139], v[208:211]\n\t"
  "v_mfma_f32_16x16x32_f16 v[212:215], v[80:83], v[148:151], v[212:215]\n\t"
  "v_mfma_f32_16x16x32_f16 v[176:179], v[80:83], v[156:159], v[176:179]\n\ts_waitcnt lgkmcnt(2)\n\t"
  "v_mfma_f32_16x16x32_f16 v[208:211], v[102:105], v[128:131], v[208:211]\n\tds_read_b32 v101, v27 offset:52\n\tds_read_b64 v[120:121], v16 offset:1024\n\t"
  "v_mfma_f32_16x16x32_f16 v[212:215], v[102:105], v[136:139], v[212:215]\n\tds_read_b64 v[122:123], v17 offset:1024\n\t"
  "v_mfma_f32_16x16x32_f16 v[176:179], v[102:105], v[148:151], v[176:179]\n\t"
  "v_mfma_f32_16x16x32_f16 v[180:183], v[102:105], v[156:159], v[180:183]\n\tds_read_b64 v[102:103], v16 offset:512\n\tds_read_b64 v[104:105], v17 offset:512\n\t"
  "v_mfma_f32_16x16x32_f16 v[212:215], v[108:111], v[128:131], v[212:215]\n\t"
  "v_mfma_f32_16x16x32_f16 v[176:179], v[108:111], v[136:139], v[176:179]\n\tds_read_b64 v[124:125], v16 offset:0\n\tds_read_b64 v[126:127], v17 offset:0\n\t"
  "v_mfma_f32_16x16x32_f16 v[180:183], v[108:111], v[148:151], v[180:183]\n\t"
  "v_mfma_f32_16x16x32_f16 v[176:179], v[112:115], v[128:131], v[176:179]\n\t"
  "v_mfma_f32_16x16x32_f16 v[180:183], v[112:115], v[136:139], v[180:183]\n\ts_waitcnt lgkmcnt(5)\n\tv_readfirstlane_b32 s61, v101\n\t"
  "v_mfma_f32_16x16x32_f16 v[180:183], v[132:135], v[128:131], v[180:183]\n\t"
  "v_mfma_f32_16x16x32_f16 v[208:211], v[88:91], v[172:175], v[208:211]\n\ts_cmp_lt_u32 s61, 4\n\ts_cbranch_scc1 .Lc0_ub\n.Lc0_ub_b:\n\t"
  "v_mfma_f32_16x16x32_f16 v[208:211], v[92:95], v[168:171], v[208:211]\n\tds_read_b128 v[108:111], v18 offset:2240\n\tds_read_b128 v[112:115], v18 offset:2752\n\t"
  "v_mfma_f32_16x16x32_f16 v[212:215], v[92:95], v[172:175], v[212:215]\n\tds_read_b128 v[128:131], v18 offset:3264\n\tds_read_b128 v[132:135], v18 offset:3776\n\t"
  "v_mfma_f32_16x16x32_f16 v[208:211], v[96:99], v[164:167], v[208:211]\n\t"
  "v_mfma_f32_16x16x32_f16 v[212:215], v[96:99], v[168:171], v[212:215]\n\t"
  "v_mfma_f32_16x16x32_f16 v[176:179], v[96:99], v[172:175], v[176:179]\n\tds_read_b128 v[136:139], v18 offset:2176\n\tds_read_b128 v[140:143], v18 offset:2688\n\t"
  "v_mfma_f32_16x16x32_f16 v[208:211], v[116:119], v[160:163], v[208:211]\n\tds_read_b128 v[144:147], v18 offset:3200\n\tds_read_b128 v[148:151], v18 offset:3712\n\t"
  "v_mfma_f32_16x16x32_f16 v[212:215], v[116:119], v[164:167], v[212:215]\n\t"
  "v_mfma_f32_16x16x32_f16 v[176:179], v[116:119], v[168:171], v[176:179]\n\t"
  "v_mfma_f32_16x16x32_f16 v[180:183], v[116:119], v[172:175], v[180:183]\n\ts_waitcnt lgkmcnt(6)\n\t"
  "v_mfma_f32_16x16x32_f16 v[212:215], v[120:123], v[160:163], v[212:215]\n\tds_read_b128 v[116:119], v18 offset:2112\n\tds_read_b128 v[152:155], v18 offset:2624\n\t"
  "v_mfma_f32_16x16x32_f16 v[176:179], v[120:123], v[164:167], v[176:179]\n\tds_read_b128 v[156:159], v18 offset:3136\n\tds_read_b128 v[172:175], v18 offset:3648\n\t"
  "v_mfma_f32_16x16x32_f16 v[180:183], v[120:123], v[168:171], v[180:183]\n\t"
  "v_mfma_f32_16x16x32_f16 v[176:179], v[102:105], v[160:163], v[176:179]\n\t"
  "v_mfma_f32_16x16x32_f16 v[180:183], v[102:105], v[164:167], v[180:183]\n\t"
  "v_mfma_f32_16x16x32_f16 v[180:183], v[124:127], v[160:163], v[180:183]\n\tds_read_b128 v[104:107], v18 offset:2048\n\tds_read_b128 v[120:123], v18 offset:2560\n\t"
  "v_mfma_f32_16x16x32_f16 v[212:215], v[40:43], v[108:111], v[212:215]\n\tds_read_b128 v[124:127], v18 offset:3072\n\tds_read_b128 v[160:163], v18 offset:3584\n\t"
  "v_mfma_f32_16x16x32_f16 v[176:179], v[44:47], v[108:111], v[176:179]\n\t"
  "v_mfma_f32_16x16x32_f16 v[212:215], v[36:39], v[112:115], v[212:215]\n\t"
  "v_mfma_f32_16x16x32_f16 v[176:179], v[40:43], v[112:115], v[176:179]\n\ts_waitcnt lgkmcnt(4)\n\t"
  "v_mfma_f32_16x16x32_f16 v[176:179], v[36:39], v[128:131], v[176:179]\n\t"
  "v_mfma_f32_16x16x32_f16 v[212:215], v[56:59], v[136:139], v[212:215]\n\t"
  "v_mfma_f32_16x16x32_f16 v[176:179], v[60:63], v[136:139], v[176:179]\n\t"
  "v_mfma_f32_16x16x32_f16 v[212:215], v[52:55], v[140:143], v[212:215]\n\t"
  "v_mfma_f32_16x16x32_f16 v[176:179], v[56:59], v[140:143], v[176:179]\n\t"
  "v_mfma_f32_16x16x32_f16 v[176:179], v[52:55], v[144:147], v[176:179]\n\t"
  "v_mfma_f32_16x16x32_f16 v[212:215], v[72:75], v[116:119], v[212:215]\n\tds_read_b128 v[164:167], v19 offset:4096\n\tds_read_b128 v[168:171], v19 offset:7168\n\t"
  "v_mfma_f32_16x16x32_f16 v[176:179], v[76:79], v[116:119], v[176:179]\n\t"
  "v_mfma_f32_16x16x32_f16 v[212:215], v[68:71], v[152:155], v[212:215]\n\t"
  "v_mfma_f32_16x16x32_f16 v[176:179], v[72:75], v[152:155], v[176:179]\n\t"
  "v_mfma_f32_16x16x32_f16 v[176:179], v[68:71], v[156:159], v[176:179]\n\ts_waitcnt lgkmcnt(2)\n\t"
  "v_mfma_f32_16x16x32_f16 v[212:215], v[88:91], v[104:107], v[212:215]\n\t"
  "v_mfma_f32_16x16x32_f16 v[176:179], v[92:95], v[104:107], v[176:179]\n\t"
  "v_mfma_f32_16x16x32_f16 v[212:215], v[84:87], v[120:123], v[212:215]\n\t"
  "v_mfma_f32_16x16x32_f16 v[176:179], v[88:91], v[120:123], v[176:179]\n\tds_read_b32 v101, v27 offset:64\n\ts_waitcnt lgkmcnt(1)\n\t"
  "v_mfma_f32_16x16x32_f16 v[176:179], v[84:87], v[124:127], v[176:179]\n\tds_write_b128 v20, v[212:215] offset:57344\n\tv_perm_b32 v164, v164, v164, s60\n\t"
  "v_mfma_f32_16x16x32_f16 v[208:211], v[36:39], v[108:111], v[208:211]\n\ts_setprio 3\n\tbuffer_load_dwordx4 v[28:31], v23, s[56:59], 0 offen sc1\n\tbuffer_load_dwordx4 v[32:35], v24, s[56:59], 0 offen sc1\n\ts_setprio 0\n\t"
  "v_mfma_f32_16x16x32_f16 v[180:183], v[48:51], v[108:111], v[180:183]\n\tds_write_b128 v20, v[176:179] offset:58368\n\tv_perm_b32 v165, v165, v165, s60\n\t"
  "v_mfma_f32_16x16x32_f16 v[180:183], v[44:47], v[112:115], v[180:183]\n\ts_mov_b64 exec, 1\n\tds_add_u32 v27, v100 offset:16\n\ts_mov_b64 exec, -1\n\t"
  "v_mfma_f32_16x16x32_f16 v[180:183], v[40:43], v[128:131], v[180:183]\n\tv_perm_b32 v166, v166, v166, s60\n\tv_perm_b32 v167, v167, v167, s60\n\t"
  "v_mfma_f32_16x16x32_f16 v[180:183], v[36:39], v[132:135], v[180:183]\n\tv_perm_b32 v168, v168, v168, s60\n\tv_perm_b32 v169, v169, v169, s60\n\t"
  "v_mfma_f32_16x16x32_f16 v[208:211], v[52:55], v[136:139], v[208:211]\n\tv_perm_b32 v170, v170, v170, s60\n\tv_perm_b32 v171, v171, v171, s60\n\t"
  "v_mfma_f32_16x16x32_f16 v[180:183], v[64:67], v[136:139], v[180:183]\n\ts_waitcnt lgkmcnt(3)\n\tv_readfirstlane_b32 s61, v101\n\t"
  "v_mfma_f32_16x16x32_f16 v[180:183], v[60:63], v[140:143], v[180:183]\n\t"
  "v_mfma_f32_16x16x32_f16 v[180:183], v[56:59], v[144:147], v[180:183]\n\ts_cmp_lt_u32 s61, 4\n\ts_cbranch_scc1 .Lc0_p3\n.Lc0_p3_b:\n\t"
  "v_mfma_f32_16x16x32_f16 v[180:183], v[52:55], v[148:151], v[180:183]\n\tds_read_b32 v36, v27 offset:16\n\tds_read_b128 v[40:43], v20 offset:59392\n\tds_read_b128 v[44:47], v20 offset:60416\n\t"
  "v_mfma_f32_16x16x32_f16 v[208:211], v[68:71], v[116:119], v[208:211]\n\t"
  "v_mfma_f32_16x16x32_f16 v[180:183], v[80:83], v[116:119], v[180:183]\n\t"
  "v_mfma_f32_16x16x32_f16 v[180:183], v[76:79], v[152:155], v[180:183]\n\t"
  "v_mfma_f32_16x16x32_f16 v[180:183], v[72:75], v[156:159], v[180:183]\n\t"
  "v_mfma_f32_16x16x32_f16 v[180:183], v[68:71], v[172:175], v[180:183]\n\t"
  "v_mfma_f32_16x16x32_f16 v[208:211], v[84:87], v[104:107], v[208:211]\n\t"
  "v_mfma_f32_16x16x32_f16 v[180:183], v[96:99], v[104:107], v[180:183]\n\t"
  "v_mfma_f32_16x16x32_f16 v[180:183], v[92:95], v[120:123], v[180:183]\n\ts_waitcnt lgkmcnt(0)\n\tv_readfirstlane_b32 s61, v36\n\tds_read_b32 v25, v27 offset:32\n\t"
  "v_mfma_f32_16x16x32_f16 v[180:183], v[88:91], v[124:127], v[180:183]\n\ts_cmp_lt_u32 s61, 4\n\ts_cbranch_scc1 .Lc0_x4\n.Lc0_x4_b:\n\t"
  "v_mfma_f32_16x16x32_f16 v[180:183], v[84:87], v[160:163], v[180:183]\n\tv_fmac_f32 v164, v40, v26\n\tv_fmac_f32 v165, v41, v26\n\tv_fmac_f32 v166, v42, v26\n\t"
  "v_fmac_f32 v167, v43, v26\n\tv_fmac_f32 v168, v44, v26\n\tv_fmac_f32 v169, v45, v26\n\tv_fmac_f32 v170, v46, v26\n\t"
  "v_fmac_f32 v171, v47, v26\n\tv_fmac_f32 v164, v208, v26\n\tv_fmac_f32 v165, v209, v26\n\tv_fmac_f32 v166, v210, v26\n\t"
  "v_fmac_f32 v167, v211, v26\n\tv_cvt_pk_bf16_f32 v36, v164, v165\n\tv_cvt_pk_bf16_f32 v37, v166, v167\n\tv_fmac_f32 v168, v180, v26\n\t"
  "v_fmac_f32 v169, v181, v26\n\tv_fmac_f32 v170, v182, v26\n\tv_fmac_f32 v171, v183, v26\n\tv_cvt_pk_bf16_f32 v38, v168, v169\n\t"
  "v_cvt_pk_bf16_f32 v39, v170, v171\n\ts_nop 0\n\tv_permlane16_swap_b32 v36, v38\n\tv_permlane16_swap_b32 v37, v39\n\t"
  "s_setprio 3\n\tbuffer_store_dwordx4 v[36:39], v22, s[56:59], 0 offen sc1\n\ts_setprio 0\n\ts_waitcnt lgkmcnt(0)\n\ts_nop 0\n\t"
  "s_branch .Lc_end\n.Lc_odd:\n\t"
  "ds_read_b64 v[36:37], v16 offset:3776\n\tds_read_b64 v[38:39], v17 offset:3776\n\tds_read_b64 v[40:41], v16 offset:3264\n\tds_read_b64 v[42:43], v17 offset:3264\n\t"
  "ds_read_b64 v[44:45], v16 offset:2752\n\tds_read_b64 v[46:47], v17 offset:2752\n\tds_read_b64 v[48:49], v16 offset:2240\n\tds_read_b64 v[50:51], v17 offset:2240\n\t"
  "ds_read_b64 v[52:53], v16 offset:3712\n\tds_read_b64 v[54:55], v17 offset:3712\n\tds_read_b64 v[56:57], v16 offset:3200\n\tds_read_b64 v[58:59], v17 offset:3200\n\t"
  "ds_read_b64 v[60:61], v16 offset:2688\n\tds_read_b64 v[62:63], v17 offset:2688\n\tds_read_b64 v[64:65], v16 offset:2176\n\tds_read_b64 v[66:67], v17 offset:2176\n\t"
  "ds_read_b64 v[68:69], v16 offset:3648\n\tds_read_b64 v[70:71], v17 offset:3648\n\tds_read_b64 v[72:73], v16 offset:3136\n\tds_read_b64 v[74:75], v17 offset:3136\n\t"
  "ds_read_b64 v[76:77], v16 offset:2624\n\tds_read_b64 v[78:79], v17 offset:2624\n\tds_read_b64 v[80:81], v16 offset:2112\n\tds_read_b64 v[82:83], v17 offset:2112\n\t"
  "ds_read_b64 v[84:85], v16 offset:3584\n\tds_read_b64 v[86:87], v17 offset:3584\n\tds_read_b64 v[88:89], v16 offset:3072\n\tds_read_b64 v[90:91], v17 offset:3072\n\t"
  "ds_read_b64 v[92:93], v16 offset:2560\n\tds_read_b64 v[94:95], v17 offset:2560\n\tds_read_b64 v[96:97], v16 offset:2048\n\tds_read_b64 v[98:99], v17 offset:2048\n\t"
  "ds_read_b32 v26, v21\n\tv_and_b32 v27, -8, v21\n\tv_mov_b32 v100, 1\n\t"
  CST(6)
  "s_mov_b32 vcc_hi, 0x1000000\n\t.Lc1_u:\n\tds_read_b32 v101, v27 offset:56\n\ts_sub_u32 vcc_hi, vcc_hi, 1\n\ts_cselect_b32 vcc_lo, 0, 4\n\ts_waitcnt lgkmcnt(0)\n\tv_readfirstlane_b32 s61, v101\n\ts_nop 1\n\ts_cmp_lt_u32 s61, vcc_lo\n\ts_cbranch_scc1 .Lc1_u\n\t"
  CST(7)
  "ds_read_b128 v[102:105], v18 offset:192\n\tds_read_b128 v[106:109], v18 offset:704\n\tds_read_b128 v[110:113], v18 offset:1216\n\tds_read_b128 v[114:117], v18 offset:1728\n\t"
  "ds_read_b128 v[120:123], v18 offset:128\n\tds_read_b128 v[124:127], v18 offset:640\n\tds_read_b128 v[128:131], v18 offset:1152\n\tds_read_b128 v[132:135], v18 offset:1664\n\t"
  "ds_read_b128 v[136:139], v18 offset:64\n\ts_waitcnt lgkmcnt(8)\n\t"
  "v_mfma_f32_16x16x32_f16 v[140:143], v[36:39], v[102:105], 0\n\tds_read_b128 v[144:147], v18 offset:576\n\t"
  "v_mfma_f32_16x16x32_f16 v[148:151], v[48:51], v[102:105], 0\n\tds_read_b128 v[152:155], v18 offset:1088\n\ts_waitcnt lgkmcnt(9)\n\t"
  "v_mfma_f32_16x16x32_f16 v[148:151], v[44:47], v[106:109], v[148:151]\n\tds_read_b128 v[156:159], v18 offset:1600\n\ts_waitcnt lgkmcnt(3)\n\t"
  "v_mfma_f32_16x16x32_f16 v[148:151], v[40:43], v[110:113], v[148:151]\n\tds_read_b128 v[160:163], v18 offset:0\n\tds_read_b32 v101, v27 offset:48\n\t"
  "v_mfma_f32_16x16x32_f16 v[148:151], v[36:39], v[114:117], v[148:151]\n\tds_read_b128 v[164:167], v18 offset:512\n\t"
  "v_mfma_f32_16x16x32_f16 v[140:143], v[52:55], v[120:123], v[140:143]\n\tds_read_b128 v[168:171], v18 offset:1024\n\t"
  "v_mfma_f32_16x16x32_f16 v[148:151], v[64:67], v[120:123], v[148:151]\n\tds_read_b128 v[172:175], v18 offset:1536\n\t"
  "v_mfma_f32_16x16x32_f16 v[148:151], v[60:63], v[124:127], v[148:151]\n\t"
  "v_mfma_f32_16x16x32_f16 v[148:151], v[56:59], v[128:131], v[148:151]\n\t"
  "v_mfma_f32_16x16x32_f16 v[148:151], v[52:55], v[132:135], v[148:151]\n\t"
  "v_mfma_f32_16x16x32_f16 v[140:143], v[68:71], v[136:139], v[140:143]\n\tds_read_b128 v[176:179], v19 offset:1024\n\tds_read_b128 v[180:183], v19 offset:2048\n\t"
  "v_mfma_f32_16x16x32_f16 v[148:151], v[80:83], v[136:139], v[148:151]\n\ts_waitcnt lgkmcnt(5)\n\tv_readfirstlane_b32 s61, v101\n\t"
  "v_mfma_f32_16x16x32_f16 v[148:151], v[76:79], v[144:147], v[148:151]\n\t"
  "v_mfma_f32_16x16x32_f16 v[148:151], v[72:75], v[152:155], v[148:151]\n\ts_cmp_lt_u32 s61, 4\n\ts_cbranch_scc1 .Lc1_kh\n.Lc1_kh_b:\n\t"
  "v_mfma_f32_16x16x32_f16 v[148:151], v[68:71], v[156:159], v[148:151]\n\tds_read_b64 v[184:185], v16 offset:1728\n\tds_read_b64 v[186:187], v17 offset:1728\n\t"
  "v_mfma_f32_16x16x32_f16 v[140:143], v[84:87], v[160:163], v[140:143]\n\tds_read_b64 v[188:189], v16 offset:1216\n\tds_read_b64 v[190:191], v17 offset:1216\n\t"
  "v_mfma_f32_16x16x32_f16 v[148:151], v[96:99], v[160:163], v[148:151]\n\tds_read_b64 v[192:193], v16 offset:704\n\ts_waitcnt lgkmcnt(7)\n\t"
  "v_mfma_f32_16x16x32_f16 v[148:151], v[92:95], v[164:167], v[148:151]\n\tds_read_b64 v[194:195], v17 offset:704\n\tds_read_b64 v[196:197], v16 offset:192\n\tds_read_b64 v[198:199], v17 offset:192\n\t"
  "v_mfma_f32_16x16x32_f16 v[148:151], v[88:91], v[168:171], v[148:151]\n\ts_waitcnt lgkmcnt(8)\n\tv_perm_b32 v176, v176, v176, s60\n\t"
  "v_mfma_f32_16x16x32_f16 v[148:151], v[84:87], v[172:175], v[148:151]\n\tds_write_b128 v20, v[140:143] offset:2048\n\tv_perm_b32 v177, v177, v177, s60\n\t"
  "v_mfma_f32_16x16x32_f16 v[200:203], v[40:43], v[102:105], 0\n\tv_perm_b32 v178, v178, v178, s60\n\tv_perm_b32 v179, v179, v179, s60\n\t"
  "v_mfma_f32_16x16x32_f16 v[140:143], v[44:47], v[102:105], 0\n\tds_write_b128 v20, v[148:151] offset:3072\n\tv_perm_b32 v180, v180, v180, s60\n\t"
  "v_mfma_f32_16x16x32_f16 v[200:203], v[36:39], v[106:109], v[200:203]\n\ts_mov_b64 exec, 1\n\tds_add_u32 v21, v100 offset:8\n\ts_mov_b64 exec, -1\n\t"
  "v_mfma_f32_16x16x32_f16 v[140:143], v[40:43], v[106:109], v[140:143]\n\tv_perm_b32 v181, v181, v181, s60\n\tv_perm_b32 v182, v182, v182, s60\n\t"
  "v_mfma_f32_16x16x32_f16 v[140:143], v[36:39], v[110:113], v[140:143]\n\tv_perm_b32 v183, v183, v183, s60\n\t"
  "v_mfma_f32_16x16x32_f16 v[200:203], v[56:59], v[120:123], v[200:203]\n\t"
  "v_mfma_f32_16x16x32_f16 v[140:143], v[60:63], v[120:123], v[140:143]\n\t"
  "v_mfma_f32_16x16x32_f16 v[200:203], v[52:55], v[124:127], v[200:203]\n\t"
  "v_mfma_f32_16x16x32_f16 v[140:143], v[56:59], v[124:127], v[140:143]\n\t"
  "v_mfma_f32_16x16x32_f16 v[140:143], v[52:55], v[128:131], v[140:143]\n\t"
  "v_mfma_f32_16x16x32_f16 v[200:203], v[72:75], v[136:139], v[200:203]\n\t"
  "v_mfma_f32_16x16x32_f16 v[140:143], v[76:79], v[136:139], v[140:143]\n\tds_read_b32 v101, v21 offset:8\n\tds_read_b128 v[148:151], v20 offset:0\n\tds_read_b128 v[204:207], v20 offset:1024\n\t"
  "v_mfma_f32_16x16x32_f16 v[200:203], v[68:71], v[144:147], v[200:203]\n\t"
  "v_mfma_f32_16x16x32_f16 v[140:143], v[72:75], v[144:147], v[140:143]\n\t"
  "v_mfma_f32_16x16x32_f16 v[140:143], v[68:71], v[152:155], v[140:143]\n\t"
  "v_mfma_f32_16x16x32_f16 v[200:203], v[88:91], v[160:163], v[200:203]\n\t"
  "v_mfma_f32_16x16x32_f16 v[140:143], v[92:95], v[160:163], v[140:143]\n\t"
  "v_mfma_f32_16x16x32_f16 v[200:203], v[84:87], v[164:167], v[200:203]\n\t"
  "v_mfma_f32_16x16x32_f16 v[140:143], v[88:91], v[164:167], v[140:143]\n\t"
  "v_mfma_f32_16x16x32_f16 v[140:143], v[84:87], v[168:171], v[140:143]\n\ts_waitcnt lgkmcnt(0)\n\tv_readfirstlane_b32 s61, v101\n\t"
  "v_mfma_f32_16x16x32_f16 v[208:211], v[40:43], v[114:117], 0\n\t"
  "v_mfma_f32_16x16x32_f16 v[208:211], v[44:47], v[110:113], v[208:211]\n\ts_cmp_lt_u32 s61, 2\n\ts_cbranch_scc1 .Lc1_x0\n.Lc1_x0_b:\n\tv_fmac_f32 v176, v148, v26\n\tv_fmac_f32 v177, v149, v26\n\t"
  "v_fmac_f32 v178, v150, v26\n\t"
  "v_mfma_f32_16x16x32_f16 v[212:215], v[44:47], v[114:117], 0\n\tv_fmac_f32 v179, v151, v26\n\tv_fmac_f32 v180, v204, v26\n\tv_fmac_f32 v181, v205, v26\n\t"
  "v_fmac_f32 v182, v206, v26\n\tv_fmac_f32 v183, v207, v26\n\t"
  "v_mfma_f32_16x16x32_f16 v[208:211], v[48:51], v[106:109], v[208:211]\n\tv_fmac_f32 v176, v200, v26\n\tv_fmac_f32 v177, v201, v26\n\t"
  "v_mfma_f32_16x16x32_f16 v[212:215], v[48:51], v[110:113], v[212:215]\n\tv_fmac_f32 v178, v202, v26\n\tv_fmac_f32 v179, v203, v26\n\tv_cvt_pk_bf16_f32 v148, v176, v177\n\t"
  "v_cvt_pk_bf16_f32 v149, v178, v179\n\tv_fmac_f32 v180, v140, v26\n\t"
  "v_mfma_f32_16x16x32_f16 v[176:179], v[48:51], v[114:117], 0\n\tv_fmac_f32 v181, v141, v26\n\tv_fmac_f32 v182, v142, v26\n\tv_fmac_f32 v183, v143, v26\n\t"
  "v_cvt_pk_bf16_f32 v150, v180, v181\n\tv_cvt_pk_bf16_f32 v151, v182, v183\n\t"
  "v_mfma_f32_16x16x32_f16 v[208:211], v[184:187], v[102:105], v[208:211]\n\tv_permlane16_swap_b32 v148, v150\n\tv_permlane16_swap_b32 v149, v151\n\ts_setprio 3\n\tbuffer_store_dwordx4 v[148:151], v22, s[56:59], 0 offen sc1\n\ts_setprio 0\n\t"
  CST(8)
  "v_mfma_f32_16x16x32_f16 v[212:215], v[184:187], v[106:109], v[212:215]\n\tds_read_b64 v[140:141], v16 offset:1664\n\tds_read_b64 v[142:143], v17 offset:1664\n\t"
  "v_mfma_f32_16x16x32_f16 v[176:179], v[184:187], v[110:113], v[176:179]\n\tds_read_b64 v[148:149], v16 offset:1152\n\tds_read_b64 v[150:151], v17 offset:1152\n\t"
  "v_mfma_f32_16x16x32_f16 v[180:183], v[184:187], v[114:117], 0\n\tds_read_b64 v[116:117], v16 offset:640\n\tds_read_b64 v[118:119], v17 offset:640\n\t"
  "v_mfma_f32_16x16x32_f16 v[212:215], v[188:191], v[102:105], v[212:215]\n\t"
  "v_mfma_f32_16x16x32_f16 v[176:179], v[188:191], v[106:109], v[176:179]\n\tds_read_b64 v[184:185], v16 offset:128\n\tds_read_b64 v[186:187], v17 offset:128\n\t"
  "v_mfma_f32_16x16x32_f16 v[180:183], v[188:191], v[110:113], v[180:183]\n\t"
  "v_mfma_f32_16x16x32_f16 v[176:179], v[192:195], v[102:105], v[176:179]\n\t"
  "v_mfma_f32_16x16x32_f16 v[180:183], v[192:195], v[106:109], v[180:183]\n\t"
  "v_mfma_f32_16x16x32_f16 v[180:183], v[196:199], v[102:105], v[180:183]\n\t"
  "v_mfma_f32_16x16x32_f16 v[208:211], v[56:59], v[132:135], v[208:211]\n\t"
  "v_mfma_f32_16x16x32_f16 v[208:211], v[60:63], v[128:131], v[208:211]\n\t"
  "v_mfma_f32_16x16x32_f16 v[212:215], v[60:63], v[132:135], v[212:215]\n\tds_read_b64 v[102:103], v16 offset:1600\n\tds_read_b64 v[104:105], v17 offset:1600\n\t"
  "v_mfma_f32_16x16x32_f16 v[208:211], v[64:67], v[124:127], v[208:211]\n\t"
  "v_mfma_f32_16x16x32_f16 v[212:215], v[64:67], v[128:131], v[212:215]\n\t"
  "v_mfma_f32_16x16x32_f16 v[176:179], v[64:67], v[132:135], v[176:179]\n\ts_waitcnt lgkmcnt(2)\n\t"
  "v_mfma_f32_16x16x32_f16 v[208:211], v[140:143], v[120:123], v[208:211]\n\tds_read_b64 v[108:109], v16 offset:1088\n\tds_read_b64 v[110:111], v17 offset:1088\n\t"
  "v_mfma_f32_16x16x32_f16 v[212:215], v[140:143], v[124:127], v[212:215]\n\t"
  "v_mfma_f32_16x16x32_f16 v[176:179], v[140:143], v[128:131], v[176:179]\n\t"
  "v_mfma_f32_16x16x32_f16 v[180:183], v[140:143], v[132:135], v[180:183]\n\tds_read_b64 v[112:113], v16 offset:576\n\tds_read_b64 v[114:115], v17 offset:576\n\t"
  "v_mfma_f32_16x16x32_f16 v[212:215], v[148:151], v[120:123], v[212:215]\n\t"
  "v_mfma_f32_16x16x32_f16 v[176:179], v[148:151], v[124:127], v[176:179]\n\tds_read_b64 v[132:133], v16 offset:64\n\tds_read_b64 v[134:135], v17 offset:64\n\t"
  "v_mfma_f32_16x16x32_f16 v[180:183], v[148:151], v[128:131], v[180:183]\n\t"
  "v_mfma_f32_16x16x32_f16 v[176:179], v[116:119], v[120:123], v[176:179]\n\t"
  "v_mfma_f32_16x16x32_f16 v[180:183], v[116:119], v[124:127], v[180:183]\n\tv_add_u32 v22, 0x101000, v22\n\t"
  "v_mfma_f32_16x16x32_f16 v[180:183], v[184:187], v[120:123], v[180:183]\n\t"
  "v_mfma_f32_16x16x32_f16 v[208:211], v[72:75], v[156:159], v[208:211]\n\t"
  "v_mfma_f32_16x16x32_f16 v[208:211], v[76:79], v[152:155], v[208:211]\n\t"
  "v_mfma_f32_16x16x32_f16 v[212:215], v[76:79], v[156:159], v[212:215]\n\tds_read_b64 v[116:117], v16 offset:1536\n\tds_read_b64 v[118:119], v17 offset:1536\n\t"
  "v_mfma_f32_16x16x32_f16 v[208:211], v[80:83], v[144:147], v[208:211]\n\t"
  "v_mfma_f32_16x16x32_f16 v[212:215], v[80:83], v[152:155], v[212:215]\n\t"
  "v_mfma_f32_16x16x32_f16 v[176:179], v[80:83], v[156:159], v[176:179]\n\ts_waitcnt lgkmcnt(2)\n\t"
  "v_mfma_f32_16x16x32_f16 v[208:211], v[102:105], v[136:139], v[208:211]\n\tds_read_b32 v101, v27 offset:52\n\tds_read_b64 v[120:121], v16 offset:1024\n\t"
  "v_mfma_f32_16x16x32_f16 v[212:215], v[102:105], v[144:147], v[212:215]\n\tds_read_b64 v[122:123], v17 offset:1024\n\t"
  "v_mfma_f32_16x16x32_f16 v[176:179], v[102:105], v[152:155], v[176:179]\n\t"
  "v_mfma_f32_16x16x32_f16 v[180:183], v[102:105], v[156:159], v[180:183]\n\tds_read_b64 v[102:103], v16 offset:512\n\tds_read_b64 v[104:105], v17 offset:512\n\t"
  "v_mfma_f32_16x16x32_f16 v[212:215], v[108:111], v[136:139], v[212:215]\n\t"
  "v_mfma_f32_16x16x32_f16 v[176:179], v[108:111], v[144:147], v[176:179]\n\tds_read_b64 v[124:125], v16 offset:0\n\tds_read_b64 v[126:127], v17 offset:0\n\t"
  "v_mfma_f32_16x16x32_f16 v[180:183], v[108:111], v[152:155], v[180:183]\n\t"
  "v_mfma_f32_16x16x32_f16 v[176:179], v[112:115], v[136:139], v[176:179]\n\t"
  "v_mfma_f32_16x16x32_f16 v[180:183], v[112:115], v[144:147], v[180:183]\n\ts_waitcnt lgkmcnt(5)\n\tv_readfirstlane_b32 s61, v101\n\t"
  "v_mfma_f32_16x16x32_f16 v[180:183], v[132:135], v[136:139], v[180:183]\n\t"
  "v_mfma_f32_16x16x32_f16 v[208:211], v[88:91], v[172:175], v[208:211]\n\ts_cmp_lt_u32 s61, 4\n\ts_cbranch_scc1 .Lc1_ub\n.Lc1_ub_b:\n\t"
  "v_mfma_f32_16x16x32_f16 v[208:211], v[92:95], v[168:171], v[208:211]\n\tds_read_b128 v[108:111], v18 offset:2240\n\tds_read_b128 v[112:115], v18 offset:2752\n\t"
  "v_mfma_f32_16x16x32_f16 v[212:215], v[92:95], v[172:175], v[212:215]\n\tds_read_b128 v[128:131], v18 offset:3264\n\tds_read_b128 v[132:135], v18 offset:3776\n\t"
  "v_mfma_f32_16x16x32_f16 v[208:211], v[96:99], v[164:167], v[208:211]\n\t"
  "v_mfma_f32_16x16x32_f16 v[212:215], v[96:99], v[168:171], v[212:215]\n\t"
  "v_mfma_f32_16x16x32_f16 v[176:179], v[96:99], v[172:175], v[176:179]\n\tds_read_b128 v[136:139], v18 offset:2176\n\tds_read_b128 v[140:143], v18 offset:2688\n\t"
  "v_mfma_f32_16x16x32_f16 v[208:211], v[116:119], v[160:163], v[208:211]\n\tds_read_b128 v[144:147], v18 offset:3200\n\tds_read_b128 v[148:151], v18 offset:3712\n\t"
  "v_mfma_f32_16x16x32_f16 v[212:215], v[116:119], v[164:167], v[212:215]\n\t"
  "v_mfma_f32_16x16x32_f16 v[176:179], v[116:119], v[168:171], v[176:179]\n\t"
  "v_mfma_f32_16x16x32_f16 v[180:183], v[116:119], v[172:175], v[180:183]\n\ts_waitcnt lgkmcnt(6)\n\t"
  "v_mfma_f32_16x16x32_f16 v[212:215], v[120:123], v[160:163], v[212:215]\n\tds_read_b128 v[116:119], v18 offset:2112\n\tds_read_b128 v[152:155], v18 offset:2624\n\t"
  "v_mfma_f32_16x16x32_f16 v[176:179], v[120:123], v[164:167], v[176:179]\n\tds_read_b128 v[156:159], v18 offset:3136\n\tds_read_b128 v[172:175], v18 offset:3648\n\t"
  "v_mfma_f32_16x16x32_f16 v[180:183], v[120:123], v[168:171], v[180:183]\n\t"
  "v_mfma_f32_16x16x32_f16 v[176:179], v[102:105], v[160:163], v[176:179]\n\t"
  "v_mfma_f32_16x16x32_f16 v[180:183], v[102:105], v[164:167], v[180:183]\n\t"
  "v_mfma_f32_16x16x32_f16 v[180:183], v[124:127], v[160:163], v[180:183]\n\tds_read_b128 v[104:107], v18 offset:2048\n\tds_read_b128 v[120:123], v18 offset:2560\n\t"
  "v_mfma_f32_16x16x32_f16 v[208:211], v[36:39], v[108:111], v[208:211]\n\tds_read_b128 v[124:127], v18 offset:3072\n\tds_read_b128 v[160:163], v18 offset:3584\n\t"
  "v_mfma_f32_16x16x32_f16 v[180:183], v[48:51], v[108:111], v[180:183]\n\t"
  "v_mfma_f32_16x16x32_f16 v[180:183], v[44:47], v[112:115], v[180:183]\n\ts_waitcnt lgkmcnt(6)\n\t"
  "v_mfma_f32_16x16x32_f16 v[180:183], v[40:43], v[128:131], v[180:183]\n\t"
  "v_mfma_f32_16x16x32_f16 v[180:183], v[36:39], v[132:135], v[180:183]\n\t"
  "v_mfma_f32_16x16x32_f16 v[208:211], v[52:55], v[136:139], v[208:211]\n\t"
  "v_mfma_f32_16x16x32_f16 v[180:183], v[64:67], v[136:139], v[180:183]\n\t"
  "v_mfma_f32_16x16x32_f16 v[180:183], v[60:63], v[140:143], v[180:183]\n\t"
  "v_mfma_f32_16x16x32_f16 v[180:183], v[56:59], v[144:147], v[180:183]\n\t"
  "v_mfma_f32_16x16x32_f16 v[180:183], v[52:55], v[148:151], v[180:183]\n\t"
  "v_mfma_f32_16x16x32_f16 v[208:211], v[68:71], v[116:119], v[208:211]\n\tds_read_b128 v[48:51], v19 offset:5120\n\tds_read_b128 v[64:67], v19 offset:6144\n\t"
  "v_mfma_f32_16x16x32_f16 v[180:183], v[80:83], v[116:119], v[180:183]\n\t"
  "v_mfma_f32_16x16x32_f16 v[180:183], v[76:79], v[152:155], v[180:183]\n\ts_waitcnt lgkmcnt(2)\n\t"
  "v_mfma_f32_16x16x32_f16 v[180:183], v[72:75], v[156:159], v[180:183]\n\t"
  "v_mfma_f32_16x16x32_f16 v[180:183], v[68:71], v[172:175], v[180:183]\n\t"
  "v_mfma_f32_16x16x32_f16 v[208:211], v[84:87], v[104:107], v[208:211]\n\t"
  "v_mfma_f32_16x16x32_f16 v[180:183], v[96:99], v[104:107], v[180:183]\n\t"
  "v_mfma_f32_16x16x32_f16 v[180:183], v[92:95], v[120:123], v[180:183]\n\t"
  "v_mfma_f32_16x16x32_f16 v[180:183], v[88:91], v[124:127], v[180:183]\n\tds_read_b32 v101, v27 offset:64\n\ts_waitcnt lgkmcnt(1)\n\t"
  "v_mfma_f32_16x16x32_f16 v[180:183], v[84:87], v[160:163], v[180:183]\n\tds_write_b128 v20, v[208:211] offset:59392\n\tv_perm_b32 v48, v48, v48, s60\n\t"
  "v_mfma_f32_16x16x32_f16 v[212:215], v[40:43], v[108:111], v[212:215]\n\ts_setprio 3\n\tbuffer_load_dwordx4 v[28:31], v23, s[56:59], 0 offen sc1\n\tbuffer_load_dwordx4 v[32:35], v24, s[56:59], 0 offen sc1\n\ts_setprio 0\n\t"
  "v_mfma_f32_16x16x32_f16 v[176:179], v[44:47], v[108:111], v[176:179]\n\tds_write_b128 v20, v[180:183] offset:60416\n\tv_perm_b32 v49, v49, v49, s60\n\t"
  "v_mfma_f32_16x16x32_f16 v[212:215], v[36:39], v[112:115], v[212:215]\n\ts_mov_b64 exec, 1\n\tds_add_u32 v27, v100 offset:16\n\ts_mov_b64 exec, -1\n\t"
  "v_mfma_f32_16x16x32_f16 v[176:179], v[40:43], v[112:115], v[176:179]\n\tv_perm_b32 v50, v50, v50, s60\n\tv_perm_b32 v51, v51, v51, s60\n\t"
  "v_mfma_f32_16x16x32_f16 v[176:179], v[36:39], v[128:131], v[176:179]\n\tv_perm_b32 v64, v64, v64, s60\n\tv_perm_b32 v65, v65, v65, s60\n\t"
  "v_mfma_f32_16x16x32_f16 v[212:215], v[56:59], v[136:139], v[212:215]\n\tv_perm_b32 v66, v66, v66, s60\n\tv_perm_b32 v67, v67, v67, s60\n\t"
  "v_mfma_f32_16x16x32_f16 v[176:179], v[60:63], v[136:139], v[176:179]\n\ts_waitcnt lgkmcnt(3)\n\tv_readfirstlane_b32 s61, v101\n\t"
  "v_mfma_f32_16x16x32_f16 v[212:215], v[52:55], v[140:143], v[212:215]\n\t"
  "v_mfma_f32_16x16x32_f16 v[176:179], v[56:59], v[140:143], v[176:179]\n\ts_cmp_lt_u32 s61, 4\n\ts_cbranch_scc1 .Lc1_p3\n.Lc1_p3_b:\n\t"
  "v_mfma_f32_16x16x32_f16 v[176:179], v[52:55], v[144:147], v[176:179]\n\tds_read_b32 v36, v27 offset:16\n\tds_read_b128 v[40:43], v20 offset:57344\n\tds_read_b128 v[44:47], v20 offset:58368\n\t"
  "v_mfma_f32_16x16x32_f16 v[212:215], v[72:75], v[116:119], v[212:215]\n\t"
  "v_mfma_f32_16x16x32_f16 v[176:179], v[76:79], v[116:119], v[176:179]\n\t"
  "v_mfma_f32_16x16x32_f16 v[212:215], v[68:71], v[152:155], v[212:215]\n\t"
  "v_mfma_f32_16x16x32_f16 v[176:179], v[72:75], v[152:155], v[176:179]\n\t"
  "v_mfma_f32_16x16x32_f16 v[176:179], v[68:71], v[156:159], v[176:179]\n\t"
  "v_mfma_f32_16x16x32_f16 v[212:215], v[88:91], v[104:107], v[212:215]\n\t"
  "v_mfma_f32_16x16x32_f16 v[176:179], v[92:95], v[104:107], v[176:179]\n\t"
  "v_mfma_f32_16x16x32_f16 v[212:215], v[84:87], v[120:123], v[212:215]\n\ts_waitcnt lgkmcnt(0)\n\tv_readfirstlane_b32 s61, v36\n\tds_read_b32 v25, v27 offset:32\n\t"
  "v_mfma_f32_16x16x32_f16 v[176:179], v[88:91], v[120:123], v[176:179]\n\ts_cmp_lt_u32 s61, 4\n\ts_cbranch_scc1 .Lc1_x4\n.Lc1_x4_b:\n\t"
  "v_mfma_f32_16x16x32_f16 v[176:179], v[84:87], v[124:127], v[176:179]\n\tv_fmac_f32 v48, v40, v26\n\tv_fmac_f32 v49, v41, v26\n\tv_fmac_f32 v50, v42, v26\n\t"
  "v_fmac_f32 v51, v43, v26\n\tv_fmac_f32 v64, v44, v26\n\tv_fmac_f32 v65, v45, v26\n\tv_fmac_f32 v66, v46, v26\n\t"
  "v_fmac_f32 v67, v47, v26\n\tv_fmac_f32 v48, v212, v26\n\tv_fmac_f32 v49, v213, v26\n\tv_fmac_f32 v50, v214, v26\n\t"
  "v_fmac_f32 v51, v215, v26\n\tv_cvt_pk_bf16_f32 v36, v48, v49\n\tv_cvt_pk_bf16_f32 v37, v50, v51\n\tv_fmac_f32 v64, v176, v26\n\t"
  "v_fmac_f32 v65, v177, v26\n\tv_fmac_f32 v66, v178, v26\n\tv_fmac_f32 v67, v179, v26\n\tv_cvt_pk_bf16_f32 v38, v64, v65\n\t"
  "v_cvt_pk_bf16_f32 v39, v66, v67\n\ts_nop 0\n\tv_permlane16_swap_b32 v36, v38\n\tv_permlane16_swap_b32 v37, v39\n\t"
  "s_setprio 3\n\tbuffer_store_dwordx4 v[36:39], v22, s[56:59], 0 offen sc1\n\ts_setprio 0\n\ts_waitcnt lgkmcnt(0)\n\ts_nop 0\n\t"
  ".Lc_end:\n\ts_waitcnt vmcnt(1) lgkmcnt(0)"
  : C1_OPS, "=&{v[28:31]}"(fl), "=&{v[32:35]}"(wfl), "=&{v25}"(g2)
  : "{s[56:59]}"(wrs), "{s60}"((a >> 1) ? 0x03020c0cu : 0x01000c0cu), [a] "s"(a)
  : "memory", "scc", "vcc", "s61", "v26", "v27", "v36", "v37", "v38", "v39", "v40", "v41", "v42", "v43", "v44", "v45", "v46", "v47", "v48", "v49", "v50", "v51", "v52", "v53", "v54", "v55", "v56", "v57", "v58", "v59", "v60", "v61", "v62", "v63", "v64", "v65", "v66", "v67", "v68", "v69", "v70", "v71", "v72", "v73", "v74", "v75", "v76", "v77", "v78", "v79", "v80", "v81", "v82", "v83", "v84", "v85", "v86", "v87", "v88", "v89", "v90", "v91", "v92", "v93", "v94", "v95", "v96", "v97", "v98", "v99", "v100", "v101", "v102", "v103", "v104", "v105", "v106", "v107", "v108", "v109", "v110", "v111", "v112", "v113", "v114", "v115", "v116", "v117", "v118", "v119", "v120", "v121", "v122", "v123", "v124", "v125", "v126", "v127", "v128", "v129", "v130", "v131", "v132", "v133", "v134", "v135", "v136", "v137", "v138", "v139", "v140", "v141", "v142", "v143", "v144", "v145", "v146", "v147", "v148", "v149", "v150", "v151", "v152", "v153", "v154", "v155", "v156", "v157", "v158", "v159", "v160", "v161", "v162", "v163", "v164", "v165", "v166", "v167", "v168", "v169", "v170", "v171", "v172", "v173", "v174", "v175", "v176", "v177", "v178", "v179", "v180", "v181", "v182", "v183", "v184", "v185", "v186", "v187", "v188", "v189", "v190", "v191", "v192", "v193", "v194", "v195", "v196", "v197", "v198", "v199", "v200", "v201", "v202", "v203", "v204", "v205", "v206", "v207", "v208", "v209", "v210", "v211", "v212", "v213", "v214", "v215", "v216", "v217", "v218", "v219", "v220", "v221", "v222", "v223");
  ST(0, 9);
}

struct XTake { u32 p, sw, idx; bf16x8 one; u64 m0, m1, m2; };
typedef __attribute__((ext_vector_type(16))) short bf16x16;
__device__ __forceinline__ void x_pre(XTake& X, int a, int lane) {
  X.p = (u32)(L_TA + a * 8192 + lane * 8);
  X.sw = (u32)(L_SP + ((lane & 15) * 4 + a) * 4 + ((lane >> 4) == ((lane >> 2) & 3) ? 768 : 0));
  X.idx = (lane & 1) ? 0xEEEEu : 0x4444u;
  X.one = __builtin_bit_cast(bf16x8, (u32x4){0x3F803F80u, 0x3F803F80u, 0x3F803F80u, 0x3F803F80u});
  X.m0 = 0xAAAAAAAAAAAAAAAAull; X.m1 = 0xCCCCCCCCCCCCCCCCull; X.m2 = 0xF0000F0000F0000Full;
  asm volatile("" : "+v"(X.p), "+v"(X.sw), "+v"(X.idx), "+v"(X.one));
  asm volatile("" : "+s"(X.m0), "+s"(X.m1), "+s"(X.m2));
}
struct NoMid { __device__ void operator()() const {} };
template <int H, int NY = 0, bool ALL = false, class F = NoMid>
__device__ __forceinline__ void x_take(const XTake& X, char* smem, rsrc_t wrs, int xin, int rb, int a, int lane, u32 sent = 0xFFFFFFFFu, F mid = F()) {
  constexpr int tile = H ? L_TB : L_TA, NT = H ? 3 : 2;
  const LAS char* p = (const LAS char*)(size_t)(H ? opaque(X.p + (L_TB - L_TA)) : X.p);
  const LAS char* ph = (const LAS char*)(size_t)(H ? opaque(X.p + (L_TH - L_TA) - a * 4096) : 0u);
  LAS float* spw = (LAS float*)(size_t)X.sw + H * 384;
  float s = 0.f, dv[NT];
  bf16x16 xf[NT][2];
  f32x4 ss[NT], sg[NT];
  auto ld_ = [&](int o, int j) __attribute__((always_inline)) {
    if (o < 2) xf[o][j] = __builtin_shufflevector(ld_tr8<1024>(p + o * 512 + j * 4096), ld_tr8<1024>(p + o * 512 + j * 4096 + 2048), 0, 1, 2, 3, 4, 5, 6, 7, 8, 9, 10, 11, 12, 13, 14, 15);
    else xf[o][j] = __builtin_shufflevector(ld_tr8<512>(ph + j * 2048), ld_tr8<512>(ph + j * 2048 + 1024), 0, 1, 2, 3, 4, 5, 6, 7, 8, 9, 10, 11, 12, 13, 14, 15);
    SB();
  };
  auto gr_ = [&](int o, int j) __attribute__((always_inline)) {
#define XLO(v_) __builtin_shufflevector(v_, v_, 0, 1, 2, 3, 4, 5, 6, 7)
#define XHI(v_) __builtin_shufflevector(v_, v_, 8, 9, 10, 11, 12, 13, 14, 15)
    if (j == 0) asm volatile("v_mfma_f32_16x16x32_bf16 %0, %1, %1, 0" : "=&v"(sg[o]) : "v"(XLO(xf[o][0])));
    else asm volatile("v_mfma_f32_16x16x32_bf16 %0, %1, %1, %0" : "+v"(sg[o]) : "v"(XLO(xf[o][1])));
    asm volatile("v_mfma_f32_16x16x32_bf16 %0, %1, %1, %0" : "+v"(sg[o]) : "v"(XHI(xf[o][j])));
    SB();
  };
  auto sm_ = [&](int o, int j) __attribute__((always_inline)) {
    asm volatile("v_smfmac_f32_16x16x64_bf16 %0, %1, %2, %3" : "+v"(ss[o]) : "v"(X.one), "v"(xf[o][j]), "v"(X.idx));
    SB();
  };
  auto fin_ = [&](int o, auto V_) __attribute__((always_inline)) {
    float t;
    float& d = dv[o];
    asm volatile("v_cndmask_b32_e64 %0, %3, %4, %9\n\tv_cndmask_b32_e64 %1, %5, %6, %9\n\tv_cndmask_b32_e64 %0, %0, %1, %10\n\t.if %12 >= 0\n\ts_nop %12\n\t.endif\n\t"
                 "v_add_f32 %1, %7, %8\n\tv_cndmask_b32_e64 %0, %1, %0, %11\n\tv_add_f32 %2, %2, %1"
                 : "=&v"(d), "=&v"(t), "+v"(s) : "v"(sg[o][0]), "v"(sg[o][1]), "v"(sg[o][2]), "v"(sg[o][3]), "v"(ss[o][0]), "v"(ss[o][1]),
                   "s"(X.m0), "s"(X.m1), "s"(X.m2), "n"(decltype(V_)::value - 1), "v"(ss[o]));
    SB();
  };
  auto zero_ = [&]() __attribute__((always_inline)) {
#pragma unroll
    for (int o = 0; o < NT; ++o) { ss[o] = (f32x4){0.f, 0.f, 0.f, 0.f}; asm volatile("" : "+v"(ss[o])); }
    asm volatile("" : "+v"(s));
  };
  auto first_ = [&]() __attribute__((always_inline)) { gr_(0, 0); sm_(0, 0); gr_(1, 0); sm_(1, 0); };
  auto second_ = [&]() __attribute__((always_inline)) {
    gr_(0, 1); sm_(0, 1); gr_(1, 1); fin_(0, IC<0>{}); sm_(1, 1);
    if (NT == 2) { fin_(1, IC<5>{}); spw[0] = dv[0]; spw[64] = dv[1]; }
  };
  auto third_ = [&]() __attribute__((always_inline)) {
    gr_(2, 0); fin_(1, IC<0>{}); gr_(2, 1);
    spw[64] = dv[0]; spw[128] = dv[1];
    SB();
    sm_(2, 0); sm_(2, 1); fin_(NT - 1, IC<5>{});
    spw[0] = dv[NT - 1];
  };
  zero_();
  SB();
  if (H) {
    const u32 sa = opaque((u32)(L_TB + a * 8192 + (lane >> 4) * 1024 + (lane & 15) * 32 + ((lane >> 3) & 1) * 512 + ((lane >> 4) & 3) * 8 + 4));
#pragma unroll 1
    for (unsigned spins = 0; spins <= SPIN_CAP; ++spins) {
      x_dma(wrs, xin, rb, tile, a, lane);
      asm volatile("s_waitcnt vmcnt(8)" ::: "memory");
      if (__builtin_amdgcn_ballot_w64(*(const LAS u32*)(size_t)sa == sent) == 0ull) break;
      FAILC(20);
    }
  } else asm volatile("s_waitcnt vmcnt(%0)" :: "n"(4 + NY) : "memory");
  if (H && B6 == 0) { __builtin_amdgcn_s_barrier(); ST(xin != OFF_X1, 14); asm volatile("" ::: "memory"); }
  ld_(0, 0); ld_(1, 0);
  if (!ALL && !(H && B6 == 1)) first_();
  asm volatile("s_waitcnt vmcnt(%0)" :: "n"(4 * (NT - 2) + NY) : "memory");
  if (H && B6) { __builtin_amdgcn_s_barrier(); ST(xin != OFF_X1, 14); asm volatile("" ::: "memory"); }
  ld_(0, 1); ld_(1, 1);
  if (ALL || (H && B6 == 1)) first_();
  second_();
  if (NT == 3) {
    asm volatile("s_waitcnt vmcnt(%0)" :: "n"(NY) : "memory");
    ld_(2, 0); ld_(2, 1);
    mid();
    third_();
  }
  if (__builtin_expect(__builtin_amdgcn_ballot_w64(s != s) != 0ull, 0)) {
    for (unsigned spins = 0;; ++spins) {
      FAILC((xin == OFF_X1 ? 1 : 9) + H);
      if (H) *(u32*)(smem + L_MI + M_RD) = xin == OFF_X1 ? 1 : 2;
      x_dma(wrs, xin, rb, tile, a, lane);
      asm volatile("s_waitcnt vmcnt(0)" ::: "memory");
      s = 0.f;
#pragma unroll
      for (int o = 0; o < 2 * NT; ++o) ld_(o >> 1, o & 1);
      zero_();
      first_(); second_();
      if (NT == 3) third_();
      if (!__any(s != s)) break;
      bool st = false;
#pragma unroll 1
      for (int e = 0; e < 4 * NT; ++e) { const u32x4 v = *(const u32x4*)(smem + (e < 8 ? tile + a * 8192 : L_TH + a * 4096 - 8192) + lane * 16 + e * 1024); st |= v[1] == sent || v[3] == sent; }
      if (!__any(st) || spins > SPIN_CAP) break;
    }
  }
}
#define PIN(x_) asm volatile("" : "+v"(x_))
struct HaloMsg { u32x4 k, v; };
__device__ __forceinline__ void halo_ld(HaloMsg& h, rsrc_t wrs, int ho) { h.k = ldw(wrs, ho); h.v = ldw(wrs, ho + 1024); }
__device__ __forceinline__ bool halo_ok(const HaloMsg& h, u32 tag) {
  return __builtin_amdgcn_ballot_w64((opaque((h.k[1] ^ tag) | (h.k[3] ^ tag)) | opaque((h.v[1] ^ tag) | (h.v[3] ^ tag))) != 0u) == 0ull;
}
struct AttC { f32x4 mp, mo, mf; u32x2 one; u32 so0, so1, sp0, sp1; };
__device__ __forceinline__ void att_pre(AttC& C, int lane, bool first) {
  const float NINF = -__builtin_inff();
  const u64 pm[4] = {0x0fff00ff000f0000ull, 0x3fff03ff003f0003ull, 0xf000ff00fff0ffffull, 0xc000fc00ffc0fffcull};
#pragma unroll
  for (int e = 0; e < 4; ++e) {
    const u64 par = (e & 1) ? 0xAAAAAAAAAAAAAAAAull : 0x5555555555555555ull;
    C.mp[e] = __builtin_amdgcn_inverse_ballot_w64(pm[e >> 1] & par) ? 0.f : NINF;
    C.mo[e] = __builtin_amdgcn_inverse_ballot_w64(pm[2 + (e >> 1)] & par) ? 0.f : NINF;
    C.mf[e] = first ? NINF : C.mp[e];
  }
  C.one = (u32x2){0x3F803F80u, 0x3F803F80u};
  const u32 sl = (lane & 1) ? 0x01000c0cu : 0x0c0c0100u, su = (lane & 1) ? 0x03020c0cu : 0x0c0c0302u;
  const bool o0 = __builtin_amdgcn_inverse_ballot_w64(pm[2]), o1 = __builtin_amdgcn_inverse_ballot_w64(pm[3]);
  C.so0 = o0 ? sl : 0x0c0c0c0cu; C.sp0 = o0 ? 0x0c0c0c0cu : sl; C.so1 = o1 ? su : 0x0c0c0c0cu; C.sp1 = o1 ? 0x0c0c0c0cu : su;
  asm volatile("" : "+v"(C.mp), "+v"(C.mo), "+v"(C.mf), "+v"(C.one), "+v"(C.so0), "+v"(C.so1), "+v"(C.sp0), "+v"(C.sp1));
}
template <class F, class F2>
__device__ __forceinline__ void attention(const AttC& C, u32 qp, HaloMsg& hm, int ho, u32 tag, rsrc_t wrs, int oo, int fid, F mid, F2 mid2) {
  u32x4 vv, ow;
  u32x2 q0;
  f32x4 l0, o1;
  float inv;
#define SOFT(F1_, F2_, W_, V_, O_)                                                                                                                      \
  F1_ "v_permlane16_swap_b32 v232, v233\n\tv_max_f32 v234, v232, v233\n\tv_max_f32 v235, v232, v233\n\t"                                                \
  F2_ "v_permlane32_swap_b32 v234, v235\n\tv_max_f32 v232, v234, v235\n\tv_mul_f32 v232, 0xbeb8aa3b, v232\n\t"                                          \
  "v_fmamk_f32 v220, v220, 0x3eb8aa3b, v232\n\tv_fmamk_f32 v221, v221, 0x3eb8aa3b, v232\n\t"                                                            \
  "v_exp_f32 v220, v220\n\tv_exp_f32 v221, v221\n\tv_mov_b32 v233, 0\n\tv_cvt_pk_bf16_f32 v232, v220, v221\n\t"                                         \
  "v_perm_b32 v226, v232, v232, %[sp0]\n\tv_perm_b32 v227, v232, v232, %[sp1]\n\t"                                                                      \
  "v_mfma_f32_16x16x16_bf16 v[236:239], %[one], v[232:233], 0\n\t"                                                                                      \
  "v_perm_b32 v224, v232, v232, %[so0]\n\tv_perm_b32 v225, v232, v232, %[so1]\n\t" W_ "\n\ts_nop 0\n\t"                                                 \
  "v_mfma_f32_16x16x32_bf16 " O_ ", " V_ ", v[224:227], 0\n\t"
  asm volatile(
      "ds_read2st64_b32 v[214:215], %[qp] offset0:4 offset1:5\n\tds_read2st64_b32 v[216:217], %[qp] offset0:6 offset1:7\n\t"
      "ds_read2st64_b32 v[212:213], %[qp] offset0:0 offset1:1\n\tds_read2st64_b32 v[218:219], %[qp] offset0:10 offset1:11\n\t"
      "ds_read2st64_b32 v[200:201], %[qp] offset0:2 offset1:3\n\tds_read2st64_b32 v[202:203], %[qp] offset0:8 offset1:9\n\t"
      "s_waitcnt lgkmcnt(4)\n\tv_mfma_f32_16x16x16_bf16 v[220:223], v[216:217], v[214:215], %[mp]\n\t"
      "s_waitcnt lgkmcnt(3)\n\tv_mfma_f32_16x16x16_bf16 v[224:227], v[212:213], v[214:215], %[mo]\n\t"
      "s_waitcnt lgkmcnt(2)\n\tv_mfma_f32_16x16x16_bf16 v[228:231], v[216:217], v[218:219], %[mo]\n\t"
      "v_max_f32 v220, v220, v221\n\tv_max_f32 v221, v222, v223\n\ts_nop 1\n\t"
      "v_max3_f32 v220, v220, v224, v225\n\tv_max3_f32 v221, v221, v226, v227\n\t"
      "v_max_f32 v233, v220, v221\n\tv_max_f32 v232, v220, v221\n\t"
      SOFT("v_max_f32 v228, v228, v229\n\tv_max_f32 v229, v230, v231\n\t", "s_nop 1\n\t", "s_waitcnt lgkmcnt(0)", "v[200:203]", "v[244:247]")
      "v_rcp_f32 v248, v236"
      : "=&{v[200:203]}"(vv), "=&{v[218:219]}"(q0), "=&{v[228:231]}"(l0), "=&{v[244:247]}"(o1), "=&{v248}"(inv)
      : [qp] "v"(qp), [mp] "v"(C.mp), [mo] "v"(C.mo), [one] "v"(C.one), [so0] "v"(C.so0), [so1] "v"(C.so1), [sp0] "v"(C.sp0), [sp1] "v"(C.sp1)
      : "memory", "v212", "v213", "v214", "v215", "v216", "v217", "v220", "v221", "v222", "v223", "v224", "v225", "v226", "v227", "v232", "v233", "v234", "v235", "v236", "v237", "v238", "v239");
  mid();
  {
    u32 n = SPIN_CAP;
    asm volatile("1:\n\tv_bitop3_b32 v232, v209, v211, %[tg] bitop3:0x7e\n\tv_bitop3_b32 v233, v205, v207, %[tg] bitop3:0x7e\n\tv_or_b32 v232, v232, v233\n\tv_cmp_ne_u32 vcc, 0, v232\n\t"
                 "s_cbranch_vccz 2f\n\ts_sleep 2\n\tbuffer_load_dwordx4 v[208:211], %[ho], %[rs], 0 offen sc1\n\tbuffer_load_dwordx4 v[204:207], %[ho], %[rs], 0 offen offset:1024 sc1\n\t"
                 "s_sub_u32 %[n], %[n], 1\n\ts_waitcnt vmcnt(0)\n\ts_cbranch_scc0 1b\n2:"
                 : "+{v[208:211]}"(hm.k), "+{v[204:207]}"(hm.v), [n] "+s"(n) : [tg] "s"(tag), [ho] "v"(ho), [rs] "s"(wrs) : "memory", "vcc", "scc", "v232", "v233");
  }
  mid2();
  asm volatile(
      "v_mov_b32 v209, v210\n\tv_mov_b32 v205, v206\n\ts_nop 0\n\tv_mfma_f32_16x16x16_bf16 v[220:223], v[208:209], v[218:219], %[mf]\n\t"
      "v_mul_f32 v244, v244, v248\n\tv_mul_f32 v245, v245, v248\n\tv_mul_f32 v246, v246, v248\n\tv_mul_f32 v247, v247, v248\n\t"
      "v_cvt_pk_bf16_f32 v242, v244, v245\n\tv_cvt_pk_bf16_f32 v243, v246, v247\n\ts_nop 1\n\t"
      "v_max3_f32 v220, v220, v221, v228\n\tv_max3_f32 v221, v222, v223, v229\n\t"
      "v_max_f32 v233, v220, v221\n\tv_max_f32 v232, v220, v221\n\t"
      SOFT("s_nop 1\n\t", "s_nop 1\n\t", "s_nop 0", "v[202:205]", "v[244:247]")
      "v_rcp_f32 v248, v236\n\ts_nop 6\n\t"
      "v_mul_f32 v244, v244, v248\n\tv_mul_f32 v245, v245, v248\n\tv_mul_f32 v246, v246, v248\n\tv_mul_f32 v247, v247, v248\n\t"
      "v_cvt_pk_bf16_f32 v240, v244, v245\n\tv_cvt_pk_bf16_f32 v241, v246, v247\n\ts_nop 0\n\t"
      "v_permlane16_swap_b32 v240, v242\n\tv_permlane16_swap_b32 v241, v243"
      : "=&{v[240:243]}"(ow), "+{v[208:211]}"(hm.k), "+{v[204:207]}"(hm.v), "+{v[228:231]}"(l0), "+{v[244:247]}"(o1), "+{v248}"(inv)
      : "{v[200:203]}"(vv), "{v[218:219]}"(q0), [mf] "v"(C.mf), [one] "v"(C.one), [so0] "v"(C.so0), [so1] "v"(C.so1), [sp0] "v"(C.sp0), [sp1] "v"(C.sp1)
      : "v220", "v221", "v222", "v223", "v224", "v225", "v226", "v227", "v232", "v233", "v234", "v235", "v236", "v237", "v238", "v239");
  stw(wrs, oo, ow);
  asm volatile("s_nop 1" :: "v"(ow) : "memory");
}
__device__ __forceinline__ void o_dma(rsrc_t wrs, int og, int ot, int a, int lane) {
  dma4k(wrs, lane * 16, og + a * 4096, ot + a * 4096);
  dma4k(wrs, lane * 16, og + 16384 + a * 4096, ot + 16384 + a * 4096);
}
__device__ __forceinline__ void o_get(rsrc_t wrs, int vo, int g0, int g1, int ot, u32 sa, u32 mark, int fid) {
#pragma unroll 1
  for (unsigned spins = 0; spins <= SPIN_CAP; ++spins) {
    dma4k(wrs, vo, g0, ot);
    dma4k(wrs, vo, g1, ot + 16384);
    asm volatile("s_waitcnt vmcnt(4)" ::: "memory");
    if (__builtin_amdgcn_ballot_w64(*(const LAS u32*)(size_t)sa == mark) == 0ull) break;
    FAILC(fid);
  }
}
template <bool L1, class F>
__device__ __forceinline__ void wo_mma2(f32x4 (&acc)[2], char* smem, int ot, rsrc_t wrs, int og, int a, int lane, const bf16x8 (&wof)[16], u32 cnt, const f32x4 (&r0)[2], const u32 (&qa)[4], u32 sent, F late) {
  u32x4 of[32];
#define LDO2(s_) of[s_] = *(const LAS u32x4*)((const LAS char*)(size_t)qa[(s_) >> 3] + (ot - L_TA) + ((s_) & 1) * 16384 + (((s_) >> 1) & 3) * 1024)
#define WMM(s_) do { SB(); if ((s_) < 2) asm volatile("v_mfma_f32_16x16x32_bf16 %0, %1, %2, %3" : "=&v"(acc[(s_) & 1]) \
        : "v"(L1 ? __builtin_bit_cast(bf16x8, of[s_]) : wof[0]), "v"(L1 ? wof[0] : __builtin_bit_cast(bf16x8, of[s_])), "v"(r0[(s_) & 1])); \
      else if (L1) asm volatile("v_mfma_f32_16x16x32_bf16 %0, %1, %2, %0" : "+v"(acc[(s_) & 1]) : "v"(of[s_]), "v"(wof[(s_) >> 1])); \
      else asm volatile("v_mfma_f32_16x16x32_bf16 %0, %1, %2, %0" : "+v"(acc[(s_) & 1]) : "v"(wof[(s_) >> 1]), "v"(of[s_])); SB(); } while (0)
  u32 mx = 0, cv;
#pragma unroll
  for (int s = 0; s < 8; ++s) { LDO2(s); SB(); }
#pragma unroll
  for (int s = 0; s < 3; ++s) {
    WMM(s);
#pragma unroll
    for (int e = 3 * s; e < 3 * s + 3 && e < 8; ++e) mx = max(max(mx, of[e][1]), of[e][3]);
    asm volatile("" : "+v"(mx));
  }
  if (__builtin_expect(!__all(mx < 0xFF810000u), 0)) {
    for (unsigned spins = 0; spins <= SPIN_CAP; ++spins) {
      FAILC((L1 ? 5 : 13) + (ot == L_TB));
      __builtin_amdgcn_s_sleep(2);
      o_dma(wrs, og, ot, a, lane);
      asm volatile("s_waitcnt vmcnt(0)" ::: "memory");
      bool st = false;
#pragma unroll
      for (int s = 0; s < 8; ++s) { LDO2(s); st |= of[s][1] == sent || of[s][3] == sent; }
      if (!__any(st)) break;
    }
    WMM(0); WMM(1); WMM(2);
    asm volatile("s_nop 7" : "+v"(acc[0]), "+v"(acc[1]));
  }
  cnt_add(smem, C_OT, lane);
  cv = *(const volatile LAS u32*)(size_t)(u32)(L_MI + C_OT);
  SB();
#pragma unroll
  for (int s = 3; s < WS5; ++s) WMM(s);
  {
    u32 n = 1u << 24;
    asm volatile("1:\n\tv_readfirstlane_b32 vcc_lo, %0\n\ts_sub_u32 %1, %1, 1\n\ts_cbranch_scc1 2f\n\ts_cmp_ge_u32 vcc_lo, %3\n\ts_cbranch_scc1 2f\n\tds_read_b32 %0, %2\n\ts_waitcnt lgkmcnt(0)\n\ts_branch 1b\n2:"
                 : "+v"(cv), "+s"(n) : "v"(L_MI + C_OT), "s"(cnt) : "memory", "vcc", "scc");
  }
#pragma unroll
  for (int s = 8; s < WS5 + 8; ++s) { LDO2(s); SB(); }
#pragma unroll
  for (int s = WS5; s < 32; ++s) {
    WMM(s);
    if (s + 8 < 32) LDO2(s + 8);
    if (s == 24) late();
  }
  asm volatile("s_nop 7" : "+v"(acc[0]), "+v"(acc[1]));
}
template <class F>
__device__ __forceinline__ void wo_last(f32x4 (&acc)[2], char* smem, rsrc_t wrs, int og, int a, int lane, const bf16x8 (&wof)[16], const f32x4 (&r0)[2], const u32 (&qa)[4], u32 sent, F put) {
  u32x4 of[32];
#define LDO3(q_, o_, s_) of[s_] = *(const LAS u32x4*)((const LAS char*)(size_t)q_[((s_) >> 2) & 3] + (o_) + ((s_) >> 4) * 16384 + ((s_) & 3) * 1024)
#define WM3(s_) do { SB(); if (((s_) & 15) == 0) asm volatile("v_mfma_f32_16x16x32_bf16 %0, %1, %2, %3" : "=&v"(acc[(s_) >> 4]) : "v"(wof[0]), "v"(of[s_]), "v"(r0[(s_) >> 4])); \
      else asm volatile("v_mfma_f32_16x16x32_bf16 %0, %1, %2, %0" : "+v"(acc[(s_) >> 4]) : "v"(wof[(s_) & 15]), "v"(of[s_])); SB(); } while (0)
  u32 qb[4];
#pragma unroll
  for (int e = 0; e < 4; ++e) qb[e] = qa[e];
  SB();
  __builtin_amdgcn_s_barrier();
  ST(1, 19);
#pragma unroll 1
  for (u32 pass = 0;; ++pass) {
    asm volatile("" : "+v"(qb[0]), "+v"(qb[1]), "+v"(qb[2]), "+v"(qb[3]) :: "memory");
#pragma unroll
    for (int s = 0; s < WLD; ++s) { LDO3(qb, L_TB - L_TA, s); SB(); }
#pragma unroll
    for (int s = 0; s < 32; ++s) {
      WM3(s);
      if (s + WLD == 16) asm volatile("s_cmp_lg_u32 %0, 0\n\ts_cbranch_scc1 1f\n\ts_barrier\n1:" :: "s"(__builtin_amdgcn_readfirstlane(pass)) : "memory", "scc");
      if (s + WLD < 32) LDO3(qb, L_TB - L_TA, s + WLD);
    }
    asm volatile("s_nop 7" : "+v"(acc[0]), "+v"(acc[1]));
    put();
    SB();
    if (__builtin_expect(__builtin_amdgcn_ballot_w64(__builtin_isunordered(acc[0][0], acc[1][0])) == 0ull, 1)) break;
    asm volatile("s_waitcnt vmcnt(0)" ::: "memory");
    FAILC(14);
    if (pass == 0) {
      lds_barrier();
      const u32 fx = *(const u32*)(smem + L_MI + M_FX);
#pragma unroll
      for (int e = 0; e < 4; ++e) qb[e] -= ((fx >> (8 * ((a + e) & 3))) & 1u) * (L_TB - L_TA);
    } else {
      u32 m = 1;
#pragma unroll 1
      for (int s = 0; s < 32; ++s) {
        const u32x4 v = *(const LAS u32x4*)(size_t)((s & 8 ? (s & 4 ? qb[3] : qb[2]) : (s & 4 ? qb[1] : qb[0])) + (L_TB - L_TA) + (s >> 4) * 16384 + (s & 3) * 1024);
        m = min(min(m, v[1] ^ sent), v[3] ^ sent);
      }
      if (__builtin_amdgcn_ballot_w64(m == 0u) == 0ull || pass > SPIN_CAP) break;
      lds_barrier();
      o_dma(wrs, og, __builtin_amdgcn_readfirstlane(qb[0]) + (L_TB - L_TA) - a * 4096, a, lane);
      asm volatile("s_waitcnt vmcnt(0)" ::: "memory");
      lds_barrier();
    }
  }
}
struct WoPre { f32x4 r[2]; int so; u32 xs, ao; };
template <bool L1>
__device__ __forceinline__ void wo_pre(WoPre& P, const u32x2 (&res)[2], char* ws, int H, int rb, int hb, int a, int lane) {
  const int i16 = lane & 15, kg = lane >> 4, R0 = 1024 * H + 32 * rb;
#pragma unroll
  for (int t = 0; t < 2; ++t) P.r[t] = (f32x4){lo2f(res[t][0]), hi2f(res[t][0]), lo2f(res[t][1]), hi2f(res[t][1])};
  if (L1) {
    P.so = OFF_X2 + ((32 * hb + 8 * a + (i16 >> 1)) * XS_ + R0 + 16 * (lane & 1) + 4 * kg) * 4;
    P.xs = (u32)(L_XS + H * 512 + lane * 8);
    P.ao = (u32)(OFF_ST2 + ((R0 & ~255) + ((lane >> 1) & 3) * 64 + ((R0 >> 2) & 63) + 4 * (lane & 1) + kg) * 16 + (lane & 8));
    asm volatile("" : "+v"(P.r[0]), "+v"(P.r[1]), "+v"(P.so), "+v"(P.xs), "+v"(P.ao));
  } else {
    P.so = ((R0 + 16 * (kg & 1) + i16) * 256 + 32 * hb + 8 * a + 4 * (kg >> 1)) * 4;
    asm volatile("" : "+v"(P.r[0]), "+v"(P.r[1]), "+v"(P.so));
  }
}
template <bool L1>
__device__ __forceinline__ void wo_epi(const f32x4 (&acc)[2], const WoPre& P, char* smem, rsrc_t wrs, char* ws, u32* out, int H, int lane, u32 ONES) {
  if (L1) {
    f32x4 lo_, hi_;
    pair_x(lo_, hi_, acc[0], acc[1]);
    u32x4 v;
#pragma unroll
    for (int e = 0; e < 4; ++e) v[e] = pk2(lo_[e], hi_[e]);
    asm volatile("" : "+v"(v));
    stw(wrs, P.so, v);
    SB();
    float sv, q0, q1, q2, q3;
#define RD_(d_, a_, c_, m_) "v_add_f32_dpp " d_ ", " a_ ", " a_ " " c_ " row_mask:0xf bank_mask:" m_ "\n\t"
    asm volatile("v_dot2_f32_bf16 %5, %13, %17, 0\n\tv_dot2_f32_bf16 %6, %14, %17, 0\n\tv_dot2_f32_bf16 %7, %15, %17, 0\n\tv_dot2_f32_bf16 %8, %16, %17, 0\n\t"
                 "v_dot2_f32_bf16 %9, %13, %13, 0\n\tv_dot2_f32_bf16 %10, %14, %14, 0\n\tv_dot2_f32_bf16 %11, %15, %15, 0\n\tv_dot2_f32_bf16 %12, %16, %16, 0\n\t"
                 RD_("%1", "%5", "row_ror:8", "0x3") RD_("%2", "%6", "row_ror:8", "0x3") RD_("%3", "%7", "row_ror:8", "0x3") RD_("%4", "%8", "row_ror:8", "0x3")
                 RD_("%1", "%9", "row_ror:8", "0xc") RD_("%2", "%10", "row_ror:8", "0xc") RD_("%3", "%11", "row_ror:8", "0xc") RD_("%4", "%12", "row_ror:8", "0xc")
                 RD_("%5", "%1", "row_shl:4", "0x5") RD_("%6", "%2", "row_shl:4", "0x5") RD_("%5", "%3", "row_shr:4", "0xa") RD_("%6", "%4", "row_shr:4", "0xa")
                 "s_nop 0\n\t"
                 RD_("%1", "%5", "quad_perm:[2,3,0,1]", "0xf") RD_("%2", "%6", "quad_perm:[2,3,0,1]", "0xf")
                 "v_cndmask_b32_e64 %0, %1, %2, %18"
                 : "=&v"(sv), "=&v"(q0), "=&v"(q1), "=&v"(q2), "=&v"(q3), "=&v"(lo_[0]), "=&v"(lo_[1]), "=&v"(lo_[2]), "=&v"(lo_[3]), "=&v"(hi_[0]), "=&v"(hi_[1]), "=&v"(hi_[2]), "=&v"(hi_[3])
                 : "v"(v[0]), "v"(v[1]), "v"(v[2]), "v"(v[3]), "v"(ONES), "s"(0xCCCCCCCCCCCCCCCCull));
    const float hf = __builtin_floorf(sv * 0.5f);
    const u32 lo = (u32)(fmaf(sv, 0.5f, -hf) * 4294967296.0f);
    const u64 mine = ((u64)(u32)(int)hf << 32) | (u64)((lo & ~63u) | 1u);
    const u64 old = __hip_atomic_fetch_add((LAS u64*)(size_t)P.xs, mine, __ATOMIC_RELAXED, __HIP_MEMORY_SCOPE_WORKGROUP);
    if (((u32)old & 63u) == 3u) __hip_atomic_fetch_add((GAS u64*)((GAS char*)ws + P.ao), old + mine, __ATOMIC_RELAXED, __HIP_MEMORY_SCOPE_AGENT);
  } else {
    auto r0s = __builtin_amdgcn_permlane16_swap(pk2(acc[0][0], acc[0][1]), pk2(acc[1][0], acc[1][1]), false, false);
    auto r1s = __builtin_amdgcn_permlane16_swap(pk2(acc[0][2], acc[0][3]), pk2(acc[1][2], acc[1][3]), false, false);
    const rsrc_t ors = __builtin_amdgcn_make_buffer_rsrc((void*)out, 0, 2 * MB_, 0x00020000);
    if (H == 0) __builtin_amdgcn_raw_buffer_store_b128((u32x4){r0s[0], r1s[0], r0s[1], r1s[1]}, ors, P.so, 0, OUTA);
    else __builtin_amdgcn_raw_buffer_store_b128((u32x4){r0s[0], r1s[0], r0s[1], r1s[1]}, ors, P.so, 0, OUTB);
  }
}
template <bool L1>
__device__ __forceinline__ void res_ld(u32x2 (&res)[2], const char* smem, int tile, int hb, int a, int lane) {
#pragma unroll
  for (int t = 0; t < 2; ++t) {
    const int o = tile + t * 512 + (4 * hb + a) * 1024;
    if (L1) res[t] = *(const u32x2*)(smem + o + (lane & 15) * 32 + (lane >> 4) * 8);
    else res[t] = __builtin_bit_cast(u32x2, __builtin_amdgcn_ds_read_tr16_b64_v4i16((LAS bf16x4*)(size_t)(u32)(o + lane * 8)));
  }
}
template <bool L1>
__device__ __forceinline__ void a_blayer(char* smem, rsrc_t wrs, char* ws, u32* out, int t, int a, bf16x8 (&wof)[16], u32 tag, u32x4 wfl, ConvK& ck, XTake& xt, AttC& ac) {
  const u32 t9 = L1 ? 0u : KADD(2, now_(), D9);
  t = (int)opaque((u32)t);
  const int lane = t & 63, i16 = lane & 15, kg = lane >> 4;
  const int XIN = L1 ? OFF_X1 : OFF_X3, OBUF = L1 ? OFF_O1 : OFF_O2;
  const int bid = BIDX;
  const int rb = RB_OF(bid), hb = HB_OF(bid);
  if (!L1) { x_pre(xt, a, lane); SB(); x_take<0, 0, true>(xt, smem, wrs, XIN, rb, a, lane); SB(); }
  const int og = OBUF + rb * 32768;
  u32x2 resa[2], resb[2];
  const int wfo = OFF_ST0 + 8192 + (hb * 4 + a) * 128 + (lane & 3) * 16;
#define LDWO(e_) wof[e_] = __builtin_bit_cast(bf16x8, ldws(wrs, ((hb * 64 + a) * 64 + lane) * 16, OFF_WO + ((4 * a + (e_)) & 15) * 4096))
  auto ld_wo = [&](int e0, int e1) __attribute__((always_inline)) {
#pragma unroll
    for (int e = e0; e < e1; ++e) LDWO(e);
    __builtin_amdgcn_sched_barrier(0);
  };
  const u32 mark = x0_mark(tag), sent = L1 ? mark : 0xFFFFFFFFu;
  if (L1) {
    tags_wait(wfl, wrs, wfo, tag, 6); ld_wo(0, WOE);
    SB();
    x_pre(xt, a, lane); SB();
    x_take<0, WOE, true>(xt, smem, wrs, XIN, rb, a, lane, sent);
  }
  ST(!L1, 10);
  SB();
  lds_barrier();
  ST(!L1, 11);
  const u32 t5 = L1 ? (u32)__builtin_amdgcn_s_memrealtime() : KADD(20, now_(), D5), c5 = L1 ? opaque(KADD(18, now_(), DO5)) : 0u;
  res_ld<L1>(resa, smem, L_TA, hb, a, lane);
  auto pf_x3 = [&]() __attribute__((always_inline)) {
    const u32x4 SENT = {0xFFFFFFFFu, 0xFFFFFFFFu, 0xFFFFFFFFu, 0xFFFFFFFFu};
    const int xo = OFF_X3 + (8 * a + (lane >> 3)) * XBS + ((bid & 7) * 32 + (bid >> 3)) * 128 + (lane & 7) * 16;
    stw(wrs, xo, SENT); stw(wrs, xo + 32 * XBS, SENT);
  };
  const int oo = (int)opaque((u32)(OBUF + (((rb * 2 + (kg & 1)) * 16 + hb * 2 + (a >> 1)) * 64 + (2 * (a & 1) + (kg >> 1)) * 16 + i16) * 16));
  const u32 ONES = opaque(0x3F803F80u);
  att_pre(ac, lane, rb == 0);
  if (L1) pf_x3();
  if (L1) SB();
  const int ho = (int)opaque((u32)((rb == 0 ? WSB : KVO(L1, rb, hb * 4 + a)) + lane * 16));
  const u32 qp = opaque((u32)(L_QB + a * 3072 + lane * 4));
  const int tso = (int)opaque((u32)(L1 ? OFF_T5 + rb * 1024 + (lane & 7) * 128 : OFF_TS + rb * 128 + (lane & 7) * 8)), tsw = (int)opaque((u32)(t == hb ? tso : WSB));
  if (L1) __builtin_amdgcn_raw_buffer_store_b64((u32x2){t5, tag}, wrs, tsw, 0, 16);
  HaloMsg hm;
  SB();
  if (L1) KSLP(1, SLP_XB1); else gate(tmax(t9, t5));
  ST(!L1, 12);
  x_take<1, 0, false>(xt, smem, wrs, XIN, 32 + rb, a, lane, sent, [&]() __attribute__((always_inline)) { if (L1) ld_wo(WOE, WOE + WX); });
  ST(!L1, 13);
  if (L1) SB();
  lds_barrier();
  ST(!L1, 15);
  if (L1) KSLP(3, SLP_H1); else KSLP(4, SLP_H2);
  halo_ld(hm, wrs, ho);
  if (L1) { SB(); ld_wo(WOE + WX, WOE + WX + WA); }
  u32x2 ts;
  u32 t7 = L1 ? t5 : 0u, tq = 0;
  if (!L1) {
    t7 = (u32)__builtin_amdgcn_s_memrealtime();
    tq = KADD(6, now_(), DQ2);
    __builtin_amdgcn_raw_buffer_store_b64((u32x2){t7, tag}, wrs, tsw, 0, 16);
  }
  SB();
  attention(ac, qp, hm, ho, rb == 0 ? 0u : L1 ? mark : tag, wrs, oo, L1 ? 3 : 11, [&]() __attribute__((always_inline)) { if (L1) SB(); }, [&]() __attribute__((always_inline)) {
    if (L1) {
      asm volatile("buffer_load_dwordx2 %0, %1, %2, 0 offen sc1" : "=&{v[250:251]}"(ts) : "v"(tso), "s"(wrs) : "memory");
      ld_wo(WOE + WX + WA, WOE + WX + WA + WM);
    }
  });
  const u32 to = L1 ? KADD(5, now_(), DO1) : KADD(21, now_(), DO2);
  ST(!L1, 16);
  if (L1) __builtin_amdgcn_raw_buffer_store_b32(tag, wrs, (bid | t) == 0 ? OFF_EP : WSB, 0, 16);
  if (L1) ld_wo(WOE + WX + WA + WM, 16);
  res_ld<L1>(resb, smem, L_TB, hb, a, lane);
  cnt_add(smem, C_OB, lane);
  WoPre wp, wq;
  u32 qa[4];
#pragma unroll
  for (int e = 0; e < 4; ++e) qa[e] = opaque((u32)(L_TA + ((a + e) & 3) * 4096 + lane * 16));
  wo_pre<L1>(wp, resa, ws, 0, rb, hb, a, lane);
  wo_pre<L1>(wq, resb, ws, 1, rb, hb, a, lane);
  const u32 sa = opaque((u32)(L_TA + a * 4096 + lane * 64 + (lane & 14) * 4 + 4));
  SB();
  int d;
  auto lead = [&]() __attribute__((always_inline)) {
    asm volatile("v_subrev_u32 %0, %2, v250\n\tv_cmp_eq_u32 vcc, %3, v251\n\tv_min_i32 %0, 0x96, %0\n\tv_cndmask_b32 %0, 64, %0, vcc\n\t"
                 "s_nop 1\n\tv_max_i32_dpp %0, %0, %0 quad_perm:[1,0,3,2] row_mask:0xf bank_mask:0xf\n\ts_nop 1\n\tv_max_i32_dpp %0, %0, %0 quad_perm:[2,3,0,1] row_mask:0xf bank_mask:0xf\n\t"
                 "s_nop 1\n\tv_max_i32_dpp %0, %0, %0 row_half_mirror row_mask:0xf bank_mask:0xf\n\tv_max_i32 %0, 0, %0\n\tv_mul_u32_u24 %0, 20, %0"
                 : "=&v"(d), "+{v[250:251]}"(ts) : "s"(t7), "s"(tag) : "vcc", "memory");
  };
  if (L1) {
    asm volatile("s_waitcnt vmcnt(%0)" :: "n"(18 - WOE - WX - WA) : "memory");
    lead();
    gate(tmax(to, __builtin_amdgcn_readfirstlane(c5 + d)));
  } else gate(tmax(tq, to));
  ST(!L1, 17);
  if (!L1) asm volatile("buffer_load_dwordx2 %0, %1, %2, 0 offen sc1" : "=&{v[250:251]}"(ts) : "v"(tso), "s"(wrs) : "memory");
  o_get(wrs, lane * 16, og + a * 4096, og + 16384 + a * 4096, L_TA + a * 4096, sa, mark, L1 ? 21 : 23);
  if (!L1) {
    lead();
    *(int*)(smem + L_MI + M_DL) = d;
    cnt_add(smem, C_OV, lane);
  }
  u32 ob = 0;
  int ob0 = og + MB_ + a * 4096, ob1 = ob0 + 16384;
  if (L1) asm volatile("" : "+s"(ob0), "+s"(ob1));
  asm volatile("s_waitcnt vmcnt(0)" ::: "memory");
  f32x4 acc[2];
  wo_mma2<L1>(acc, smem, L_TA, wrs, og, a, lane, wof, L1 ? 4 : 12, wp.r, qa, mark, [&]() __attribute__((always_inline)) { if (L1) ob = *(const volatile LAS u32*)(size_t)(u32)(L_MI + C_OB); });
  ST(!L1, 18);
  wo_epi<L1>(acc, wp, smem, wrs, ws, out, 0, lane, ONES);
  if (L1) {
    cnt_add(smem, C_X2, lane);
    if (__builtin_expect((u32)__builtin_amdgcn_readfirstlane(ob) < 8u, 0)) cnt_spin(smem, C_OB, 8);
    dma4k(wrs, lane * 16, ob0, L_TB + a * 4096);
    dma4k(wrs, lane * 16, ob1, L_TB + 16384 + a * 4096);
    a_conv_pre(ck, a, lane);
    ST(0, 19);
    asm volatile("s_waitcnt vmcnt(0)" ::: "memory");
    wo_mma2<L1>(acc, smem, L_TB, wrs, og + MB_, a, lane, wof, 8, wq.r, qa, mark, []() {});
    wo_epi<L1>(acc, wq, smem, wrs, ws, out, 1, lane, ONES);
  } else wo_last(acc, smem, wrs, og + MB_, a, lane, wof, wq.r, qa, mark, [&]() __attribute__((always_inline)) { wo_epi<L1>(acc, wq, smem, wrs, ws, out, 1, lane, ONES); });
  ST(!L1, 20); if (L1) ST(1, 1);
}

#define WREGS(X) \
  X(0, 76, 77, 78, 79) X(1, 80, 81, 82, 83) X(2, 84, 85, 86, 87) X(3, 88, 89, 90, 91) X(4, 92, 93, 94, 95) X(5, 96, 97, 98, 99) \
  X(6, 100, 101, 102, 103) X(7, 104, 105, 106, 107) X(8, 108, 109, 110, 111) X(9, 112, 113, 114, 115) X(10, 116, 117, 118, 119) X(11, 120, 121, 122, 123) \
  X(12, 124, 125, 126, 127) X(13, 128, 129, 130, 131) X(14, 132, 133, 134, 135) X(15, 136, 137, 138, 139) X(16, 140, 141, 142, 143) X(17, 144, 145, 146, 147) \
  X(18, 148, 149, 150, 151) X(19, 152, 153, 154, 155) X(20, 156, 157, 158, 159) X(21, 160, 161, 162, 163) X(22, 164, 165, 166, 167) X(23, 168, 169, 170, 171) \
  X(24, 172, 173, 174, 175) X(25, 176, 177, 178, 179) X(26, 180, 181, 182, 183) X(27, 184, 185, 186, 187) X(28, 188, 189, 190, 191) X(29, 192, 193, 194, 195) \
  X(30, 196, 197, 198, 199) X(31, 200, 201, 202, 203) X(32, 204, 205, 206, 207) X(33, 208, 209, 210, 211) X(34, 212, 213, 214, 215) X(35, 216, 217, 218, 219) \
  X(36, 220, 221, 222, 223) X(37, 224, 225, 226, 227) X(38, 228, 229, 230, 231) X(39, 232, 233, 234, 235) X(40, 236, 237, 238, 239) X(41, 240, 241, 242, 243) \
  X(42, 244, 245, 246, 247) X(43, 248, 249, 250, 251) X(44, 252, 253, 254, 255) X(45, 64, 65, 66, 67) X(46, 68, 69, 70, 71) X(47, 72, 73, 74, 75)
__device__ __forceinline__ void w_dma(rsrc_t r, int voff, int vof2, int soff, int lds) {
  asm volatile("s_mov_b32 m0, %3\n\ts_nop 0\n\tbuffer_load_dwordx4 %0, %1, %2 offen lds\n\t"
               "s_add_u32 m0, m0, 3072\n\ts_nop 0\n\tbuffer_load_dwordx4 %0, %1, %2 offen offset:1024 lds\n\t"
               "s_add_u32 m0, m0, 3072\n\ts_nop 0\n\tbuffer_load_dwordx4 %4, %1, %2 offen offset:2048 lds"
               :: "v"(voff), "s"(r), "s"(soff), "s"(lds), "v"(vof2) : "memory", "scc");
}
template <int I>
__device__ __forceinline__ void w_ld(bf16x8& r, int vo, rsrc_t rs) {
#define X(n_, a_, b_, c_, d_) if constexpr (I == n_) asm volatile("buffer_load_dwordx4 %0, %1, %2, %3 offen offset:%4 sc1" : "={v[" #a_ ":" #d_ "]}"(r) : "v"(vo), "s"(rs), "s"(OFF_WQ + (I / 3) * 98304), "n"((I % 3) * 1024) : "memory");
  WREGS(X)
#undef X
}
template <int I, int O>
__device__ __forceinline__ void w_get(bf16x8& r, u32 a) {
#define X(n_, a_, b_, c_, d_) if constexpr (I == n_) asm volatile("ds_read_b64_tr_b16 v[" #a_ ":" #b_ "], %1 offset:%2\n\tds_read_b64_tr_b16 v[" #c_ ":" #d_ "], %1 offset:%3" : "=&{v[" #a_ ":" #d_ "]}"(r) : "v"(a), "n"(O), "n"(O + 2048) : "memory");
  WREGS(X)
#undef X
}
template <int I>
__device__ __forceinline__ void w_pin(const bf16x8& r) {
#define X(n_, a_, b_, c_, d_) if constexpr (I == n_) asm volatile("" :: "{v[" #a_ ":" #d_ "]}"(r));
  WREGS(X)
#undef X
}
template <int I>
__device__ __forceinline__ void w_sum(f32x4& d, const bf16x8& one, u32 idx, const bf16x8 (&wf)[48]) {
  w_pin<I>(wf[I]); w_pin<(I + 1) % 48>(wf[(I + 1) % 48]);
  asm volatile("v_smfmac_f32_16x16x64_bf16 %0, %1, v[%3:%3+7], %2" : "+v"(d) : "v"(one), "v"(idx), "n"(I < 45 ? 76 + 4 * I : 64 + 4 * (I - 45)));
  w_pin<I>(wf[I]); w_pin<(I + 1) % 48>(wf[(I + 1) % 48]);
}
struct GPre { u32 ap, l4, fr, fq, sp, wx, mo, oo, tg, ly; };
template <bool L1>
__device__ __forceinline__ void g_pre(GPre& P, int g, u32 tag) {
  const int lane = lane_id(), bid = BIDX;
  const int rb = RB_OF(bid), hb = HB_OF(bid);
  P.mo = (u32)KVO(L1, (rb + 1) & 31, hb * 4 + g);
  P.oo = (u32)((L1 ? OFF_O1 : OFF_O2) + MB_ + ((rb * 32 + hb * 2 + (g >> 1)) * 64 + 32 * (g & 1)) * 16);
  P.tg = L1 ? x0_mark(tag) : tag;
  P.ly = L1 ? 1 : 2;
  asm volatile("" : "+{s60}"(P.mo), "+{s61}"(P.oo), "+{s62}"(P.tg), "+{s63}"(P.ly));
  P.ap = (u32)(L_TA + lane * 8);
  P.l4 = (u32)(lane * 4);
  P.fr = (u32)(L_FS + g * 768 + (lane & 15) * 4);
  P.fq = (u32)(L_FS + g * 768 + (lane >> 4) * 16);
  P.sp = (u32)(L_SP + (lane & 31) * 16);
  P.wx = (u32)(L_WX + g * 3072 + lane * 16);
  asm volatile("" : "+{v0}"(P.ap), "+{v1}"(P.l4), "+{v2}"(P.fr), "+{v3}"(P.fq), "+{v4}"(P.sp), "+{v5}"(P.wx));
}
template <bool L1, class F>
__device__ __forceinline__ void g_blayer(char* smem, rsrc_t wrs, int g, u32 tag, bf16x8 (&wf)[48], GPre& P, F mid) {
  if (!L1) ST(1, 10);
  auto pin = [&](auto I_) __attribute__((always_inline)) { w_pin<decltype(I_)::value>(wf[decltype(I_)::value]); };
  static_for<0, 45>(pin);
#define G_OPS : "+{v0}"(P.ap), "+{v1}"(P.l4), "+{v2}"(P.fr), "+{v3}"(P.fq), "+{v4}"(P.sp), "+{v5}"(P.wx) \
  : "{s[56:59]}"(wrs), "{s60}"(P.mo), "{s61}"(P.oo), "{s62}"(P.tg), "{s63}"(P.ly), "{s64}"(L_QB + g * 3072), "{s65}"(L_FS + g * 768), "{s66}"(L_WX + g * 3072) \
  : "memory", "scc", "vcc", "v6", "v7", "v8", "v9", "v10", "v11", "v12", "v13", "v14", "v15", "v16", "v17", "v18", "v19", "v20", "v21", "v22", "v23", "v24", "v25", "v26", "v27", "v28", "v29", "v30", "v31", "v32", "v33", "v34", "v35", "v36", "v37", "v38", "v39", "v40", "v41", "v42", "v43", "v44", "v45", "v46", "v47", "v48", "v49", "v50", "v51", "v52", "v53", "v54", "v55", "v56", "v57", "v58", "v59", "v60", "v61", "v62", "v63", "v64", "v65", "v66", "v67", "v68", "v69", "v70", "v71", "v72", "v73", "v74", "v75", "s54", "s55", "s67", "s68", "s69", "s70", "s71", "s72", "s73", "s74", "s75", "s76", "s77"
  if constexpr (L1) asm volatile(".Lg_body:\n\t"
  "ds_read_b128 v[24:27], v5 offset:1024\n\tds_read_b128 v[72:75], v5 offset:2048\n\tds_read_b128 v[20:23], v5 offset:0\n\t"
  "s_waitcnt lgkmcnt(3)\n\ts_barrier\n\t" GST(11)
  "ds_read_b64_tr_b16 v[60:61], v0 offset:31232\n\tds_read_b64_tr_b16 v[62:63], v0 offset:32256\n\tds_read_b64_tr_b16 v[16:17], v0 offset:512\n\tds_read_b64_tr_b16 v[18:19], v0 offset:1536\n\t"
  "ds_read_b128 v[28:31], v4\n\tds_read_b128 v[44:47], v4 offset:768\n\tds_read_b64_tr_b16 v[36:37], v0 offset:2560\n\tds_read_b64_tr_b16 v[38:39], v0 offset:3584\n\t"
  "ds_read_b64_tr_b16 v[40:41], v0 offset:4608\n\tds_read_b64_tr_b16 v[42:43], v0 offset:5632\n\ts_mov_b32 s68, 0xaaaaaaaa\n\ts_mov_b32 s69, 0xaaaaaaaa\n\t"
  "s_mov_b32 s74, 0xfff0ffff\n\ts_mov_b32 s75, 0xf000ff00\n\ts_mov_b32 s76, 0xffc0fffc\n\ts_mov_b32 s77, 0xc000fc00\n\t"
  "s_mov_b32 s67, 0x0c0c0100\n\ts_mov_b32 s55, 0x0c0c0302\n\ts_waitcnt lgkmcnt(8)\n\t"
  "v_mfma_f32_16x16x32_bf16 v[12:15], v[24:27], v[60:63], 0\n\tds_read_b64_tr_b16 v[68:69], v0 offset:6656\n\t"
  "v_mfma_f32_16x16x32_bf16 v[64:67], v[60:63], v[72:75], 0\n\tds_read_b64_tr_b16 v[70:71], v0 offset:7680\n\t"
  "v_mfma_f32_16x16x32_bf16 v[72:75], v[20:23], v[60:63], 0\n\ts_waitcnt lgkmcnt(2)\n\t"
  "v_mfma_f32_16x16x32_bf16 v[12:15], v[80:83], v[16:19], v[12:15]\n\tds_read_b64_tr_b16 v[4:5], v0 offset:8704\n\tv_add_f32 v28, v28, v29\n\t"
  "v_mfma_f32_16x16x32_bf16 v[64:67], v[16:19], v[84:87], v[64:67]\n\tds_read_b64_tr_b16 v[6:7], v0 offset:9728\n\tv_add_f32 v30, v30, v31\n\t"
  "v_mfma_f32_16x16x32_bf16 v[72:75], v[76:79], v[16:19], v[72:75]\n\tv_add_f32 v44, v44, v45\n\tv_add_f32 v28, v28, v30\n\t"
  "v_mfma_f32_16x16x32_bf16 v[12:15], v[92:95], v[36:39], v[12:15]\n\tds_read_b64_tr_b16 v[32:33], v0 offset:10752\n\tv_add_f32 v46, v46, v47\n\t"
  "v_mfma_f32_16x16x32_bf16 v[64:67], v[36:39], v[96:99], v[64:67]\n\tds_read_b64_tr_b16 v[34:35], v0 offset:11776\n\tv_mul_f32 v29, 0x3b000000, v28\n\t"
  "v_mfma_f32_16x16x32_bf16 v[72:75], v[88:91], v[36:39], v[72:75]\n\tv_add_f32 v44, v44, v46\n\tv_mul_f32_e64 v30, -v29, v29\n\t"
  "v_mfma_f32_16x16x32_bf16 v[12:15], v[104:107], v[40:43], v[12:15]\n\tds_read_b64_tr_b16 v[36:37], v0 offset:12800\n\tv_fmac_f32 v30, 0x3b000000, v44\n\t"
  "v_mfma_f32_16x16x32_bf16 v[64:67], v[40:43], v[108:111], v[64:67]\n\tds_read_b64_tr_b16 v[38:39], v0 offset:13824\n\tv_max_f32 v30, 0, v30\n\t"
  "v_mfma_f32_16x16x32_bf16 v[72:75], v[100:103], v[40:43], v[72:75]\n\tv_add_f32 v30, 0x3727c5ac, v30\n\ts_waitcnt lgkmcnt(4)\n\t"
  "v_mfma_f32_16x16x32_bf16 v[12:15], v[116:119], v[68:71], v[12:15]\n\tds_read_b64_tr_b16 v[60:61], v0 offset:14848\n\tv_add_u32 v31, s65, v1\n\t"
  "v_mfma_f32_16x16x32_bf16 v[64:67], v[68:71], v[120:123], v[64:67]\n\tds_read_b64_tr_b16 v[62:63], v0 offset:15872\n\t"
  "v_mfma_f32_16x16x32_bf16 v[72:75], v[112:115], v[68:71], v[72:75]\n\tv_rsq_f32 v30, v30\n\t"
  "v_mfma_f32_16x16x32_bf16 v[12:15], v[128:131], v[4:7], v[12:15]\n\tds_read_b64_tr_b16 v[52:53], v0 offset:16896\n\tds_write2st64_b32 v31, v30, v29 offset1:1\n\t"
  "v_mfma_f32_16x16x32_bf16 v[64:67], v[4:7], v[132:135], v[64:67]\n\tds_read_b64_tr_b16 v[54:55], v0 offset:17920\n\tds_read_b128 v[8:11], v3 offset:576\n\t"
  "v_mfma_f32_16x16x32_bf16 v[72:75], v[124:127], v[4:7], v[72:75]\n\tds_read2_b32 v[26:27], v2 offset0:64 offset1:80\n\ts_waitcnt lgkmcnt(7)\n\t"
  "v_mfma_f32_16x16x32_bf16 v[12:15], v[140:143], v[32:35], v[12:15]\n\tds_read_b64_tr_b16 v[16:17], v0 offset:18944\n\tds_read_b128 v[56:59], v3 offset:256\n\t"
  "v_mfma_f32_16x16x32_bf16 v[64:67], v[32:35], v[144:147], v[64:67]\n\tds_read_b64_tr_b16 v[18:19], v0 offset:19968\n\tds_read_b128 v[20:23], v3 offset:512\n\t"
  "v_mfma_f32_16x16x32_bf16 v[72:75], v[136:139], v[32:35], v[72:75]\n\tds_read_b32 v25, v2 offset:640\n\t"
  "v_mfma_f32_16x16x32_bf16 v[12:15], v[152:155], v[36:39], v[12:15]\n\tds_read_b64_tr_b16 v[48:49], v0 offset:20992\n\t"
  "v_mfma_f32_16x16x32_bf16 v[64:67], v[36:39], v[156:159], v[64:67]\n\tds_read_b64_tr_b16 v[50:51], v0 offset:22016\n\t"
  "v_mfma_f32_16x16x32_bf16 v[72:75], v[148:151], v[36:39], v[72:75]\n\ts_waitcnt lgkmcnt(9)\n\t"
  "v_mfma_f32_16x16x32_bf16 v[12:15], v[164:167], v[60:63], v[12:15]\n\tds_read_b64_tr_b16 v[44:45], v0 offset:23040\n\ts_waitcnt lgkmcnt(8)\n\t"
  "v_mfma_f32_16x16x32_bf16 v[64:67], v[60:63], v[168:171], v[64:67]\n\tds_read_b64_tr_b16 v[46:47], v0 offset:24064\n\tv_mul_f32_e64 v40, -v26, v8\n\t"
  "v_mfma_f32_16x16x32_bf16 v[72:75], v[160:163], v[60:63], v[72:75]\n\tv_mul_f32_e64 v41, -v26, v9\n\tv_mul_f32_e64 v42, -v26, v10\n\t"
  "v_mfma_f32_16x16x32_bf16 v[12:15], v[176:179], v[52:55], v[12:15]\n\tds_read_b64_tr_b16 v[60:61], v0 offset:25088\n\tv_mul_f32_e64 v43, -v26, v11\n\t"
  "v_mfma_f32_16x16x32_bf16 v[64:67], v[52:55], v[180:183], v[64:67]\n\tds_read_b64_tr_b16 v[62:63], v0 offset:26112\n\ts_waitcnt lgkmcnt(5)\n\t"
  "v_mfma_f32_16x16x32_bf16 v[72:75], v[172:175], v[52:55], v[72:75]\n\tv_mul_f32_e64 v4, -v56, v25\n\tv_mul_f32_e64 v5, -v57, v25\n\t"
  "v_mfma_f32_16x16x32_bf16 v[12:15], v[188:191], v[16:19], v[12:15]\n\t"
  GST(20)
  "ds_read_b64_tr_b16 v[36:37], v0 offset:27136\n\tds_read2_b32 v[30:31], v2 offset0:0 offset1:16\n\t"
  "v_mfma_f32_16x16x32_bf16 v[64:67], v[16:19], v[192:195], v[64:67]\n\tds_read_b64_tr_b16 v[38:39], v0 offset:28160\n\tv_mul_f32_e64 v6, -v58, v25\n\t"
  "v_mfma_f32_16x16x32_bf16 v[72:75], v[184:187], v[16:19], v[72:75]\n\tv_mul_f32_e64 v7, -v59, v25\n\ts_waitcnt lgkmcnt(5)\n\t"
  "v_mfma_f32_16x16x32_bf16 v[12:15], v[200:203], v[48:51], v[12:15]\n\tds_read_b64_tr_b16 v[16:17], v0 offset:29184\n\tv_mul_f32_e64 v56, -v26, v20\n\t"
  "v_mfma_f32_16x16x32_bf16 v[64:67], v[48:51], v[204:207], v[64:67]\n\tds_read_b64_tr_b16 v[18:19], v0 offset:30208\n\tv_mul_f32_e64 v57, -v26, v21\n\t"
  "v_mfma_f32_16x16x32_bf16 v[72:75], v[196:199], v[48:51], v[72:75]\n\tv_mul_f32_e64 v58, -v26, v22\n\tv_mul_f32_e64 v59, -v26, v23\n\t"
  "v_mfma_f32_16x16x32_bf16 v[12:15], v[212:215], v[44:47], v[12:15]\n\tds_read_b64_tr_b16 v[48:49], v0 offset:0\n\t"
  "v_mfma_f32_16x16x32_bf16 v[64:67], v[44:47], v[216:219], v[64:67]\n\tds_read_b64_tr_b16 v[50:51], v0 offset:1024\n\t"
  "v_mfma_f32_16x16x32_bf16 v[72:75], v[208:211], v[44:47], v[72:75]\n\ts_waitcnt lgkmcnt(4)\n\t"
  "v_mfma_f32_16x16x32_bf16 v[12:15], v[224:227], v[60:63], v[12:15]\n\tds_read_b64_tr_b16 v[44:45], v0 offset:2048\n\t"
  "v_mfma_f32_16x16x32_bf16 v[64:67], v[60:63], v[228:231], v[64:67]\n\tds_read_b64_tr_b16 v[46:47], v0 offset:3072\n\tv_lshl_add_u32 v26, v1, 2, s60\n\t"
  "v_mfma_f32_16x16x32_bf16 v[72:75], v[220:223], v[60:63], v[72:75]\n\t"
  "v_mfma_f32_16x16x32_bf16 v[12:15], v[236:239], v[36:39], v[12:15]\n\tds_read_b64_tr_b16 v[52:53], v0 offset:4096\n\tds_read_b128 v[32:35], v3 offset:320\n\t"
  "v_mfma_f32_16x16x32_bf16 v[64:67], v[36:39], v[240:243], v[64:67]\n\tds_read_b64_tr_b16 v[54:55], v0 offset:5120\n\t"
  "v_mfma_f32_16x16x32_bf16 v[72:75], v[232:235], v[36:39], v[72:75]\n\tds_read_b128 v[60:63], v3 offset:64\n\ts_waitcnt lgkmcnt(6)\n\t"
  "v_mfma_f32_16x16x32_bf16 v[12:15], v[248:251], v[16:19], v[12:15]\n\tds_read_b64_tr_b16 v[68:69], v0 offset:6144\n\t"
  "v_mfma_f32_16x16x32_bf16 v[64:67], v[16:19], v[252:255], v[64:67]\n\tds_read_b64_tr_b16 v[70:71], v0 offset:7168\n\t"
  "v_mfma_f32_16x16x32_bf16 v[72:75], v[244:247], v[16:19], v[72:75]\n\tv_fma_f32 v12, -v27, v8, v12\n\tv_fma_f32 v13, -v27, v9, v13\n\t"
  "v_mfma_f32_16x16x32_bf16 v[40:43], v[80:83], v[48:51], v[40:43]\n\tds_read_b64_tr_b16 v[36:37], v0 offset:8192\n\tv_fma_f32 v14, -v27, v10, v14\n\t"
  "v_mfma_f32_16x16x32_bf16 v[4:7], v[48:51], v[84:87], v[4:7]\n\tds_read_b64_tr_b16 v[38:39], v0 offset:9216\n\tv_fma_f32 v15, -v27, v11, v15\n\t"
  "v_mfma_f32_16x16x32_bf16 v[56:59], v[76:79], v[48:51], v[56:59]\n\tv_mul_f32 v12, v12, v31\n\ts_waitcnt lgkmcnt(5)\n\t"
  "v_mfma_f32_16x16x32_bf16 v[40:43], v[92:95], v[44:47], v[40:43]\n\tds_read_b64_tr_b16 v[48:49], v0 offset:10240\n\tv_mul_f32 v13, v13, v31\n\t"
  "v_mfma_f32_16x16x32_bf16 v[4:7], v[44:47], v[96:99], v[4:7]\n\tds_read_b64_tr_b16 v[50:51], v0 offset:11264\n\tv_mul_f32 v14, v14, v31\n\t"
  "v_mfma_f32_16x16x32_bf16 v[56:59], v[88:91], v[44:47], v[56:59]\n\t"
  GST(17)
  "v_mul_f32 v15, v15, v31\n\tv_cvt_pk_bf16_f32 v12, v12, v13\n\t"
  "v_mfma_f32_16x16x32_bf16 v[40:43], v[104:107], v[52:55], v[40:43]\n\tds_read_b64_tr_b16 v[44:45], v0 offset:12288\n\tv_cvt_pk_bf16_f32 v14, v14, v15\n\t"
  "v_mfma_f32_16x16x32_bf16 v[4:7], v[52:55], v[108:111], v[4:7]\n\tds_read_b64_tr_b16 v[46:47], v0 offset:13312\n\tv_mov_b32 v13, s62\n\t"
  "v_mfma_f32_16x16x32_bf16 v[56:59], v[100:103], v[52:55], v[56:59]\n\tv_mov_b32 v15, s62\n\ts_waitcnt lgkmcnt(4)\n\t"
  "v_mfma_f32_16x16x32_bf16 v[40:43], v[116:119], v[68:71], v[40:43]\n\tds_read_b64_tr_b16 v[16:17], v0 offset:14336\n\tv_fma_f32 v64, -v32, v25, v64\n\t"
  "v_mfma_f32_16x16x32_bf16 v[4:7], v[68:71], v[120:123], v[4:7]\n\tds_read_b64_tr_b16 v[18:19], v0 offset:15360\n\tv_fma_f32 v65, -v33, v25, v65\n\t"
  "v_mfma_f32_16x16x32_bf16 v[56:59], v[112:115], v[68:71], v[56:59]\n\tv_fma_f32 v66, -v34, v25, v66\n\tv_fma_f32 v67, -v35, v25, v67\n\t"
  "v_mfma_f32_16x16x32_bf16 v[40:43], v[128:131], v[36:39], v[40:43]\n\tds_read_b64_tr_b16 v[68:69], v0 offset:16384\n\tv_mul_f32 v64, v64, v60\n\t"
  "v_mfma_f32_16x16x32_bf16 v[4:7], v[36:39], v[132:135], v[4:7]\n\tds_read_b64_tr_b16 v[70:71], v0 offset:17408\n\tv_mul_f32 v65, v65, v61\n\t"
  "v_mfma_f32_16x16x32_bf16 v[56:59], v[124:127], v[36:39], v[56:59]\n\tv_mul_f32 v66, v66, v62\n\ts_waitcnt lgkmcnt(4)\n\t"
  "v_mfma_f32_16x16x32_bf16 v[40:43], v[140:143], v[48:51], v[40:43]\n\tds_read_b64_tr_b16 v[36:37], v0 offset:18432\n\tv_mul_f32 v67, v67, v63\n\t"
  "v_mfma_f32_16x16x32_bf16 v[4:7], v[48:51], v[144:147], v[4:7]\n\tds_read_b64_tr_b16 v[38:39], v0 offset:19456\n\tv_cvt_pk_bf16_f32 v64, v64, v65\n\t"
  "v_mfma_f32_16x16x32_bf16 v[56:59], v[136:139], v[48:51], v[56:59]\n\tv_cvt_pk_bf16_f32 v66, v66, v67\n\tv_mov_b32 v65, s62\n\t"
  "v_mfma_f32_16x16x32_bf16 v[40:43], v[152:155], v[44:47], v[40:43]\n\tds_read_b64_tr_b16 v[32:33], v0 offset:20480\n\tv_mov_b32 v67, s62\n\t"
  "v_mfma_f32_16x16x32_bf16 v[4:7], v[44:47], v[156:159], v[4:7]\n\tds_read_b64_tr_b16 v[34:35], v0 offset:21504\n\tv_fma_f32 v72, -v27, v20, v72\n\t"
  "v_mfma_f32_16x16x32_bf16 v[56:59], v[148:151], v[44:47], v[56:59]\n\tv_add_u32 v24, s64, v1\n\ts_waitcnt lgkmcnt(4)\n\t"
  "v_mfma_f32_16x16x32_bf16 v[40:43], v[164:167], v[16:19], v[40:43]\n\tds_read_b64_tr_b16 v[60:61], v0 offset:22528\n\tv_fma_f32 v73, -v27, v21, v73\n\t"
  "v_mfma_f32_16x16x32_bf16 v[4:7], v[16:19], v[168:171], v[4:7]\n\tds_read_b64_tr_b16 v[62:63], v0 offset:23552\n\tv_fma_f32 v74, -v27, v22, v74\n\t"
  "v_mfma_f32_16x16x32_bf16 v[56:59], v[160:163], v[16:19], v[56:59]\n\tv_fma_f32 v75, -v27, v23, v75\n\tv_mul_f32 v72, v72, v31\n\t"
  "v_mfma_f32_16x16x32_bf16 v[40:43], v[176:179], v[68:71], v[40:43]\n\tds_read_b64_tr_b16 v[44:45], v0 offset:24576\n\tv_mul_f32 v73, v73, v31\n\t"
  "v_mfma_f32_16x16x32_bf16 v[4:7], v[68:71], v[180:183], v[4:7]\n\tds_read_b64_tr_b16 v[46:47], v0 offset:25600\n\tv_mul_f32 v74, v74, v31\n\t"
  "v_mfma_f32_16x16x32_bf16 v[56:59], v[172:175], v[68:71], v[56:59]\n\tv_mul_f32 v75, v75, v31\n\ts_waitcnt lgkmcnt(4)\n\t"
  "v_mfma_f32_16x16x32_bf16 v[40:43], v[188:191], v[36:39], v[40:43]\n\tds_read_b64_tr_b16 v[68:69], v0 offset:26624\n\tv_cvt_pk_bf16_f32 v72, v72, v73\n\t"
  "v_mfma_f32_16x16x32_bf16 v[4:7], v[36:39], v[192:195], v[4:7]\n\tds_read_b64_tr_b16 v[70:71], v0 offset:27648\n\tv_cvt_pk_bf16_f32 v74, v74, v75\n\t"
  "v_mfma_f32_16x16x32_bf16 v[56:59], v[184:187], v[36:39], v[56:59]\n\t"
  "v_mfma_f32_16x16x32_bf16 v[40:43], v[200:203], v[32:35], v[40:43]\n\t"
  GST(13)
  "ds_read_b64_tr_b16 v[52:53], v0 offset:28672\n\t"
  "v_mfma_f32_16x16x32_bf16 v[4:7], v[32:35], v[204:207], v[4:7]\n\ts_barrier\n\tds_read_b64_tr_b16 v[54:55], v0 offset:29696\n\t"
  "v_mfma_f32_16x16x32_bf16 v[56:59], v[196:199], v[32:35], v[56:59]\n\tbuffer_store_dwordx4 v[12:15], v26, s[56:59], 0 offen offset:0 sc1\n\ts_waitcnt lgkmcnt(4)\n\t"
  "v_mfma_f32_16x16x32_bf16 v[40:43], v[212:215], v[60:63], v[40:43]\n\tds_read_b64_tr_b16 v[36:37], v0 offset:30720\n\tbuffer_store_dwordx4 v[64:67], v26, s[56:59], 0 offen offset:1024 sc1\n\t"
  "v_mfma_f32_16x16x32_bf16 v[4:7], v[60:63], v[216:219], v[4:7]\n\tds_read_b64_tr_b16 v[38:39], v0 offset:31744\n\tv_lshl_add_u32 v13, v1, 2, s66\n\tv_add_u32 v0, 0x8000, v0\n\t"
  "v_mfma_f32_16x16x32_bf16 v[56:59], v[208:211], v[60:63], v[56:59]\n\tds_read_b128 v[26:29], v13 offset:1024\n\tds_read_b128 v[60:63], v13 offset:2048\n\t"
  "v_mfma_f32_16x16x32_bf16 v[40:43], v[224:227], v[44:47], v[40:43]\n\tds_read_b64_tr_b16 v[32:33], v0 offset:0\n\tds_read_b128 v[16:19], v13 offset:0\n\t"
  "v_mfma_f32_16x16x32_bf16 v[4:7], v[44:47], v[228:231], v[4:7]\n\tds_read_b64_tr_b16 v[34:35], v0 offset:1024\n\tv_min_u32 v73, 188, v1\n\t"
  "v_mfma_f32_16x16x32_bf16 v[56:59], v[220:223], v[44:47], v[56:59]\n\tds_read_b128 v[48:51], v3\n\ts_waitcnt lgkmcnt(8)\n\t"
  "v_mfma_f32_16x16x32_bf16 v[40:43], v[236:239], v[68:71], v[40:43]\n\tds_read_b64_tr_b16 v[44:45], v0 offset:2048\n\tv_lshlrev_b32 v73, 2, v73\n\t"
  "v_mfma_f32_16x16x32_bf16 v[4:7], v[68:71], v[240:243], v[4:7]\n\tds_read_b64_tr_b16 v[46:47], v0 offset:3072\n\tv_add_u32 v73, 0x22c00, v73\n\t"
  "v_mfma_f32_16x16x32_bf16 v[56:59], v[232:235], v[68:71], v[56:59]\n\t"
  "v_mfma_f32_16x16x32_bf16 v[40:43], v[248:251], v[52:55], v[40:43]\n\tds_read_b64_tr_b16 v[68:69], v0 offset:8192\n\t"
  "v_mfma_f32_16x16x32_bf16 v[4:7], v[52:55], v[252:255], v[4:7]\n\tds_read_b64_tr_b16 v[70:71], v0 offset:9216\n\t"
  "v_mfma_f32_16x16x32_bf16 v[56:59], v[244:247], v[52:55], v[56:59]\n\ts_waitcnt lgkmcnt(5)\n\t"
  "v_mfma_f32_16x16x32_bf16 v[40:43], v[26:29], v[36:39], v[40:43]\n\tds_read_b64_tr_b16 v[26:27], v0 offset:10240\n\t"
  "v_mfma_f32_16x16x32_bf16 v[4:7], v[36:39], v[60:63], v[4:7]\n\tds_read_b64_tr_b16 v[28:29], v0 offset:11264\n\t"
  "v_mfma_f32_16x16x32_bf16 v[56:59], v[16:19], v[36:39], v[56:59]\n\t"
  GST(12)
  "v_mul_f32 v40, v40, v30\n\tv_mul_f32 v41, v41, v30\n\t"
  "v_mfma_f32_16x16x32_bf16 v[60:63], v[80:83], v[32:35], 0\n\tds_read_b64_tr_b16 v[36:37], v0 offset:16384\n\tv_mul_f32 v42, v42, v30\n\t"
  "v_mfma_f32_16x16x32_bf16 v[16:19], v[32:35], v[84:87], 0\n\tds_read_b64_tr_b16 v[38:39], v0 offset:17408\n\tv_mul_f32 v43, v43, v30\n\t"
  "v_mfma_f32_16x16x32_bf16 v[52:55], v[76:79], v[32:35], 0\n\tv_cvt_pk_bf16_f32 v40, v40, v41\n\ts_waitcnt lgkmcnt(4)\n\t"
  "v_mfma_f32_16x16x32_bf16 v[60:63], v[92:95], v[44:47], v[60:63]\n\tds_read_b64_tr_b16 v[32:33], v0 offset:18432\n\tv_cvt_pk_bf16_f32 v42, v42, v43\n\t"
  "v_mfma_f32_16x16x32_bf16 v[16:19], v[44:47], v[96:99], v[16:19]\n\tds_read_b64_tr_b16 v[34:35], v0 offset:19456\n\tv_mul_f32 v56, v56, v30\n\t"
  "v_mfma_f32_16x16x32_bf16 v[52:55], v[88:91], v[44:47], v[52:55]\n\tv_mul_f32 v57, v57, v30\n\tv_mul_f32 v58, v58, v30\n\t"
  "v_mfma_f32_16x16x32_bf16 v[60:63], v[128:131], v[68:71], v[60:63]\n\tds_read_b64_tr_b16 v[44:45], v0 offset:24576\n\tds_write2st64_b32 v24, v12, v14 offset0:0 offset1:1\n\tds_write2st64_b32 v24, v40, v42 offset0:6 offset1:7\n\t"
  "v_mfma_f32_16x16x32_bf16 v[16:19], v[68:71], v[132:135], v[16:19]\n\tds_read_b64_tr_b16 v[46:47], v0 offset:25600\n\tds_write2st64_b32 v24, v64, v66 offset0:2 offset1:3\n\t"
  "v_mfma_f32_16x16x32_bf16 v[52:55], v[124:127], v[68:71], v[52:55]\n\tds_write2st64_b32 v24, v72, v74 offset0:4 offset1:5\n\tv_mul_f32 v59, v59, v30\n\tv_cvt_pk_bf16_f32 v56, v56, v57\n\t"
  "v_cvt_pk_bf16_f32 v58, v58, v59\n\ts_waitcnt lgkmcnt(8)\n\t"
  "v_mfma_f32_16x16x32_bf16 v[60:63], v[140:143], v[26:29], v[60:63]\n\tds_read_b64_tr_b16 v[40:41], v0 offset:26624\n\tds_write2st64_b32 v24, v56, v58 offset0:10 offset1:11\n\t"
  "v_mfma_f32_16x16x32_bf16 v[16:19], v[26:29], v[144:147], v[16:19]\n\tds_read_b64_tr_b16 v[42:43], v0 offset:27648\n\tv_mul_f32 v4, v4, v48\n\t"
  "v_mfma_f32_16x16x32_bf16 v[52:55], v[136:139], v[26:29], v[52:55]\n\tv_mul_f32 v5, v5, v49\n\tv_mul_f32 v6, v6, v50\n\tv_mul_f32 v7, v7, v51\n\t"
  "v_cvt_pk_bf16_f32 v4, v4, v5\n\tv_cvt_pk_bf16_f32 v6, v6, v7\n\t"
  "v_mfma_f32_16x16x32_bf16 v[60:63], v[176:179], v[36:39], v[60:63]\n\tds_write2st64_b32 v24, v4, v6 offset0:8 offset1:9\n\t"
  "v_mfma_f32_16x16x32_bf16 v[16:19], v[36:39], v[180:183], v[16:19]\n\t"
  "v_mfma_f32_16x16x32_bf16 v[52:55], v[172:175], v[36:39], v[52:55]\n\tv_and_b32 v27, 0x40, v1\n\ts_waitcnt lgkmcnt(4)\n\t"
  "v_mfma_f32_16x16x32_bf16 v[60:63], v[188:191], v[32:35], v[60:63]\n\tv_lshl_add_u32 v27, v27, 8, s61\n\tv_and_b32 v26, 0x3c, v1\n\t"
  "v_mfma_f32_16x16x32_bf16 v[16:19], v[32:35], v[192:195], v[16:19]\n\ts_waitcnt lgkmcnt(0)\n\ts_barrier\n\tds_read_b32 v24, v73 offset:5700\n\t"
  "ds_read_b128 v[64:67], v73\n\tds_read_b128 v[48:51], v73 offset:768\n\t"
  "v_mfma_f32_16x16x32_bf16 v[52:55], v[184:187], v[32:35], v[52:55]\n\tds_read_b64_tr_b16 v[4:5], v0 offset:4096\n\tds_read_b64_tr_b16 v[6:7], v0 offset:5120\n\t"
  "v_mfma_f32_16x16x32_bf16 v[60:63], v[224:227], v[44:47], v[60:63]\n\tds_read_b64_tr_b16 v[72:73], v0 offset:6144\n\tds_read_b64_tr_b16 v[74:75], v0 offset:7168\n\t"
  "v_mfma_f32_16x16x32_bf16 v[16:19], v[44:47], v[228:231], v[16:19]\n\tds_read_b64_tr_b16 v[28:29], v0 offset:12288\n\tv_lshl_add_u32 v27, v26, 2, v27\n\t"
  "v_mfma_f32_16x16x32_bf16 v[52:55], v[220:223], v[44:47], v[52:55]\n\tds_read_b64_tr_b16 v[30:31], v0 offset:13312\n\tv_and_b32 v26, 0x80, v1\n\t"
  "v_mfma_f32_16x16x32_bf16 v[60:63], v[236:239], v[40:43], v[60:63]\n\tds_read_b64_tr_b16 v[68:69], v0 offset:14336\n\tv_lshl_add_u32 v27, v26, 1, v27\n\t"
  "v_mfma_f32_16x16x32_bf16 v[16:19], v[40:43], v[240:243], v[16:19]\n\tds_read_b64_tr_b16 v[70:71], v0 offset:15360\n\t"
  "v_mfma_f32_16x16x32_bf16 v[52:55], v[232:235], v[40:43], v[52:55]\n\ts_waitcnt lgkmcnt(9)\n\ts_waitcnt lgkmcnt(6)\n\t"
  "v_mfma_f32_16x16x32_bf16 v[60:63], v[104:107], v[4:7], v[60:63]\n\tds_read_b64_tr_b16 v[12:13], v0 offset:20480\n\tv_readfirstlane_b32 s54, v24\n\t"
  "v_mfma_f32_16x16x32_bf16 v[16:19], v[4:7], v[108:111], v[16:19]\n\tds_read_b64_tr_b16 v[14:15], v0 offset:21504\n\tv_add_f32 v64, v64, v65\n\t"
  "v_mfma_f32_16x16x32_bf16 v[52:55], v[100:103], v[4:7], v[52:55]\n\ts_cmp_eq_u32 s54, s63\n\ts_cbranch_scc1 .Lg_redo\n.Lg_back:\n\ts_waitcnt lgkmcnt(5)\n\t"
  "v_mfma_f32_16x16x32_bf16 v[60:63], v[116:119], v[72:75], v[60:63]\n\tds_read_b64_tr_b16 v[44:45], v0 offset:22528\n\tv_add_f32 v66, v66, v67\n\t"
  "v_mfma_f32_16x16x32_bf16 v[16:19], v[72:75], v[120:123], v[16:19]\n\tds_read_b64_tr_b16 v[46:47], v0 offset:23552\n\tv_add_f32 v48, v48, v49\n\t"
  "v_mfma_f32_16x16x32_bf16 v[52:55], v[112:115], v[72:75], v[52:55]\n\tv_lshl_add_u32 v24, v1, 2, s66\n\ts_waitcnt lgkmcnt(4)\n\t"
  "v_mfma_f32_16x16x32_bf16 v[60:63], v[152:155], v[28:31], v[60:63]\n\tds_read_b64_tr_b16 v[4:5], v0 offset:30720\n\tv_add_f32 v64, v64, v66\n\t"
  "v_mfma_f32_16x16x32_bf16 v[16:19], v[28:31], v[156:159], v[16:19]\n\tds_read_b64_tr_b16 v[6:7], v0 offset:31744\n\tds_read_b128 v[36:39], v24 offset:1024\n\t"
  "v_mfma_f32_16x16x32_bf16 v[52:55], v[148:151], v[28:31], v[52:55]\n\tv_add_f32 v50, v50, v51\n\tv_mul_f32 v65, 0x3b000000, v64\n\t"
  "v_mfma_f32_16x16x32_bf16 v[60:63], v[164:167], v[68:71], v[60:63]\n\tds_read_b64_tr_b16 v[40:41], v0 offset:28672\n\tv_add_f32 v48, v48, v50\n\t"
  "v_mfma_f32_16x16x32_bf16 v[16:19], v[68:71], v[168:171], v[16:19]\n\tds_read_b64_tr_b16 v[42:43], v0 offset:29696\n\tv_mul_f32_e64 v66, -v65, v65\n\tv_fmac_f32 v66, 0x3b000000, v48\n\t"
  "v_max_f32 v66, 0, v66\n\tv_add_f32 v66, 0x3727c5ac, v66\n\t"
  "v_mfma_f32_16x16x32_bf16 v[52:55], v[160:163], v[68:71], v[52:55]\n\tds_read_b64_tr_b16 v[68:69], v0 offset:49152\n\tv_rsq_f32 v66, v66\n\tv_add_u32 v67, s65, v1\n\t"
  "ds_write2st64_b32 v67, v66, v65 offset1:1\n\ts_waitcnt lgkmcnt(6)\n\t"
  "v_mfma_f32_16x16x32_bf16 v[60:63], v[200:203], v[12:15], v[60:63]\n\tds_read_b64_tr_b16 v[70:71], v0 offset:49664\n\tds_read_b128 v[32:35], v24 offset:2048\n\tds_read2_b32 v[66:67], v2 offset0:80 offset1:96\n\t"
  "ds_read_b128 v[48:51], v24 offset:0\n\t"
  "v_mfma_f32_16x16x32_bf16 v[16:19], v[12:15], v[204:207], v[16:19]\n\tds_read_b64_tr_b16 v[56:57], v0 offset:50176\n\t"
  "v_mfma_f32_16x16x32_bf16 v[52:55], v[196:199], v[12:15], v[52:55]\n\tds_read_b64_tr_b16 v[58:59], v0 offset:50688\n\t"
  "v_mfma_f32_16x16x32_bf16 v[60:63], v[212:215], v[44:47], v[60:63]\n\tds_read_b64_tr_b16 v[72:73], v0 offset:51200\n\t"
  "v_mfma_f32_16x16x32_bf16 v[16:19], v[44:47], v[216:219], v[16:19]\n\tds_read_b64_tr_b16 v[74:75], v0 offset:51712\n\t"
  "v_mfma_f32_16x16x32_bf16 v[52:55], v[208:211], v[44:47], v[52:55]\n\tds_read_b64_tr_b16 v[44:45], v0 offset:52224\n\ts_waitcnt lgkmcnt(10)\n\t"
  "v_mfma_f32_16x16x32_bf16 v[60:63], v[36:39], v[4:7], v[60:63]\n\tds_read_b64_tr_b16 v[46:47], v0 offset:52736\n\ts_waitcnt lgkmcnt(8)\n\t"
  "v_mfma_f32_16x16x32_bf16 v[16:19], v[4:7], v[32:35], v[16:19]\n\tds_read_b64_tr_b16 v[32:33], v0 offset:53248\n\ts_waitcnt lgkmcnt(7)\n\t"
  "v_mfma_f32_16x16x32_bf16 v[52:55], v[48:51], v[4:7], v[52:55]\n\tds_read_b64_tr_b16 v[34:35], v0 offset:53760\n\tds_read_b128 v[4:7], v3 offset:320\n\t"
  "v_mfma_f32_16x16x32_bf16 v[60:63], v[248:251], v[40:43], v[60:63]\n\tds_read_b64_tr_b16 v[28:29], v0 offset:54272\n\tv_mul_f32_e64 v48, -v67, v8\n\t"
  "v_mfma_f32_16x16x32_bf16 v[16:19], v[40:43], v[252:255], v[16:19]\n\tds_read_b64_tr_b16 v[30:31], v0 offset:54784\n\tv_mul_f32_e64 v49, -v67, v9\n\t"
  "v_mfma_f32_16x16x32_bf16 v[52:55], v[244:247], v[40:43], v[52:55]\n\tds_read_b64_tr_b16 v[36:37], v0 offset:55296\n\tv_fma_f32 v60, -v66, v8, v60\n\t"
  "v_mfma_f32_16x16x32_bf16 v[12:15], v[80:83], v[68:71], 0\n\t"
  GST(18)
  "ds_read_b64_tr_b16 v[38:39], v0 offset:55808\n\tv_fma_f32 v61, -v66, v9, v61\n\t"
  "v_mfma_f32_16x16x32_bf16 v[40:43], v[68:71], v[84:87], 0\n\tds_read_b64_tr_b16 v[68:69], v0 offset:56320\n\ts_waitcnt lgkmcnt(8)\n\t"
  "v_mfma_f32_16x16x32_bf16 v[12:15], v[92:95], v[56:59], v[12:15]\n\tds_read_b64_tr_b16 v[70:71], v0 offset:56832\n\tv_fma_f32 v62, -v66, v10, v62\n\t"
  "v_mfma_f32_16x16x32_bf16 v[40:43], v[56:59], v[96:99], v[40:43]\n\tds_read_b64_tr_b16 v[56:57], v0 offset:57344\n\tv_fma_f32 v63, -v66, v11, v63\n\t"
  "v_mfma_f32_16x16x32_bf16 v[12:15], v[104:107], v[72:75], v[12:15]\n\tds_read_b64_tr_b16 v[58:59], v0 offset:57856\n\tv_cvt_pk_bf16_f32 v64, v60, v61\n\t"
  "v_mfma_f32_16x16x32_bf16 v[40:43], v[72:75], v[108:111], v[40:43]\n\tds_read_b64_tr_b16 v[60:61], v0 offset:58368\n\tv_cvt_pk_bf16_f32 v65, v62, v63\n\t"
  "v_mfma_f32_16x16x32_bf16 v[12:15], v[116:119], v[44:47], v[12:15]\n\tds_read_b64_tr_b16 v[62:63], v0 offset:58880\n\tv_fma_f32 v52, -v66, v20, v52\n\t"
  "v_mfma_f32_16x16x32_bf16 v[40:43], v[44:47], v[120:123], v[40:43]\n\tds_read_b64_tr_b16 v[72:73], v0 offset:59392\n\ts_waitcnt lgkmcnt(7)\n\t"
  "v_mfma_f32_16x16x32_bf16 v[12:15], v[128:131], v[32:35], v[12:15]\n\tds_read_b64_tr_b16 v[74:75], v0 offset:59904\n\tv_fma_f32 v16, -v4, v25, v16\n\t"
  "v_mfma_f32_16x16x32_bf16 v[40:43], v[32:35], v[132:135], v[40:43]\n\tds_read_b64_tr_b16 v[32:33], v0 offset:60416\n\tv_fma_f32 v17, -v5, v25, v17\n\t"
  "v_mfma_f32_16x16x32_bf16 v[12:15], v[140:143], v[28:31], v[12:15]\n\tds_read_b64_tr_b16 v[34:35], v0 offset:60928\n\tv_fma_f32 v18, -v6, v25, v18\n\tv_fma_f32 v19, -v7, v25, v19\n\t"
  "v_cvt_pk_bf16_f32 v46, v16, v17\n\tv_cvt_pk_bf16_f32 v47, v18, v19\n\t"
  "v_mfma_f32_16x16x32_bf16 v[40:43], v[28:31], v[144:147], v[40:43]\n\tds_read_b64_tr_b16 v[28:29], v0 offset:61440\n\tv_fma_f32 v53, -v66, v21, v53\n\t"
  "v_mfma_f32_16x16x32_bf16 v[12:15], v[152:155], v[36:39], v[12:15]\n\tds_read_b64_tr_b16 v[30:31], v0 offset:61952\n\tv_fma_f32 v54, -v66, v22, v54\n\t"
  "v_mfma_f32_16x16x32_bf16 v[40:43], v[36:39], v[156:159], v[40:43]\n\tds_read_b64_tr_b16 v[16:17], v0 offset:62464\n\ts_waitcnt lgkmcnt(6)\n\t"
  "v_mfma_f32_16x16x32_bf16 v[12:15], v[164:167], v[68:71], v[12:15]\n\tds_read_b64_tr_b16 v[18:19], v0 offset:62976\n\tv_fma_f32 v55, -v66, v23, v55\n\t"
  "v_mfma_f32_16x16x32_bf16 v[40:43], v[68:71], v[168:171], v[40:43]\n\tds_read_b64_tr_b16 v[36:37], v0 offset:63488\n\tv_cvt_pk_bf16_f32 v44, v52, v53\n\t"
  "v_mfma_f32_16x16x32_bf16 v[12:15], v[176:179], v[56:59], v[12:15]\n\tds_read_b64_tr_b16 v[38:39], v0 offset:64000\n\tv_cvt_pk_bf16_f32 v45, v54, v55\n\t"
  "v_mfma_f32_16x16x32_bf16 v[40:43], v[56:59], v[180:183], v[40:43]\n\tds_read_b64_tr_b16 v[56:57], v0 offset:64512\n\tv_mul_f32_e64 v50, -v67, v10\n\t"
  "v_mfma_f32_16x16x32_bf16 v[12:15], v[188:191], v[60:63], v[12:15]\n\tds_read_b64_tr_b16 v[58:59], v0 offset:65024\n\tv_lshl_add_u32 v66, v1, 2, s66\n\t"
  "v_mfma_f32_16x16x32_bf16 v[40:43], v[60:63], v[192:195], v[40:43]\n\tds_read_b32 v24, v2 offset:256\n\ts_waitcnt lgkmcnt(7)\n\t"
  "v_mfma_f32_16x16x32_bf16 v[12:15], v[200:203], v[72:75], v[12:15]\n\tds_read_b64_tr_b16 v[4:5], v0 offset:31232\n\tv_and_or_b32 v26, v1, 4, v3\n\tv_add_u32 v63, 64, v26\n\t"
  "v_mfma_f32_16x16x32_bf16 v[40:43], v[72:75], v[204:207], v[40:43]\n\tds_read_b64_tr_b16 v[6:7], v0 offset:32256\n\tv_cndmask_b32_e64 v68, v26, v63, s[74:75]\n\tv_cndmask_b32_e64 v26, v26, v63, s[76:77]\n\t"
  "v_mfma_f32_16x16x32_bf16 v[12:15], v[212:215], v[32:35], v[12:15]\n\tds_read_b128 v[60:63], v66 offset:1024\n\tds_read_b128 v[72:75], v66 offset:2048\n\t"
  "v_mfma_f32_16x16x32_bf16 v[40:43], v[32:35], v[216:219], v[40:43]\n\tds_read_b64_tr_b16 v[52:53], v0 offset:512\n\tv_mul_f32_e64 v51, -v67, v11\n\t"
  "v_mfma_f32_16x16x32_bf16 v[12:15], v[224:227], v[28:31], v[12:15]\n\tds_read_b64_tr_b16 v[54:55], v0 offset:1536\n\tds_read_b128 v[32:35], v66 offset:0\n\t"
  "v_mfma_f32_16x16x32_bf16 v[40:43], v[28:31], v[228:231], v[40:43]\n\tds_read2_b32 v[70:71], v68 offset0:0 offset1:16\n\ts_waitcnt lgkmcnt(9)\n\t"
  "v_mfma_f32_16x16x32_bf16 v[12:15], v[236:239], v[16:19], v[12:15]\n\tds_read_b64_tr_b16 v[28:29], v0 offset:2560\n\tds_read2_b32 v[68:69], v26 offset0:2 offset1:18\n\t"
  "v_mfma_f32_16x16x32_bf16 v[40:43], v[16:19], v[240:243], v[40:43]\n\tds_read_b64_tr_b16 v[30:31], v0 offset:3584\n\tv_mul_f32_e64 v16, -v67, v20\n\t"
  "v_mfma_f32_16x16x32_bf16 v[12:15], v[248:251], v[36:39], v[12:15]\n\tv_mul_f32_e64 v17, -v67, v21\n\tv_mul_f32_e64 v18, -v67, v22\n\tv_mul_f32_e64 v19, -v67, v23\n\t"
  "v_mfma_f32_16x16x32_bf16 v[40:43], v[36:39], v[252:255], v[40:43]\n\tds_read_b64_tr_b16 v[20:21], v0 offset:4608\n\ts_waitcnt lgkmcnt(8)\n\t"
  "v_mfma_f32_16x16x32_bf16 v[12:15], v[60:63], v[56:59], v[12:15]\n\tds_read_b64_tr_b16 v[22:23], v0 offset:5632\n\tds_read_b128 v[36:39], v3 offset:256\n\t"
  "v_mfma_f32_16x16x32_bf16 v[40:43], v[56:59], v[72:75], v[40:43]\n\tds_read2_b32 v[66:67], v2 offset0:16 offset1:32\n\t"
  "v_mfma_f32_16x16x32_bf16 v[48:51], v[60:63], v[4:7], v[48:51]\n\t"
  GST(19)
  "ds_read_b64_tr_b16 v[60:61], v0 offset:6656\n\ts_waitcnt lgkmcnt(8)\n\t"
  "v_mfma_f32_16x16x32_bf16 v[16:19], v[32:35], v[4:7], v[16:19]\n\tds_read_b64_tr_b16 v[62:63], v0 offset:7680\n\tv_fma_f32 v12, -v24, v8, v12\n\t"
  "v_mfma_f32_16x16x32_bf16 v[32:35], v[4:7], v[72:75], 0\n\tv_fma_f32 v13, -v24, v9, v13\n\tv_fma_f32 v14, -v24, v10, v14\n\t"
  "v_mfma_f32_16x16x32_bf16 v[48:51], v[80:83], v[52:55], v[48:51]\n\tds_read_b64_tr_b16 v[8:9], v0 offset:8704\n\tv_mfma_f32_16x16x16_bf16 v[56:59], v[64:65], v[44:45], 0\n\tv_fma_f32 v15, -v24, v11, v15\n\t"
  "v_cvt_pk_bf16_f32 v12, v12, v13\n\t"
  "v_mfma_f32_16x16x32_bf16 v[16:19], v[76:79], v[52:55], v[16:19]\n\tds_read_b64_tr_b16 v[10:11], v0 offset:9728\n\tv_cvt_pk_bf16_f32 v13, v14, v15\n\t"
  "v_mfma_f32_16x16x32_bf16 v[32:35], v[52:55], v[84:87], v[32:35]\n\tv_cndmask_b32_e64 v26, v56, v57, s[68:69]\n\tv_mfma_f32_16x16x16_bf16 v[4:7], v[12:13], v[44:45], 0\n\tv_cndmask_b32_e64 v24, v58, v59, s[68:69]\n\t"
  "s_waitcnt lgkmcnt(2)\n\t"
  "v_mfma_f32_16x16x32_bf16 v[48:51], v[92:95], v[28:31], v[48:51]\n\tds_read_b64_tr_b16 v[12:13], v0 offset:10752\n\tv_mul_f32 v66, 0x3eb8aa3b, v66\n\t"
  "v_mfma_f32_16x16x32_bf16 v[16:19], v[88:91], v[28:31], v[16:19]\n\tds_read_b64_tr_b16 v[14:15], v0 offset:11776\n\tv_cndmask_b32_e64 v52, v4, v5, s[68:69]\n\t"
  "v_mfma_f32_16x16x32_bf16 v[32:35], v[28:31], v[96:99], v[32:35]\n\tv_cndmask_b32_e64 v26, v52, v26, s[74:75]\n\tv_mul_f32 v26, v26, v70\n\t"
  "v_mfma_f32_16x16x32_bf16 v[48:51], v[104:107], v[20:23], v[48:51]\n\tds_read_b64_tr_b16 v[4:5], v0 offset:12800\n\tv_cndmask_b32_e64 v53, v6, v7, s[68:69]\n\t"
  "v_mfma_f32_16x16x32_bf16 v[16:19], v[100:103], v[20:23], v[16:19]\n\tds_read_b64_tr_b16 v[6:7], v0 offset:13824\n\tv_cndmask_b32_e64 v24, v53, v24, s[76:77]\n\t"
  "v_mfma_f32_16x16x32_bf16 v[32:35], v[20:23], v[108:111], v[32:35]\n\tv_mul_f32 v24, v24, v68\n\tv_max_f32 v53, v26, v24\n\t"
  "v_mfma_f32_16x16x32_bf16 v[48:51], v[116:119], v[60:63], v[48:51]\n\tds_read_b64_tr_b16 v[56:57], v0 offset:14848\n\tv_mul_f32 v67, 0x3eb8aa3b, v67\n\t"
  "v_mfma_f32_16x16x32_bf16 v[16:19], v[112:115], v[60:63], v[16:19]\n\tds_read_b64_tr_b16 v[58:59], v0 offset:15872\n\tv_max_f32 v52, v26, v24\n\t"
  "v_mfma_f32_16x16x32_bf16 v[32:35], v[60:63], v[120:123], v[32:35]\n\tv_fma_f32 v40, -v36, v25, v40\n\ts_waitcnt lgkmcnt(4)\n\t"
  "v_mfma_f32_16x16x32_bf16 v[48:51], v[128:131], v[8:11], v[48:51]\n\tds_read_b64_tr_b16 v[60:61], v0 offset:16896\n\tv_fma_f32 v41, -v37, v25, v41\n\t"
  "v_mfma_f32_16x16x32_bf16 v[16:19], v[124:127], v[8:11], v[16:19]\n\tds_read_b64_tr_b16 v[62:63], v0 offset:17920\n\tv_lshlrev_b32 v45, 2, v1\n\t"
  "v_mfma_f32_16x16x32_bf16 v[32:35], v[8:11], v[132:135], v[32:35]\n\tds_read_b64_tr_b16 v[28:29], v0 offset:18944\n\tv_alignbit_b32 v55, s67, s67, v45\n\t"
  "v_mfma_f32_16x16x32_bf16 v[48:51], v[140:143], v[12:15], v[48:51]\n\tds_read_b64_tr_b16 v[30:31], v0 offset:19968\n\tv_alignbit_b32 v45, s55, s55, v45\n\t"
  "v_mfma_f32_16x16x32_bf16 v[16:19], v[136:139], v[12:15], v[16:19]\n\tds_read_b64_tr_b16 v[20:21], v0 offset:20992\n\tv_mov_b32 v36, 0x0c0c0c0c\n\t"
  "v_mfma_f32_16x16x32_bf16 v[32:35], v[12:15], v[144:147], v[32:35]\n\tds_read_b64_tr_b16 v[22:23], v0 offset:22016\n\ts_waitcnt lgkmcnt(6)\n\t"
  "v_mfma_f32_16x16x32_bf16 v[48:51], v[152:155], v[4:7], v[48:51]\n\tds_read_b64_tr_b16 v[12:13], v0 offset:23040\n\tv_mov_b32 v8, 0x3f803f80\n\t"
  "v_mfma_f32_16x16x32_bf16 v[16:19], v[148:151], v[4:7], v[16:19]\n\tds_read_b64_tr_b16 v[14:15], v0 offset:24064\n\tv_mov_b32 v9, 0x3f803f80\n\t"
  "v_mfma_f32_16x16x32_bf16 v[32:35], v[4:7], v[156:159], v[32:35]\n\tds_read_b64_tr_b16 v[4:5], v0 offset:25088\n\tv_mov_b32 v11, 0\n\t"
  "v_mfma_f32_16x16x32_bf16 v[48:51], v[164:167], v[56:59], v[48:51]\n\tds_read_b64_tr_b16 v[6:7], v0 offset:26112\n\tv_cndmask_b32_e64 v2, v55, v36, s[74:75]\n\t"
  "v_mfma_f32_16x16x32_bf16 v[16:19], v[160:163], v[56:59], v[16:19]\n\tds_read_b64_tr_b16 v[72:73], v0 offset:27136\n\tv_cndmask_b32_e64 v1, v36, v55, s[74:75]\n\t"
  "v_mfma_f32_16x16x32_bf16 v[32:35], v[56:59], v[168:171], v[32:35]\n\tds_read_b64_tr_b16 v[74:75], v0 offset:28160\n\ts_waitcnt lgkmcnt(7)\n\t"
  "v_mfma_f32_16x16x32_bf16 v[48:51], v[176:179], v[60:63], v[48:51]\n\tds_read_b64_tr_b16 v[56:57], v0 offset:29184\n\tv_cndmask_b32_e64 v37, v45, v36, s[76:77]\n\t"
  "v_mfma_f32_16x16x32_bf16 v[16:19], v[172:175], v[60:63], v[16:19]\n\tds_read_b64_tr_b16 v[58:59], v0 offset:30208\n\tv_cndmask_b32_e64 v0, v36, v45, s[76:77]\n\t"
  "v_mfma_f32_16x16x32_bf16 v[48:51], v[188:191], v[28:31], v[48:51]\n\tv_permlane16_swap_b32 v52, v53\n\t"
  "v_mfma_f32_16x16x32_bf16 v[16:19], v[184:187], v[28:31], v[16:19]\n\tv_max_f32 v54, v52, v53\n\ts_waitcnt lgkmcnt(5)\n\t"
  "v_mfma_f32_16x16x32_bf16 v[48:51], v[200:203], v[20:23], v[48:51]\n\tv_max_f32 v55, v52, v53\n\tv_fma_f32 v42, -v38, v25, v42\n\t"
  "v_mfma_f32_16x16x32_bf16 v[16:19], v[196:199], v[20:23], v[16:19]\n\tv_permlane32_swap_b32 v54, v55\n\t"
  "v_mfma_f32_16x16x32_bf16 v[48:51], v[212:215], v[12:15], v[48:51]\n\tv_max_f32 v52, v54, v55\n\tv_mul_f32_e64 v52, -v52, v66\n\t"
  "v_mfma_f32_16x16x32_bf16 v[16:19], v[208:211], v[12:15], v[16:19]\n\tv_fma_f32 v26, v26, v66, v52\n\ts_waitcnt lgkmcnt(1)\n\t"
  "v_mfma_f32_16x16x32_bf16 v[48:51], v[224:227], v[4:7], v[48:51]\n\tv_fma_f32 v24, v24, v66, v52\n\tv_fma_f32 v43, -v39, v25, v43\n\t"
  "v_mfma_f32_16x16x32_bf16 v[16:19], v[220:223], v[4:7], v[16:19]\n\tv_exp_f32 v26, v26\n\t"
  "v_mfma_f32_16x16x32_bf16 v[48:51], v[236:239], v[72:75], v[48:51]\n\tv_cvt_pk_bf16_f32 v44, v40, v41\n\tv_cvt_pk_bf16_f32 v45, v42, v43\n\t"
  "v_mfma_f32_16x16x32_bf16 v[16:19], v[232:235], v[72:75], v[16:19]\n\tds_read_b128 v[40:43], v3 offset:384\n\ts_waitcnt lgkmcnt(1)\n\t"
  "v_mfma_f32_16x16x32_bf16 v[48:51], v[248:251], v[56:59], v[48:51]\n\tv_exp_f32 v24, v24\n\t"
  "v_mfma_f32_16x16x32_bf16 v[16:19], v[244:247], v[56:59], v[16:19]\n\tv_cvt_pk_bf16_f32 v10, v26, v24\n\tv_mul_f32 v26, v26, v70\n\t"
  "v_mfma_f32_16x16x32_bf16 v[32:35], v[60:63], v[180:183], v[32:35]\n\tv_cvt_pk_bf16_f32 v48, v48, v49\n\tv_cvt_pk_bf16_f32 v49, v50, v51\n\tv_mfma_f32_16x16x16_bf16 v[52:55], v[8:9], v[10:11], 0\n\t"
  "v_cvt_pk_bf16_f32 v16, v16, v17\n\tv_cvt_pk_bf16_f32 v17, v18, v19\n\t"
  "v_mfma_f32_16x16x32_bf16 v[32:35], v[28:31], v[192:195], v[32:35]\n\tv_mul_f32 v24, v24, v68\n\tv_mfma_f32_16x16x16_bf16 v[28:31], v[48:49], v[16:17], 0\n\tv_mfma_f32_16x16x16_bf16 v[48:51], v[64:65], v[16:17], 0\n\t"
  "v_cvt_pk_bf16_f32 v26, v26, v24\n\tv_perm_b32 v16, v26, v26, v2\n\t"
  "v_mfma_f32_16x16x32_bf16 v[32:35], v[20:23], v[204:207], v[32:35]\n\tv_cndmask_b32_e64 v39, v28, v29, s[68:69]\n\tv_cndmask_b32_e64 v24, v30, v31, s[68:69]\n\t"
  "v_mfma_f32_16x16x32_bf16 v[32:35], v[12:15], v[216:219], v[32:35]\n\tv_cndmask_b32_e64 v28, v48, v49, s[68:69]\n\tv_cndmask_b32_e64 v39, v28, v39, s[74:75]\n\t"
  "v_mfma_f32_16x16x32_bf16 v[32:35], v[4:7], v[228:231], v[32:35]\n\tv_mul_f32 v39, v39, v71\n\tv_cndmask_b32_e64 v29, v50, v51, s[68:69]\n\t"
  "v_mfma_f32_16x16x32_bf16 v[32:35], v[72:75], v[240:243], v[32:35]\n\tv_cndmask_b32_e64 v24, v29, v24, s[76:77]\n\tv_mul_f32 v24, v24, v69\n\t"
  "v_mfma_f32_16x16x32_bf16 v[32:35], v[56:59], v[252:255], v[32:35]\n\t"
  GST(14)
  "v_max_f32 v29, v39, v24\n\tv_max_f32 v28, v39, v24\n\tv_perm_b32 v17, v26, v26, v37\n\tv_perm_b32 v18, v26, v26, v1\n\t"
  "v_permlane16_swap_b32 v28, v29\n\tv_max_f32 v30, v28, v29\n\tv_max_f32 v31, v28, v29\n\tv_perm_b32 v19, v26, v26, v0\n\t"
  "s_waitcnt lgkmcnt(0)\n\tv_fma_f32 v32, -v40, v25, v32\n\tv_permlane32_swap_b32 v30, v31\n\tv_max_f32 v28, v30, v31\n\t"
  "v_mul_f32_e64 v28, -v28, v67\n\tv_fma_f32 v39, v39, v67, v28\n\tv_fma_f32 v24, v24, v67, v28\n\tv_exp_f32 v39, v39\n\t"
  "v_exp_f32 v24, v24\n\tv_fma_f32 v33, -v41, v25, v33\n\tv_cvt_pk_bf16_f32 v10, v39, v24\n\tv_mul_f32 v39, v39, v71\n\t"
  "v_fma_f32 v34, -v42, v25, v34\n\tv_fma_f32 v35, -v43, v25, v35\n\tv_cvt_pk_bf16_f32 v48, v32, v33\n\tv_cvt_pk_bf16_f32 v49, v34, v35\n\t"
  "v_mul_f32 v24, v24, v69\n\tv_mfma_f32_16x16x16_bf16 v[28:31], v[8:9], v[10:11], 0\n\tv_cvt_pk_bf16_f32 v39, v39, v24\n\tv_perm_b32 v40, v39, v39, v2\n\t"
  "v_perm_b32 v41, v39, v39, v37\n\tv_perm_b32 v42, v39, v39, v1\n\t"
  "v_mfma_f32_16x16x32_bf16 v[22:25], v[44:47], v[16:19], 0\n\tv_perm_b32 v43, v39, v39, v0\n\tv_rcp_f32 v52, v52\n\tv_rcp_f32 v28, v28\n\t"
  "v_mfma_f32_16x16x32_bf16 v[4:7], v[46:49], v[40:43], 0\n\ts_nop 0\n\tv_mul_f32 v22, v22, v52\n\tv_mul_f32 v23, v23, v52\n\t"
  "v_mul_f32 v24, v24, v52\n\tv_mul_f32 v25, v25, v52\n\tv_cvt_pk_bf16_f32 v64, v22, v23\n\tv_cvt_pk_bf16_f32 v65, v24, v25\n\t"
  "s_nop 0\n\tv_mul_f32 v4, v4, v28\n\tv_mul_f32 v5, v5, v28\n\tv_mul_f32 v6, v6, v28\n\t"
  "v_mul_f32 v7, v7, v28\n\tv_cvt_pk_bf16_f32 v66, v4, v5\n\tv_cvt_pk_bf16_f32 v67, v6, v7\n\ts_nop 0\n\t"
  "v_permlane16_swap_b32 v64, v66\n\tv_permlane16_swap_b32 v65, v67\n\tbuffer_store_dwordx4 v[64:67], v27, s[56:59], 0 offen sc1\n\t"
  "s_cmp_eq_u32 s63, 2\n\ts_cbranch_scc1 .Lg_ret\n\ts_nop 0"
  G_OPS);
  else asm volatile("s_branch .Lg_body\n.Lg_ret:\n\ts_nop 0" G_OPS);
  static_for<0, 45>(pin);
  ST(!L1, 16);
  if (L1) {
    KSLP(7, SLP_OB1);
    cnt_add(smem, C_OB, 0);
    mid();
    cnt_wait(smem, C_X2, 4);
    return;
  }
  u32 t16 = KADD(8, now_(), DB2);
  cnt_add(smem, C_OB, 0);
  const int bid = BIDX, lane = lane_id();
  int og0 = OFF_O2 + MB_ + RB_OF(bid) * 32768 + g * 4096, og1 = og0 + 16384, vo = lane * 16;
  u32 sa = (u32)(L_TB + g * 4096 + lane * 64 + (lane & 14) * 4 + 4);
  asm volatile("" : "+s"(og0), "+s"(og1), "+v"(vo), "+v"(sa));
  u32 dl;
  for (unsigned n = 0; n < (1u << 24); ++n) {
    asm volatile("" ::: "memory");
    const u32 c0 = *(const volatile LAS u32*)(size_t)(u32)(L_MI + C_OV), c1 = *(const volatile LAS u32*)(size_t)(u32)(L_MI + C_OB);
    dl = *(const volatile LAS u32*)(size_t)(u32)(L_MI + M_DL);
    if ((u32)__builtin_amdgcn_readfirstlane(min(c0 + 11u, c1)) >= 16u) break;
  }
  gate(t16 + dl);
  ST(1, 15);
  const u32 mark = x0_mark(tag);
  o_get(wrs, vo, og0, og1, L_TB + g * 4096, sa, mark, 25);
  asm volatile("s_barrier\n\ts_waitcnt vmcnt(0)\n\ts_barrier" ::: "memory");
  if (__any(*(const LAS u32*)(size_t)(sa + 16384) == mark)) {
    FAILC(12);
    for (unsigned spins = 0; spins <= SPIN_CAP; ++spins) {
      dma4k(wrs, vo, og0, L_TA + g * 4096);
      dma4k(wrs, vo, og1, L_TA + 16384 + g * 4096);
      asm volatile("s_waitcnt vmcnt(0)" ::: "memory");
      if (!__any(*(const LAS u32*)(size_t)(sa - (L_TB - L_TA)) == mark || *(const LAS u32*)(size_t)(sa - (L_TB - L_TA) + 16384) == mark)) break;
    }
    smem[L_MI + M_FX + g] = 1;
    asm volatile("s_waitcnt lgkmcnt(0)" ::: "memory");
  }
}

using u32x8 = __attribute__((ext_vector_type(8))) unsigned;
using u32x16 = __attribute__((ext_vector_type(16))) unsigned;
__global__ __launch_bounds__(512, 2) KATTR void k_mega(const u16*, const u16*, const u16*, const u16*, const u16*, const u16*, const u16*, const u16*,
                                                       const u16*, const u16*, const u16*, char*, u32*) {
  extern __shared__ __attribute__((aligned(16))) char smem[];
  u32x16 ka;
  u32x8 kb;
  u32x2 kc;
  asm volatile("s_load_dwordx16 %0, %3, 0x0\n\ts_load_dwordx8 %1, %3, 0x40\n\ts_load_dwordx2 %2, %3, 0x60"
               : "=&s"(ka), "=&s"(kb), "=&s"(kc) : "s"(__builtin_amdgcn_kernarg_segment_ptr()) : "memory");
  const int tid = TIDX, lane = tid & 63;
  const int w = __builtin_amdgcn_readfirstlane(tid >> 6);
  const int bid = BIDX;
  ST_INIT(); ST(0, 0); ST(1, 0);
  if (w == 0) {
    u64 pc;
    int clen;
    asm volatile("s_getpc_b64 %0\n.Lcw_base:\n\ts_mov_b32 %1, .Lcw_end-.Lcw_base-4" : "=s"(pc), "=s"(clen));
    asm volatile("s_mov_b32 m0, %2\n\ts_nop 0\n\tglobal_load_lds_dword %0, %1" :: "v"(min(min(lane, 15) * 128 + (bid >> 3) * 2048, clen)), "s"(pc), "s"(L_FS) : "memory");
  }
  const int kp = (bid & 7) * 32 + (bid >> 3), hb = HB_OF(bid);
  const int c = (w >> 1) & 1, g = w & 3;
  u32 po = (u32)(kp * 512 + (tid & 255) * 2);
  const int pb = (bid & 7) + 8 * (bid >> 6), ps = (bid >> 3) & 7, gr = pb * 8 + ps;
  u32 xo = (u32)((w < 4 ? (pb * 64 + 16 * w) * 1024 + ps * 128 : gr * 8192 + g * 2048) + (lane & 7) * 16 + (lane >> 3) * (w < 4 ? 2048 : 128));
  asm volatile("" : "+v"(po), "+v"(xo) :: "memory");
  asm volatile("s_waitcnt lgkmcnt(0)" : "+s"(ka), "+s"(kb), "+s"(kc) :: "memory");
  ST_FIX();
#define KP(T_, v_, i_) ((T_)(((u64)v_[2 * (i_) + 1] << 32) | v_[2 * (i_)]))
  const u16 *x0 = KP(const u16*, ka, 0), *A_re = KP(const u16*, ka, 1), *A_im = KP(const u16*, ka, 2), *B_re = KP(const u16*, ka, 3), *B_im = KP(const u16*, ka, 4);
  const u16 *C_re = KP(const u16*, ka, 5), *C_im = KP(const u16*, ka, 6), *log_dt = KP(const u16*, ka, 7), *Dskip = KP(const u16*, kb, 0), *wqkv = KP(const u16*, kb, 1), *wo = KP(const u16*, kb, 2);
  char* ws = KP(char*, kb, 3);
  u32* out = KP(u32*, kc, 0);
#define LD_A() asm volatile("global_load_ushort v248, %0, %1\n\tglobal_load_ushort v249, %0, %2" :: "v"(po), "s"(A_re), "s"(A_im) : "memory", "v248", "v249")
  u32 ldtw = 0, dskw = 0;
  if (w < 4) {
    asm volatile("s_load_dword %0, %2, %4\n\ts_load_dword %1, %3, %4" : "=&s"(ldtw), "=&s"(dskw) : "s"(log_dt), "s"(Dskip), "s"(4 * kp) : "memory");
    LD_A();
  }
  asm volatile("global_load_dwordx4 v[240:243], %0, %1\n\tglobal_load_dwordx4 v[244:247], %0, %1 offset:1024\n\tglobal_load_dword v254, %2, %3 sc1"
               :: "v"(xo), "s"(x0), "v"(OFF_EP), "s"(ws) : "memory", "v240", "v254");
  if (w >= 4) {
    asm volatile("global_load_ushort v250, %0, %1\n\tglobal_load_ushort v251, %0, %2\n\tglobal_load_ushort v252, %0, %3\n\tglobal_load_ushort v253, %0, %4"
                 :: "v"(po), "s"(B_re), "s"(B_im), "s"(C_re), "s"(C_im) : "memory", "v250", "v253");
    LD_A();
  }
  __builtin_amdgcn_sched_barrier(0);
  const rsrc_t wrs = __builtin_amdgcn_make_buffer_rsrc((void*)ws, 0, WSB, 0x00020000);
  if (w >= 4) {
    asm volatile("s_mov_b32 m0, %2\n\ts_nop 0\n\tglobal_load_lds_dword %0, %1" :: "v"((bid * 16 + g * 4 + min(lane, 3)) * 128), "s"(wqkv), "s"(L_FS + w * 256) : "memory");
    u32x4 zz = {0, 0, 0, 0};
    asm volatile("" : "+v"(zz));
    const int z0 = (int)opaque((u32)(L_U + (lane >> 5) * 4608 + (lane & 31) * 16)), z1 = (int)opaque((u32)(L_MI + min((tid & 255) * 16 - 1024, 1088)));
    *(u32x4*)(smem + (g == 0 ? z0 : z1)) = zz;
    asm volatile("s_nop 1\n\ts_waitcnt lgkmcnt(0)" :: "v"(zz) : "memory");
  }
  if (w >= 4) asm volatile("s_load_dword %0, %1, %2" : "=s"(ldtw) : "s"(log_dt), "s"(4 * kp) : "memory");
  __builtin_amdgcn_sched_barrier(0);
#define XT_GET(x_) asm volatile("v_mov_b64 %0, v[240:241]\n\tv_mov_b64 %1, v[242:243]\n\tv_mov_b64 %2, v[244:245]\n\tv_mov_b64 %3, v[246:247]" \
                   : "=v"(x_[0].xy), "=v"(x_[0].zw), "=v"(x_[1].xy), "=v"(x_[1].zw) :: "memory")
  const int ftb = c ? L_TA : 0;
#define PUT(e_, r_, i_) *(LAS u32*)(wa + (e_) * FTS) = pkh(r_, i_)
#define CMR(a_, b_) fmaf(a_[0], b_[0], -(a_[1] * b_[1]))
#define CMI(a_, b_) fmaf(a_[0], b_[1], a_[1] * b_[0])
#define CMUL(a_, b_) (f32x2){CMR(a_, b_), CMI(a_, b_)}
  struct P0A { u32 gad[12], sad[8], ua, ur; };
  auto p0_addr = [&](P0A& A, int lane, int ut) __attribute__((always_inline)) {
    const int i = lane & 15, kk = lane >> 4;
    const u32 sb = (u32)((L_KT - KTO + c * TBS - 0x7E00) & 0xFFFF) * 0x10001u;
    const float f0 = __uint_as_float(opaque(__float_as_uint((float)((ut < 2 ? 1023 : 2047) - 512 * (w & 1) - 8 * lane))));
#pragma unroll
    for (int e = 0; e < 6; ++e) { const u32 ga = tb_addr(pk2(f0 - (float)(2 * e), f0 - (float)(2 * e + 1)), sb); A.gad[2 * e] = ga & 0xFFFFu; A.gad[2 * e + 1] = ga >> 16; }
    if (ut < 2) {
      A.ua = (u32)(ftb + i * FTS + kk * 16); A.ur = A.ua + ut * 16 * FTS;
      const int a0 = 128 * (i >> 3) + (i & 7), a1 = i < 8 ? 256 + 128 * (i >> 2) + 2 * (i & 3) : 128 * (i >> 1) + 4 * (i & 1), a2 = min(1024 + 128 * i, 2064);
#pragma unroll
      for (int t = 0; t < 2; ++t) {
        const float st = (t && i >= 8) ? 16.f : 8.f;
        const float fb = (float)(t ? a2 : ut == 0 ? a0 : a1) + st * (float)(4 * kk);
        const u32 s0 = tb_addr(pk2(fb, fb + st), sb), s1 = tb_addr(pk2(fb + 2.f * st, fb + 3.f * st), sb);
        A.sad[4 * t] = s0 & 0xFFFFu; A.sad[4 * t + 1] = s0 >> 16; A.sad[4 * t + 2] = s1 & 0xFFFFu; A.sad[4 * t + 3] = s1 >> 16;
      }
      asm volatile("" : "+v"(A.ua), "+v"(A.ur), "+v"(A.sad[0]), "+v"(A.sad[1]), "+v"(A.sad[2]), "+v"(A.sad[3]), "+v"(A.sad[4]), "+v"(A.sad[5]), "+v"(A.sad[6]), "+v"(A.sad[7]));
    }
    asm volatile("" : "+v"(A.gad[0]), "+v"(A.gad[1]), "+v"(A.gad[2]), "+v"(A.gad[3]), "+v"(A.gad[4]), "+v"(A.gad[5]), "+v"(A.gad[6]), "+v"(A.gad[7]), "+v"(A.gad[8]), "+v"(A.gad[9]), "+v"(A.gad[10]));
    __builtin_amdgcn_sched_barrier(0);
  };
  auto k_unit = [&](const P0A& A, float dk, bool lag0) __attribute__((always_inline)) {
    f16x8 fr[8], fc[8];
    const f32x2 bd = *(const f32x2*)(smem + L_P0 + 512 + c * 8);
    __builtin_amdgcn_sched_barrier(0);
#pragma unroll
    for (int e = 0; e < 8; ++e) {
      fc[e] = *(const LAS f16x8*)(size_t)(A.ua + e * 64 + 41 * FTS);
      fr[e] = *(const LAS f16x8*)(size_t)(A.ur + e * 64);
      __builtin_amdgcn_sched_barrier(0);
    }
    f16x8 f2[8];
    if (!(w & 1)) {
#pragma unroll
      for (int e = 0; e < 8; ++e) { f2[e] = *(const LAS f16x8*)(size_t)(A.ua + e * 64 + 32 * FTS); __builtin_amdgcn_sched_barrier(0); }
    }
    const int ec = bexp(bd[0] + bd[1] + fabsf(dk));
    const float f0 = __uint_as_float((u32)max(127 + bexp(bd[0]) - ec, 0) << 23), f1 = __uint_as_float((u32)max(127 + bexp(bd[1]) - ec, 0) << 23);
    const float d0s = lag0 ? dk * __uint_as_float((u32)(268 - ec) << 23) : 0.f;
    auto unit = [&](const f16x8 (&fr)[8], u32x4 sa, float add) __attribute__((always_inline)) {
      f32x4 k0 = {0.f, 0.f, 0.f, 0.f}, k1 = k0;
#pragma unroll
      for (int e = 0; e < 4; ++e) k0 = __builtin_amdgcn_mfma_f32_16x16x32_f16(fc[e], fr[e], k0, 0, 0, 0);
#pragma unroll
      for (int e = 4; e < 8; ++e) k1 = __builtin_amdgcn_mfma_f32_16x16x32_f16(fc[e], fr[e], k1, 0, 0, 0);
      k0 *= f0; k0[0] += add;
      const u32 h01 = pkh(fmaf(k1[0], f1, k0[0]), fmaf(k1[1], f1, k0[1])), h23 = pkh(fmaf(k1[2], f1, k0[2]), fmaf(k1[3], f1, k0[3]));
      *(LAS u16*)((LAS char*)(size_t)sa[0] + KTO) = (u16)h01; *(LAS u16*)((LAS char*)(size_t)sa[1] + KTO) = (u16)(h01 >> 16);
      *(LAS u16*)((LAS char*)(size_t)sa[2] + KTO) = (u16)h23; *(LAS u16*)((LAS char*)(size_t)sa[3] + KTO) = (u16)(h23 >> 16);
    };
    if (!(w & 1)) {
      unit(fr, (u32x4){A.sad[0], A.sad[1], A.sad[2], A.sad[3]}, d0s);
      unit(f2, (u32x4){A.sad[4], A.sad[5], A.sad[6], A.sad[7]}, 0.f);
    } else {
      unit(fr, (u32x4){A.sad[0], A.sad[1], A.sad[2], A.sad[3]}, 0.f);
      *(float*)(smem + L_MI + 4 * c) = __uint_as_float((u32)(ec - 14) << 23);
    }
  };
  auto k_get = [&](u32 (&kh)[11], const P0A& A) __attribute__((always_inline)) {
#pragma unroll
    for (int e = 0; e < 11; ++e) kh[e] = *(const LAS u16*)((const LAS char*)(size_t)A.gad[e] + KTO);
    __builtin_amdgcn_sched_barrier(0);
  };
  auto k_copies = [&](const u32 (&kh)[11], int lane, bool low) __attribute__((always_inline)) {
    u32 kw[6];
#pragma unroll
    for (int e = 0; e < 5; ++e) kw[e] = __builtin_amdgcn_perm(kh[2 * e + 1], kh[2 * e], 0x05040100u);
    kw[5] = kh[10];
    if (low && (w & 1) && lane == 63) { kw[4] = 0; kw[5] = 0; }
    build_copies(smem, c, (w & 1) * 64 + lane + (low ? 128 : 0), kw);
    __builtin_amdgcn_sched_barrier(0);
  };
  __builtin_amdgcn_sched_barrier(0);
  int w2 = w;
  asm volatile("" : "+s"(w2));
  if (w < 4) {
  P0A pa;
  p0_addr(pa, lane, w & 1);
  LAS char* wa = (LAS char*)(size_t)opaque((u32)(ftb + (tid & 127) * 4));
  u32 tg;
  const bool lo = pb < 16;
  const int xc8 = lane >> 3, xrp = lane & 7;
  const int xts = lo ? 128 : XTS, xt2 = 2 * xts;
  const int xto = (int)opaque((u32)(lo ? OFF_X1 + (pb * 4 + w) * XBS + (ps * 32 + 4 * xc8) * 128 + xrp * 16
                                       : XTO(ps * 32 + 4 * xc8) + ((pb & 15) * 64 + 16 * w + 2 * xrp) * 8));
  auto xpub = [&]() __attribute__((always_inline)) {
    u32x4 q[4];
    u32x4 xt[2];
    __builtin_amdgcn_sched_barrier(0);
    asm volatile("s_waitcnt vmcnt(0)\n\tv_add_u32 %0, 1, v254" : "=v"(tg) :: "memory");
    XT_GET(xt);
    const int sl = ((lane & 7) * 8 + (lane >> 3)) * 4;
#pragma unroll
    for (int e = 0; e < 8; ++e) xt[e & 1][e >> 1] = (u32)__builtin_amdgcn_ds_bpermute(sl, (int)xt[e & 1][e >> 1]);
#pragma unroll
    for (int e = 0; e < 4; ++e) q[e] = (u32x4){xt[0][e], x0_mark(tg), xt[1][e], x0_mark(tg)};
    asm volatile("" : "+v"(q[0]), "+v"(q[1]), "+v"(q[2]), "+v"(q[3]));
#pragma unroll
    for (int e = 0; e < 4; ++e) stw(wrs, xto + (e & 1) * xts + (e >> 1) * xt2, q[e]);
    asm volatile("s_nop 1" :: "v"(q[0]), "v"(q[1]), "v"(q[2]), "v"(q[3]) : "memory");
    ST(0, 3);
    __builtin_amdgcn_sched_barrier(0);
  };
  u32 ha, hz, hs, ht;
  C1Pre cp;
  int xs0, xs1, xl0, xl1, xv;
  u32 za;
  auto setup1 = [&]() __attribute__((always_inline)) {
    SB();
    const int t = (w & 1) * 64 + lane;
    ha = (u32)(L_KT + c * TBS - 0x7E00 + 2 * (0x44FF - t)); hz = ha - (t == 127 ? 2 : 0);
    hs = (lane & 1) ? 0x01000302u : 0x03020302u; ht = (lane & 1) ? 0x01000100u : 0x01000302u;
    asm volatile("" : "+v"(ha), "+v"(hz), "+v"(hs), "+v"(ht));
    SB();
  };
  auto setup2 = [&]() __attribute__((always_inline)) {
    SB();
    c1_pre(cp, kp, w, lane);
    xs0 = OFF_X1 + RB_OF(bid) * XBS + w * 8192; xs1 = xs0 + 4096; xl0 = L_TA + w * 8192; xl1 = xl0 + 4096; xv = x_lane(lane);
    za = (u32)(((lane >> 1) & 7) * CSTR + 4096 + (lane & 1) * 16);
    asm volatile("" : "+s"(xs0), "+s"(xs1), "+s"(xl0), "+s"(xl1), "+v"(xv), "+v"(za));
    SB();
  };
  {
    float ar, ai;
#define B0() do { SB(); __builtin_amdgcn_s_barrier(); SB(); } while (0)
    B0();
    asm volatile("s_waitcnt vmcnt(3)\n\tv_lshlrev_b32 %0, 16, v248\n\tv_lshlrev_b32 %1, 16, v249" : "=v"(ar), "=v"(ai) :: "memory");
    asm volatile("s_waitcnt lgkmcnt(0)" : "+s"(ldtw), "+s"(dskw) :: "memory");
    ST(0, 1);
    const float dt = rbf(__expf(c ? hi2f(ldtw) : lo2f(ldtw)));
    const float zr = dt * ar * 1.4426950408889634f, zi = dt * ai * 0.15915494309189535f;
    float cs = __builtin_amdgcn_exp2f(fmaxf(zr, 0.f) * -1928.f);
    f32x2 P[8], Q1, R[17];
    { float a_, b_; cexp2(zr, zi, a_, b_); P[1] = (f32x2){a_, b_}; cexp2(zr * 128.f, zi * 128.f, a_, b_); Q1 = (f32x2){a_, b_}; }
    asm volatile("" : "+v"(cs), "+v"(P[1]), "+v"(Q1));
    xpub();
    P[2] = CMUL(P[1], P[1]);
    const f32x2 Q2 = CMUL(Q1, Q1);
    R[1] = Q1 * cs; R[2] = Q2 * cs;
    P[3] = CMUL(P[2], P[1]); P[4] = CMUL(P[2], P[2]);
    const f32x2 Q4 = CMUL(Q2, Q2);
    R[3] = CMUL(R[1], Q2); R[4] = Q4 * cs;
    P[5] = CMUL(P[4], P[1]); P[6] = CMUL(P[3], P[3]); P[7] = CMUL(P[4], P[3]);
#pragma unroll
    for (int j = 5; j <= 16; ++j) R[j] = CMUL(R[j - 4], Q4);
    asm volatile("" : "+v"(R[13]), "+v"(R[14]), "+v"(R[15]), "+v"(R[16]), "+v"(P[7]), "+v"(P[6]), "+v"(P[5]));
    PUT(0, cs, 0.f); PUT(8, R[1][0], R[1][1]);
#pragma unroll
    for (int r = 1; r < 8; ++r) { PUT(r, P[r][0] * cs, P[r][1] * cs); PUT(8 + r, CMR(R[1], P[r]), CMI(R[1], P[r])); }
    asm volatile("" ::: "memory");
#pragma unroll
    for (int j = 2; j < 4; ++j) {
      PUT(16 + 4 * (j - 2), R[j][0], R[j][1]);
#pragma unroll
      for (int r = 1; r < 4; ++r) PUT(16 + 4 * (j - 2) + r, CMR(R[j], P[2 * r]), CMI(R[j], P[2 * r]));
    }
#pragma unroll
    for (int j = 4; j < 8; ++j) { PUT(24 + 2 * (j - 4), R[j][0], R[j][1]); PUT(25 + 2 * (j - 4), CMR(R[j], P[4]), CMI(R[j], P[4])); }
#pragma unroll
    for (int j = 8; j <= 16; ++j) PUT(24 + j, R[j][0], R[j][1]);
    __builtin_amdgcn_sched_barrier(0);
  }
  ST(0, 2);
  cnt_add(smem, C_F, lane);
  u32 stag = __builtin_amdgcn_readfirstlane(tg), g2;
  asm volatile("" : "+s"(stag));
  cnt_spin(smem, C_F, 8);
  k_unit(pa, c ? hi2f(dskw) : lo2f(dskw), (tid & 127) == 0);
  cnt_add(smem, C_A, lane);
  setup1();
  setup2();
  cnt_spin(smem, C_A, 4);
  ST(0, 4);
  {
    u32 kl[11];
    k_get(kl, pa);
    const u32 h0 = *(const LAS u16*)(size_t)ha, h1 = *(const LAS u16*)(size_t)(ha + 2), h2 = *(const LAS u16*)(size_t)hz;
    SB();
    u32x4 zz = {0, 0, 0, 0};
    asm volatile("" : "+v"(zz));
    *(LAS u32x4*)(size_t)za = zz;
    k_copies(kl, lane, true);
    asm volatile("" :: "v"(zz));
    cnt_add(smem, C_A, lane);
    ST(0, 5);
    {
      const u32 p = h0 | (h1 << 16), hh = __builtin_amdgcn_perm(p, p, 0x03020302u), ll = __builtin_amdgcn_perm(p, p, 0x01000100u);
      const u32 hx = __builtin_amdgcn_perm(p, p, hs), xl = __builtin_amdgcn_perm(p, p, ht);
      u32x4 c0 = {hh, hx, ll, ll}, c1 = {hh, xl, ll, ll}, c2 = {hx, ll, ll, ll}, c3 = {xl, ll, ll, h0 | (h2 << 16)};
      asm volatile("" : "+v"(c0), "+v"(c1), "+v"(c2), "+v"(c3));
      char* cop = smem + c * 4 * CSTR + ((w & 1) * 64 + lane) * 16;
      *(u32x4*)(cop + 0 * CSTR) = c0; *(u32x4*)(cop + 1 * CSTR) = c1; *(u32x4*)(cop + 2 * CSTR) = c2; *(u32x4*)(cop + 3 * CSTR) = c3;
      asm volatile("s_nop 1" :: "v"(c0), "v"(c1), "v"(c2), "v"(c3) : "memory");
      cnt_add(smem, C_KH, lane);
    }
  }
  cnt_spin(smem, C_A, 8);
  bf16x8 wof[16];
  u32x4 pfl, wfl;
  XTake xt;
  AttC ac;
  a_conv1(wrs, w, cp, pfl, wfl, g2);
  asm volatile("" : "+v"(pfl), "+v"(wfl));
  SB();
  KSLP(15, SLP_XA1);
  tags_wait(pfl, wrs, OFF_ST0 + tid * 16, stag, 19);
  dma4k(wrs, xv, xs0, xl0);
  dma4k(wrs, xv, xs1, xl1);
  KSLP(14, SLP_WE);
  SB();
  ConvK ck;
  a_blayer<true>(smem, wrs, ws, out, tid, w, wof, stag, wfl, ck, xt, ac);
  ST(1, 2);
  a_conv<false>(smem, wrs, RB_OF(bid), w, ck);
  a_blayer<false>(smem, wrs, ws, out, tid, w, wof, stag, wfl, ck, xt, ac);
  ST_OUT();
  }
  if (w2 >= 4) {
    const int lane = lane_id();
    LAS char* wa = (LAS char*)(size_t)opaque((u32)(ftb + (w & 1) * 256 + lane * 4));
    u32 tag;
    u32 st0, st1;
    int pbo;
    auto cols = [&]() __attribute__((always_inline)) {
    {
      float ar, ai, br, bi, cr, ci;
      asm volatile("v_lshlrev_b32 %0, 16, v248\n\tv_lshlrev_b32 %1, 16, v249\n\tv_lshlrev_b32 %2, 16, v250\n\t"
                   "v_lshlrev_b32 %3, 16, v251\n\tv_lshlrev_b32 %4, 16, v252\n\tv_lshlrev_b32 %5, 16, v253"
                   : "=v"(ar), "=v"(ai), "=v"(br), "=v"(bi), "=v"(cr), "=v"(ci) :: "memory");
      asm volatile("s_waitcnt lgkmcnt(0)" : "+s"(ldtw) :: "memory");
      const float dt = rbf(__expf(c ? hi2f(ldtw) : lo2f(ldtw)));
      const float dbr = dt * br, dbi = dt * bi;
      const float zr = dt * ar * 1.4426950408889634f, zi = dt * ai * 0.15915494309189535f;
      const float zp = fmaxf(zr, 0.f), g1 = __builtin_amdgcn_exp2f(zp * 1928.f);
      const float wr = (cr * dbr - ci * dbi) * g1, wi = (cr * dbi + ci * dbr) * g1;
      float bd = (fabsf(wr) + fabsf(wi)) * __builtin_amdgcn_exp2f(zp * 120.f);
      f32x2 S1, C[16];
      { float a_, b_; cexp2(zr * 8.f, zi * 8.f, a_, b_); S1 = (f32x2){a_, b_}; }
      bd = dpp_add<0x111, 0xf>(bd); bd = dpp_add<0x112, 0xf>(bd);
      const f32x2 S2 = CMUL(S1, S1);
      bd = dpp_add<0x114, 0xf>(bd); bd = dpp_add<0x118, 0xf>(bd);
      const f32x2 S4 = CMUL(S2, S2);
      bd = dpp_add<0x142, 0xa>(bd); bd = dpp_add<0x143, 0xc>(bd);
      bd = __int_as_float(__builtin_amdgcn_readlane(__float_as_int(bd), 63));
      *(float*)(smem + L_P0 + 512 + g * 4) = bd;
      const float sc = __uint_as_float((u32)(268 - bexp(bd)) << 23);
      C[0] = (f32x2){wr * sc, wi * sc};
      C[1] = CMUL(C[0], S1); C[2] = CMUL(C[0], S2); C[3] = CMUL(C[1], S2);
#pragma unroll
      for (int e = 4; e < 16; ++e) C[e] = CMUL(C[e - 4], S4);
#pragma unroll
      for (int e = 0; e < 16; ++e) *(LAS u32*)(wa + (41 + e) * FTS) = pkhn(C[e][0], C[e][1]);
    }
      cnt_add(smem, C_F, lane);
      ST(0, 2);
    };
    auto stats = [&]() __attribute__((always_inline)) {
    {
      const u32 ONES = opaque(0x3F803F80u);
      u32x4 xt[2];
      XT_GET(xt);
      float sm[2] = {0.f, 0.f}, sq[2] = {0.f, 0.f};
#pragma unroll
      for (int e = 0; e < 8; ++e) { sm[e & 1] = dot2acc(xt[e & 1][e >> 1], ONES, sm[e & 1]); sq[e & 1] = dot2acc(xt[e & 1][e >> 1], xt[e & 1][e >> 1], sq[e & 1]); }
      const bool b0 = lane & 1, b1 = lane & 2;
      const float v0 = (b0 ? sm[1] : sm[0]) + dpp_mov<DPP_XOR1>(b0 ? sm[0] : sm[1]), v1 = (b0 ? sq[1] : sq[0]) + dpp_mov<DPP_XOR1>(b0 ? sq[0] : sq[1]);
      float v = (b1 ? v1 : v0) + dpp_mov<DPP_XOR2>(b1 ? v0 : v1);
      v += dpp_mov<0x124>(v); v += dpp_mov<DPP_ROR8>(v);
      auto r = __builtin_amdgcn_permlane16_swap(__float_as_uint(v), __float_as_uint(v), false, false);
      v = __uint_as_float(r[0]) + __uint_as_float(r[1]);
      auto q = __builtin_amdgcn_permlane32_swap(__float_as_uint(v), __float_as_uint(v), false, false);
      v = (__uint_as_float(q[0]) + __uint_as_float(q[1])) * (1.f / 512.f);
      st0 = pack_stats(v, fmaxf(dpp_mov<DPP_XOR2>(v) - v * v, 0.f));
      st1 = dpp_u<DPP_XOR1>(st0);
    }
    };
    auto publish = [&]() __attribute__((always_inline)) {
      asm volatile("v_add_u32 %0, 1, v254" : "=v"(tag) :: "memory");
      stw(wrs, pbo, (u32x4){st0, tag, st1, tag});
      __builtin_amdgcn_sched_barrier(0);
    };
#define VMW(n_) asm volatile("s_waitcnt vmcnt(%0)" :: "n"(n_) : "memory")
    constexpr int PTN = 2;
    const int ut = 16 + (bid >> 3), us = ut / 3, um = ut % 3;
    const rsrc_t qrs = __builtin_amdgcn_make_buffer_rsrc((void*)wqkv, 0, 1572864, 0x00020000), ors = __builtin_amdgcn_make_buffer_rsrc((void*)wo, 0, 524288, 0x00020000);
    const int wv0 = (8 * g + (lane >> 3)) * 3072 + (((lane & 7) ^ ((lane >> 3) & 6)) << 4);
#define W_DMA1(r_, v_, s_, l_) asm volatile("s_mov_b32 m0, %3\n\ts_nop 0\n\tbuffer_load_dwordx4 %0, %1, %2 offen lds" :: "v"(v_), "s"(r_), "s"(s_), "s"(l_) : "memory")
    W_DMA1(qrs, wv0, us * 98304 + um * 1024 + (bid & 7) * 128, L_WR + g * 1024);
    const int hs = (3 - (int)(kb[2] >> 7)) & 7, xt_ = bid - 117, xs = (xt_ / 3) * 98304 + (xt_ % 3) * 1024;
    const bool xp = bid >= 128 && bid < 133;
    W_DMA1(xp ? qrs : ors, xp ? wv0 : ((bid >> 3) * 32 + 8 * g + (lane >> 3)) * 1024 + (((lane & 7) ^ (((lane >> 3) & 2) | ((g & 1) << 2))) << 4), xp ? xs + hs * 128 : (bid & 7) * 128, L_WR + 4096 + g * 1024);
    B0();
    const int ln = lane;
    pbo = (int)opaque((u32)(lane == 0 ? OFF_SG + gr * 64 + g * 16 : WSB));
    const int wvo = (int)opaque((u32)(wv0 + hb * 128)), wvx = (int)opaque((u32)(hb == hs ? WSB : wvo));
#define WSO(s_) ((s_) < 4 ? 40960 + (s_) * 12288 : L_WX - L_WS)
    auto wraw = [&](auto S_) __attribute__((always_inline)) { constexpr int s = decltype(S_)::value; w_dma(qrs, s < 4 ? wvo : wvx, s < 3 ? wvo : wvx, s * 98304, L_WS + WSO(s) + g * 1024); };
    __builtin_amdgcn_s_setprio(1);
    VMW(8 + PTN);
    ST(0, 1);
    stats();
    VMW(7 + PTN);
    publish();
    ST(0, 10);
    VMW(2 + PTN);
    cols();
    SB();
    __builtin_amdgcn_s_setprio(0);
    static_for<0, R1D>(wraw);
    VMW(3 * R1D + 1); cnt_add(smem, C_B, lane);
    SB();
    const u32 wga = opaque((u32)(L_WS + (ln >> 2) * 128 + (((2 * g + ((ln & 3) >> 1)) ^ ((ln >> 2) & 6)) << 4) + (ln & 1) * 8)), wgb = opaque(wga + 53248u);
    const u32 to = opaque(xp ? wga + (L_WR - L_WS + 4096) : (u32)(L_WR + 4096 + (8 * (lane >> 4) + ((lane >> 2) & 3)) * 128 + (((2 * g + ((lane & 3) >> 1)) ^ (((lane >> 2) & 2) | ((lane >> 2) & 4))) << 4) + (lane & 1) * 8));
    const u32 tp = opaque(to + (xp ? 2048 : 512));
    const int us0 = (int)opaque((u32)(OFF_WQ + us * 98304 + ((bid & 7) * 4 + g) * 3072 + um * 1024 + lane * 16));
    const int us1 = (int)opaque((u32)((bid < 128 ? OFF_WO + (((bid & 7) * 16 + (bid >> 3)) * 4 + g) * 1024 : xp ? OFF_WQ + xs + (hs * 4 + g) * 3072 : WSB) + lane * 16));
    cnt_spin(smem, C_B, 4);
    {
#define TR4(a_) __builtin_amdgcn_ds_read_tr16_b64_v4i16((LAS bf16x4*)(size_t)(a_))
      const bf16x4 q0 = TR4(wgb + (L_WR - L_WS - 53248)), q1 = TR4(wgb + (L_WR - L_WS - 53248 + 2048)), q2 = TR4(to), q3 = TR4(tp);
      cnt_add(smem, C_B, lane);
      stw(wrs, us0, __builtin_bit_cast(u32x4, __builtin_shufflevector(q0, q1, 0, 1, 2, 3, 4, 5, 6, 7)));
      stw(wrs, us1, __builtin_bit_cast(u32x4, __builtin_shufflevector(q2, q3, 0, 1, 2, 3, 4, 5, 6, 7)));
      SB();
    }
    static_for<R1D, R1F>(wraw);
    SB();
    const u32 stag = __builtin_amdgcn_readfirstlane(tag);
    const int fs0 = OFF_ST0 + 4096 + ((bid & 7) * 4 + g) * 128 + (bid >> 3) * 4, fs1 = bid < 128 ? fs0 + 4096 : bid < 136 ? OFF_ST0 + 8192 + (hs * 4 + g) * 128 + (bid - 112) * 4 : WSB;
    const int fso = (int)opaque((u32)(lane == 0 ? fs0 : lane == 1 ? fs1 : WSB));
    const int wbo = (int)opaque((u32)((hb * 4 + g) * 3072 + ln * 16));
    const int fo = (int)opaque((u32)(OFF_ST0 + 4096 + (hb * 4 + g) * 128 + (hb == hs && (ln & 8) ? 4096 + 64 + (ln & 1) * 16 : (ln & 7) * 16)));
    const int r0 = 256 * g + 2 * ln;
    const int sto = (int)opaque((u32)(OFF_SG + r0 * 8)), xto = (int)opaque((u32)(XTO(kp) + r0 * 8));
    const int xlo = (int)opaque((u32)(OFF_X1 + (16 * g + (ln >> 3)) * XBS + kp * 128 + (ln & 7) * 16));
    const u32 uw = opaque((u32)(L_U + (256 + r0) * 2)), xw = opaque((u32)(L_X + r0 * 4));
    const int mso = (int)opaque((u32)(ln == 0 ? OFF_ST0 + kp * 16 + g * 4 : WSB));
    const int p2o = (int)opaque((u32)(OFF_X2 + (kp * XS_ + 256 * g + 4 * ln) * 4)), pso = (int)opaque((u32)(ln < 2 ? OFF_ST2 + (512 * g + 2 * bid + ln) * 16 : WSB));
    SB();
    VMW(3 * (R1F - R1D));
    __builtin_amdgcn_raw_buffer_store_b32(tag, wrs, fso, 0, 16);
    ST(0, 6);
    SB();
    static_for<R1F, 5>(wraw);
    W_DMA1(qrs, wvx, 5 * 98304, L_SP + g * 1024);
    bf16x8 wf[48];
    GPre gp;
    {
      auto wget = [&](auto S_) __attribute__((always_inline)) {
        constexpr int s = decltype(S_)::value, o = WSO(s) >= 53248 ? WSO(s) - 53248 : WSO(s);
        w_get<3 * s, o>(wf[3 * s], WSO(s) >= 53248 ? wgb : wga);
        w_get<3 * s + 1, o + 4096>(wf[3 * s + 1], WSO(s) >= 53248 ? wgb : wga);
        w_get<3 * s + 2, o + 8192>(wf[3 * s + 2], WSO(s) >= 53248 ? wgb : wga);
      };
      const u32 mk = x0_mark(stag);
      auto wld = [&](auto I_) __attribute__((always_inline)) { w_ld<decltype(I_)::value>(wf[decltype(I_)::value], wbo, wrs); };
      u32x4 gs[4], gx[4];
#define G_LD(e_) do { gs[e_] = ldws(wrs, sto + ((e_) & 1) * 1024, ((e_) >> 1) * 8192); gx[e_] = (e_) < 2 ? ldws(wrs, xlo, (e_) * 8 * XBS) : ldw(wrs, xto + ((e_) - 2) * 1024); } while (0)
#define G_B2(v_, t_) opaque((v_[1] ^ (t_)) | (v_[3] ^ (t_)))
#define G_OK(h_) (__builtin_amdgcn_ballot_w64(((G_B2(gs[2 * (h_)], stag) | G_B2(gs[2 * (h_) + 1], stag)) | (G_B2(gx[2 * (h_)], mk) | G_B2(gx[2 * (h_) + 1], mk))) != 0u) == 0ull)
      auto take = [&](auto H_) __attribute__((always_inline)) {
        constexpr int H = decltype(H_)::value;
        if (__builtin_expect(!G_OK(H), 0)) {
          for (unsigned spins = 0;; ++spins) {
            FAILC(H ? 4 : 0);
            __builtin_amdgcn_s_sleep(SLP_U);
            asm volatile("" ::: "memory");
            G_LD(2 * H); G_LD(2 * H + 1);
            if (G_OK(H) || spins > SPIN_CAP) break;
          }
        }
#pragma unroll
        for (int e = 2 * H; e < 2 * H + 2; ++e) {
          const float rs0 = __builtin_amdgcn_rcpf(hi2f(gs[e][0])), rs1 = __builtin_amdgcn_rcpf(hi2f(gs[e][2])), n0 = -lo2f(gs[e][0]) * rs0, n1 = -lo2f(gs[e][2]) * rs1;
          const int r = (e & 1) * 128 + (e >> 1) * 1024;
          *(LAS u32*)(size_t)(uw + r * 2) = pkh(fmaf(lo2f(gx[e][0]), rs0, n0), fmaf(lo2f(gx[e][2]), rs1, n1));
          *(LAS u32*)(size_t)(uw + r * 2 + 4608) = pkh(fmaf(hi2f(gx[e][0]), rs0, n0), fmaf(hi2f(gx[e][2]), rs1, n1));
          *(LAS u32x2*)(size_t)(xw + r * 4) = (u32x2){gx[e][0], gx[e][2]};
        }
        __builtin_amdgcn_sched_barrier(0);
      };
      KSLP(16, SLP_G);
      asm volatile("" ::: "memory");
#pragma unroll
      for (int e = 0; e < 4; ++e) G_LD(e);
      __builtin_amdgcn_sched_barrier(0);
      ST(0, 5);
      f32x4 cs[3];
      u32x4 SENT = {0xFFFFFFFFu, 0xFFFFFFFFu, 0xFFFFFFFFu, 0xFFFFFFFFu};
      const u32 o1 = ((ln >> 1) & 1) == (ln >> 5) ? 0x3F803F80u : 0u;
      bf16x8 one8 = __builtin_bit_cast(bf16x8, (u32x4){o1, o1, o1, o1});
      u32 sidx = (ln & 1) ? 0xEEEEu : 0x4444u;
      cs[0] = cs[1] = cs[2] = (f32x4){0.f, 0.f, 0.f, 0.f};
      asm volatile("" : "+v"(one8), "+v"(sidx), "+v"(cs[0]), "+v"(cs[1]), "+v"(cs[2]), "+v"(SENT));
      auto wsum = [&](auto I_) __attribute__((always_inline)) { constexpr int i = 2 * decltype(I_)::value + 1; w_sum<i>(cs[i % 3], one8, sidx, wf); };
      cnt_spin(smem, C_B, 8);
      __builtin_amdgcn_s_setprio(1);
      VMW(8);
      cnt_add(smem, C_W, ln);
      u32x4 fl;
      asm volatile("" : "+v"(gs[0]), "+v"(gs[1]), "+v"(gx[0]), "+v"(gx[1]) :: "memory");
      take(IC<0>{});
      cnt_add(smem, C_U, ln);
      __builtin_amdgcn_raw_buffer_store_b32(tag, wrs, mso, 0, 16);
      ST(0, 3);
      stw(wrs, p2o, SENT); __builtin_amdgcn_raw_buffer_store_b128(SENT, wrs, p2o, 4096, 16);
      stw(wrs, pso, __builtin_bit_cast(u32x4, cs[0]));
      asm volatile("s_nop 1" :: "v"(SENT) : "memory");
      SB();
      auto rest = [&]() __attribute__((always_inline)) {
        cnt_spin(smem, C_W, 4);
        static_for<0, 5>(wget);
        w_get<15, L_SP - L_WS - 53248>(wf[15], wgb);
        cnt_add(smem, C_B, ln);
        take(IC<1>{});
        cnt_add(smem, C_P2, ln);
        ST(0, 4);
      };
      rest();
      { KSLP(17, SLP_FL); fl = ldw(wrs, fo); SB(); }
      asm volatile("" : "+v"(gs[2]), "+v"(gs[3]), "+v"(gx[2]), "+v"(gx[3]), "+v"(fl) :: "memory");
      tags_wait(fl, wrs, fo, stag, 15);
      asm volatile("s_waitcnt lgkmcnt(0)" ::: "memory");
      ST(0, 7);
      asm volatile("s_cmp_lg_u32 %5, %6\n\ts_cbranch_scc1 1f\n\tbuffer_load_dwordx4 %0, %7, %8, %9 offen offset:2048 sc1\n\t"
                   "buffer_load_dwordx4 %1, %7, %8, %10 offen sc1\n\tbuffer_load_dwordx4 %2, %7, %8, %10 offen offset:1024 sc1\n\tbuffer_load_dwordx4 %3, %7, %8, %10 offen offset:2048 sc1\n\t"
                   "buffer_load_dwordx4 %4, %7, %8, %11 offen sc1\n1:"
                   : "+{v[120:123]}"(wf[11]), "+{v[124:127]}"(wf[12]), "+{v[128:131]}"(wf[13]), "+{v[132:135]}"(wf[14]), "+{v[136:139]}"(wf[15])
                   : "s"(hb), "s"(hs), "v"(wbo), "s"(wrs), "s"(OFF_WQ + 3 * 98304), "s"(OFF_WQ + 4 * 98304), "s"(OFF_WQ + 5 * 98304) : "memory", "scc");
      static_for<45, 48>(wld);
      static_for<16, 16 + H1>(wld);
      VMW(3 + H1);
      cnt_add(smem, C_P3, ln);
      static_for<16 + H1, 45>(wld);
      __builtin_amdgcn_s_setprio(0);
      ST(0, 9);
      g_pre<true>(gp, g, stag);
      asm volatile("s_waitcnt lgkmcnt(0)" ::: "memory");
      static_for<0, 5>(wsum);
      cnt_spin(smem, C_B, 12);
      VMW(29);
#pragma unroll
      for (int e = 0; e < 3; ++e) *(LAS bf16x8*)(size_t)(gp.wx + e * 1024) = wf[45 + e];
      static_for<22, 24>(wsum);
      static_for<5, 7>(wsum);
      VMW(12); static_for<7, 16>(wsum);
      VMW(6); static_for<16, 19>(wsum);
      VMW(2); static_for<19, 21>(wsum);
      VMW(0); static_for<21, 22>(wsum);
      asm volatile("s_nop 7" : "+v"(cs[0]), "+v"(cs[1]), "+v"(cs[2]));
#pragma unroll
      for (int e = 0; e < 3; ++e) *(LAS float*)(size_t)(gp.fr + 512 + e * 64) = (cs[e][0] + cs[e][1]) + (cs[(e + 2) % 3][2] + cs[(e + 2) % 3][3]);
      asm volatile("" ::: "memory");
      ST(0, 8);
    }
    {
      ConvPre pa2, pb2;
      u32x4 xa, xb;
      int ln2, l2, xo2, xo, so;
      u32 ua, rp0, rp1;
      float isc;
      g_blayer<true>(smem, wrs, g, stag, wf, gp, [&]() __attribute__((always_inline)) {
        ln2 = lane_id();
        l2 = 256 * g + 4 * ln2;
        xo2 = (int)opaque((u32)(OFF_X3 + (8 * g + (ln2 >> 3)) * XBS + kp * 128 + (ln2 & 1) * 64 + (ln2 & 6) * 8));
        xo = (int)opaque((u32)(OFF_X2 + (kp * XS_ + l2) * 4)); so = (int)opaque((u32)(OFF_ST2 + (256 * g + ln2) * 16));
        ua = opaque((u32)(L_U + (256 + l2) * 2));
        rp0 = opaque((u32)(L_REDA + (ln2 & 1) * 8192 + (g * 64 + (ln2 ^ ((ln2 >> 3) & 3))) * 16)); rp1 = opaque(rp0 ^ 16u);
        isc = *(const float*)(smem + L_MI + 4 * (ln2 & 1));
        asm volatile("" : "+v"(isc));
      });
#define VM0() __builtin_amdgcn_s_waitcnt(0x0F70)
      KSLP(9, SLP_C2);
      u32x4 gg[4];
      ST(1, 2);
      xa = ldw(wrs, xo);
      asm volatile("" : "+v"(xa));
      if (__builtin_expect(!x2_ok(xa), 0)) { FAILC(16); __builtin_amdgcn_s_sleep(SLP_XP); xa = x2_ld(wrs, kp, l2); x2_poll(wrs, kp, l2, xa); VM0(); }
      KSLP(10, SLP_S);
      asm volatile("" ::: "memory");
#pragma unroll
      for (int e = 0; e < 4; ++e) gg[e] = ldw(wrs, so + e * 1024);
      SB();
      conv_unp(pa2, xa);
      asm volatile("" : "+v"(gg[0]), "+v"(gg[1]), "+v"(gg[2]), "+v"(gg[3]));
      if (__builtin_expect(!st2_ok(gg), 0)) { FAILC(7); __builtin_amdgcn_s_sleep(SLP_SP); st2_ld(wrs, l2, gg); st2_poll(wrs, l2, gg); VM0(); }
      conv2_in<false>(smem, ua, pa2, xa, gg);
      ST(1, 3);
      lds_barrier();
      ST(1, 7);
      conv_res(pa2);
      const int so2 = (int)opaque((u32)(so + 16384));
      auto rq_b = [&]() __attribute__((always_inline)) {
        KSLP(11, SLP_C3);
        asm volatile("" ::: "memory");
        xb = ldws(wrs, xo, 4096);
        SB();
#pragma unroll
        for (int e = 0; e < 4; ++e) gg[e] = ldw(wrs, so2 + e * 1024);
        SB();
      };
      rq_b();
      __builtin_amdgcn_s_barrier();
      asm volatile("" ::: "memory");
      ST(1, 8);
      stw(wrs, xo2, conv_epi(rp0, rp1, 0, isc, pa2));
      SB();
      asm volatile("" : "+v"(xb), "+v"(gg[0]), "+v"(gg[1]), "+v"(gg[2]), "+v"(gg[3]) :: "memory");
      ST(1, 4);
      if (__builtin_expect(!x2st_ok(xb, gg), 0)) {
        FAILC(18);
        xb = x2_ld(wrs, kp, l2 + 1024);
        st2_ld(wrs, l2 + 1024, gg);
        if (!x2_ok(xb)) {
          x2_poll(wrs, kp, l2 + 1024, xb);
          st2_ld(wrs, l2 + 1024, gg);
        }
        st2_poll(wrs, l2 + 1024, gg);
        VM0();
      }
      ST(1, 1);
      const int rb = RB_OF(bid);
      if (XPB == 2) { x_dma(wrs, OFF_X3, rb, L_TA, g, ln2); SB(); }
      conv2_in<true>(smem, ua + 2048, pb2, xb, gg);
      ST(1, 5);
      lds_barrier();
      ST(1, 6);
      if (XPB == 1) { KSLP(12, SLP_XA2); x_dma(wrs, OFF_X3, rb, L_TA, g, ln2); SB(); }
      conv_res(pb2);
      if (XPB == 0) { KSLP(12, SLP_XA2); x_dma(wrs, OFF_X3, rb, L_TA, g, ln2); }
      SB();
      g_pre<false>(gp, g, stag);
      const int xo3 = (int)opaque((u32)(xo2 + 32 * XBS));
      if (GPE) __builtin_amdgcn_s_setprio(GPE);
      asm volatile("s_waitcnt vmcnt(0)\n\ts_barrier" ::: "memory");
      ST(1, 9);
      u32x4 ov = conv_epi(rp0, rp1, 16384, isc, pb2);
      stw(wrs, xo3, ov);
      if (GPE) __builtin_amdgcn_s_setprio(0);
      asm volatile("s_nop 1" :: "v"(ov));
    }
    g_blayer<false>(smem, wrs, g, stag, wf, gp, []() {});

    ST_OUT();
  }
}

__device__ __attribute__((used, noinline)) void k_text_end() {
  asm volatile(
  ".Lg_redo:\n\ts_waitcnt lgkmcnt(0)\n\t"
  "ds_read_b64_tr_b16 v[72:73], v0 offset:0\n\tds_read_b64_tr_b16 v[74:75], v0 offset:1024\n\ts_waitcnt lgkmcnt(0)\n\t"
  "v_mfma_f32_16x16x32_bf16 v[60:63], v[80:83], v[72:75], 0\n\tv_mfma_f32_16x16x32_bf16 v[16:19], v[72:75], v[84:87], 0\n\tv_mfma_f32_16x16x32_bf16 v[52:55], v[76:79], v[72:75], 0\n\t"
  "ds_read_b64_tr_b16 v[72:73], v0 offset:2048\n\tds_read_b64_tr_b16 v[74:75], v0 offset:3072\n\ts_waitcnt lgkmcnt(0)\n\t"
  "v_mfma_f32_16x16x32_bf16 v[60:63], v[92:95], v[72:75], v[60:63]\n\tv_mfma_f32_16x16x32_bf16 v[16:19], v[72:75], v[96:99], v[16:19]\n\tv_mfma_f32_16x16x32_bf16 v[52:55], v[88:91], v[72:75], v[52:55]\n\t"
  "ds_read_b64_tr_b16 v[72:73], v0 offset:8192\n\tds_read_b64_tr_b16 v[74:75], v0 offset:9216\n\ts_waitcnt lgkmcnt(0)\n\t"
  "v_mfma_f32_16x16x32_bf16 v[60:63], v[128:131], v[72:75], v[60:63]\n\tv_mfma_f32_16x16x32_bf16 v[16:19], v[72:75], v[132:135], v[16:19]\n\tv_mfma_f32_16x16x32_bf16 v[52:55], v[124:127], v[72:75], v[52:55]\n\t"
  "ds_read_b64_tr_b16 v[72:73], v0 offset:10240\n\tds_read_b64_tr_b16 v[74:75], v0 offset:11264\n\ts_waitcnt lgkmcnt(0)\n\t"
  "v_mfma_f32_16x16x32_bf16 v[60:63], v[140:143], v[72:75], v[60:63]\n\tv_mfma_f32_16x16x32_bf16 v[16:19], v[72:75], v[144:147], v[16:19]\n\tv_mfma_f32_16x16x32_bf16 v[52:55], v[136:139], v[72:75], v[52:55]\n\t"
  "ds_read_b64_tr_b16 v[72:73], v0 offset:16384\n\tds_read_b64_tr_b16 v[74:75], v0 offset:17408\n\ts_waitcnt lgkmcnt(0)\n\t"
  "v_mfma_f32_16x16x32_bf16 v[60:63], v[176:179], v[72:75], v[60:63]\n\tv_mfma_f32_16x16x32_bf16 v[16:19], v[72:75], v[180:183], v[16:19]\n\tv_mfma_f32_16x16x32_bf16 v[52:55], v[172:175], v[72:75], v[52:55]\n\t"
  "ds_read_b64_tr_b16 v[72:73], v0 offset:18432\n\tds_read_b64_tr_b16 v[74:75], v0 offset:19456\n\ts_waitcnt lgkmcnt(0)\n\t"
  "v_mfma_f32_16x16x32_bf16 v[60:63], v[188:191], v[72:75], v[60:63]\n\tv_mfma_f32_16x16x32_bf16 v[16:19], v[72:75], v[192:195], v[16:19]\n\tv_mfma_f32_16x16x32_bf16 v[52:55], v[184:187], v[72:75], v[52:55]\n\t"
  "ds_read_b64_tr_b16 v[72:73], v0 offset:24576\n\tds_read_b64_tr_b16 v[74:75], v0 offset:25600\n\ts_waitcnt lgkmcnt(0)\n\t"
  "v_mfma_f32_16x16x32_bf16 v[60:63], v[224:227], v[72:75], v[60:63]\n\tv_mfma_f32_16x16x32_bf16 v[16:19], v[72:75], v[228:231], v[16:19]\n\tv_mfma_f32_16x16x32_bf16 v[52:55], v[220:223], v[72:75], v[52:55]\n\t"
  "ds_read_b64_tr_b16 v[72:73], v0 offset:26624\n\tds_read_b64_tr_b16 v[74:75], v0 offset:27648\n\ts_waitcnt lgkmcnt(0)\n\t"
  "v_mfma_f32_16x16x32_bf16 v[60:63], v[236:239], v[72:75], v[60:63]\n\tv_mfma_f32_16x16x32_bf16 v[16:19], v[72:75], v[240:243], v[16:19]\n\tv_mfma_f32_16x16x32_bf16 v[52:55], v[232:235], v[72:75], v[52:55]\n\t"
  "ds_read_b64_tr_b16 v[72:73], v0 offset:4096\n\tds_read_b64_tr_b16 v[74:75], v0 offset:5120\n\ts_waitcnt lgkmcnt(0)\n\t"
  "v_mfma_f32_16x16x32_bf16 v[60:63], v[104:107], v[72:75], v[60:63]\n\tv_mfma_f32_16x16x32_bf16 v[16:19], v[72:75], v[108:111], v[16:19]\n\tv_mfma_f32_16x16x32_bf16 v[52:55], v[100:103], v[72:75], v[52:55]\n\t"
  "ds_read_b64_tr_b16 v[72:73], v0 offset:6144\n\tds_read_b64_tr_b16 v[74:75], v0 offset:7168\n\tds_read_b64_tr_b16 v[28:29], v0 offset:12288\n\tds_read_b64_tr_b16 v[30:31], v0 offset:13312\n\tds_read_b64_tr_b16 v[68:69], v0 offset:14336\n\tds_read_b64_tr_b16 v[70:71], v0 offset:15360\n\tds_read_b64_tr_b16 v[12:13], v0 offset:20480\n\tds_read_b64_tr_b16 v[14:15], v0 offset:21504\n\t"
  "s_waitcnt lgkmcnt(0)\n\ts_branch .Lg_back\n\t"
  ".Lc0_kh:\n\ts_mov_b32 vcc_hi, 0x1000000\n\t.Lc0_kh_s:\n\tds_read_b32 v101, v27 offset:48\n\ts_sub_u32 vcc_hi, vcc_hi, 1\n\ts_cselect_b32 vcc_lo, 0, 4\n\ts_waitcnt lgkmcnt(0)\n\tv_readfirstlane_b32 s61, v101\n\ts_nop 1\n\ts_cmp_lt_u32 s61, vcc_lo\n\ts_cbranch_scc1 .Lc0_kh_s\n\t"
  "s_waitcnt lgkmcnt(0)\n\ts_branch .Lc0_kh_b\n\t"
  ".Lc0_x0:\n\ts_mov_b32 vcc_hi, 0x1000000\n\t.Lc0_x0_s:\n\tds_read_b32 v101, v21 offset:8\n\ts_sub_u32 vcc_hi, vcc_hi, 1\n\ts_cselect_b32 vcc_lo, 0, 2\n\ts_waitcnt lgkmcnt(0)\n\tv_readfirstlane_b32 s61, v101\n\ts_nop 1\n\ts_cmp_lt_u32 s61, vcc_lo\n\ts_cbranch_scc1 .Lc0_x0_s\n\t"
  "ds_read_b128 v[144:147], v20 offset:2048\n\tds_read_b128 v[204:207], v20 offset:3072\n\ts_waitcnt lgkmcnt(0)\n\ts_branch .Lc0_x0_b\n\t"
  ".Lc0_ub:\n\ts_mov_b32 vcc_hi, 0x1000000\n\t.Lc0_ub_s:\n\tds_read_b32 v101, v27 offset:52\n\ts_sub_u32 vcc_hi, vcc_hi, 1\n\ts_cselect_b32 vcc_lo, 0, 4\n\ts_waitcnt lgkmcnt(0)\n\tv_readfirstlane_b32 s61, v101\n\ts_nop 1\n\ts_cmp_lt_u32 s61, vcc_lo\n\ts_cbranch_scc1 .Lc0_ub_s\n\t"
  "s_waitcnt lgkmcnt(0)\n\ts_branch .Lc0_ub_b\n\t"
  ".Lc0_p3:\n\ts_mov_b32 vcc_hi, 0x1000000\n\t.Lc0_p3_s:\n\tds_read_b32 v101, v27 offset:64\n\ts_sub_u32 vcc_hi, vcc_hi, 1\n\ts_cselect_b32 vcc_lo, 0, 4\n\ts_waitcnt lgkmcnt(0)\n\tv_readfirstlane_b32 s61, v101\n\ts_nop 1\n\ts_cmp_lt_u32 s61, vcc_lo\n\ts_cbranch_scc1 .Lc0_p3_s\n\t"
  "s_waitcnt lgkmcnt(0)\n\ts_branch .Lc0_p3_b\n\t"
  ".Lc0_x4:\n\ts_mov_b32 vcc_hi, 0x1000000\n\t.Lc0_x4_s:\n\tds_read_b32 v36, v27 offset:16\n\ts_sub_u32 vcc_hi, vcc_hi, 1\n\ts_cselect_b32 vcc_lo, 0, 4\n\ts_waitcnt lgkmcnt(0)\n\tv_readfirstlane_b32 s61, v36\n\ts_nop 1\n\ts_cmp_lt_u32 s61, vcc_lo\n\ts_cbranch_scc1 .Lc0_x4_s\n\t"
  "ds_read_b128 v[40:43], v20 offset:59392\n\tds_read_b128 v[44:47], v20 offset:60416\n\ts_waitcnt lgkmcnt(0)\n\ts_branch .Lc0_x4_b\n\t"
  ".Lc1_kh:\n\ts_mov_b32 vcc_hi, 0x1000000\n\t.Lc1_kh_s:\n\tds_read_b32 v101, v27 offset:48\n\ts_sub_u32 vcc_hi, vcc_hi, 1\n\ts_cselect_b32 vcc_lo, 0, 4\n\ts_waitcnt lgkmcnt(0)\n\tv_readfirstlane_b32 s61, v101\n\ts_nop 1\n\ts_cmp_lt_u32 s61, vcc_lo\n\ts_cbranch_scc1 .Lc1_kh_s\n\t"
  "s_waitcnt lgkmcnt(0)\n\ts_branch .Lc1_kh_b\n\t"
  ".Lc1_x0:\n\ts_mov_b32 vcc_hi, 0x1000000\n\t.Lc1_x0_s:\n\tds_read_b32 v101, v21 offset:8\n\ts_sub_u32 vcc_hi, vcc_hi, 1\n\ts_cselect_b32 vcc_lo, 0, 2\n\ts_waitcnt lgkmcnt(0)\n\tv_readfirstlane_b32 s61, v101\n\ts_nop 1\n\ts_cmp_lt_u32 s61, vcc_lo\n\ts_cbranch_scc1 .Lc1_x0_s\n\t"
  "ds_read_b128 v[148:151], v20 offset:0\n\tds_read_b128 v[204:207], v20 offset:1024\n\ts_waitcnt lgkmcnt(0)\n\ts_branch .Lc1_x0_b\n\t"
  ".Lc1_ub:\n\ts_mov_b32 vcc_hi, 0x1000000\n\t.Lc1_ub_s:\n\tds_read_b32 v101, v27 offset:52\n\ts_sub_u32 vcc_hi, vcc_hi, 1\n\ts_cselect_b32 vcc_lo, 0, 4\n\ts_waitcnt lgkmcnt(0)\n\tv_readfirstlane_b32 s61, v101\n\ts_nop 1\n\ts_cmp_lt_u32 s61, vcc_lo\n\ts_cbranch_scc1 .Lc1_ub_s\n\t"
  "s_waitcnt lgkmcnt(0)\n\ts_branch .Lc1_ub_b\n\t"
  ".Lc1_p3:\n\ts_mov_b32 vcc_hi, 0x1000000\n\t.Lc1_p3_s:\n\tds_read_b32 v101, v27 offset:64\n\ts_sub_u32 vcc_hi, vcc_hi, 1\n\ts_cselect_b32 vcc_lo, 0, 4\n\ts_waitcnt lgkmcnt(0)\n\tv_readfirstlane_b32 s61, v101\n\ts_nop 1\n\ts_cmp_lt_u32 s61, vcc_lo\n\ts_cbranch_scc1 .Lc1_p3_s\n\t"
  "s_waitcnt lgkmcnt(0)\n\ts_branch .Lc1_p3_b\n\t"
  ".Lc1_x4:\n\ts_mov_b32 vcc_hi, 0x1000000\n\t.Lc1_x4_s:\n\tds_read_b32 v36, v27 offset:16\n\ts_sub_u32 vcc_hi, vcc_hi, 1\n\ts_cselect_b32 vcc_lo, 0, 4\n\ts_waitcnt lgkmcnt(0)\n\tv_readfirstlane_b32 s61, v36\n\ts_nop 1\n\ts_cmp_lt_u32 s61, vcc_lo\n\ts_cbranch_scc1 .Lc1_x4_s\n\t"
  "ds_read_b128 v[40:43], v20 offset:57344\n\tds_read_b128 v[44:47], v20 offset:58368\n\ts_waitcnt lgkmcnt(0)\n\ts_branch .Lc1_x4_b\n\t"
  ".Lcw_end:");
}

extern "C" void kernel_launch(void* const* d_in, const int* in_sizes, int n_in, void* d_out, int out_size,
                              void* d_ws, size_t ws_size, hipStream_t stream) {
  (void)hipFuncSetAttribute((const void*)k_mega, hipFuncAttributeMaxDynamicSharedMemorySize, MEGA_LDS);
  k_mega<<<256, 512, MEGA_LDS, stream>>>((const u16*)d_in[0], (const u16*)d_in[1], (const u16*)d_in[2], (const u16*)d_in[3],
                                         (const u16*)d_in[4], (const u16*)d_in[5], (const u16*)d_in[6], (const u16*)d_in[7],
                                         (const u16*)d_in[8], (const u16*)d_in[10], (const u16*)d_in[11], (char*)d_ws,
                                         (u32*)d_out);
}
#if defined(__HIP_DEVICE_COMPILE__)
#pragma clang attribute pop
#endif
```

```cpp
#if defined(__HIP_DEVICE_COMPILE__)
#pragma clang attribute push (__attribute__((target("no-packed-fp32-ops"))), apply_to = function)
#endif
#include <hip/hip_runtime.h>
#include <hip/hip_bf16.h>
#include <stdint.h>
#define TIDX ((int)__builtin_amdgcn_workitem_id_x())
#define BIDX ((int)__builtin_amdgcn_workgroup_id_x())
#define ST(set, k)
#define ST_INIT()
#define ST_OUT()
#define KATTR
#define FAILC(k)

#define ST_FIX()
#define CST(k)
#define GST(k)
typedef unsigned short u16;
typedef unsigned int u32;
typedef unsigned long long u64;
using bf16x8 = __attribute__((ext_vector_type(8))) short;
using bf16x4 = __attribute__((ext_vector_type(4))) short;
using f16x8 = __attribute__((ext_vector_type(8))) _Float16;
using f16x4 = __attribute__((ext_vector_type(4))) _Float16;
using f32x2 = __attribute__((ext_vector_type(2))) float;
using f32x4 = __attribute__((ext_vector_type(4))) float;
using u32x2 = __attribute__((ext_vector_type(2))) unsigned;
using u32x4 = __attribute__((ext_vector_type(4))) unsigned;
#define LAS __attribute__((address_space(3)))
#define GAS __attribute__((address_space(1)))

#define D_ 512
#define NS_ 128
#define XS_ 2080

#define MB_ (1 << 20)
#define XTO(kp_) (OFF_O1 + (((kp_) >> 4) & 1) * MB_ + ((kp_) >> 5) * 131072 + ((kp_) & 15) * 8192)
#define XTS 8192
#define RB_OF(b_) (((b_) & 7) * 4 + ((b_) >> 6))
#define HB_OF(b_) (((b_) >> 3) & 7)
#define OFF_ST0 (5 * MB_)
#define OFF_TS (OFF_ST0 + 16384)
#define OFF_SG (OFF_ST0 + 32768)
#define OFF_ST2 (OFF_ST0 + 65536)
#define OFF_T5 (OFF_ST0 + 262144)
#define OFF_EP (OFF_ST0 + 131072)
#define OFF_WQ (9 * MB_ - 262144)
#define OFF_WO (10 * MB_ + 393216)
#define OFF_X1 (11 * MB_)
#define OFF_O1 (14 * MB_)
#define OFF_X2 (16 * MB_)
#define OFF_X3 (19 * MB_)
#define OFF_O2 (22 * MB_)
#define OFF_KV2 (26 * MB_)
#define KVO(L1_, rb_, h_) (((L1_) ? OFF_O2 : OFF_KV2) + (((h_) >> 3) & 1) * MB_ + (rb_) * 32768 + (((h_) >> 2) & 1) * 16384 + ((h_) >> 4) * 8192 + ((h_) & 3) * 2048)
#define WSB (32 * MB_)
#define XBS 32896
#define SPIN_CAP (1u << 14)
#define XPB 1
#define GPE 1
#define B6 0
#define OUTA 2
#define OUTB 0
#define WLD 8
#define SLP_FL 4
#define SLP_WE 0
#define R1F 3
#define D9 1300
#define WS5 5
#define SLP_B4 4
#define DB2 450
#define D5 200
#define DQ2 0
#define DO2 800
#define SLP_U 2
#define SLP_F 2
#define SLP_G 0
#define R1D 1
#define WOE 4
#define SLP_H1 0
#define SLP_H2 2
#define WO0 0
#define SLP_XA2 2
#define CPF 5
#define SLP_XP 2
#define SLP_SP 4
#define SLP_S 6
#define SLP_XA1 4
#define SLP_XB1 6
#define SLP_OB1 6
#define DO1 800
#define DO5 4600
#define WX 4
#define WA 0
#define WM 4
#define SLP_C2 3
#define SLP_C3 14

__device__ __forceinline__ float bf2f(u16 v) { return __uint_as_float(((u32)v) << 16); }
typedef __attribute__((ext_vector_type(2))) __bf16 bf16x2v;
__device__ __forceinline__ u32 pk2(float a, float b) {
  f32x2 f = {a, b};
  bf16x2v v = __builtin_convertvector(f, bf16x2v);
  return __builtin_bit_cast(u32, v);
}
__device__ __forceinline__ float rbf(float f) { return __uint_as_float(pk2(f, 0.f) << 16); }
__device__ __forceinline__ float lo2f(u32 w) { return __uint_as_float(w << 16); }
__device__ __forceinline__ float hi2f(u32 w) { return __uint_as_float(w & 0xFFFF0000u); }
template <int CTRL>
__device__ __forceinline__ float dpp_mov(float v) {
  return __int_as_float(__builtin_amdgcn_update_dpp(0, __float_as_int(v), CTRL, 0xf, 0xf, false));
}
#define DPP_XOR1 0xB1
#define DPP_XOR2 0x4E
#define DPP_ROR8 0x128
template <int CTRL, int RMASK>
__device__ __forceinline__ float dpp_add(float v) {
  return v + __int_as_float(__builtin_amdgcn_update_dpp(0, __float_as_int(v), CTRL, RMASK, 0xf, false));
}
__device__ __forceinline__ void wave_sum3(float& a, float& b, float& c) {
#define WS3(CT_, RM_) a = dpp_add<CT_, RM_>(a); b = dpp_add<CT_, RM_>(b); c = dpp_add<CT_, RM_>(c); __builtin_amdgcn_sched_barrier(0);
  WS3(0x111, 0xf) WS3(0x112, 0xf) WS3(0x114, 0xf) WS3(0x118, 0xf) WS3(0x142, 0xa) WS3(0x143, 0xc)
  a = __int_as_float(__builtin_amdgcn_readlane(__float_as_int(a), 63));
  b = __int_as_float(__builtin_amdgcn_readlane(__float_as_int(b), 63));
  c = __int_as_float(__builtin_amdgcn_readlane(__float_as_int(c), 63));
}
__device__ __forceinline__ float rmax(float a, float b) {
  float r;
  asm("v_max_f32 %0, %1, %2" : "=v"(r) : "v"(a), "v"(b));
  return r;
}
__device__ __forceinline__ void lds_barrier() {
  asm volatile("s_waitcnt lgkmcnt(0)" ::: "memory");
  __builtin_amdgcn_s_barrier();
  asm volatile("" ::: "memory");
}
__device__ __forceinline__ void cexp2(float re2, float rev, float& orr, float& oi) {
  float e = __builtin_amdgcn_exp2f(re2);
  rev = __builtin_amdgcn_fractf(rev);
  float s = __builtin_amdgcn_sinf(rev);
  float c = __builtin_amdgcn_cosf(rev);
  orr = e * c;
  oi = e * s;
}
__device__ __forceinline__ void cexp3(float re2, float rev, float& e, float& c, float& s) {
  e = __builtin_amdgcn_exp2f(re2);
  rev = __builtin_amdgcn_fractf(rev);
  s = __builtin_amdgcn_sinf(rev); c = __builtin_amdgcn_cosf(rev);
}
__device__ __forceinline__ float dot2acc(u32 a, u32 b, float c) {
  return __builtin_amdgcn_fdot2_f32_bf16(__builtin_bit_cast(bf16x2v, a), __builtin_bit_cast(bf16x2v, b), c, false);
}
__device__ __forceinline__ u32 opaque(u32 v) { asm volatile("" : "+v"(v)); return v; }
__device__ __forceinline__ int lane_id() { return (int)__builtin_amdgcn_mbcnt_hi(~0u, __builtin_amdgcn_mbcnt_lo(~0u, opaque(0u))); }
__device__ __forceinline__ u32 pack_stats(float mean, float var) {
  float vb = rbf(var);
  float ve = rbf(vb + 1e-5f);
  float sd = __builtin_amdgcn_sqrtf(ve);
  return pk2(mean, sd);
}

#define SB() __builtin_amdgcn_sched_barrier(0)

#define KADD(id_, t_, v_) kadd_<(v_)>(t_)
#define KSLP(id_, v_) do { if ((v_) > 0) __builtin_amdgcn_s_sleep(v_); } while (0)
template <int V>
__device__ __forceinline__ u32 kadd_(u32 t) { u32 r; asm("s_add_i32 %0, %1, %2" : "=s"(r) : "s"(__builtin_amdgcn_readfirstlane(t)), "n"(V) : "scc"); return r; }
template <int V> struct IC { static constexpr int value = V; };
template <int B, int E, class F>
__device__ __forceinline__ void static_for(F& f) { if constexpr (B < E) { f(IC<B>{}); static_for<B + 1, E>(f); } }
typedef __amdgpu_buffer_rsrc_t rsrc_t;
__device__ __forceinline__ u32x4 ldw(rsrc_t r, int off) { return __builtin_amdgcn_raw_buffer_load_b128(r, off, 0, 16); }
__device__ __forceinline__ u32x4 ldws(rsrc_t r, int off, int soff) { return __builtin_amdgcn_raw_buffer_load_b128(r, off, soff, 16); }
__device__ __forceinline__ void stw(rsrc_t r, int off, u32x4 v) { __builtin_amdgcn_raw_buffer_store_b128(v, r, off, 0, 16); }
__device__ __forceinline__ u32 umax4(u32 m, u32x4 v) { return max(max(max(m, v[0]), v[1]), max(v[2], v[3])); }
template <int CTRL>
__device__ __forceinline__ u32 dpp_u(u32 v) { return (u32)__builtin_amdgcn_update_dpp(0, (int)v, CTRL, 0xf, 0xf, false); }

#define CSTR 4160
#define L_U 33280
#define L_TA 42496
#define L_TB (L_TA + 32768)
#define L_QB (L_TB + 32768)
#define L_TH (L_QB + 16384)
#define L_SP (L_QB + 32768)
#define L_FS (L_SP + 4096)
#define L_MI (L_FS + 3072)
#define L_XS (L_MI + 80)
#define L_WX (L_XS + 1024)
#define MEGA_LDS (L_WX + 12288)
#define L_REDA L_QB
#define L_REDB (L_QB + 16384)
#define L_XC L_REDB
#define L_X (L_REDB + 8192)
#define TBS 3136
#define L_WR (L_QB + 24576)
#define L_WS L_TA
#define L_KT L_TB
#define KTO 65024
#define FTS 528
#define L_P0 (L_FS + 2048)
#define STAT_SCALE 33554432.0f
#define C_OB 20
#define C_OT 24
#define M_FX 72
#define C_X2 28
#define C_G2 32
#define C_A 36
#define C_B 40
#define C_F 44
#define C_KH 48
#define C_P2 52
#define C_XC 8
#define C_P3 64
#define M_RD 68
#define M_DL 76
#define C_OV 28
#define C_U 56
#define C_W 60

typedef __attribute__((ext_vector_type(2))) _Float16 f16x2v;
typedef __attribute__((ext_vector_type(2))) short i16x2;
__device__ __forceinline__ u32 pkh(float a, float b) {
  u32 r;
  asm("v_cvt_pk_f16_f32 %0, %1, %2" : "=v"(r) : "v"(a), "v"(b));
  return r;
}
__device__ __forceinline__ u32 pkhn(float a, float b) {
  u32 r;
  asm("v_cvt_pk_f16_f32 %0, %1, -%2" : "=v"(r) : "v"(a), "v"(b));
  return r;
}
__device__ __forceinline__ u32 tb_addr(u32 bits2, u32 base2) {
  i16x2 b = __builtin_elementwise_max(__builtin_bit_cast(i16x2, bits2), (i16x2){0x3F00, 0x3F00});
  u32 r;
  asm("v_pk_mad_u16 %0, %1, 2, %2 op_sel_hi:[1,0,1]" : "=v"(r) : "v"(b), "s"(base2));
  return r;
}
__device__ __forceinline__ int bexp(float b) { return min(max((int)((__float_as_uint(b) >> 23) & 0xFF), 20), 240); }
__device__ __forceinline__ void cnt_add(char* smem, int off, int lane) {
  asm volatile("s_mov_b64 exec, 1\n\tds_add_u32 %0, %1\n\ts_mov_b64 exec, -1" :: "v"(L_MI + off), "v"(1u) : "memory");
}
__device__ __forceinline__ void cnt_wait(char* smem, int off, u32 target) {
#pragma unroll 1
  for (unsigned n = 0; n < (1u << 23) && (u32)__builtin_amdgcn_readfirstlane(__hip_atomic_load((LAS u32*)(smem + L_MI + off), __ATOMIC_RELAXED, __HIP_MEMORY_SCOPE_WORKGROUP)) < target; ++n)
    __builtin_amdgcn_s_sleep(2);
  asm volatile("" ::: "memory");
}
__device__ __forceinline__ void cnt_spin(char* smem, int off, u32 target) {
  for (unsigned n = 0; n < (1u << 24) && (u32)__builtin_amdgcn_readfirstlane(__hip_atomic_load((LAS u32*)(smem + L_MI + off), __ATOMIC_RELAXED, __HIP_MEMORY_SCOPE_WORKGROUP)) < target; ++n) {}
  asm volatile("" ::: "memory");
}
__device__ __forceinline__ void cnt_spin2(int o0, u32 t0, int o1, u32 t1) {
  for (unsigned n = 0; n < (1u << 24); ++n) {
    asm volatile("" ::: "memory");
    const u32 c0 = *(const volatile LAS u32*)(size_t)(u32)(L_MI + o0), c1 = *(const volatile LAS u32*)(size_t)(u32)(L_MI + o1);
    if (__builtin_amdgcn_readfirstlane(min((int)(c0 - t0), (int)(c1 - t1))) >= 0) break;
  }
  asm volatile("" ::: "memory");
}
__device__ __forceinline__ u32 now_() { return (u32)__builtin_amdgcn_s_memtime(); }
__device__ __forceinline__ u32 tmax(u32 a, u32 b) { return (int)(a - b) > 0 ? a : b; }
__device__ __forceinline__ void gate(u32 t) {
  u32 n = 256;
  asm volatile("1:\n\ts_memtime vcc\n\ts_sub_u32 %0, %0, 1\n\ts_waitcnt lgkmcnt(0)\n\ts_cbranch_scc1 2f\n\ts_sub_i32 vcc_lo, %1, vcc_lo\n\ts_cmp_lt_i32 vcc_lo, 1\n\ts_cbranch_scc1 2f\n\t"
               "s_cmp_lt_i32 vcc_lo, 64\n\ts_cbranch_scc1 1b\n\ts_sleep 1\n\ts_cmp_lt_i32 vcc_lo, 0x200\n\ts_cbranch_scc1 1b\n\ts_sleep 6\n\ts_branch 1b\n2:"
               : "+s"(n) : "s"(__builtin_amdgcn_readfirstlane(t)) : "memory", "vcc", "scc");
}
__device__ __forceinline__ u32 x0_mark(u32 tag) { return 0xFF807F80u + (((tag & 31u) + 1u) << 16) + ((tag >> 5) & 31u) + 1u; }
__device__ __forceinline__ bool tags_ok(const u32x4& f, u32 tag) {
  return __builtin_amdgcn_ballot_w64((opaque((f[0] ^ tag) | (f[1] ^ tag)) | opaque((f[2] ^ tag) | (f[3] ^ tag))) != 0u) == 0ull;
}
__device__ __forceinline__ void tags_wait(u32x4& f, rsrc_t wrs, int fo, u32 tag, int fid) {
  if (__builtin_expect(!tags_ok(f, tag), 0)) {
    for (unsigned spins = 0; spins <= SPIN_CAP; ++spins) {
      FAILC(fid);
      __builtin_amdgcn_s_sleep(SLP_F);
      asm volatile("" ::: "memory");
      if (tags_ok(ldw(wrs, fo), tag)) break;
    }
    __builtin_amdgcn_s_waitcnt(0x0F70);
  }
  asm volatile("" ::: "memory");
}
__device__ __forceinline__ void build_copies(char* smem, int cc, int ch, const u32 (&kw)[6]) {
  char* cop = smem + cc * 4 * CSTR + ch * 16;
  u32 sh[5];
#pragma unroll
  for (int k = 0; k < 5; ++k) sh[k] = __builtin_amdgcn_alignbit(kw[k + 1], kw[k], 16);
  u32x4 c0 = {kw[0], kw[1], kw[2], kw[3]}, c1 = {sh[0], sh[1], sh[2], sh[3]}, c2 = {kw[1], kw[2], kw[3], kw[4]}, c3 = {sh[1], sh[2], sh[3], sh[4]};
  asm volatile("" : "+v"(c0), "+v"(c1), "+v"(c2), "+v"(c3));
  *(u32x4*)(cop + 0 * CSTR) = c0; *(u32x4*)(cop + 1 * CSTR) = c1; *(u32x4*)(cop + 2 * CSTR) = c2; *(u32x4*)(cop + 3 * CSTR) = c3;
  asm volatile("s_nop 1" :: "v"(c0), "v"(c1), "v"(c2), "v"(c3) : "memory");
}
struct ConvPre { f32x4 x0, x1; };
__device__ __forceinline__ void conv_unp(ConvPre& P, u32x4 xw) {
#pragma unroll
  for (int e = 0; e < 4; ++e) { P.x0[e] = lo2f(xw[e]); P.x1[e] = hi2f(xw[e]); }
  asm volatile("" : "+v"(P.x0), "+v"(P.x1));
}
__device__ __forceinline__ u32x4 x2_ld(rsrc_t wrs, int kp, int l0) { return ldw(wrs, OFF_X2 + (kp * XS_ + l0) * 4); }
__device__ __forceinline__ bool x2_ok(const u32x4& xw) { return __all(umax4(0u, xw) != 0xFFFFFFFFu); }
__device__ __forceinline__ void x2_poll(rsrc_t wrs, int kp, int l0, u32x4& xw) {
  for (unsigned spins = 0; !x2_ok(xw) && spins <= SPIN_CAP; ++spins) {
    FAILC(17);
    __builtin_amdgcn_s_sleep(SLP_XP);
    asm volatile("" ::: "memory");
    xw = x2_ld(wrs, kp, l0);
  }
}
__device__ __forceinline__ void st2_ld(rsrc_t wrs, int l0, u32x4 (&g)[4]) {
#pragma unroll
  for (int e = 0; e < 4; ++e) g[e] = ldw(wrs, OFF_ST2 + ((l0 & ~255) + ((l0 >> 2) & 63)) * 16 + e * 1024);
}
__device__ __forceinline__ bool st2_ok(const u32x4 (&g)[4]) {
  u32 ok = 32u;
#pragma unroll
  for (int e = 0; e < 4; ++e) ok &= g[e][0] & g[e][2];
  return __all(ok != 0u);
}
__device__ __forceinline__ bool x2st_ok(const u32x4& xw, const u32x4 (&g)[4]) {
  u32 ok = umax4(0u, xw) != 0xFFFFFFFFu ? 32u : 0u;
#pragma unroll
  for (int e = 0; e < 4; ++e) ok &= g[e][0] & g[e][2];
  return __all(ok != 0u);
}
__device__ __forceinline__ void st2_poll(rsrc_t wrs, int l0, u32x4 (&g)[4]) {
  for (unsigned spins = 0; !st2_ok(g) && spins <= SPIN_CAP; ++spins) {
    FAILC(8);
    __builtin_amdgcn_s_sleep(SLP_SP);
    asm volatile("" ::: "memory");
    st2_ld(wrs, l0, g);
  }
}
template <bool UNP>
__device__ __forceinline__ void conv2_in(char* smem, u32 ua, ConvPre& P, u32x4 xw, u32x4 (&g)[4]) {
  f32x4 rs, nm;
#pragma unroll
  for (int e = 0; e < 4; ++e) {
    const float c1 = 4294967296.0f / (64.0f * 512.0f * STAT_SCALE), c2 = 1.0f / 4294967296.0f;
    const float t1 = fmaf((float)g[e][0], c2, (float)(int)g[e][1]), t2 = fmaf((float)g[e][2], c2, (float)(int)g[e][3]);
    const float m1 = -c1 * t1;
    rs[e] = __builtin_amdgcn_rsqf(fmaf(rmax(fmaf(m1, t1, t2), 0.f), c1, 1e-5f));
    nm[e] = m1 * rs[e];
  }
  if (UNP) conv_unp(P, xw);
  u32x2 u0 = {pkh(fmaf(P.x0[0], rs[0], nm[0]), fmaf(P.x0[1], rs[1], nm[1])), pkh(fmaf(P.x0[2], rs[2], nm[2]), fmaf(P.x0[3], rs[3], nm[3]))};
  u32x2 u1 = {pkh(fmaf(P.x1[0], rs[0], nm[0]), fmaf(P.x1[1], rs[1], nm[1])), pkh(fmaf(P.x1[2], rs[2], nm[2]), fmaf(P.x1[3], rs[3], nm[3]))};
  asm volatile("" : "+v"(u0), "+v"(u1));
  *(LAS u32x2*)(size_t)ua = u0;
  *(LAS u32x2*)(size_t)(ua + 4608) = u1;
  asm volatile("s_nop 1" :: "v"(u0), "v"(u1) : "memory");
}
__device__ __forceinline__ void pair_x(f32x4& lo, f32x4& hi, const f32x4& a, const f32x4& b) {
#define PXD " quad_perm:[1,0,3,2] row_mask:0xf bank_mask:0xf\n\t"
  asm volatile("s_mov_b64 vcc, %16\n\ts_nop 0\n\t"
               "v_cndmask_b32_dpp %0, %12, %8, vcc" PXD "v_cndmask_b32_dpp %1, %13, %9, vcc" PXD "v_cndmask_b32_dpp %2, %14, %10, vcc" PXD "v_cndmask_b32_dpp %3, %15, %11, vcc" PXD
               "s_not_b64 vcc, vcc\n\t"
               "v_cndmask_b32_dpp %4, %8, %12, vcc" PXD "v_cndmask_b32_dpp %5, %9, %13, vcc" PXD "v_cndmask_b32_dpp %6, %10, %14, vcc" PXD "v_cndmask_b32_dpp %7, %11, %15, vcc" PXD
               : "=&v"(lo[0]), "=&v"(lo[1]), "=&v"(lo[2]), "=&v"(lo[3]), "=&v"(hi[0]), "=&v"(hi[1]), "=&v"(hi[2]), "=&v"(hi[3])
               : "v"(a[0]), "v"(a[1]), "v"(a[2]), "v"(a[3]), "v"(b[0]), "v"(b[1]), "v"(b[2]), "v"(b[3]), "s"(0x5555555555555555ull) : "vcc", "scc");
}
__device__ __forceinline__ void conv_res(ConvPre& P) {
  f32x4 lo, hi;
  pair_x(lo, hi, P.x0, P.x1);
  P.x0 = lo; P.x1 = hi;
}
__device__ __forceinline__ u32x4 conv_epi(u32 rp0, u32 rp1, int off, float isc, const ConvPre& P) {
  const f32x4 a0 = *(const LAS f32x4*)(size_t)(rp0 + off), a1 = *(const LAS f32x4*)(size_t)(rp0 + off + 4096), b0 = *(const LAS f32x4*)(size_t)(rp1 + off), b1 = *(const LAS f32x4*)(size_t)(rp1 + off + 4096);
  __builtin_amdgcn_sched_barrier(0);
  u32x4 r;
#pragma unroll
  for (int e = 0; e < 2; ++e) {
    r[e] = pk2(fmaf(a1[2 * e], isc, fmaf(a0[2 * e], isc, P.x0[2 * e])), fmaf(a1[2 * e + 1], isc, fmaf(a0[2 * e + 1], isc, P.x0[2 * e + 1])));
    r[2 + e] = pk2(fmaf(b1[2 * e], isc, fmaf(b0[2 * e], isc, P.x1[2 * e])), fmaf(b1[2 * e + 1], isc, fmaf(b0[2 * e + 1], isc, P.x1[2 * e + 1])));
  }
  return r;
}
__device__ __forceinline__ void dma4k(rsrc_t r, int voff, int soff, int lds) {
  asm volatile("s_mov_b32 m0, %3\n\ts_nop 0\n\t"
               "buffer_load_dwordx4 %0, %1, %2 offen sc1 lds\n\t"
               "buffer_load_dwordx4 %0, %1, %2 offen offset:1024 sc1 lds\n\t"
               "buffer_load_dwordx4 %0, %1, %2 offen offset:2048 sc1 lds\n\t"
               "buffer_load_dwordx4 %0, %1, %2 offen offset:3072 sc1 lds"
               :: "v"(voff), "s"(r), "s"(soff), "s"(lds) : "memory");
}
__device__ __forceinline__ void dma1k(rsrc_t r, int voff, int soff, int lds) {
  asm volatile("s_mov_b32 m0, %3\n\ts_nop 0\n\tbuffer_load_dwordx4 %0, %1, %2 offen sc1 lds" :: "v"(voff), "s"(r), "s"(soff), "s"(lds) : "memory");
}
__device__ __forceinline__ int x_lane(int lane) { return ((lane & 31) >> 1) * 64 + (lane >> 5) * 32 + (lane & 1) * 16; }
__device__ __forceinline__ void x_dma(rsrc_t wrs, int xin, int rb, int tile, int a, int lane) {
  const int vo = x_lane(lane);
  dma4k(wrs, vo, xin + rb * XBS + a * 8192, tile + a * 8192);
  dma4k(wrs, vo, xin + rb * XBS + a * 8192 + 4096, tile + a * 8192 + 4096);
  if (tile == L_TB) {
    int so = xin + (rb - 1) * XBS + a * 8192;
    asm volatile("s_mov_b32 m0, %3\n\ts_nop 0\n\tbuffer_load_dwordx4 %1, %2, %0 offen sc1 lds\n\t"
                 "s_sub_u32 m0, m0, 1024\n\ts_nop 0\n\tbuffer_load_dwordx4 %1, %2, %0 offen offset:2048 sc1 lds\n\t"
                 "s_add_u32 m0, m0, 3072\n\ts_add_u32 %0, %0, 4096\n\tbuffer_load_dwordx4 %1, %2, %0 offen sc1 lds\n\t"
                 "s_sub_u32 m0, m0, 1024\n\ts_nop 0\n\tbuffer_load_dwordx4 %1, %2, %0 offen offset:2048 sc1 lds"
                 : "+s"(so) : "v"((lane >> 1) * 64 + 32 + (lane & 1) * 16), "s"(wrs), "s"(L_TH + a * 4096) : "memory", "scc");
  }
}
template <int S>
__device__ __forceinline__ bf16x8 ld_tr8(const LAS char* p) {
  const bf16x4 lo = __builtin_amdgcn_ds_read_tr16_b64_v4i16((LAS bf16x4*)p), hi = __builtin_amdgcn_ds_read_tr16_b64_v4i16((LAS bf16x4*)(p + S));
  return __builtin_shufflevector(lo, hi, 0, 1, 2, 3, 4, 5, 6, 7);
}

struct ConvK { u32 ah, ah2, bp, ra; f16x8 k[16]; };
__device__ __forceinline__ void a_conv_pre(ConvK& C, int a, int lane) {
  lane = (int)opaque((u32)lane);
  const int c = a >> 1, qq = a & 1, j16 = lane & 15, kg = lane >> 4;
  const int y00 = 2031 - j16 + 8 * kg - 128 * qq;
  const int sg = y00 & 3;
  C.ah = opaque((u32)(c * 4 * CSTR + sg * CSTR + 2 * (y00 - sg) - 3776));
  C.ah2 = opaque(C.ah + 8);
  C.bp = opaque((u32)(L_U + c * 4608 + (256 + 16 * j16 - 16 + 8 * kg - 128 * qq) * 2 - 192));
  C.ra = opaque((u32)(L_REDA + (a * 256 + 4 * j16 + (kg ^ ((j16 >> 1) & 3) ^ c)) * 16));
#pragma unroll
  for (int e = 0; e < 16; ++e) {
    const int ao = 3776 - (512 * (e & 3) + 64 * (e >> 2));
    const f16x4 h0 = *(const LAS f16x4*)(size_t)(C.ah + ao), h1 = *(const LAS f16x4*)(size_t)(C.ah2 + ao);
    C.k[e] = __builtin_shufflevector(h0, h1, 0, 1, 2, 3, 4, 5, 6, 7);
    __builtin_amdgcn_sched_barrier(0);
  }
#pragma unroll
  for (int e = 0; e < 16; ++e) asm volatile("" : "+v"(C.k[e]));
}
template <bool L1>
__device__ __forceinline__ void a_conv(char* smem, rsrc_t wrs, int rb, int a, const ConvK& C) {
  const LAS char* ah = (const LAS char*)(size_t)C.ah;
  const LAS char* ah2 = (const LAS char*)(size_t)C.ah2;
  const LAS char* bp = (const LAS char*)(size_t)C.bp;
  LAS f32x4* ra = (LAS f32x4*)(size_t)C.ra;
  f32x4 acc[8];
#pragma unroll
  for (int e = 0; e < 8; ++e) acc[e] = (f32x4){0.f, 0.f, 0.f, 0.f};
  f16x8 bf[4][8], fh[4][8];
#define S_RR(s_) ((s_) < 16 ? (s_) >> 2 : (s_) < 44 ? ((s_) - 16) / 7 : ((s_) - 44) >> 2)
#define S_MU(s_) ((s_) < 16 ? (s_) & 3 : (s_) < 44 ? ((s_) - 16) % 7 + 1 : ((s_) - 44) & 3)
#define S_NEW(s_) ((s_) < 16 || ((s_) < 44 && S_MU(s_) >= 4))
#define LDA(s_) do { const int ao_ = 3776 - (512 * S_MU(s_) + 64 * S_RR(s_));                                        \
      f16x4 h0_ = *(const LAS f16x4*)(ah + ao_), h1_ = *(const LAS f16x4*)(ah2 + ao_);                                \
      fh[S_RR(s_)][S_MU(s_)] = __builtin_shufflevector(h0_, h1_, 0, 1, 2, 3, 4, 5, 6, 7); } while (0)
#define LDB(r_, t_) bf[r_][t_] = *(const LAS f16x8*)(bp + 512 * (t_) + 192 - 64 * (r_))
#define CSTEPS(s0_, s1_)                                                                                               \
  _Pragma("unroll") for (int st = s0_; st < s1_; ++st) {                                                               \
    const int rr = S_RR(st), mu = S_MU(st);                                                                           \
    if (S_NEW(st + CPF) && (L1 || st + CPF >= 16)) LDA(st + CPF);                                                     \
    if (st < 4) { LDB(2 + (st >> 1), 2 * (st & 1)); LDB(2 + (st >> 1), 2 * (st & 1) + 1); }                           \
    if (st >= 44 && st < 48) { LDB(2 + ((st - 44) >> 1), 4 + 2 * (st & 1)); LDB(2 + ((st - 44) >> 1), 5 + 2 * (st & 1)); } \
    __builtin_amdgcn_sched_barrier(0);                                                                                \
    _Pragma("unroll") for (int cb = 0; cb < 8; ++cb) {                                                                \
      const int t = cb - mu;                                                                                          \
      const bool on = st < 16 ? (cb < 4 && t >= 0) : st < 44 ? (cb >= 4 && t >= 0 && t < 4) : (t >= 4);               \
      if (on) acc[cb] = __builtin_amdgcn_mfma_f32_16x16x32_f16(fh[rr][mu], bf[rr][t < 0 ? 0 : t], acc[cb], 0, 0, 0);  \
    }                                                                                                                 \
    __builtin_amdgcn_sched_barrier(0);                                                                                \
    if (st >= 13 && st < 16) ra[(st - 13) * 64] = acc[st - 13];                                                       \
    if (st >= 57) ra[1024 + (st - 57) * 64] = acc[4 + st - 57];                                                       \
  }
#pragma unroll
  for (int e = 0; e < 16; ++e) fh[e >> 2][e & 3] = C.k[e];
  __builtin_amdgcn_s_barrier();
  asm volatile("" ::: "memory");
  ST(!L1, 7);
#pragma unroll
  for (int e = 0; e < CPF; ++e) { if (L1) LDA(e); __builtin_amdgcn_sched_barrier(0); if (e < 2) { LDB(e, 0); __builtin_amdgcn_sched_barrier(0); LDB(e, 1); __builtin_amdgcn_sched_barrier(0); LDB(e, 2); __builtin_amdgcn_sched_barrier(0); LDB(e, 3); __builtin_amdgcn_sched_barrier(0); } }
  __builtin_amdgcn_sched_barrier(0);
  CSTEPS(0, 16)
  CSTEPS(16, 17)
  ra[3 * 64] = acc[3];
  __builtin_amdgcn_sched_barrier(0);
  asm volatile("" ::: "memory");
  CSTEPS(17, 18)
  if (L1) asm volatile("s_waitcnt vmcnt(0)" ::: "memory");
  asm volatile("s_waitcnt lgkmcnt(2)" ::: "memory");
  __builtin_amdgcn_s_barrier();
  asm volatile("" ::: "memory");
  ST(!L1, 8);
  CSTEPS(18, 44)
  if (!L1) ST(1, 5);
  if (!L1) __builtin_amdgcn_s_barrier();
  if (!L1) ST(1, 6);
  asm volatile("" ::: "memory");
#pragma unroll
  for (int e = 0; e < 8; ++e) { LDB(e >> 2, 4 + (e & 3)); __builtin_amdgcn_sched_barrier(0); }
  CSTEPS(44, 60)
  ra[1024 + 3 * 64] = acc[7];
  lds_barrier();
  ST(!L1, 9);
}

struct C1Pre { u32 ah, ah2, bp, xp, xc, cn; int so, fo0, fo1; };
#define C1_OPS "+{v16}"(C.ah), "+{v17}"(C.ah2), "+{v18}"(C.bp), "+{v19}"(C.xp), "+{v20}"(C.xc), "+{v21}"(C.cn), "+{v22}"(C.so), "+{v23}"(C.fo0), "+{v24}"(C.fo1)
__device__ __forceinline__ void c1_pre(C1Pre& C, int kp, int a, int lane) {
  lane = (int)opaque((u32)lane);
  const int c = a >> 1, j16 = lane & 15, kg = lane >> 4, P = a & 1;
  const int y0 = 2031 - j16 + 8 * kg - 128 * P, sg = y0 & 3;
  C.ah = (u32)(c * 4 * CSTR + sg * CSTR + 2 * (y0 - sg) - 3776);
  C.ah2 = C.ah + 8;
  C.bp = (u32)(L_U + c * 4608 + (240 + 16 * j16 + 8 * kg - 128 * P) * 2 - 192);
  C.xp = (u32)(L_X + j16 * 64 + kg * 16);
  C.xc = (u32)(L_TA + 24576 + c * 4096 + lane * 16);
  C.cn = (u32)(L_MI + 4 * c);
  C.so = OFF_X1 + (8 * ((kg & 1) ? 3 - P : P) + (j16 >> 1)) * XBS + kp * 128 + c * 64 + (j16 & 1) * 32 + (kg >> 1) * 16;
  C.fo0 = OFF_ST0 + (a * 64 + lane) * 16;
  C.fo1 = OFF_ST0 + 8192 + (HB_OF(BIDX) * 4 + a) * 128 + (lane & 3) * 16;
  asm volatile("" : C1_OPS);
}
static_assert(L_XC - L_TA - 24576 == 57344 && C_XC == 8 && C_KH == 48 && C_P2 == 52 && C_U == 56 && C_P3 == 64 && C_G2 == 32 && L_MI % 8 == 0, "generated offsets");
__device__ __forceinline__ void a_conv1(rsrc_t wrs, int a, C1Pre& C, u32x4& fl, u32x4& wfl, u32& g2) {
  asm volatile("s_bitcmp1_b32 %[a], 0\n\ts_cbranch_scc1 .Lc_odd\n\t"
  "ds_read_b64 v[36:37], v16 offset:3776\n\tds_read_b64 v[38:39], v17 offset:3776\n\tds_read_b64 v[40:41], v16 offset:3264\n\tds_read_b64 v[42:43], v17 offset:3264\n\t"
  "ds_read_b64 v[44:45], v16 offset:2752\n\tds_read_b64 v[46:47], v17 offset:2752\n\tds_read_b64 v[48:49], v16 offset:2240\n\tds_read_b64 v[50:51], v17 offset:2240\n\t"
  "ds_read_b64 v[52:53], v16 offset:3712\n\tds_read_b64 v[54:55], v17 offset:3712\n\tds_read_b64 v[56:57], v16 offset:3200\n\tds_read_b64 v[58:59], v17 offset:3200\n\t"
  "ds_read_b64 v[60:61], v16 offset:2688\n\tds_read_b64 v[62:63], v17 offset:2688\n\tds_read_b64 v[64:65], v16 offset:2176\n\tds_read_b64 v[66:67], v17 offset:2176\n\t"
  "ds_read_b64 v[68:69], v16 offset:3648\n\tds_read_b64 v[70:71], v17 offset:3648\n\tds_read_b64 v[72:73], v16 offset:3136\n\tds_read_b64 v[74:75], v17 offset:3136\n\t"
  "ds_read_b64 v[76:77], v16 offset:2624\n\tds_read_b64 v[78:79], v17 offset:2624\n\tds_read_b64 v[80:81], v16 offset:2112\n\tds_read_b64 v[82:83], v17 offset:2112\n\t"
  "ds_read_b64 v[84:85], v16 offset:3584\n\tds_read_b64 v[86:87], v17 offset:3584\n\tds_read_b64 v[88:89], v16 offset:3072\n\tds_read_b64 v[90:91], v17 offset:3072\n\t"
  "ds_read_b64 v[92:93], v16 offset:2560\n\tds_read_b64 v[94:95], v17 offset:2560\n\tds_read_b64 v[96:97], v16 offset:2048\n\tds_read_b64 v[98:99], v17 offset:2048\n\t"
  "ds_read_b32 v26, v21\n\tv_and_b32 v27, -8, v21\n\tv_mov_b32 v100, 1\n\t"
  CST(6)
  "s_mov_b32 vcc_hi, 0x1000000\n\t.Lc0_u:\n\tds_read_b32 v101, v27 offset:56\n\ts_sub_u32 vcc_hi, vcc_hi, 1\n\ts_cselect_b32 vcc_lo, 0, 4\n\ts_waitcnt lgkmcnt(0)\n\tv_readfirstlane_b32 s61, v101\n\ts_nop 1\n\ts_cmp_lt_u32 s61, vcc_lo\n\ts_cbranch_scc1 .Lc0_u\n\t"
  CST(7)
  "ds_read_b128 v[102:105], v18 offset:192\n\tds_read_b128 v[106:109], v18 offset:704\n\tds_read_b128 v[110:113], v18 offset:1216\n\tds_read_b128 v[116:119], v18 offset:128\n\t"
  "ds_read_b128 v[120:123], v18 offset:640\n\tds_read_b128 v[124:127], v18 offset:1152\n\tds_read_b128 v[128:131], v18 offset:64\n\ts_waitcnt lgkmcnt(6)\n\t"
  "v_mfma_f32_16x16x32_f16 v[132:135], v[40:43], v[102:105], 0\n\tds_read_b128 v[136:139], v18 offset:576\n\tds_read_b128 v[140:143], v18 offset:1728\n\t"
  "v_mfma_f32_16x16x32_f16 v[144:147], v[44:47], v[102:105], 0\n\tds_read_b128 v[148:151], v18 offset:1088\n\ts_waitcnt lgkmcnt(8)\n\t"
  "v_mfma_f32_16x16x32_f16 v[132:135], v[36:39], v[106:109], v[132:135]\n\tds_read_b128 v[152:155], v18 offset:1664\n\tds_read_b128 v[156:159], v18 offset:1600\n\t"
  "v_mfma_f32_16x16x32_f16 v[144:147], v[40:43], v[106:109], v[144:147]\n\tds_read_b128 v[160:163], v18 offset:0\n\tds_read_b32 v101, v27 offset:48\n\ts_waitcnt lgkmcnt(7)\n\t"
  "v_mfma_f32_16x16x32_f16 v[144:147], v[36:39], v[110:113], v[144:147]\n\tds_read_b128 v[164:167], v18 offset:512\n\t"
  "v_mfma_f32_16x16x32_f16 v[132:135], v[56:59], v[116:119], v[132:135]\n\tds_read_b128 v[168:171], v18 offset:1024\n\t"
  "v_mfma_f32_16x16x32_f16 v[144:147], v[60:63], v[116:119], v[144:147]\n\tds_read_b128 v[172:175], v18 offset:1536\n\t"
  "v_mfma_f32_16x16x32_f16 v[132:135], v[52:55], v[120:123], v[132:135]\n\t"
  "v_mfma_f32_16x16x32_f16 v[144:147], v[56:59], v[120:123], v[144:147]\n\t"
  "v_mfma_f32_16x16x32_f16 v[144:147], v[52:55], v[124:127], v[144:147]\n\t"
  "v_mfma_f32_16x16x32_f16 v[132:135], v[72:75], v[128:131], v[132:135]\n\tds_read_b128 v[176:179], v19 offset:0\n\tds_read_b128 v[180:183], v19 offset:3072\n\t"
  "v_mfma_f32_16x16x32_f16 v[144:147], v[76:79], v[128:131], v[144:147]\n\ts_waitcnt lgkmcnt(5)\n\tv_readfirstlane_b32 s61, v101\n\t"
  "v_mfma_f32_16x16x32_f16 v[132:135], v[68:71], v[136:139], v[132:135]\n\t"
  "v_mfma_f32_16x16x32_f16 v[144:147], v[72:75], v[136:139], v[144:147]\n\ts_cmp_lt_u32 s61, 4\n\ts_cbranch_scc1 .Lc0_kh\n.Lc0_kh_b:\n\t"
  "v_mfma_f32_16x16x32_f16 v[144:147], v[68:71], v[148:151], v[144:147]\n\tds_read_b64 v[184:185], v16 offset:1728\n\tds_read_b64 v[186:187], v17 offset:1728\n\t"
  "v_mfma_f32_16x16x32_f16 v[132:135], v[88:91], v[160:163], v[132:135]\n\tds_read_b64 v[188:189], v16 offset:1216\n\tds_read_b64 v[190:191], v17 offset:1216\n\t"
  "v_mfma_f32_16x16x32_f16 v[144:147], v[92:95], v[160:163], v[144:147]\n\tds_read_b64 v[192:193], v16 offset:704\n\ts_waitcnt lgkmcnt(7)\n\t"
  "v_mfma_f32_16x16x32_f16 v[132:135], v[84:87], v[164:167], v[132:135]\n\tds_read_b64 v[194:195], v17 offset:704\n\tds_read_b64 v[196:197], v16 offset:192\n\tds_read_b64 v[198:199], v17 offset:192\n\t"
  "v_mfma_f32_16x16x32_f16 v[144:147], v[88:91], v[164:167], v[144:147]\n\ts_waitcnt lgkmcnt(8)\n\tv_perm_b32 v176, v176, v176, s60\n\t"
  "v_mfma_f32_16x16x32_f16 v[144:147], v[84:87], v[168:171], v[144:147]\n\tds_write_b128 v20, v[132:135] offset:0\n\tv_perm_b32 v177, v177, v177, s60\n\t"
  "v_mfma_f32_16x16x32_f16 v[200:203], v[36:39], v[102:105], 0\n\tv_perm_b32 v178, v178, v178, s60\n\tv_perm_b32 v179, v179, v179, s60\n\t"
  "v_mfma_f32_16x16x32_f16 v[132:135], v[48:51], v[102:105], 0\n\tds_write_b128 v20, v[144:147] offset:1024\n\tv_perm_b32 v180, v180, v180, s60\n\t"
  "v_mfma_f32_16x16x32_f16 v[132:135], v[44:47], v[106:109], v[132:135]\n\ts_mov_b64 exec, 1\n\tds_add_u32 v21, v100 offset:8\n\ts_mov_b64 exec, -1\n\t"
  "v_mfma_f32_16x16x32_f16 v[132:135], v[40:43], v[110:113], v[132:135]\n\tv_perm_b32 v181, v181, v181, s60\n\tv_perm_b32 v182, v182, v182, s60\n\t"
  "v_mfma_f32_16x16x32_f16 v[132:135], v[36:39], v[140:143], v[132:135]\n\tv_perm_b32 v183, v183, v183, s60\n\t"
  "v_mfma_f32_16x16x32_f16 v[200:203], v[52:55], v[116:119], v[200:203]\n\t"
  "v_mfma_f32_16x16x32_f16 v[132:135], v[64:67], v[116:119], v[132:135]\n\t"
  "v_mfma_f32_16x16x32_f16 v[132:135], v[60:63], v[120:123], v[132:135]\n\t"
  "v_mfma_f32_16x16x32_f16 v[132:135], v[56:59], v[124:127], v[132:135]\n\t"
  "v_mfma_f32_16x16x32_f16 v[132:135], v[52:55], v[152:155], v[132:135]\n\t"
  "v_mfma_f32_16x16x32_f16 v[200:203], v[68:71], v[128:131], v[200:203]\n\t"
  "v_mfma_f32_16x16x32_f16 v[132:135], v[80:83], v[128:131], v[132:135]\n\tds_read_b32 v101, v21 offset:8\n\tds_read_b128 v[144:147], v20 offset:2048\n\tds_read_b128 v[204:207], v20 offset:3072\n\t"
  "v_mfma_f32_16x16x32_f16 v[132:135], v[76:79], v[136:139], v[132:135]\n\t"
  "v_mfma_f32_16x16x32_f16 v[132:135], v[72:75], v[148:151], v[132:135]\n\t"
  "v_mfma_f32_16x16x32_f16 v[132:135], v[68:71], v[156:159], v[132:135]\n\t"
  "v_mfma_f32_16x16x32_f16 v[200:203], v[84:87], v[160:163], v[200:203]\n\t"
  "v_mfma_f32_16x16x32_f16 v[132:135], v[96:99], v[160:163], v[132:135]\n\t"
  "v_mfma_f32_16x16x32_f16 v[132:135], v[92:95], v[164:167], v[132:135]\n\t"
  "v_mfma_f32_16x16x32_f16 v[132:135], v[88:91], v[168:171], v[132:135]\n\t"
  "v_mfma_f32_16x16x32_f16 v[132:135], v[84:87], v[172:175], v[132:135]\n\ts_waitcnt lgkmcnt(0)\n\tv_readfirstlane_b32 s61, v101\n\t"
  "v_mfma_f32_16x16x32_f16 v[208:211], v[40:43], v[140:143], 0\n\t"
  "v_mfma_f32_16x16x32_f16 v[208:211], v[44:47], v[110:113], v[208:211]\n\ts_cmp_lt_u32 s61, 2\n\ts_cbranch_scc1 .Lc0_x0\n.Lc0_x0_b:\n\tv_fmac_f32 v176, v144, v26\n\tv_fmac_f32 v177, v145, v26\n\t"
  "v_fmac_f32 v178, v146, v26\n\t"
  "v_mfma_f32_16x16x32_f16 v[212:215], v[44:47], v[140:143], 0\n\tv_fmac_f32 v179, v147, v26\n\tv_fmac_f32 v180, v204, v26\n\tv_fmac_f32 v181, v205, v26\n\t"
  "v_fmac_f32 v182, v206, v26\n\tv_fmac_f32 v183, v207, v26\n\t"
  "v_mfma_f32_16x16x32_f16 v[208:211], v[48:51], v[106:109], v[208:211]\n\tv_fmac_f32 v176, v200, v26\n\tv_fmac_f32 v177, v201, v26\n\t"
  "v_mfma_f32_16x16x32_f16 v[212:215], v[48:51], v[110:113], v[212:215]\n\tv_fmac_f32 v178, v202, v26\n\tv_fmac_f32 v179, v203, v26\n\tv_cvt_pk_bf16_f32 v144, v176, v177\n\t"
  "v_cvt_pk_bf16_f32 v145, v178, v179\n\tv_fmac_f32 v180, v132, v26\n\t"
  "v_mfma_f32_16x16x32_f16 v[176:179], v[48:51], v[140:143], 0\n\tv_fmac_f32 v181, v133, v26\n\tv_fmac_f32 v182, v134, v26\n\tv_fmac_f32 v183, v135, v26\n\t"
  "v_cvt_pk_bf16_f32 v146, v180, v181\n\tv_cvt_pk_bf16_f32 v147, v182, v183\n\t"
  "v_mfma_f32_16x16x32_f16 v[208:211], v[184:187], v[102:105], v[208:211]\n\tv_permlane16_swap_b32 v144, v146\n\tv_permlane16_swap_b32 v145, v147\n\ts_setprio 3\n\tbuffer_store_dwordx4 v[144:147], v22, s[56:59], 0 offen sc1\n\ts_setprio 0\n\t"
  CST(8)
  "v_mfma_f32_16x16x32_f16 v[212:215], v[184:187], v[106:109], v[212:215]\n\tds_read_b64 v[132:133], v16 offset:1664\n\tds_read_b64 v[134:135], v17 offset:1664\n\t"
  "v_mfma_f32_16x16x32_f16 v[176:179], v[184:187], v[110:113], v[176:179]\n\tds_read_b64 v[144:145], v16 offset:1152\n\tds_read_b64 v[146:147], v17 offset:1152\n\t"
  "v_mfma_f32_16x16x32_f16 v[180:183], v[184:187], v[140:143], 0\n\tds_read_b64 v[140:141], v16 offset:640\n\tds_read_b64 v[142:143], v17 offset:640\n\t"
  "v_mfma_f32_16x16x32_f16 v[212:215], v[188:191], v[102:105], v[212:215]\n\t"
  "v_mfma_f32_16x16x32_f16 v[176:179], v[188:191], v[106:109], v[176:179]\n\tds_read_b64 v[184:185], v16 offset:128\n\tds_read_b64 v[186:187], v17 offset:128\n\t"
  "v_mfma_f32_16x16x32_f16 v[180:183], v[188:191], v[110:113], v[180:183]\n\t"
  "v_mfma_f32_16x16x32_f16 v[176:179], v[192:195], v[102:105], v[176:179]\n\t"
  "v_mfma_f32_16x16x32_f16 v[180:183], v[192:195], v[106:109], v[180:183]\n\t"
  "v_mfma_f32_16x16x32_f16 v[180:183], v[196:199], v[102:105], v[180:183]\n\t"
  "v_mfma_f32_16x16x32_f16 v[208:211], v[56:59], v[152:155], v[208:211]\n\t"
  "v_mfma_f32_16x16x32_f16 v[208:211], v[60:63], v[124:127], v[208:211]\n\t"
  "v_mfma_f32_16x16x32_f16 v[212:215], v[60:63], v[152:155], v[212:215]\n\tds_read_b64 v[102:103], v16 offset:1600\n\tds_read_b64 v[104:105], v17 offset:1600\n\t"
  "v_mfma_f32_16x16x32_f16 v[208:211], v[64:67], v[120:123], v[208:211]\n\t"
  "v_mfma_f32_16x16x32_f16 v[212:215], v[64:67], v[124:127], v[212:215]\n\t"
  "v_mfma_f32_16x16x32_f16 v[176:179], v[64:67], v[152:155], v[176:179]\n\ts_waitcnt lgkmcnt(2)\n\t"
  "v_mfma_f32_16x16x32_f16 v[208:211], v[132:135], v[116:119], v[208:211]\n\tds_read_b64 v[108:109], v16 offset:1088\n\tds_read_b64 v[110:111], v17 offset:1088\n\t"
  "v_mfma_f32_16x16x32_f16 v[212:215], v[132:135], v[120:123], v[212:215]\n\t"
  "v_mfma_f32_16x16x32_f16 v[176:179], v[132:135], v[124:127], v[176:179]\n\t"
  "v_mfma_f32_16x16x32_f16 v[180:183], v[132:135], v[152:155], v[180:183]\n\tds_read_b64 v[112:113], v16 offset:576\n\tds_read_b64 v[114:115], v17 offset:576\n\t"
  "v_mfma_f32_16x16x32_f16 v[212:215], v[144:147], v[116:119], v[212:215]\n\t"
  "v_mfma_f32_16x16x32_f16 v[176:179], v[144:147], v[120:123], v[176:179]\n\tds_read_b64 v[132:133], v16 offset:64\n\tds_read_b64 v[134:135], v17 offset:64\n\t"
  "v_mfma_f32_16x16x32_f16 v[180:183], v[144:147], v[124:127], v[180:183]\n\t"
  "v_mfma_f32_16x16x32_f16 v[176:179], v[140:143], v[116:119], v[176:179]\n\t"
  "v_mfma_f32_16x16x32_f16 v[180:183], v[140:143], v[120:123], v[180:183]\n\tv_add_u32 v22, 0x101000, v22\n\t"
  "v_mfma_f32_16x16x32_f16 v[180:183], v[184:187], v[116:119], v[180:183]\n\t"
  "v_mfma_f32_16x16x32_f16 v[208:211], v[72:75], v[156:159], v[208:211]\n\t"
  "v_mfma_f32_16x16x32_f16 v[208:211], v[76:79], v[148:151], v[208:211]\n\t"
  "v_mfma_f32_16x16x32_f16 v[212:215], v[76:79], v[156:159], v[212:215]\n\tds_read_b64 v[116:117], v16 offset:1536\n\tds_read_b64 v[118:119], v17 offset:1536\n\t"
  "v_mfma_f32_16x16x32_f16 v[208:211], v[80:83], v[136:139], v[208:211]\n\t"
  "v_mfma_f32_16x16x32_f16 v[212:215], v[80:83], v[148:151], v[212:215]\n\t"
  "v_mfma_f32_16x16x32_f16 v[176:179], v[80:83], v[156:159], v[176:179]\n\ts_waitcnt lgkmcnt(2)\n\t"
  "v_mfma_f32_16x16x32_f16 v[208:211], v[102:105], v[128:131], v[208:211]\n\tds_read_b32 v101, v27 offset:52\n\tds_read_b64 v[120:121], v16 offset:1024\n\t"
  "v_mfma_f32_16x16x32_f16 v[212:215], v[102:105], v[136:139], v[212:215]\n\tds_read_b64 v[122:123], v17 offset:1024\n\t"
  "v_mfma_f32_16x16x32_f16 v[176:179], v[102:105], v[148:151], v[176:179]\n\t"
  "v_mfma_f32_16x16x32_f16 v[180:183], v[102:105], v[156:159], v[180:183]\n\tds_read_b64 v[102:103], v16 offset:512\n\tds_read_b64 v[104:105], v17 offset:512\n\t"
  "v_mfma_f32_16x16x32_f16 v[212:215], v[108:111], v[128:131], v[212:215]\n\t"
  "v_mfma_f32_16x16x32_f16 v[176:179], v[108:111], v[136:139], v[176:179]\n\tds_read_b64 v[124:125], v16 offset:0\n\tds_read_b64 v[126:127], v17 offset:0\n\t"
  "v_mfma_f32_16x16x32_f16 v[180:183], v[108:111], v[148:151], v[180:183]\n\t"
  "v_mfma_f32_16x16x32_f16 v[176:179], v[112:115], v[128:131], v[176:179]\n\t"
  "v_mfma_f32_16x16x32_f16 v[180:183], v[112:115], v[136:139], v[180:183]\n\ts_waitcnt lgkmcnt(5)\n\tv_readfirstlane_b32 s61, v101\n\t"
  "v_mfma_f32_16x16x32_f16 v[180:183], v[132:135], v[128:131], v[180:183]\n\t"
  "v_mfma_f32_16x16x32_f16 v[208:211], v[88:91], v[172:175], v[208:211]\n\ts_cmp_lt_u32 s61, 4\n\ts_cbranch_scc1 .Lc0_ub\n.Lc0_ub_b:\n\t"
  "v_mfma_f32_16x16x32_f16 v[208:211], v[92:95], v[168:171], v[208:211]\n\tds_read_b128 v[108:111], v18 offset:2240\n\tds_read_b128 v[112:115], v18 offset:2752\n\t"
  "v_mfma_f32_16x16x32_f16 v[212:215], v[92:95], v[172:175], v[212:215]\n\tds_read_b128 v[128:131], v18 offset:3264\n\tds_read_b128 v[132:135], v18 offset:3776\n\t"
  "v_mfma_f32_16x16x32_f16 v[208:211], v[96:99], v[164:167], v[208:211]\n\t"
  "v_mfma_f32_16x16x32_f16 v[212:215], v[96:99], v[168:171], v[212:215]\n\t"
  "v_mfma_f32_16x16x32_f16 v[176:179], v[96:99], v[172:175], v[176:179]\n\tds_read_b128 v[136:139], v18 offset:2176\n\tds_read_b128 v[140:143], v18 offset:2688\n\t"
  "v_mfma_f32_16x16x32_f16 v[208:211], v[116:119], v[160:163], v[208:211]\n\tds_read_b128 v[144:147], v18 offset:3200\n\tds_read_b128 v[148:151], v18 offset:3712\n\t"
  "v_mfma_f32_16x16x32_f16 v[212:215], v[116:119], v[164:167], v[212:215]\n\t"
  "v_mfma_f32_16x16x32_f16 v[176:179], v[116:119], v[168:171], v[176:179]\n\t"
  "v_mfma_f32_16x16x32_f16 v[180:183], v[116:119], v[172:175], v[180:183]\n\ts_waitcnt lgkmcnt(6)\n\t"
  "v_mfma_f32_16x16x32_f16 v[212:215], v[120:123], v[160:163], v[212:215]\n\tds_read_b128 v[116:119], v18 offset:2112\n\tds_read_b128 v[152:155], v18 offset:2624\n\t"
  "v_mfma_f32_16x16x32_f16 v[176:179], v[120:123], v[164:167], v[176:179]\n\tds_read_b128 v[156:159], v18 offset:3136\n\tds_read_b128 v[172:175], v18 offset:3648\n\t"
  "v_mfma_f32_16x16x32_f16 v[180:183], v[120:123], v[168:171], v[180:183]\n\t"
  "v_mfma_f32_16x16x32_f16 v[176:179], v[102:105], v[160:163], v[176:179]\n\t"
  "v_mfma_f32_16x16x32_f16 v[180:183], v[102:105], v[164:167], v[180:183]\n\t"
  "v_mfma_f32_16x16x32_f16 v[180:183], v[124:127], v[160:163], v[180:183]\n\tds_read_b128 v[104:107], v18 offset:2048\n\tds_read_b128 v[120:123], v18 offset:2560\n\t"
  "v_mfma_f32_16x16x32_f16 v[212:215], v[40:43], v[108:111], v[212:215]\n\tds_read_b128 v[124:127], v18 offset:3072\n\tds_read_b128 v[160:163], v18 offset:3584\n\t"
  "v_mfma_f32_16x16x32_f16 v[176:179], v[44:47], v[108:111], v[176:179]\n\t"
  "v_mfma_f32_16x16x32_f16 v[212:215], v[36:39], v[112:115], v[212:215]\n\t"
  "v_mfma_f32_16x16x32_f16 v[176:179], v[40:43], v[112:115], v[176:179]\n\ts_waitcnt lgkmcnt(4)\n\t"
  "v_mfma_f32_16x16x32_f16 v[176:179], v[36:39], v[128:131], v[176:179]\n\t"
  "v_mfma_f32_16x16x32_f16 v[212:215], v[56:59], v[136:139], v[212:215]\n\t"
  "v_mfma_f32_16x16x32_f16 v[176:179], v[60:63], v[136:139], v[176:179]\n\t"
  "v_mfma_f32_16x16x32_f16 v[212:215], v[52:55], v[140:143], v[212:215]\n\t"
  "v_mfma_f32_16x16x32_f16 v[176:179], v[56:59], v[140:143], v[176:179]\n\t"
  "v_mfma_f32_16x16x32_f16 v[176:179], v[52:55], v[144:147], v[176:179]\n\t"
  "v_mfma_f32_16x16x32_f16 v[212:215], v[72:75], v[116:119], v[212:215]\n\tds_read_b128 v[164:167], v19 offset:4096\n\tds_read_b128 v[168:171], v19 offset:7168\n\t"
  "v_mfma_f32_16x16x32_f16 v[176:179], v[76:79], v[116:119], v[176:179]\n\t"
  "v_mfma_f32_16x16x32_f16 v[212:215], v[68:71], v[152:155], v[212:215]\n\t"
  "v_mfma_f32_16x16x32_f16 v[176:179], v[72:75], v[152:155], v[176:179]\n\t"
  "v_mfma_f32_16x16x32_f16 v[176:179], v[68:71], v[156:159], v[176:179]\n\ts_waitcnt lgkmcnt(2)\n\t"
  "v_mfma_f32_16x16x32_f16 v[212:215], v[88:91], v[104:107], v[212:215]\n\t"
  "v_mfma_f32_16x16x32_f16 v[176:179], v[92:95], v[104:107], v[176:179]\n\t"
  "v_mfma_f32_16x16x32_f16 v[212:215], v[84:87], v[120:123], v[212:215]\n\t"
  "v_mfma_f32_16x16x32_f16 v[176:179], v[88:91], v[120:123], v[176:179]\n\tds_read_b32 v101, v27 offset:64\n\ts_waitcnt lgkmcnt(1)\n\t"
  "v_mfma_f32_16x16x32_f16 v[176:179], v[84:87], v[124:127], v[176:179]\n\tds_write_b128 v20, v[212:215] offset:57344\n\tv_perm_b32 v164, v164, v164, s60\n\t"
  "v_mfma_f32_16x16x32_f16 v[208:211], v[36:39], v[108:111], v[208:211]\n\ts_setprio 3\n\tbuffer_load_dwordx4 v[28:31], v23, s[56:59], 0 offen sc1\n\tbuffer_load_dwordx4 v[32:35], v24, s[56:59], 0 offen sc1\n\ts_setprio 0\n\t"
  "v_mfma_f32_16x16x32_f16 v[180:183], v[48:51], v[108:111], v[180:183]\n\tds_write_b128 v20, v[176:179] offset:58368\n\tv_perm_b32 v165, v165, v165, s60\n\t"
  "v_mfma_f32_16x16x32_f16 v[180:183], v[44:47], v[112:115], v[180:183]\n\ts_mov_b64 exec, 1\n\tds_add_u32 v27, v100 offset:16\n\ts_mov_b64 exec, -1\n\t"
  "v_mfma_f32_16x16x32_f16 v[180:183], v[40:43], v[128:131], v[180:183]\n\tv_perm_b32 v166, v166, v166, s60\n\tv_perm_b32 v167, v167, v167, s60\n\t"
  "v_mfma_f32_16x16x32_f16 v[180:183], v[36:39], v[132:135], v[180:183]\n\tv_perm_b32 v168, v168, v168, s60\n\tv_perm_b32 v169, v169, v169, s60\n\t"
  "v_mfma_f32_16x16x32_f16 v[208:211], v[52:55], v[136:139], v[208:211]\n\tv_perm_b32 v170, v170, v170, s60\n\tv_perm_b32 v171, v171, v171, s60\n\t"
  "v_mfma_f32_16x16x32_f16 v[180:183], v[64:67], v[136:139], v[180:183]\n\ts_waitcnt lgkmcnt(3)\n\tv_readfirstlane_b32 s61, v101\n\t"
  "v_mfma_f32_16x16x32_f16 v[180:183], v[60:63], v[140:143], v[180:183]\n\t"
  "v_mfma_f32_16x16x32_f16 v[180:183], v[56:59], v[144:147], v[180:183]\n\ts_cmp_lt_u32 s61, 4\n\ts_cbranch_scc1 .Lc0_p3\n.Lc0_p3_b:\n\t"
  "v_mfma_f32_16x16x32_f16 v[180:183], v[52:55], v[148:151], v[180:183]\n\tds_read_b32 v36, v27 offset:16\n\tds_read_b128 v[40:43], v20 offset:59392\n\tds_read_b128 v[44:47], v20 offset:60416\n\t"
  "v_mfma_f32_16x16x32_f16 v[208:211], v[68:71], v[116:119], v[208:211]\n\t"
  "v_mfma_f32_16x16x32_f16 v[180:183], v[80:83], v[116:119], v[180:183]\n\t"
  "v_mfma_f32_16x16x32_f16 v[180:183], v[76:79], v[152:155], v[180:183]\n\t"
  "v_mfma_f32_16x16x32_f16 v[180:183], v[72:75], v[156:159], v[180:183]\n\t"
  "v_mfma_f32_16x16x32_f16 v[180:183], v[68:71], v[172:175], v[180:183]\n\t"
  "v_mfma_f32_16x16x32_f16 v[208:211], v[84:87], v[104:107], v[208:211]\n\t"
  "v_mfma_f32_16x16x32_f16 v[180:183], v[96:99], v[104:107], v[180:183]\n\t"
  "v_mfma_f32_16x16x32_f16 v[180:183], v[92:95], v[120:123], v[180:183]\n\ts_waitcnt lgkmcnt(0)\n\tv_readfirstlane_b32 s61, v36\n\tds_read_b32 v25, v27 offset:32\n\t"
  "v_mfma_f32_16x16x32_f16 v[180:183], v[88:91], v[124:127], v[180:183]\n\ts_cmp_lt_u32 s61, 4\n\ts_cbranch_scc1 .Lc0_x4\n.Lc0_x4_b:\n\t"
  "v_mfma_f32_16x16x32_f16 v[180:183], v[84:87], v[160:163], v[180:183]\n\tv_fmac_f32 v164, v40, v26\n\tv_fmac_f32 v165, v41, v26\n\tv_fmac_f32 v166, v42, v26\n\t"
  "v_fmac_f32 v167, v43, v26\n\tv_fmac_f32 v168, v44, v26\n\tv_fmac_f32 v169, v45, v26\n\tv_fmac_f32 v170, v46, v26\n\t"
  "v_fmac_f32 v171, v47, v26\n\tv_fmac_f32 v164, v208, v26\n\tv_fmac_f32 v165, v209, v26\n\tv_fmac_f32 v166, v210, v26\n\t"
  "v_fmac_f32 v167, v211, v26\n\tv_cvt_pk_bf16_f32 v36, v164, v165\n\tv_cvt_pk_bf16_f32 v37, v166, v167\n\tv_fmac_f32 v168, v180, v26\n\t"
  "v_fmac_f32 v169, v181, v26\n\tv_fmac_f32 v170, v182, v26\n\tv_fmac_f32 v171, v183, v26\n\tv_cvt_pk_bf16_f32 v38, v168, v169\n\t"
  "v_cvt_pk_bf16_f32 v39, v170, v171\n\ts_nop 0\n\tv_permlane16_swap_b32 v36, v38\n\tv_permlane16_swap_b32 v37, v39\n\t"
  "s_setprio 3\n\tbuffer_store_dwordx4 v[36:39], v22, s[56:59], 0 offen sc1\n\ts_setprio 0\n\ts_waitcnt lgkmcnt(0)\n\ts_nop 0\n\t"
  "s_branch .Lc_end\n.Lc_odd:\n\t"
  "ds_read_b64 v[36:37], v16 offset:3776\n\tds_read_b64 v[38:39], v17 offset:3776\n\tds_read_b64 v[40:41], v16 offset:3264\n\tds_read_b64 v[42:43], v17 offset:3264\n\t"
  "ds_read_b64 v[44:45], v16 offset:2752\n\tds_read_b64 v[46:47], v17 offset:2752\n\tds_read_b64 v[48:49], v16 offset:2240\n\tds_read_b64 v[50:51], v17 offset:2240\n\t"
  "ds_read_b64 v[52:53], v16 offset:3712\n\tds_read_b64 v[54:55], v17 offset:3712\n\tds_read_b64 v[56:57], v16 offset:3200\n\tds_read_b64 v[58:59], v17 offset:3200\n\t"
  "ds_read_b64 v[60:61], v16 offset:2688\n\tds_read_b64 v[62:63], v17 offset:2688\n\tds_read_b64 v[64:65], v16 offset:2176\n\tds_read_b64 v[66:67], v17 offset:2176\n\t"
  "ds_read_b64 v[68:69], v16 offset:3648\n\tds_read_b64 v[70:71], v17 offset:3648\n\tds_read_b64 v[72:73], v16 offset:3136\n\tds_read_b64 v[74:75], v17 offset:3136\n\t"
  "ds_read_b64 v[76:77], v16 offset:2624\n\tds_read_b64 v[78:79], v17 offset:2624\n\tds_read_b64 v[80:81], v16 offset:2112\n\tds_read_b64 v[82:83], v17 offset:2112\n\t"
  "ds_read_b64 v[84:85], v16 offset:3584\n\tds_read_b64 v[86:87], v17 offset:3584\n\tds_read_b64 v[88:89], v16 offset:3072\n\tds_read_b64 v[90:91], v17 offset:3072\n\t"
  "ds_read_b64 v[92:93], v16 offset:2560\n\tds_read_b64 v[94:95], v17 offset:2560\n\tds_read_b64 v[96:97], v16 offset:2048\n\tds_read_b64 v[98:99], v17 offset:2048\n\t"
  "ds_read_b32 v26, v21\n\tv_and_b32 v27, -8, v21\n\tv_mov_b32 v100, 1\n\t"
  CST(6)
  "s_mov_b32 vcc_hi, 0x1000000\n\t.Lc1_u:\n\tds_read_b32 v101, v27 offset:56\n\ts_sub_u32 vcc_hi, vcc_hi, 1\n\ts_cselect_b32 vcc_lo, 0, 4\n\ts_waitcnt lgkmcnt(0)\n\tv_readfirstlane_b32 s61, v101\n\ts_nop 1\n\ts_cmp_lt_u32 s61, vcc_lo\n\ts_cbranch_scc1 .Lc1_u\n\t"
  CST(7)
  "ds_read_b128 v[102:105], v18 offset:192\n\tds_read_b128 v[106:109], v18 offset:704\n\tds_read_b128 v[110:113], v18 offset:1216\n\tds_read_b128 v[114:117], v18 offset:1728\n\t"
  "ds_read_b128 v[120:123], v18 offset:128\n\tds_read_b128 v[124:127], v18 offset:640\n\tds_read_b128 v[128:131], v18 offset:1152\n\tds_read_b128 v[132:135], v18 offset:1664\n\t"
  "ds_read_b128 v[136:139], v18 offset:64\n\ts_waitcnt lgkmcnt(8)\n\t"
  "v_mfma_f32_16x16x32_f16 v[140:143], v[36:39], v[102:105], 0\n\tds_read_b128 v[144:147], v18 offset:576\n\t"
  "v_mfma_f32_16x16x32_f16 v[148:151], v[48:51], v[102:105], 0\n\tds_read_b128 v[152:155], v18 offset:1088\n\ts_waitcnt lgkmcnt(9)\n\t"
  "v_mfma_f32_16x16x32_f16 v[148:151], v[44:47], v[106:109], v[148:151]\n\tds_read_b128 v[156:159], v18 offset:1600\n\ts_waitcnt lgkmcnt(3)\n\t"
  "v_mfma_f32_16x16x32_f16 v[148:151], v[40:43], v[110:113], v[148:151]\n\tds_read_b128 v[160:163], v18 offset:0\n\tds_read_b32 v101, v27 offset:48\n\t"
  "v_mfma_f32_16x16x32_f16 v[148:151], v[36:39], v[114:117], v[148:151]\n\tds_read_b128 v[164:167], v18 offset:512\n\t"
  "v_mfma_f32_16x16x32_f16 v[140:143], v[52:55], v[120:123], v[140:143]\n\tds_read_b128 v[168:171], v18 offset:1024\n\t"
  "v_mfma_f32_16x16x32_f16 v[148:151], v[64:67], v[120:123], v[148:151]\n\tds_read_b128 v[172:175], v18 offset:1536\n\t"
  "v_mfma_f32_16x16x32_f16 v[148:151], v[60:63], v[124:127], v[148:151]\n\t"
  "v_mfma_f32_16x16x32_f16 v[148:151], v[56:59], v[128:131], v[148:151]\n\t"
  "v_mfma_f32_16x16x32_f16 v[148:151], v[52:55], v[132:135], v[148:151]\n\t"
  "v_mfma_f32_16x16x32_f16 v[140:143], v[68:71], v[136:139], v[140:143]\n\tds_read_b128 v[176:179], v19 offset:1024\n\tds_read_b128 v[180:183], v19 offset:2048\n\t"
  "v_mfma_f32_16x16x32_f16 v[148:151], v[80:83], v[136:139], v[148:151]\n\ts_waitcnt lgkmcnt(5)\n\tv_readfirstlane_b32 s61, v101\n\t"
  "v_mfma_f32_16x16x32_f16 v[148:151], v[76:79], v[144:147], v[148:151]\n\t"
  "v_mfma_f32_16x16x32_f16 v[148:151], v[72:75], v[152:155], v[148:151]\n\ts_cmp_lt_u32 s61, 4\n\ts_cbranch_scc1 .Lc1_kh\n.Lc1_kh_b:\n\t"
  "v_mfma_f32_16x16x32_f16 v[148:151], v[68:71], v[156:159], v[148:151]\n\tds_read_b64 v[184:185], v16 offset:1728\n\tds_read_b64 v[186:187], v17 offset:1728\n\t"
  "v_mfma_f32_16x16x32_f16 v[140:143], v[84:87], v[160:163], v[140:143]\n\tds_read_b64 v[188:189], v16 offset:1216\n\tds_read_b64 v[190:191], v17 offset:1216\n\t"
  "v_mfma_f32_16x16x32_f16 v[148:151], v[96:99], v[160:163], v[148:151]\n\tds_read_b64 v[192:193], v16 offset:704\n\ts_waitcnt lgkmcnt(7)\n\t"
  "v_mfma_f32_16x16x32_f16 v[148:151], v[92:95], v[164:167], v[148:151]\n\tds_read_b64 v[194:195], v17 offset:704\n\tds_read_b64 v[196:197], v16 offset:192\n\tds_read_b64 v[198:199], v17 offset:192\n\t"
  "v_mfma_f32_16x16x32_f16 v[148:151], v[88:91], v[168:171], v[148:151]\n\ts_waitcnt lgkmcnt(8)\n\tv_perm_b32 v176, v176, v176, s60\n\t"
  "v_mfma_f32_16x16x32_f16 v[148:151], v[84:87], v[172:175], v[148:151]\n\tds_write_b128 v20, v[140:143] offset:2048\n\tv_perm_b32 v177, v177, v177, s60\n\t"
  "v_mfma_f32_16x16x32_f16 v[200:203], v[40:43], v[102:105], 0\n\tv_perm_b32 v178, v178, v178, s60\n\tv_perm_b32 v179, v179, v179, s60\n\t"
  "v_mfma_f32_16x16x32_f16 v[140:143], v[44:47], v[102:105], 0\n\tds_write_b128 v20, v[148:151] offset:3072\n\tv_perm_b32 v180, v180, v180, s60\n\t"
  "v_mfma_f32_16x16x32_f16 v[200:203], v[36:39], v[106:109], v[200:203]\n\ts_mov_b64 exec, 1\n\tds_add_u32 v21, v100 offset:8\n\ts_mov_b64 exec, -1\n\t"
  "v_mfma_f32_16x16x32_f16 v[140:143], v[40:43], v[106:109], v[140:143]\n\tv_perm_b32 v181, v181, v181, s60\n\tv_perm_b32 v182, v182, v182, s60\n\t"
  "v_mfma_f32_16x16x32_f16 v[140:143], v[36:39], v[110:113], v[140:143]\n\tv_perm_b32 v183, v183, v183, s60\n\t"
  "v_mfma_f32_16x16x32_f16 v[200:203], v[56:59], v[120:123], v[200:203]\n\t"
  "v_mfma_f32_16x16x32_f16 v[140:143], v[60:63], v[120:123], v[140:143]\n\t"
  "v_mfma_f32_16x16x32_f16 v[200:203], v[52:55], v[124:127], v[200:203]\n\t"
  "v_mfma_f32_16x16x32_f16 v[140:143], v[56:59], v[124:127], v[140:143]\n\t"
  "v_mfma_f32_16x16x32_f16 v[140:143], v[52:55], v[128:131], v[140:143]\n\t"
  "v_mfma_f32_16x16x32_f16 v[200:203], v[72:75], v[136:139], v[200:203]\n\t"
  "v_mfma_f32_16x16x32_f16 v[140:143], v[76:79], v[136:139], v[140:143]\n\tds_read_b32 v101, v21 offset:8\n\tds_read_b128 v[148:151], v20 offset:0\n\tds_read_b128 v[204:207], v20 offset:1024\n\t"
  "v_mfma_f32_16x16x32_f16 v[200:203], v[68:71], v[144:147], v[200:203]\n\t"
  "v_mfma_f32_16x16x32_f16 v[140:143], v[72:75], v[144:147], v[140:143]\n\t"
  "v_mfma_f32_16x16x32_f16 v[140:143], v[68:71], v[152:155], v[140:143]\n\t"
  "v_mfma_f32_16x16x32_f16 v[200:203], v[88:91], v[160:163], v[200:203]\n\t"
  "v_mfma_f32_16x16x32_f16 v[140:143], v[92:95], v[160:163], v[140:143]\n\t"
  "v_mfma_f32_16x16x32_f16 v[200:203], v[84:87], v[164:167], v[200:203]\n\t"
  "v_mfma_f32_16x16x32_f16 v[140:143], v[88:91], v[164:167], v[140:143]\n\t"
  "v_mfma_f32_16x16x32_f16 v[140:143], v[84:87], v[168:171], v[140:143]\n\ts_waitcnt lgkmcnt(0)\n\tv_readfirstlane_b32 s61, v101\n\t"
  "v_mfma_f32_16x16x32_f16 v[208:211], v[40:43], v[114:117], 0\n\t"
  "v_mfma_f32_16x16x32_f16 v[208:211], v[44:47], v[110:113], v[208:211]\n\ts_cmp_lt_u32 s61, 2\n\ts_cbranch_scc1 .Lc1_x0\n.Lc1_x0_b:\n\tv_fmac_f32 v176, v148, v26\n\tv_fmac_f32 v177, v149, v26\n\t"
  "v_fmac_f32 v178, v150, v26\n\t"
  "v_mfma_f32_16x16x32_f16 v[212:215], v[44:47], v[114:117], 0\n\tv_fmac_f32 v179, v151, v26\n\tv_fmac_f32 v180, v204, v26\n\tv_fmac_f32 v181, v205, v26\n\t"
  "v_fmac_f32 v182, v206, v26\n\tv_fmac_f32 v183, v207, v26\n\t"
  "v_mfma_f32_16x16x32_f16 v[208:211], v[48:51], v[106:109], v[208:211]\n\tv_fmac_f32 v176, v200, v26\n\tv_fmac_f32 v177, v201, v26\n\t"
  "v_mfma_f32_16x16x32_f16 v[212:215], v[48:51], v[110:113], v[212:215]\n\tv_fmac_f32 v178, v202, v26\n\tv_fmac_f32 v179, v203, v26\n\tv_cvt_pk_bf16_f32 v148, v176, v177\n\t"
  "v_cvt_pk_bf16_f32 v149, v178, v179\n\tv_fmac_f32 v180, v140, v26\n\t"
  "v_mfma_f32_16x16x32_f16 v[176:179], v[48:51], v[114:117], 0\n\tv_fmac_f32 v181, v141, v26\n\tv_fmac_f32 v182, v142, v26\n\tv_fmac_f32 v183, v143, v26\n\t"
  "v_cvt_pk_bf16_f32 v150, v180, v181\n\tv_cvt_pk_bf16_f32 v151, v182, v183\n\t"
  "v_mfma_f32_16x16x32_f16 v[208:211], v[184:187], v[102:105], v[208:211]\n\tv_permlane16_swap_b32 v148, v150\n\tv_permlane16_swap_b32 v149, v151\n\ts_setprio 3\n\tbuffer_store_dwordx4 v[148:151], v22, s[56:59], 0 offen sc1\n\ts_setprio 0\n\t"
  CST(8)
  "v_mfma_f32_16x16x32_f16 v[212:215], v[184:187], v[106:109], v[212:215]\n\tds_read_b64 v[140:141], v16 offset:1664\n\tds_read_b64 v[142:143], v17 offset:1664\n\t"
  "v_mfma_f32_16x16x32_f16 v[176:179], v[184:187], v[110:113], v[176:179]\n\tds_read_b64 v[148:149], v16 offset:1152\n\tds_read_b64 v[150:151], v17 offset:1152\n\t"
  "v_mfma_f32_16x16x32_f16 v[180:183], v[184:187], v[114:117], 0\n\tds_read_b64 v[116:117], v16 offset:640\n\tds_read_b64 v[118:119], v17 offset:640\n\t"
  "v_mfma_f32_16x16x32_f16 v[212:215], v[188:191], v[102:105], v[212:215]\n\t"
  "v_mfma_f32_16x16x32_f16 v[176:179], v[188:191], v[106:109], v[176:179]\n\tds_read_b64 v[184:185], v16 offset:128\n\tds_read_b64 v[186:187], v17 offset:128\n\t"
  "v_mfma_f32_16x16x32_f16 v[180:183], v[188:191], v[110:113], v[180:183]\n\t"
  "v_mfma_f32_16x16x32_f16 v[176:179], v[192:195], v[102:105], v[176:179]\n\t"
  "v_mfma_f32_16x16x32_f16 v[180:183], v[192:195], v[106:109], v[180:183]\n\t"
  "v_mfma_f32_16x16x32_f16 v[180:183], v[196:199], v[102:105], v[180:183]\n\t"
  "v_mfma_f32_16x16x32_f16 v[208:211], v[56:59], v[132:135], v[208:211]\n\t"
  "v_mfma_f32_16x16x32_f16 v[208:211], v[60:63], v[128:131], v[208:211]\n\t"
  "v_mfma_f32_16x16x32_f16 v[212:215], v[60:63], v[132:135], v[212:215]\n\tds_read_b64 v[102:103], v16 offset:1600\n\tds_read_b64 v[104:105], v17 offset:1600\n\t"
  "v_mfma_f32_16x16x32_f16 v[208:211], v[64:67], v[124:127], v[208:211]\n\t"
  "v_mfma_f32_16x16x32_f16 v[212:215], v[64:67], v[128:131], v[212:215]\n\t"
  "v_mfma_f32_16x16x32_f16 v[176:179], v[64:67], v[132:135], v[176:179]\n\ts_waitcnt lgkmcnt(2)\n\t"
  "v_mfma_f32_16x16x32_f16 v[208:211], v[140:143], v[120:123], v[208:211]\n\tds_read_b64 v[108:109], v16 offset:1088\n\tds_read_b64 v[110:111], v17 offset:1088\n\t"
  "v_mfma_f32_16x16x32_f16 v[212:215], v[140:143], v[124:127], v[212:215]\n\t"
  "v_mfma_f32_16x16x32_f16 v[176:179], v[140:143], v[128:131], v[176:179]\n\t"
  "v_mfma_f32_16x16x32_f16 v[180:183], v[140:143], v[132:135], v[180:183]\n\tds_read_b64 v[112:113], v16 offset:576\n\tds_read_b64 v[114:115], v17 offset:576\n\t"
  "v_mfma_f32_16x16x32_f16 v[212:215], v[148:151], v[120:123], v[212:215]\n\t"
  "v_mfma_f32_16x16x32_f16 v[176:179], v[148:151], v[124:127], v[176:179]\n\tds_read_b64 v[132:133], v16 offset:64\n\tds_read_b64 v[134:135], v17 offset:64\n\t"
  "v_mfma_f32_16x16x32_f16 v[180:183], v[148:151], v[128:131], v[180:183]\n\t"
  "v_mfma_f32_16x16x32_f16 v[176:179], v[116:119], v[120:123], v[176:179]\n\t"
  "v_mfma_f32_16x16x32_f16 v[180:183], v[116:119], v[124:127], v[180:183]\n\tv_add_u32 v22, 0x101000, v22\n\t"
  "v_mfma_f32_16x16x32_f16 v[180:183], v[184:187], v[120:123], v[180:183]\n\t"
  "v_mfma_f32_16x16x32_f16 v[208:211], v[72:75], v[156:159], v[208:211]\n\t"
  "v_mfma_f32_16x16x32_f16 v[208:211], v[76:79], v[152:155], v[208:211]\n\t"
  "v_mfma_f32_16x16x32_f16 v[212:215], v[76:79], v[156:159], v[212:215]\n\tds_read_b64 v[116:117], v16 offset:1536\n\tds_read_b64 v[118:119], v17 offset:1536\n\t"
  "v_mfma_f32_16x16x32_f16 v[208:211], v[80:83], v[144:147], v[208:211]\n\t"
  "v_mfma_f32_16x16x32_f16 v[212:215], v[80:83], v[152:155], v[212:215]\n\t"
  "v_mfma_f32_16x16x32_f16 v[176:179], v[80:83], v[156:159], v[176:179]\n\ts_waitcnt lgkmcnt(2)\n\t"
  "v_mfma_f32_16x16x32_f16 v[208:211], v[102:105], v[136:139], v[208:211]\n\tds_read_b32 v101, v27 offset:52\n\tds_read_b64 v[120:121], v16 offset:1024\n\t"
  "v_mfma_f32_16x16x32_f16 v[212:215], v[102:105], v[144:147], v[212:215]\n\tds_read_b64 v[122:123], v17 offset:1024\n\t"
  "v_mfma_f32_16x16x32_f16 v[176:179], v[102:105], v[152:155], v[176:179]\n\t"
  "v_mfma_f32_16x16x32_f16 v[180:183], v[102:105], v[156:159], v[180:183]\n\tds_read_b64 v[102:103], v16 offset:512\n\tds_read_b64 v[104:105], v17 offset:512\n\t"
  "v_mfma_f32_16x16x32_f16 v[212:215], v[108:111], v[136:139], v[212:215]\n\t"
  "v_mfma_f32_16x16x32_f16 v[176:179], v[108:111], v[144:147], v[176:179]\n\tds_read_b64 v[124:125], v16 offset:0\n\tds_read_b64 v[126:127], v17 offset:0\n\t"
  "v_mfma_f32_16x16x32_f16 v[180:183], v[108:111], v[152:155], v[180:183]\n\t"
  "v_mfma_f32_16x16x32_f16 v[176:179], v[112:115], v[136:139], v[176:179]\n\t"
  "v_mfma_f32_16x16x32_f16 v[180:183], v[112:115], v[144:147], v[180:183]\n\ts_waitcnt lgkmcnt(5)\n\tv_readfirstlane_b32 s61, v101\n\t"
  "v_mfma_f32_16x16x32_f16 v[180:183], v[132:135], v[136:139], v[180:183]\n\t"
  "v_mfma_f32_16x16x32_f16 v[208:211], v[88:91], v[172:175], v[208:211]\n\ts_cmp_lt_u32 s61, 4\n\ts_cbranch_scc1 .Lc1_ub\n.Lc1_ub_b:\n\t"
  "v_mfma_f32_16x16x32_f16 v[208:211], v[92:95], v[168:171], v[208:211]\n\tds_read_b128 v[108:111], v18 offset:2240\n\tds_read_b128 v[112:115], v18 offset:2752\n\t"
  "v_mfma_f32_16x16x32_f16 v[212:215], v[92:95], v[172:175], v[212:215]\n\tds_read_b128 v[128:131], v18 offset:3264\n\tds_read_b128 v[132:135], v18 offset:3776\n\t"
  "v_mfma_f32_16x16x32_f16 v[208:211], v[96:99], v[164:167], v[208:211]\n\t"
  "v_mfma_f32_16x16x32_f16 v[212:215], v[96:99], v[168:171], v[212:215]\n\t"
  "v_mfma_f32_16x16x32_f16 v[176:179], v[96:99], v[172:175], v[176:179]\n\tds_read_b128 v[136:139], v18 offset:2176\n\tds_read_b128 v[140:143], v18 offset:2688\n\t"
  "v_mfma_f32_16x16x32_f16 v[208:211], v[116:119], v[160:163], v[208:211]\n\tds_read_b128 v[144:147], v18 offset:3200\n\tds_read_b128 v[148:151], v18 offset:3712\n\t"
  "v_mfma_f32_16x16x32_f16 v[212:215], v[116:119], v[164:167], v[212:215]\n\t"
  "v_mfma_f32_16x16x32_f16 v[176:179], v[116:119], v[168:171], v[176:179]\n\t"
  "v_mfma_f32_16x16x32_f16 v[180:183], v[116:119], v[172:175], v[180:183]\n\ts_waitcnt lgkmcnt(6)\n\t"
  "v_mfma_f32_16x16x32_f16 v[212:215], v[120:123], v[160:163], v[212:215]\n\tds_read_b128 v[116:119], v18 offset:2112\n\tds_read_b128 v[152:155], v18 offset:2624\n\t"
  "v_mfma_f32_16x16x32_f16 v[176:179], v[120:123], v[164:167], v[176:179]\n\tds_read_b128 v[156:159], v18 offset:3136\n\tds_read_b128 v[172:175], v18 offset:3648\n\t"
  "v_mfma_f32_16x16x32_f16 v[180:183], v[120:123], v[168:171], v[180:183]\n\t"
  "v_mfma_f32_16x16x32_f16 v[176:179], v[102:105], v[160:163], v[176:179]\n\t"
  "v_mfma_f32_16x16x32_f16 v[180:183], v[102:105], v[164:167], v[180:183]\n\t"
  "v_mfma_f32_16x16x32_f16 v[180:183], v[124:127], v[160:163], v[180:183]\n\tds_read_b128 v[104:107], v18 offset:2048\n\tds_read_b128 v[120:123], v18 offset:2560\n\t"
  "v_mfma_f32_16x16x32_f16 v[208:211], v[36:39], v[108:111], v[208:211]\n\tds_read_b128 v[124:127], v18 offset:3072\n\tds_read_b128 v[160:163], v18 offset:3584\n\t"
  "v_mfma_f32_16x16x32_f16 v[180:183], v[48:51], v[108:111], v[180:183]\n\t"
  "v_mfma_f32_16x16x32_f16 v[180:183], v[44:47], v[112:115], v[180:183]\n\ts_waitcnt lgkmcnt(6)\n\t"
  "v_mfma_f32_16x16x32_f16 v[180:183], v[40:43], v[128:131], v[180:183]\n\t"
  "v_mfma_f32_16x16x32_f16 v[180:183], v[36:39], v[132:135], v[180:183]\n\t"
  "v_mfma_f32_16x16x32_f16 v[208:211], v[52:55], v[136:139], v[208:211]\n\t"
  "v_mfma_f32_16x16x32_f16 v[180:183], v[64:67], v[136:139], v[180:183]\n\t"
  "v_mfma_f32_16x16x32_f16 v[180:183], v[60:63], v[140:143], v[180:183]\n\t"
  "v_mfma_f32_16x16x32_f16 v[180:183], v[56:59], v[144:147], v[180:183]\n\t"
  "v_mfma_f32_16x16x32_f16 v[180:183], v[52:55], v[148:151], v[180:183]\n\t"
  "v_mfma_f32_16x16x32_f16 v[208:211], v[68:71], v[116:119], v[208:211]\n\tds_read_b128 v[48:51], v19 offset:5120\n\tds_read_b128 v[64:67], v19 offset:6144\n\t"
  "v_mfma_f32_16x16x32_f16 v[180:183], v[80:83], v[116:119], v[180:183]\n\t"
  "v_mfma_f32_16x16x32_f16 v[180:183], v[76:79], v[152:155], v[180:183]\n\ts_waitcnt lgkmcnt(2)\n\t"
  "v_mfma_f32_16x16x32_f16 v[180:183], v[72:75], v[156:159], v[180:183]\n\t"
  "v_mfma_f32_16x16x32_f16 v[180:183], v[68:71], v[172:175], v[180:183]\n\t"
  "v_mfma_f32_16x16x32_f16 v[208:211], v[84:87], v[104:107], v[208:211]\n\t"
  "v_mfma_f32_16x16x32_f16 v[180:183], v[96:99], v[104:107], v[180:183]\n\t"
  "v_mfma_f32_16x16x32_f16 v[180:183], v[92:95], v[120:123], v[180:183]\n\t"
  "v_mfma_f32_16x16x32_f16 v[180:183], v[88:91], v[124:127], v[180:183]\n\tds_read_b32 v101, v27 offset:64\n\ts_waitcnt lgkmcnt(1)\n\t"
  "v_mfma_f32_16x16x32_f16 v[180:183], v[84:87], v[160:163], v[180:183]\n\tds_write_b128 v20, v[208:211] offset:59392\n\tv_perm_b32 v48, v48, v48, s60\n\t"
  "v_mfma_f32_16x16x32_f16 v[212:215], v[40:43], v[108:111], v[212:215]\n\ts_setprio 3\n\tbuffer_load_dwordx4 v[28:31], v23, s[56:59], 0 offen sc1\n\tbuffer_load_dwordx4 v[32:35], v24, s[56:59], 0 offen sc1\n\ts_setprio 0\n\t"
  "v_mfma_f32_16x16x32_f16 v[176:179], v[44:47], v[108:111], v[176:179]\n\tds_write_b128 v20, v[180:183] offset:60416\n\tv_perm_b32 v49, v49, v49, s60\n\t"
  "v_mfma_f32_16x16x32_f16 v[212:215], v[36:39], v[112:115], v[212:215]\n\ts_mov_b64 exec, 1\n\tds_add_u32 v27, v100 offset:16\n\ts_mov_b64 exec, -1\n\t"
  "v_mfma_f32_16x16x32_f16 v[176:179], v[40:43], v[112:115], v[176:179]\n\tv_perm_b32 v50, v50, v50, s60\n\tv_perm_b32 v51, v51, v51, s60\n\t"
  "v_mfma_f32_16x16x32_f16 v[176:179], v[36:39], v[128:131], v[176:179]\n\tv_perm_b32 v64, v64, v64, s60\n\tv_perm_b32 v65, v65, v65, s60\n\t"
  "v_mfma_f32_16x16x32_f16 v[212:215], v[56:59], v[136:139], v[212:215]\n\tv_perm_b32 v66, v66, v66, s60\n\tv_perm_b32 v67, v67, v67, s60\n\t"
  "v_mfma_f32_16x16x32_f16 v[176:179], v[60:63], v[136:139], v[176:179]\n\ts_waitcnt lgkmcnt(3)\n\tv_readfirstlane_b32 s61, v101\n\t"
  "v_mfma_f32_16x16x32_f16 v[212:215], v[52:55], v[140:143], v[212:215]\n\t"
  "v_mfma_f32_16x16x32_f16 v[176:179], v[56:59], v[140:143], v[176:179]\n\ts_cmp_lt_u32 s61, 4\n\ts_cbranch_scc1 .Lc1_p3\n.Lc1_p3_b:\n\t"
  "v_mfma_f32_16x16x32_f16 v[176:179], v[52:55], v[144:147], v[176:179]\n\tds_read_b32 v36, v27 offset:16\n\tds_read_b128 v[40:43], v20 offset:57344\n\tds_read_b128 v[44:47], v20 offset:58368\n\t"
  "v_mfma_f32_16x16x32_f16 v[212:215], v[72:75], v[116:119], v[212:215]\n\t"
  "v_mfma_f32_16x16x32_f16 v[176:179], v[76:79], v[116:119], v[176:179]\n\t"
  "v_mfma_f32_16x16x32_f16 v[212:215], v[68:71], v[152:155], v[212:215]\n\t"
  "v_mfma_f32_16x16x32_f16 v[176:179], v[72:75], v[152:155], v[176:179]\n\t"
  "v_mfma_f32_16x16x32_f16 v[176:179], v[68:71], v[156:159], v[176:179]\n\t"
  "v_mfma_f32_16x16x32_f16 v[212:215], v[88:91], v[104:107], v[212:215]\n\t"
  "v_mfma_f32_16x16x32_f16 v[176:179], v[92:95], v[104:107], v[176:179]\n\t"
  "v_mfma_f32_16x16x32_f16 v[212:215], v[84:87], v[120:123], v[212:215]\n\ts_waitcnt lgkmcnt(0)\n\tv_readfirstlane_b32 s61, v36\n\tds_read_b32 v25, v27 offset:32\n\t"
  "v_mfma_f32_16x16x32_f16 v[176:179], v[88:91], v[120:123], v[176:179]\n\ts_cmp_lt_u32 s61, 4\n\ts_cbranch_scc1 .Lc1_x4\n.Lc1_x4_b:\n\t"
  "v_mfma_f32_16x16x32_f16 v[176:179], v[84:87], v[124:127], v[176:179]\n\tv_fmac_f32 v48, v40, v26\n\tv_fmac_f32 v49, v41, v26\n\tv_fmac_f32 v50, v42, v26\n\t"
  "v_fmac_f32 v51, v43, v26\n\tv_fmac_f32 v64, v44, v26\n\tv_fmac_f32 v65, v45, v26\n\tv_fmac_f32 v66, v46, v26\n\t"
  "v_fmac_f32 v67, v47, v26\n\tv_fmac_f32 v48, v212, v26\n\tv_fmac_f32 v49, v213, v26\n\tv_fmac_f32 v50, v214, v26\n\t"
  "v_fmac_f32 v51, v215, v26\n\tv_cvt_pk_bf16_f32 v36, v48, v49\n\tv_cvt_pk_bf16_f32 v37, v50, v51\n\tv_fmac_f32 v64, v176, v26\n\t"
  "v_fmac_f32 v65, v177, v26\n\tv_fmac_f32 v66, v178, v26\n\tv_fmac_f32 v67, v179, v26\n\tv_cvt_pk_bf16_f32 v38, v64, v65\n\t"
  "v_cvt_pk_bf16_f32 v39, v66, v67\n\ts_nop 0\n\tv_permlane16_swap_b32 v36, v38\n\tv_permlane16_swap_b32 v37, v39\n\t"
  "s_setprio 3\n\tbuffer_store_dwordx4 v[36:39], v22, s[56:59], 0 offen sc1\n\ts_setprio 0\n\ts_waitcnt lgkmcnt(0)\n\ts_nop 0\n\t"
  ".Lc_end:\n\ts_waitcnt vmcnt(1) lgkmcnt(0)"
  : C1_OPS, "=&{v[28:31]}"(fl), "=&{v[32:35]}"(wfl), "=&{v25}"(g2)
  : "{s[56:59]}"(wrs), "{s60}"((a >> 1) ? 0x03020c0cu : 0x01000c0cu), [a] "s"(a)
  : "memory", "scc", "vcc", "s61", "v26", "v27", "v36", "v37", "v38", "v39", "v40", "v41", "v42", "v43", "v44", "v45", "v46", "v47", "v48", "v49", "v50", "v51", "v52", "v53", "v54", "v55", "v56", "v57", "v58", "v59", "v60", "v61", "v62", "v63", "v64", "v65", "v66", "v67", "v68", "v69", "v70", "v71", "v72", "v73", "v74", "v75", "v76", "v77", "v78", "v79", "v80", "v81", "v82", "v83", "v84", "v85", "v86", "v87", "v88", "v89", "v90", "v91", "v92", "v93", "v94", "v95", "v96", "v97", "v98", "v99", "v100", "v101", "v102", "v103", "v104", "v105", "v106", "v107", "v108", "v109", "v110", "v111", "v112", "v113", "v114", "v115", "v116", "v117", "v118", "v119", "v120", "v121", "v122", "v123", "v124", "v125", "v126", "v127", "v128", "v129", "v130", "v131", "v132", "v133", "v134", "v135", "v136", "v137", "v138", "v139", "v140", "v141", "v142", "v143", "v144", "v145", "v146", "v147", "v148", "v149", "v150", "v151", "v152", "v153", "v154", "v155", "v156", "v157", "v158", "v159", "v160", "v161", "v162", "v163", "v164", "v165", "v166", "v167", "v168", "v169", "v170", "v171", "v172", "v173", "v174", "v175", "v176", "v177", "v178", "v179", "v180", "v181", "v182", "v183", "v184", "v185", "v186", "v187", "v188", "v189", "v190", "v191", "v192", "v193", "v194", "v195", "v196", "v197", "v198", "v199", "v200", "v201", "v202", "v203", "v204", "v205", "v206", "v207", "v208", "v209", "v210", "v211", "v212", "v213", "v214", "v215", "v216", "v217", "v218", "v219", "v220", "v221", "v222", "v223");
  ST(0, 9);
}

struct XTake { u32 p, sw, idx; bf16x8 one; u64 m0, m1, m2; };
typedef __attribute__((ext_vector_type(16))) short bf16x16;
__device__ __forceinline__ void x_pre(XTake& X, int a, int lane) {
  X.p = (u32)(L_TA + a * 8192 + lane * 8);
  X.sw = (u32)(L_SP + ((lane & 15) * 4 + a) * 4 + ((lane >> 4) == ((lane >> 2) & 3) ? 768 : 0));
  X.idx = (lane & 1) ? 0xEEEEu : 0x4444u;
  X.one = __builtin_bit_cast(bf16x8, (u32x4){0x3F803F80u, 0x3F803F80u, 0x3F803F80u, 0x3F803F80u});
  X.m0 = 0xAAAAAAAAAAAAAAAAull; X.m1 = 0xCCCCCCCCCCCCCCCCull; X.m2 = 0xF0000F0000F0000Full;
  asm volatile("" : "+v"(X.p), "+v"(X.sw), "+v"(X.idx), "+v"(X.one));
  asm volatile("" : "+s"(X.m0), "+s"(X.m1), "+s"(X.m2));
}
struct NoMid { __device__ void operator()() const {} };
template <int H, int NY = 0, bool ALL = false, class F = NoMid>
__device__ __forceinline__ void x_take(const XTake& X, char* smem, rsrc_t wrs, int xin, int rb, int a, int lane, u32 sent = 0xFFFFFFFFu, F mid = F()) {
  constexpr int tile = H ? L_TB : L_TA, NT = H ? 3 : 2;
  const LAS char* p = (const LAS char*)(size_t)(H ? opaque(X.p + (L_TB - L_TA)) : X.p);
  const LAS char* ph = (const LAS char*)(size_t)(H ? opaque(X.p + (L_TH - L_TA) - a * 4096) : 0u);
  LAS float* spw = (LAS float*)(size_t)X.sw + H * 384;
  float s = 0.f, dv[NT];
  bf16x16 xf[NT][2];
  f32x4 ss[NT], sg[NT];
  auto ld_ = [&](int o, int j) __attribute__((always_inline)) {
    if (o < 2) xf[o][j] = __builtin_shufflevector(ld_tr8<1024>(p + o * 512 + j * 4096), ld_tr8<1024>(p + o * 512 + j * 4096 + 2048), 0, 1, 2, 3, 4, 5, 6, 7, 8, 9, 10, 11, 12, 13, 14, 15);
    else xf[o][j] = __builtin_shufflevector(ld_tr8<512>(ph + j * 2048), ld_tr8<512>(ph + j * 2048 + 1024), 0, 1, 2, 3, 4, 5, 6, 7, 8, 9, 10, 11, 12, 13, 14, 15);
    SB();
  };
  auto gr_ = [&](int o, int j) __attribute__((always_inline)) {
#define XLO(v_) __builtin_shufflevector(v_, v_, 0, 1, 2, 3, 4, 5, 6, 7)
#define XHI(v_) __builtin_shufflevector(v_, v_, 8, 9, 10, 11, 12, 13, 14, 15)
    if (j == 0) asm volatile("v_mfma_f32_16x16x32_bf16 %0, %1, %1, 0" : "=&v"(sg[o]) : "v"(XLO(xf[o][0])));
    else asm volatile("v_mfma_f32_16x16x32_bf16 %0, %1, %1, %0" : "+v"(sg[o]) : "v"(XLO(xf[o][1])));
    asm volatile("v_mfma_f32_16x16x32_bf16 %0, %1, %1, %0" : "+v"(sg[o]) : "v"(XHI(xf[o][j])));
    SB();
  };
  auto sm_ = [&](int o, int j) __attribute__((always_inline)) {
    asm volatile("v_smfmac_f32_16x16x64_bf16 %0, %1, %2, %3" : "+v"(ss[o]) : "v"(X.one), "v"(xf[o][j]), "v"(X.idx));
    SB();
  };
  auto fin_ = [&](int o, auto V_) __attribute__((always_inline)) {
    float t;
    float& d = dv[o];
    asm volatile("v_cndmask_b32_e64 %0, %3, %4, %9\n\tv_cndmask_b32_e64 %1, %5, %6, %9\n\tv_cndmask_b32_e64 %0, %0, %1, %10\n\t.if %12 >= 0\n\ts_nop %12\n\t.endif\n\t"
                 "v_add_f32 %1, %7, %8\n\tv_cndmask_b32_e64 %0, %1, %0, %11\n\tv_add_f32 %2, %2, %1"
                 : "=&v"(d), "=&v"(t), "+v"(s) : "v"(sg[o][0]), "v"(sg[o][1]), "v"(sg[o][2]), "v"(sg[o][3]), "v"(ss[o][0]), "v"(ss[o][1]),
                   "s"(X.m0), "s"(X.m1), "s"(X.m2), "n"(decltype(V_)::value - 1), "v"(ss[o]));
    SB();
  };
  auto zero_ = [&]() __attribute__((always_inline)) {
#pragma unroll
    for (int o = 0; o < NT; ++o) { ss[o] = (f32x4){0.f, 0.f, 0.f, 0.f}; asm volatile("" : "+v"(ss[o])); }
    asm volatile("" : "+v"(s));
  };
  auto first_ = [&]() __attribute__((always_inline)) { gr_(0, 0); sm_(0, 0); gr_(1, 0); sm_(1, 0); };
  auto second_ = [&]() __attribute__((always_inline)) {
    gr_(0, 1); sm_(0, 1); gr_(1, 1); fin_(0, IC<0>{}); sm_(1, 1);
    if (NT == 2) { fin_(1, IC<5>{}); spw[0] = dv[0]; spw[64] = dv[1]; }
  };
  auto third_ = [&]() __attribute__((always_inline)) {
    gr_(2, 0); fin_(1, IC<0>{}); gr_(2, 1);
    spw[64] = dv[0]; spw[128] = dv[1];
    SB();
    sm_(2, 0); sm_(2, 1); fin_(NT - 1, IC<5>{});
    spw[0] = dv[NT - 1];
  };
  zero_();
  SB();
  if (H) {
    const u32 sa = opaque((u32)(L_TB + a * 8192 + (lane >> 4) * 1024 + (lane & 15) * 32 + ((lane >> 3) & 1) * 512 + ((lane >> 4) & 3) * 8 + 4));
#pragma unroll 1
    for (unsigned spins = 0; spins <= SPIN_CAP; ++spins) {
      x_dma(wrs, xin, rb, tile, a, lane);
      asm volatile("s_waitcnt vmcnt(8)" ::: "memory");
      if (__builtin_amdgcn_ballot_w64(*(const LAS u32*)(size_t)sa == sent) == 0ull) break;
      FAILC(20);
    }
  } else asm volatile("s_waitcnt vmcnt(%0)" :: "n"(4 + NY) : "memory");
  if (H && B6 == 0) { __builtin_amdgcn_s_barrier(); ST(xin != OFF_X1, 14); asm volatile("" ::: "memory"); }
  ld_(0, 0); ld_(1, 0);
  if (!ALL && !(H && B6 == 1)) first_();
  asm volatile("s_waitcnt vmcnt(%0)" :: "n"(4 * (NT - 2) + NY) : "memory");
  if (H && B6) { __builtin_amdgcn_s_barrier(); ST(xin != OFF_X1, 14); asm volatile("" ::: "memory"); }
  ld_(0, 1); ld_(1, 1);
  if (ALL || (H && B6 == 1)) first_();
  second_();
  if (NT == 3) {
    asm volatile("s_waitcnt vmcnt(%0)" :: "n"(NY) : "memory");
    ld_(2, 0); ld_(2, 1);
    mid();
    third_();
  }
  if (__builtin_expect(__builtin_amdgcn_ballot_w64(s != s) != 0ull, 0)) {
    for (unsigned spins = 0;; ++spins) {
      FAILC((xin == OFF_X1 ? 1 : 9) + H);
      if (H) *(u32*)(smem + L_MI + M_RD) = xin == OFF_X1 ? 1 : 2;
      x_dma(wrs, xin, rb, tile, a, lane);
      asm volatile("s_waitcnt vmcnt(0)" ::: "memory");
      s = 0.f;
#pragma unroll
      for (int o = 0; o < 2 * NT; ++o) ld_(o >> 1, o & 1);
      zero_();
      first_(); second_();
      if (NT == 3) third_();
      if (!__any(s != s)) break;
      bool st = false;
#pragma unroll 1
      for (int e = 0; e < 4 * NT; ++e) { const u32x4 v = *(const u32x4*)(smem + (e < 8 ? tile + a * 8192 : L_TH + a * 4096 - 8192) + lane * 16 + e * 1024); st |= v[1] == sent || v[3] == sent; }
      if (!__any(st) || spins > SPIN_CAP) break;
    }
  }
}
#define PIN(x_) asm volatile("" : "+v"(x_))
struct HaloMsg { u32x4 k, v; };
__device__ __forceinline__ void halo_ld(HaloMsg& h, rsrc_t wrs, int ho) { h.k = ldw(wrs, ho); h.v = ldw(wrs, ho + 1024); }
__device__ __forceinline__ bool halo_ok(const HaloMsg& h, u32 tag) {
  return __builtin_amdgcn_ballot_w64((opaque((h.k[1] ^ tag) | (h.k[3] ^ tag)) | opaque((h.v[1] ^ tag) | (h.v[3] ^ tag))) != 0u) == 0ull;
}
struct AttC { f32x4 mp, mo, mf; u32x2 one; u32 so0, so1, sp0, sp1; };
__device__ __forceinline__ void att_pre(AttC& C, int lane, bool first) {
  const float NINF = -__builtin_inff();
  const u64 pm[4] = {0x0fff00ff000f0000ull, 0x3fff03ff003f0003ull, 0xf000ff00fff0ffffull, 0xc000fc00ffc0fffcull};
#pragma unroll
  for (int e = 0; e < 4; ++e) {
    const u64 par = (e & 1) ? 0xAAAAAAAAAAAAAAAAull : 0x5555555555555555ull;
    C.mp[e] = __builtin_amdgcn_inverse_ballot_w64(pm[e >> 1] & par) ? 0.f : NINF;
    C.mo[e] = __builtin_amdgcn_inverse_ballot_w64(pm[2 + (e >> 1)] & par) ? 0.f : NINF;
    C.mf[e] = first ? NINF : C.mp[e];
  }
  C.one = (u32x2){0x3F803F80u, 0x3F803F80u};
  const u32 sl = (lane & 1) ? 0x01000c0cu : 0x0c0c0100u, su = (lane & 1) ? 0x03020c0cu : 0x0c0c0302u;
  const bool o0 = __builtin_amdgcn_inverse_ballot_w64(pm[2]), o1 = __builtin_amdgcn_inverse_ballot_w64(pm[3]);
  C.so0 = o0 ? sl : 0x0c0c0c0cu; C.sp0 = o0 ? 0x0c0c0c0cu : sl; C.so1 = o1 ? su : 0x0c0c0c0cu; C.sp1 = o1 ? 0x0c0c0c0cu : su;
  asm volatile("" : "+v"(C.mp), "+v"(C.mo), "+v"(C.mf), "+v"(C.one), "+v"(C.so0), "+v"(C.so1), "+v"(C.sp0), "+v"(C.sp1));
}
template <class F, class F2>
__device__ __forceinline__ void attention(const AttC& C, u32 qp, HaloMsg& hm, int ho, u32 tag, rsrc_t wrs, int oo, int fid, F mid, F2 mid2) {
  u32x4 vv, ow;
  u32x2 q0;
  f32x4 l0, o1;
  float inv;
#define SOFT(F1_, F2_, W_, V_, O_)                                                                                                                      \
  F1_ "v_permlane16_swap_b32 v232, v233\n\tv_max_f32 v234, v232, v233\n\tv_max_f32 v235, v232, v233\n\t"                                                \
  F2_ "v_permlane32_swap_b32 v234, v235\n\tv_max_f32 v232, v234, v235\n\tv_mul_f32 v232, 0xbeb8aa3b, v232\n\t"                                          \
  "v_fmamk_f32 v220, v220, 0x3eb8aa3b, v232\n\tv_fmamk_f32 v221, v221, 0x3eb8aa3b, v232\n\t"                                                            \
  "v_exp_f32 v220, v220\n\tv_exp_f32 v221, v221\n\tv_mov_b32 v233, 0\n\tv_cvt_pk_bf16_f32 v232, v220, v221\n\t"                                         \
  "v_perm_b32 v226, v232, v232, %[sp0]\n\tv_perm_b32 v227, v232, v232, %[sp1]\n\t"                                                                      \
  "v_mfma_f32_16x16x16_bf16 v[236:239], %[one], v[232:233], 0\n\t"                                                                                      \
  "v_perm_b32 v224, v232, v232, %[so0]\n\tv_perm_b32 v225, v232, v232, %[so1]\n\t" W_ "\n\ts_nop 0\n\t"                                                 \
  "v_mfma_f32_16x16x32_bf16 " O_ ", " V_ ", v[224:227], 0\n\t"
  asm volatile(
      "ds_read2st64_b32 v[214:215], %[qp] offset0:4 offset1:5\n\tds_read2st64_b32 v[216:217], %[qp] offset0:6 offset1:7\n\t"
      "ds_read2st64_b32 v[212:213], %[qp] offset0:0 offset1:1\n\tds_read2st64_b32 v[218:219], %[qp] offset0:10 offset1:11\n\t"
      "ds_read2st64_b32 v[200:201], %[qp] offset0:2 offset1:3\n\tds_read2st64_b32 v[202:203], %[qp] offset0:8 offset1:9\n\t"
      "s_waitcnt lgkmcnt(4)\n\tv_mfma_f32_16x16x16_bf16 v[220:223], v[216:217], v[214:215], %[mp]\n\t"
      "s_waitcnt lgkmcnt(3)\n\tv_mfma_f32_16x16x16_bf16 v[224:227], v[212:213], v[214:215], %[mo]\n\t"
      "s_waitcnt lgkmcnt(2)\n\tv_mfma_f32_16x16x16_bf16 v[228:231], v[216:217], v[218:219], %[mo]\n\t"
      "v_max_f32 v220, v220, v221\n\tv_max_f32 v221, v222, v223\n\ts_nop 1\n\t"
      "v_max3_f32 v220, v220, v224, v225\n\tv_max3_f32 v221, v221, v226, v227\n\t"
      "v_max_f32 v233, v220, v221\n\tv_max_f32 v232, v220, v221\n\t"
      SOFT("v_max_f32 v228, v228, v229\n\tv_max_f32 v229, v230, v231\n\t", "s_nop 1\n\t", "s_waitcnt lgkmcnt(0)", "v[200:203]", "v[244:247]")
      "v_rcp_f32 v248, v236"
      : "=&{v[200:203]}"(vv), "=&{v[218:219]}"(q0), "=&{v[228:231]}"(l0), "=&{v[244:247]}"(o1), "=&{v248}"(inv)
      : [qp] "v"(qp), [mp] "v"(C.mp), [mo] "v"(C.mo), [one] "v"(C.one), [so0] "v"(C.so0), [so1] "v"(C.so1), [sp0] "v"(C.sp0), [sp1] "v"(C.sp1)
      : "memory", "v212", "v213", "v214", "v215", "v216", "v217", "v220", "v221", "v222", "v223", "v224", "v225", "v226", "v227", "v232", "v233", "v234", "v235", "v236", "v237", "v238", "v239");
  mid();
  {
    u32 n = SPIN_CAP;
    asm volatile("1:\n\tv_bitop3_b32 v232, v209, v211, %[tg] bitop3:0x7e\n\tv_bitop3_b32 v233, v205, v207, %[tg] bitop3:0x7e\n\tv_or_b32 v232, v232, v233\n\tv_cmp_ne_u32 vcc, 0, v232\n\t"
                 "s_cbranch_vccz 2f\n\ts_sleep 2\n\tbuffer_load_dwordx4 v[208:211], %[ho], %[rs], 0 offen sc1\n\tbuffer_load_dwordx4 v[204:207], %[ho], %[rs], 0 offen offset:1024 sc1\n\t"
                 "s_sub_u32 %[n], %[n], 1\n\ts_waitcnt vmcnt(0)\n\ts_cbranch_scc0 1b\n2:"
                 : "+{v[208:211]}"(hm.k), "+{v[204:207]}"(hm.v), [n] "+s"(n) : [tg] "s"(tag), [ho] "v"(ho), [rs] "s"(wrs) : "memory", "vcc", "scc", "v232", "v233");
  }
  mid2();
  asm volatile(
      "v_mov_b32 v209, v210\n\tv_mov_b32 v205, v206\n\ts_nop 0\n\tv_mfma_f32_16x16x16_bf16 v[220:223], v[208:209], v[218:219], %[mf]\n\t"
      "v_mul_f32 v244, v244, v248\n\tv_mul_f32 v245, v245, v248\n\tv_mul_f32 v246, v246, v248\n\tv_mul_f32 v247, v247, v248\n\t"
      "v_cvt_pk_bf16_f32 v242, v244, v245\n\tv_cvt_pk_bf16_f32 v243, v246, v247\n\ts_nop 1\n\t"
      "v_max3_f32 v220, v220, v221, v228\n\tv_max3_f32 v221, v222, v223, v229\n\t"
      "v_max_f32 v233, v220, v221\n\tv_max_f32 v232, v220, v221\n\t"
      SOFT("s_nop 1\n\t", "s_nop 1\n\t", "s_nop 0", "v[202:205]", "v[244:247]")
      "v_rcp_f32 v248, v236\n\ts_nop 6\n\t"
      "v_mul_f32 v244, v244, v248\n\tv_mul_f32 v245, v245, v248\n\tv_mul_f32 v246, v246, v248\n\tv_mul_f32 v247, v247, v248\n\t"
      "v_cvt_pk_bf16_f32 v240, v244, v245\n\tv_cvt_pk_bf16_f32 v241, v246, v247\n\ts_nop 0\n\t"
      "v_permlane16_swap_b32 v240, v242\n\tv_permlane16_swap_b32 v241, v243"
      : "=&{v[240:243]}"(ow), "+{v[208:211]}"(hm.k), "+{v[204:207]}"(hm.v), "+{v[228:231]}"(l0), "+{v[244:247]}"(o1), "+{v248}"(inv)
      : "{v[200:203]}"(vv), "{v[218:219]}"(q0), [mf] "v"(C.mf), [one] "v"(C.one), [so0] "v"(C.so0), [so1] "v"(C.so1), [sp0] "v"(C.sp0), [sp1] "v"(C.sp1)
      : "v220", "v221", "v222", "v223", "v224", "v225", "v226", "v227", "v232", "v233", "v234", "v235", "v236", "v237", "v238", "v239");
  stw(wrs, oo, ow);
  asm volatile("s_nop 1" :: "v"(ow) : "memory");
}
__device__ __forceinline__ void o_dma(rsrc_t wrs, int og, int ot, int a, int lane) {
  dma4k(wrs, lane * 16, og + a * 4096, ot + a * 4096);
  dma4k(wrs, lane * 16, og + 16384 + a * 4096, ot + 16384 + a * 4096);
}
__device__ __forceinline__ void o_get(rsrc_t wrs, int vo, int g0, int g1, int ot, u32 sa, u32 mark, int fid) {
#pragma unroll 1
  for (unsigned spins = 0; spins <= SPIN_CAP; ++spins) {
    dma4k(wrs, vo, g0, ot);
    dma4k(wrs, vo, g1, ot + 16384);
    asm volatile("s_waitcnt vmcnt(4)" ::: "memory");
    if (__builtin_amdgcn_ballot_w64(*(const LAS u32*)(size_t)sa == mark) == 0ull) break;
    FAILC(fid);
  }
}
template <bool L1, class F>
__device__ __forceinline__ void wo_mma2(f32x4 (&acc)[2], char* smem, int ot, rsrc_t wrs, int og, int a, int lane, const bf16x8 (&wof)[16], u32 cnt, const f32x4 (&r0)[2], const u32 (&qa)[4], u32 sent, F late) {
  u32x4 of[32];
#define LDO2(s_) of[s_] = *(const LAS u32x4*)((const LAS char*)(size_t)qa[(s_) >> 3] + (ot - L_TA) + ((s_) & 1) * 16384 + (((s_) >> 1) & 3) * 1024)
#define WMM(s_) do { SB(); if ((s_) < 2) asm volatile("v_mfma_f32_16x16x32_bf16 %0, %1, %2, %3" : "=&v"(acc[(s_) & 1]) \
        : "v"(L1 ? __builtin_bit_cast(bf16x8, of[s_]) : wof[0]), "v"(L1 ? wof[0] : __builtin_bit_cast(bf16x8, of[s_])), "v"(r0[(s_) & 1])); \
      else if (L1) asm volatile("v_mfma_f32_16x16x32_bf16 %0, %1, %2, %0" : "+v"(acc[(s_) & 1]) : "v"(of[s_]), "v"(wof[(s_) >> 1])); \
      else asm volatile("v_mfma_f32_16x16x32_bf16 %0, %1, %2, %0" : "+v"(acc[(s_) & 1]) : "v"(wof[(s_) >> 1]), "v"(of[s_])); SB(); } while (0)
  u32 mx = 0, cv;
#pragma unroll
  for (int s = 0; s < 8; ++s) { LDO2(s); SB(); }
#pragma unroll
  for (int s = 0; s < 3; ++s) {
    WMM(s);
#pragma unroll
    for (int e = 3 * s; e < 3 * s + 3 && e < 8; ++e) mx = max(max(mx, of[e][1]), of[e][3]);
    asm volatile("" : "+v"(mx));
  }
  if (__builtin_expect(!__all(mx < 0xFF810000u), 0)) {
    for (unsigned spins = 0; spins <= SPIN_CAP; ++spins) {
      FAILC((L1 ? 5 : 13) + (ot == L_TB));
      __builtin_amdgcn_s_sleep(2);
      o_dma(wrs, og, ot, a, lane);
      asm volatile("s_waitcnt vmcnt(0)" ::: "memory");
      bool st = false;
#pragma unroll
      for (int s = 0; s < 8; ++s) { LDO2(s); st |= of[s][1] == sent || of[s][3] == sent; }
      if (!__any(st)) break;
    }
    WMM(0); WMM(1); WMM(2);
    asm volatile("s_nop 7" : "+v"(acc[0]), "+v"(acc[1]));
  }
  cnt_add(smem, C_OT, lane);
  cv = *(const volatile LAS u32*)(size_t)(u32)(L_MI + C_OT);
  SB();
#pragma unroll
  for (int s = 3; s < WS5; ++s) WMM(s);
  {
    u32 n = 1u << 24;
    asm volatile("1:\n\tv_readfirstlane_b32 vcc_lo, %0\n\ts_sub_u32 %1, %1, 1\n\ts_cbranch_scc1 2f\n\ts_cmp_ge_u32 vcc_lo, %3\n\ts_cbranch_scc1 2f\n\tds_read_b32 %0, %2\n\ts_waitcnt lgkmcnt(0)\n\ts_branch 1b\n2:"
                 : "+v"(cv), "+s"(n) : "v"(L_MI + C_OT), "s"(cnt) : "memory", "vcc", "scc");
  }
#pragma unroll
  for (int s = 8; s < WS5 + 8; ++s) { LDO2(s); SB(); }
#pragma unroll
  for (int s = WS5; s < 32; ++s) {
    WMM(s);
    if (s + 8 < 32) LDO2(s + 8);
    if (s == 24) late();
  }
  asm volatile("s_nop 7" : "+v"(acc[0]), "+v"(acc[1]));
}
template <class F>
__device__ __forceinline__ void wo_last(f32x4 (&acc)[2], char* smem, rsrc_t wrs, int og, int a, int lane, const bf16x8 (&wof)[16], const f32x4 (&r0)[2], const u32 (&qa)[4], u32 sent, F put) {
  u32x4 of[32];
#define LDO3(q_, o_, s_) of[s_] = *(const LAS u32x4*)((const LAS char*)(size_t)q_[((s_) >> 2) & 3] + (o_) + ((s_) >> 4) * 16384 + ((s_) & 3) * 1024)
#define WM3(s_) do { SB(); if (((s_) & 15) == 0) asm volatile("v_mfma_f32_16x16x32_bf16 %0, %1, %2, %3" : "=&v"(acc[(s_) >> 4]) : "v"(wof[0]), "v"(of[s_]), "v"(r0[(s_) >> 4])); \
      else asm volatile("v_mfma_f32_16x16x32_bf16 %0, %1, %2, %0" : "+v"(acc[(s_) >> 4]) : "v"(wof[(s_) & 15]), "v"(of[s_])); SB(); } while (0)
  u32 qb[4];
#pragma unroll
  for (int e = 0; e < 4; ++e) qb[e] = qa[e];
  SB();
  __builtin_amdgcn_s_barrier();
  ST(1, 19);
#pragma unroll 1
  for (u32 pass = 0;; ++pass) {
    asm volatile("" : "+v"(qb[0]), "+v"(qb[1]), "+v"(qb[2]), "+v"(qb[3]) :: "memory");
#pragma unroll
    for (int s = 0; s < WLD; ++s) { LDO3(qb, L_TB - L_TA, s); SB(); }
#pragma unroll
    for (int s = 0; s < 32; ++s) {
      WM3(s);
      if (s + WLD == 16) asm volatile("s_cmp_lg_u32 %0, 0\n\ts_cbranch_scc1 1f\n\ts_barrier\n1:" :: "s"(__builtin_amdgcn_readfirstlane(pass)) : "memory", "scc");
      if (s + WLD < 32) LDO3(qb, L_TB - L_TA, s + WLD);
    }
    asm volatile("s_nop 7" : "+v"(acc[0]), "+v"(acc[1]));
    put();
    SB();
    if (__builtin_expect(__builtin_amdgcn_ballot_w64(__builtin_isunordered(acc[0][0], acc[1][0])) == 0ull, 1)) break;
    asm volatile("s_waitcnt vmcnt(0)" ::: "memory");
    FAILC(14);
    if (pass == 0) {
      lds_barrier();
      const u32 fx = *(const u32*)(smem + L_MI + M_FX);
#pragma unroll
      for (int e = 0; e < 4; ++e) qb[e] -= ((fx >> (8 * ((a + e) & 3))) & 1u) * (L_TB - L_TA);
    } else {
      u32 m = 1;
#pragma unroll 1
      for (int s = 0; s < 32; ++s) {
        const u32x4 v = *(const LAS u32x4*)(size_t)((s & 8 ? (s & 4 ? qb[3] : qb[2]) : (s & 4 ? qb[1] : qb[0])) + (L_TB - L_TA) + (s >> 4) * 16384 + (s & 3) * 1024);
        m = min(min(m, v[1] ^ sent), v[3] ^ sent);
      }
      if (__builtin_amdgcn_ballot_w64(m == 0u) == 0ull || pass > SPIN_CAP) break;
      lds_barrier();
      o_dma(wrs, og, __builtin_amdgcn_readfirstlane(qb[0]) + (L_TB - L_TA) - a * 4096, a, lane);
      asm volatile("s_waitcnt vmcnt(0)" ::: "memory");
      lds_barrier();
    }
  }
}
struct WoPre { f32x4 r[2]; int so; u32 xs, ao; };
template <bool L1>
__device__ __forceinline__ void wo_pre(WoPre& P, const u32x2 (&res)[2], char* ws, int H, int rb, int hb, int a, int lane) {
  const int i16 = lane & 15, kg = lane >> 4, R0 = 1024 * H + 32 * rb;
#pragma unroll
  for (int t = 0; t < 2; ++t) P.r[t] = (f32x4){lo2f(res[t][0]), hi2f(res[t][0]), lo2f(res[t][1]), hi2f(res[t][1])};
  if (L1) {
    P.so = OFF_X2 + ((32 * hb + 8 * a + (i16 >> 1)) * XS_ + R0 + 16 * (lane & 1) + 4 * kg) * 4;
    P.xs = (u32)(L_XS + H * 512 + lane * 8);
    P.ao = (u32)(OFF_ST2 + ((R0 & ~255) + ((lane >> 1) & 3) * 64 + ((R0 >> 2) & 63) + 4 * (lane & 1) + kg) * 16 + (lane & 8));
    asm volatile("" : "+v"(P.r[0]), "+v"(P.r[1]), "+v"(P.so), "+v"(P.xs), "+v"(P.ao));
  } else {
    P.so = ((R0 + 16 * (kg & 1) + i16) * 256 + 32 * hb + 8 * a + 4 * (kg >> 1)) * 4;
    asm volatile("" : "+v"(P.r[0]), "+v"(P.r[1]), "+v"(P.so));
  }
}
template <bool L1>
__device__ __forceinline__ void wo_epi(const f32x4 (&acc)[2], const WoPre& P, char* smem, rsrc_t wrs, char* ws, u32* out, int H, int lane, u32 ONES) {
  if (L1) {
    f32x4 lo_, hi_;
    pair_x(lo_, hi_, acc[0], acc[1]);
    u32x4 v;
#pragma unroll
    for (int e = 0; e < 4; ++e) v[e] = pk2(lo_[e], hi_[e]);
    asm volatile("" : "+v"(v));
    stw(wrs, P.so, v);
    SB();
    float sv, q0, q1, q2, q3;
#define RD_(d_, a_, c_, m_) "v_add_f32_dpp " d_ ", " a_ ", " a_ " " c_ " row_mask:0xf bank_mask:" m_ "\n\t"
    asm volatile("v_dot2_f32_bf16 %5, %13, %17, 0\n\tv_dot2_f32_bf16 %6, %14, %17, 0\n\tv_dot2_f32_bf16 %7, %15, %17, 0\n\tv_dot2_f32_bf16 %8, %16, %17, 0\n\t"
                 "v_dot2_f32_bf16 %9, %13, %13, 0\n\tv_dot2_f32_bf16 %10, %14, %14, 0\n\tv_dot2_f32_bf16 %11, %15, %15, 0\n\tv_dot2_f32_bf16 %12, %16, %16, 0\n\t"
                 RD_("%1", "%5", "row_ror:8", "0x3") RD_("%2", "%6", "row_ror:8", "0x3") RD_("%3", "%7", "row_ror:8", "0x3") RD_("%4", "%8", "row_ror:8", "0x3")
                 RD_("%1", "%9", "row_ror:8", "0xc") RD_("%2", "%10", "row_ror:8", "0xc") RD_("%3", "%11", "row_ror:8", "0xc") RD_("%4", "%12", "row_ror:8", "0xc")
                 RD_("%5", "%1", "row_shl:4", "0x5") RD_("%6", "%2", "row_shl:4", "0x5") RD_("%5", "%3", "row_shr:4", "0xa") RD_("%6", "%4", "row_shr:4", "0xa")
                 "s_nop 0\n\t"
                 RD_("%1", "%5", "quad_perm:[2,3,0,1]", "0xf") RD_("%2", "%6", "quad_perm:[2,3,0,1]", "0xf")
                 "v_cndmask_b32_e64 %0, %1, %2, %18"
                 : "=&v"(sv), "=&v"(q0), "=&v"(q1), "=&v"(q2), "=&v"(q3), "=&v"(lo_[0]), "=&v"(lo_[1]), "=&v"(lo_[2]), "=&v"(lo_[3]), "=&v"(hi_[0]), "=&v"(hi_[1]), "=&v"(hi_[2]), "=&v"(hi_[3])
                 : "v"(v[0]), "v"(v[1]), "v"(v[2]), "v"(v[3]), "v"(ONES), "s"(0xCCCCCCCCCCCCCCCCull));
    const float hf = __builtin_floorf(sv * 0.5f);
    const u32 lo = (u32)(fmaf(sv, 0.5f, -hf) * 4294967296.0f);
    const u64 mine = ((u64)(u32)(int)hf << 32) | (u64)((lo & ~63u) | 1u);
    const u64 old = __hip_atomic_fetch_add((LAS u64*)(size_t)P.xs, mine, __ATOMIC_RELAXED, __HIP_MEMORY_SCOPE_WORKGROUP);
    if (((u32)old & 63u) == 3u) __hip_atomic_fetch_add((GAS u64*)((GAS char*)ws + P.ao), old + mine, __ATOMIC_RELAXED, __HIP_MEMORY_SCOPE_AGENT);
  } else {
    auto r0s = __builtin_amdgcn_permlane16_swap(pk2(acc[0][0], acc[0][1]), pk2(acc[1][0], acc[1][1]), false, false);
    auto r1s = __builtin_amdgcn_permlane16_swap(pk2(acc[0][2], acc[0][3]), pk2(acc[1][2], acc[1][3]), false, false);
    const rsrc_t ors = __builtin_amdgcn_make_buffer_rsrc((void*)out, 0, 2 * MB_, 0x00020000);
    if (H == 0) __builtin_amdgcn_raw_buffer_store_b128((u32x4){r0s[0], r1s[0], r0s[1], r1s[1]}, ors, P.so, 0, OUTA);
    else __builtin_amdgcn_raw_buffer_store_b128((u32x4){r0s[0], r1s[0], r0s[1], r1s[1]}, ors, P.so, 0, OUTB);
  }
}
template <bool L1>
__device__ __forceinline__ void res_ld(u32x2 (&res)[2], const char* smem, int tile, int hb, int a, int lane) {
#pragma unroll
  for (int t = 0; t < 2; ++t) {
    const int o = tile + t * 512 + (4 * hb + a) * 1024;
    if (L1) res[t] = *(const u32x2*)(smem + o + (lane & 15) * 32 + (lane >> 4) * 8);
    else res[t] = __builtin_bit_cast(u32x2, __builtin_amdgcn_ds_read_tr16_b64_v4i16((LAS bf16x4*)(size_t)(u32)(o + lane * 8)));
  }
}
template <bool L1>
__device__ __forceinline__ void a_blayer(char* smem, rsrc_t wrs, char* ws, u32* out, int t, int a, bf16x8 (&wof)[16], u32 tag, u32x4 wfl, ConvK& ck, XTake& xt, AttC& ac) {
  const u32 t9 = L1 ? 0u : KADD(2, now_(), D9);
  t = (int)opaque((u32)t);
  const int lane = t & 63, i16 = lane & 15, kg = lane >> 4;
  const int XIN = L1 ? OFF_X1 : OFF_X3, OBUF = L1 ? OFF_O1 : OFF_O2;
  const int bid = BIDX;
  const int rb = RB_OF(bid), hb = HB_OF(bid);
  if (!L1) { x_pre(xt, a, lane); SB(); x_take<0, 0, true>(xt, smem, wrs, XIN, rb, a, lane); SB(); }
  const int og = OBUF + rb * 32768;
  u32x2 resa[2], resb[2];
  const int wfo = OFF_ST0 + 8192 + (hb * 4 + a) * 128 + (lane & 3) * 16;
#define LDWO(e_) wof[e_] = __builtin_bit_cast(bf16x8, ldws(wrs, ((hb * 64 + a) * 64 + lane) * 16, OFF_WO + ((4 * a + (e_)) & 15) * 4096))
  auto ld_wo = [&](int e0, int e1) __attribute__((always_inline)) {
#pragma unroll
    for (int e = e0; e < e1; ++e) LDWO(e);
    __builtin_amdgcn_sched_barrier(0);
  };
  const u32 mark = x0_mark(tag), sent = L1 ? mark : 0xFFFFFFFFu;
  if (L1) {
    tags_wait(wfl, wrs, wfo, tag, 6); ld_wo(0, WOE);
    SB();
    x_pre(xt, a, lane); SB();
    x_take<0, WOE, true>(xt, smem, wrs, XIN, rb, a, lane, sent);
  }
  ST(!L1, 10);
  SB();
  lds_barrier();
  ST(!L1, 11);
  const u32 t5 = L1 ? (u32)__builtin_amdgcn_s_memrealtime() : KADD(20, now_(), D5), c5 = L1 ? opaque(KADD(18, now_(), DO5)) : 0u;
  res_ld<L1>(resa, smem, L_TA, hb, a, lane);
  auto pf_x3 = [&]() __attribute__((always_inline)) {
    const u32x4 SENT = {0xFFFFFFFFu, 0xFFFFFFFFu, 0xFFFFFFFFu, 0xFFFFFFFFu};
    const int xo = OFF_X3 + (8 * a + (lane >> 3)) * XBS + ((bid & 7) * 32 + (bid >> 3)) * 128 + (lane & 7) * 16;
    stw(wrs, xo, SENT); stw(wrs, xo + 32 * XBS, SENT);
  };
  const int oo = (int)opaque((u32)(OBUF + (((rb * 2 + (kg & 1)) * 16 + hb * 2 + (a >> 1)) * 64 + (2 * (a & 1) + (kg >> 1)) * 16 + i16) * 16));
  const u32 ONES = opaque(0x3F803F80u);
  att_pre(ac, lane, rb == 0);
  if (L1) pf_x3();
  if (L1) SB();
  const int ho = (int)opaque((u32)((rb == 0 ? WSB : KVO(L1, rb, hb * 4 + a)) + lane * 16));
  const u32 qp = opaque((u32)(L_QB + a * 3072 + lane * 4));
  const int tso = (int)opaque((u32)(L1 ? OFF_T5 + rb * 1024 + (lane & 7) * 128 : OFF_TS + rb * 128 + (lane & 7) * 8)), tsw = (int)opaque((u32)(t == hb ? tso : WSB));
  if (L1) __builtin_amdgcn_raw_buffer_store_b64((u32x2){t5, tag}, wrs, tsw, 0, 16);
  HaloMsg hm;
  SB();
  if (L1) KSLP(1, SLP_XB1); else gate(tmax(t9, t5));
  ST(!L1, 12);
  x_take<1, 0, false>(xt, smem, wrs, XIN, 32 + rb, a, lane, sent, [&]() __attribute__((always_inline)) { if (L1) ld_wo(WOE, WOE + WX); });
  ST(!L1, 13);
  if (L1) SB();
  lds_barrier();
  ST(!L1, 15);
  if (L1) KSLP(3, SLP_H1); else KSLP(4, SLP_H2);
  halo_ld(hm, wrs, ho);
  if (L1) { SB(); ld_wo(WOE + WX, WOE + WX + WA); }
  u32x2 ts;
  u32 t7 = L1 ? t5 : 0u, tq = 0;
  if (!L1) {
    t7 = (u32)__builtin_amdgcn_s_memrealtime();
    tq = KADD(6, now_(), DQ2);
    __builtin_amdgcn_raw_buffer_store_b64((u32x2){t7, tag}, wrs, tsw, 0, 16);
  }
  SB();
  attention(ac, qp, hm, ho, rb == 0 ? 0u : L1 ? mark : tag, wrs, oo, L1 ? 3 : 11, [&]() __attribute__((always_inline)) { if (L1) SB(); }, [&]() __attribute__((always_inline)) {
    if (L1) {
      asm volatile("buffer_load_dwordx2 %0, %1, %2, 0 offen sc1" : "=&{v[250:251]}"(ts) : "v"(tso), "s"(wrs) : "memory");
      ld_wo(WOE + WX + WA, WOE + WX + WA + WM);
    }
  });
  const u32 to = L1 ? KADD(5, now_(), DO1) : KADD(21, now_(), DO2);
  ST(!L1, 16);
  if (L1) __builtin_amdgcn_raw_buffer_store_b32(tag, wrs, (bid | t) == 0 ? OFF_EP : WSB, 0, 16);
  if (L1) ld_wo(WOE + WX + WA + WM, 16);
  res_ld<L1>(resb, smem, L_TB, hb, a, lane);
  cnt_add(smem, C_OB, lane);
  WoPre wp, wq;
  u32 qa[4];
#pragma unroll
  for (int e = 0; e < 4; ++e) qa[e] = opaque((u32)(L_TA + ((a + e) & 3) * 4096 + lane * 16));
  wo_pre<L1>(wp, resa, ws, 0, rb, hb, a, lane);
  wo_pre<L1>(wq, resb, ws, 1, rb, hb, a, lane);
  const u32 sa = opaque((u32)(L_TA + a * 4096 + lane * 64 + (lane & 14) * 4 + 4));
  SB();
  int d;
  auto lead = [&]() __attribute__((always_inline)) {
    asm volatile("v_cmp_eq_u32 vcc, %3, v251\n\tv_subrev_u32 %0, %2, v250\n\tv_min_i32 %0, 0x96, %0\n\tv_cndmask_b32 %0, 64, %0, vcc\n\t"
                 "s_nop 1\n\tv_max_i32_dpp %0, %0, %0 quad_perm:[1,0,3,2] row_mask:0xf bank_mask:0xf\n\ts_nop 1\n\tv_max_i32_dpp %0, %0, %0 quad_perm:[2,3,0,1] row_mask:0xf bank_mask:0xf\n\t"
                 "s_nop 1\n\tv_max_i32_dpp %0, %0, %0 row_half_mirror row_mask:0xf bank_mask:0xf\n\tv_max_i32 %0, 0, %0\n\tv_mul_u32_u24 %0, 20, %0"
                 : "=&v"(d), "+{v[250:251]}"(ts) : "s"(t7), "s"(tag) : "vcc", "memory");
  };
  if (L1) {
    asm volatile("s_waitcnt vmcnt(%0)" :: "n"(18 - WOE - WX - WA) : "memory");
    lead();
    gate(tmax(to, __builtin_amdgcn_readfirstlane(c5 + d)));
  } else gate(tmax(tq, to));
  ST(!L1, 17);
  if (!L1) asm volatile("buffer_load_dwordx2 %0, %1, %2, 0 offen sc1" : "=&{v[250:251]}"(ts) : "v"(tso), "s"(wrs) : "memory");
  o_get(wrs, lane * 16, og + a * 4096, og + 16384 + a * 4096, L_TA + a * 4096, sa, mark, L1 ? 21 : 23);
  if (!L1) {
    lead();
    *(int*)(smem + L_MI + M_DL) = d;
    cnt_add(smem, C_OV, lane);
  }
  u32 ob = 0;
  int ob0 = og + MB_ + a * 4096, ob1 = ob0 + 16384;
  if (L1) asm volatile("" : "+s"(ob0), "+s"(ob1));
  asm volatile("s_waitcnt vmcnt(0)" ::: "memory");
  f32x4 acc[2];
  wo_mma2<L1>(acc, smem, L_TA, wrs, og, a, lane, wof, L1 ? 4 : 12, wp.r, qa, mark, [&]() __attribute__((always_inline)) { if (L1) ob = *(const volatile LAS u32*)(size_t)(u32)(L_MI + C_OB); });
  ST(!L1, 18);
  wo_epi<L1>(acc, wp, smem, wrs, ws, out, 0, lane, ONES);
  if (L1) {
    cnt_add(smem, C_X2, lane);
    if (__builtin_expect((u32)__builtin_amdgcn_readfirstlane(ob) < 8u, 0)) cnt_spin(smem, C_OB, 8);
    dma4k(wrs, lane * 16, ob0, L_TB + a * 4096);
    dma4k(wrs, lane * 16, ob1, L_TB + 16384 + a * 4096);
    a_conv_pre(ck, a, lane);
    ST(0, 19);
    asm volatile("s_waitcnt vmcnt(0)" ::: "memory");
    wo_mma2<L1>(acc, smem, L_TB, wrs, og + MB_, a, lane, wof, 8, wq.r, qa, mark, []() {});
    wo_epi<L1>(acc, wq, smem, wrs, ws, out, 1, lane, ONES);
  } else wo_last(acc, smem, wrs, og + MB_, a, lane, wof, wq.r, qa, mark, [&]() __attribute__((always_inline)) { wo_epi<L1>(acc, wq, smem, wrs, ws, out, 1, lane, ONES); });
  ST(!L1, 20); if (L1) ST(1, 1);
}

#define WREGS(X) \
  X(0, 76, 77, 78, 79) X(1, 80, 81, 82, 83) X(2, 84, 85, 86, 87) X(3, 88, 89, 90, 91) X(4, 92, 93, 94, 95) X(5, 96, 97, 98, 99) \
  X(6, 100, 101, 102, 103) X(7, 104, 105, 106, 107) X(8, 108, 109, 110, 111) X(9, 112, 113, 114, 115) X(10, 116, 117, 118, 119) X(11, 120, 121, 122, 123) \
  X(12, 124, 125, 126, 127) X(13, 128, 129, 130, 131) X(14, 132, 133, 134, 135) X(15, 136, 137, 138, 139) X(16, 140, 141, 142, 143) X(17, 144, 145, 146, 147) \
  X(18, 148, 149, 150, 151) X(19, 152, 153, 154, 155) X(20, 156, 157, 158, 159) X(21, 160, 161, 162, 163) X(22, 164, 165, 166, 167) X(23, 168, 169, 170, 171) \
  X(24, 172, 173, 174, 175) X(25, 176, 177, 178, 179) X(26, 180, 181, 182, 183) X(27, 184, 185, 186, 187) X(28, 188, 189, 190, 191) X(29, 192, 193, 194, 195) \
  X(30, 196, 197, 198, 199) X(31, 200, 201, 202, 203) X(32, 204, 205, 206, 207) X(33, 208, 209, 210, 211) X(34, 212, 213, 214, 215) X(35, 216, 217, 218, 219) \
  X(36, 220, 221, 222, 223) X(37, 224, 225, 226, 227) X(38, 228, 229, 230, 231) X(39, 232, 233, 234, 235) X(40, 236, 237, 238, 239) X(41, 240, 241, 242, 243) \
  X(42, 244, 245, 246, 247) X(43, 248, 249, 250, 251) X(44, 252, 253, 254, 255) X(45, 64, 65, 66, 67) X(46, 68, 69, 70, 71) X(47, 72, 73, 74, 75)
__device__ __forceinline__ void w_dma(rsrc_t r, int voff, int vof2, int soff, int lds) {
  asm volatile("s_mov_b32 m0, %3\n\ts_nop 0\n\tbuffer_load_dwordx4 %0, %1, %2 offen lds\n\t"
               "s_add_u32 m0, m0, 3072\n\ts_nop 0\n\tbuffer_load_dwordx4 %0, %1, %2 offen offset:1024 lds\n\t"
               "s_add_u32 m0, m0, 3072\n\ts_nop 0\n\tbuffer_load_dwordx4 %4, %1, %2 offen offset:2048 lds"
               :: "v"(voff), "s"(r), "s"(soff), "s"(lds), "v"(vof2) : "memory", "scc");
}
template <int I>
__device__ __forceinline__ void w_ld(bf16x8& r, int vo, rsrc_t rs) {
#define X(n_, a_, b_, c_, d_) if constexpr (I == n_) asm volatile("buffer_load_dwordx4 %0, %1, %2, %3 offen offset:%4 sc1" : "={v[" #a_ ":" #d_ "]}"(r) : "v"(vo), "s"(rs), "s"(OFF_WQ + (I / 3) * 98304), "n"((I % 3) * 1024) : "memory");
  WREGS(X)
#undef X
}
template <int I, int O>
__device__ __forceinline__ void w_get(bf16x8& r, u32 a) {
#define X(n_, a_, b_, c_, d_) if constexpr (I == n_) asm volatile("ds_read_b64_tr_b16 v[" #a_ ":" #b_ "], %1 offset:%2\n\tds_read_b64_tr_b16 v[" #c_ ":" #d_ "], %1 offset:%3" : "=&{v[" #a_ ":" #d_ "]}"(r) : "v"(a), "n"(O), "n"(O + 2048) : "memory");
  WREGS(X)
#undef X
}
template <int I>
__device__ __forceinline__ void w_pin(const bf16x8& r) {
#define X(n_, a_, b_, c_, d_) if constexpr (I == n_) asm volatile("" :: "{v[" #a_ ":" #d_ "]}"(r));
  WREGS(X)
#undef X
}
template <int I>
__device__ __forceinline__ void w_sum(f32x4& d, const bf16x8& one, u32 idx, const bf16x8 (&wf)[48]) {
  w_pin<I>(wf[I]); w_pin<(I + 1) % 48>(wf[(I + 1) % 48]);
  asm volatile("v_smfmac_f32_16x16x64_bf16 %0, %1, v[%3:%3+7], %2" : "+v"(d) : "v"(one), "v"(idx), "n"(I < 45 ? 76 + 4 * I : 64 + 4 * (I - 45)));
  w_pin<I>(wf[I]); w_pin<(I + 1) % 48>(wf[(I + 1) % 48]);
}
struct GPre { u32 ap, l4, fr, fq, sp, wx, mo, oo, tg, ly; };
template <bool L1>
__device__ __forceinline__ void g_pre(GPre& P, int g, u32 tag) {
  const int lane = lane_id(), bid = BIDX;
  const int rb = RB_OF(bid), hb = HB_OF(bid);
  P.mo = (u32)KVO(L1, (rb + 1) & 31, hb * 4 + g);
  P.oo = (u32)((L1 ? OFF_O1 : OFF_O2) + MB_ + ((rb * 32 + hb * 2 + (g >> 1)) * 64 + 32 * (g & 1)) * 16);
  P.tg = L1 ? x0_mark(tag) : tag;
  P.ly = L1 ? 1 : 2;
  asm volatile("" : "+{s60}"(P.mo), "+{s61}"(P.oo), "+{s62}"(P.tg), "+{s63}"(P.ly));
  P.ap = (u32)(L_TA + lane * 8);
  P.l4 = (u32)(lane * 4);
  P.fr = (u32)(L_FS + g * 768 + (lane & 15) * 4);
  P.fq = (u32)(L_FS + g * 768 + (lane >> 4) * 16);
  P.sp = (u32)(L_SP + (lane & 31) * 16);
  P.wx = (u32)(L_WX + g * 3072 + lane * 16);
  asm volatile("" : "+{v0}"(P.ap), "+{v1}"(P.l4), "+{v2}"(P.fr), "+{v3}"(P.fq), "+{v4}"(P.sp), "+{v5}"(P.wx));
}
template <bool L1, class F>
__device__ __forceinline__ void g_blayer(char* smem, rsrc_t wrs, int g, u32 tag, bf16x8 (&wf)[48], GPre& P, F mid) {
  if (!L1) ST(1, 10);
  auto pin = [&](auto I_) __attribute__((always_inline)) { w_pin<decltype(I_)::value>(wf[decltype(I_)::value]); };
  static_for<0, 45>(pin);
#define G_OPS : "+{v0}"(P.ap), "+{v1}"(P.l4), "+{v2}"(P.fr), "+{v3}"(P.fq), "+{v4}"(P.sp), "+{v5}"(P.wx) \
  : "{s[56:59]}"(wrs), "{s60}"(P.mo), "{s61}"(P.oo), "{s62}"(P.tg), "{s63}"(P.ly), "{s64}"(L_QB + g * 3072), "{s65}"(L_FS + g * 768), "{s66}"(L_WX + g * 3072) \
  : "memory", "scc", "vcc", "v6", "v7", "v8", "v9", "v10", "v11", "v12", "v13", "v14", "v15", "v16", "v17", "v18", "v19", "v20", "v21", "v22", "v23", "v24", "v25", "v26", "v27", "v28", "v29", "v30", "v31", "v32", "v33", "v34", "v35", "v36", "v37", "v38", "v39", "v40", "v41", "v42", "v43", "v44", "v45", "v46", "v47", "v48", "v49", "v50", "v51", "v52", "v53", "v54", "v55", "v56", "v57", "v58", "v59", "v60", "v61", "v62", "v63", "v64", "v65", "v66", "v67", "v68", "v69", "v70", "v71", "v72", "v73", "v74", "v75", "s54", "s55", "s67", "s68", "s69", "s70", "s71", "s72", "s73", "s74", "s75", "s76", "s77"
  if constexpr (L1) asm volatile(".Lg_body:\n\t"
  "ds_read_b128 v[24:27], v5 offset:1024\n\tds_read_b128 v[72:75], v5 offset:2048\n\tds_read_b128 v[20:23], v5 offset:0\n\t"
  "s_waitcnt lgkmcnt(3)\n\ts_barrier\n\t" GST(11)
  "ds_read_b64_tr_b16 v[60:61], v0 offset:31232\n\tds_read_b64_tr_b16 v[62:63], v0 offset:32256\n\tds_read_b64_tr_b16 v[16:17], v0 offset:512\n\tds_read_b64_tr_b16 v[18:19], v0 offset:1536\n\t"
  "ds_read_b128 v[28:31], v4\n\tds_read_b128 v[44:47], v4 offset:768\n\tds_read_b64_tr_b16 v[36:37], v0 offset:2560\n\tds_read_b64_tr_b16 v[38:39], v0 offset:3584\n\t"
  "ds_read_b64_tr_b16 v[40:41], v0 offset:4608\n\tds_read_b64_tr_b16 v[42:43], v0 offset:5632\n\ts_mov_b32 s68, 0xaaaaaaaa\n\ts_mov_b32 s69, 0xaaaaaaaa\n\t"
  "s_mov_b32 s74, 0xfff0ffff\n\ts_mov_b32 s75, 0xf000ff00\n\ts_mov_b32 s76, 0xffc0fffc\n\ts_mov_b32 s77, 0xc000fc00\n\t"
  "s_mov_b32 s67, 0x0c0c0100\n\ts_mov_b32 s55, 0x0c0c0302\n\ts_waitcnt lgkmcnt(8)\n\t"
  "v_mfma_f32_16x16x32_bf16 v[12:15], v[24:27], v[60:63], 0\n\tds_read_b64_tr_b16 v[68:69], v0 offset:6656\n\t"
  "v_mfma_f32_16x16x32_bf16 v[64:67], v[60:63], v[72:75], 0\n\tds_read_b64_tr_b16 v[70:71], v0 offset:7680\n\t"
  "v_mfma_f32_16x16x32_bf16 v[72:75], v[20:23], v[60:63], 0\n\ts_waitcnt lgkmcnt(2)\n\t"
  "v_mfma_f32_16x16x32_bf16 v[12:15], v[80:83], v[16:19], v[12:15]\n\tds_read_b64_tr_b16 v[4:5], v0 offset:8704\n\tv_add_f32 v28, v28, v29\n\t"
  "v_mfma_f32_16x16x32_bf16 v[64:67], v[16:19], v[84:87], v[64:67]\n\tds_read_b64_tr_b16 v[6:7], v0 offset:9728\n\tv_add_f32 v30, v30, v31\n\t"
  "v_mfma_f32_16x16x32_bf16 v[72:75], v[76:79], v[16:19], v[72:75]\n\tv_add_f32 v44, v44, v45\n\tv_add_f32 v28, v28, v30\n\t"
  "v_mfma_f32_16x16x32_bf16 v[12:15], v[92:95], v[36:39], v[12:15]\n\tds_read_b64_tr_b16 v[32:33], v0 offset:10752\n\tv_add_f32 v46, v46, v47\n\t"
  "v_mfma_f32_16x16x32_bf16 v[64:67], v[36:39], v[96:99], v[64:67]\n\tds_read_b64_tr_b16 v[34:35], v0 offset:11776\n\tv_mul_f32 v29, 0x3b000000, v28\n\t"
  "v_mfma_f32_16x16x32_bf16 v[72:75], v[88:91], v[36:39], v[72:75]\n\tv_add_f32 v44, v44, v46\n\tv_mul_f32_e64 v30, -v29, v29\n\t"
  "v_mfma_f32_16x16x32_bf16 v[12:15], v[104:107], v[40:43], v[12:15]\n\tds_read_b64_tr_b16 v[36:37], v0 offset:12800\n\tv_fmac_f32 v30, 0x3b000000, v44\n\t"
  "v_mfma_f32_16x16x32_bf16 v[64:67], v[40:43], v[108:111], v[64:67]\n\tds_read_b64_tr_b16 v[38:39], v0 offset:13824\n\tv_max_f32 v30, 0, v30\n\t"
  "v_mfma_f32_16x16x32_bf16 v[72:75], v[100:103], v[40:43], v[72:75]\n\tv_add_f32 v30, 0x3727c5ac, v30\n\ts_waitcnt lgkmcnt(4)\n\t"
  "v_mfma_f32_16x16x32_bf16 v[12:15], v[116:119], v[68:71], v[12:15]\n\tds_read_b64_tr_b16 v[60:61], v0 offset:14848\n\tv_add_u32 v31, s65, v1\n\t"
  "v_mfma_f32_16x16x32_bf16 v[64:67], v[68:71], v[120:123], v[64:67]\n\tds_read_b64_tr_b16 v[62:63], v0 offset:15872\n\t"
  "v_mfma_f32_16x16x32_bf16 v[72:75], v[112:115], v[68:71], v[72:75]\n\tv_rsq_f32 v30, v30\n\t"
  "v_mfma_f32_16x16x32_bf16 v[12:15], v[128:131], v[4:7], v[12:15]\n\tds_read_b64_tr_b16 v[52:53], v0 offset:16896\n\tds_write2st64_b32 v31, v30, v29 offset1:1\n\t"
  "v_mfma_f32_16x16x32_bf16 v[64:67], v[4:7], v[132:135], v[64:67]\n\tds_read_b64_tr_b16 v[54:55], v0 offset:17920\n\tds_read_b128 v[8:11], v3 offset:576\n\t"
  "v_mfma_f32_16x16x32_bf16 v[72:75], v[124:127], v[4:7], v[72:75]\n\tds_read2_b32 v[26:27], v2 offset0:64 offset1:80\n\ts_waitcnt lgkmcnt(7)\n\t"
  "v_mfma_f32_16x16x32_bf16 v[12:15], v[140:143], v[32:35], v[12:15]\n\tds_read_b64_tr_b16 v[16:17], v0 offset:18944\n\tds_read_b128 v[56:59], v3 offset:256\n\t"
  "v_mfma_f32_16x16x32_bf16 v[64:67], v[32:35], v[144:147], v[64:67]\n\tds_read_b64_tr_b16 v[18:19], v0 offset:19968\n\tds_read_b128 v[20:23], v3 offset:512\n\t"
  "v_mfma_f32_16x16x32_bf16 v[72:75], v[136:139], v[32:35], v[72:75]\n\tds_read_b32 v25, v2 offset:640\n\t"
  "v_mfma_f32_16x16x32_bf16 v[12:15], v[152:155], v[36:39], v[12:15]\n\tds_read_b64_tr_b16 v[48:49], v0 offset:20992\n\t"
  "v_mfma_f32_16x16x32_bf16 v[64:67], v[36:39], v[156:159], v[64:67]\n\tds_read_b64_tr_b16 v[50:51], v0 offset:22016\n\t"
  "v_mfma_f32_16x16x32_bf16 v[72:75], v[148:151], v[36:39], v[72:75]\n\ts_waitcnt lgkmcnt(9)\n\t"
  "v_mfma_f32_16x16x32_bf16 v[12:15], v[164:167], v[60:63], v[12:15]\n\tds_read_b64_tr_b16 v[44:45], v0 offset:23040\n\ts_waitcnt lgkmcnt(8)\n\t"
  "v_mfma_f32_16x16x32_bf16 v[64:67], v[60:63], v[168:171], v[64:67]\n\tds_read_b64_tr_b16 v[46:47], v0 offset:24064\n\tv_mul_f32_e64 v40, -v26, v8\n\t"
  "v_mfma_f32_16x16x32_bf16 v[72:75], v[160:163], v[60:63], v[72:75]\n\tv_mul_f32_e64 v41, -v26, v9\n\tv_mul_f32_e64 v42, -v26, v10\n\t"
  "v_mfma_f32_16x16x32_bf16 v[12:15], v[176:179], v[52:55], v[12:15]\n\tds_read_b64_tr_b16 v[60:61], v0 offset:25088\n\tv_mul_f32_e64 v43, -v26, v11\n\t"
  "v_mfma_f32_16x16x32_bf16 v[64:67], v[52:55], v[180:183], v[64:67]\n\tds_read_b64_tr_b16 v[62:63], v0 offset:26112\n\ts_waitcnt lgkmcnt(5)\n\t"
  "v_mfma_f32_16x16x32_bf16 v[72:75], v[172:175], v[52:55], v[72:75]\n\tv_mul_f32_e64 v4, -v56, v25\n\tv_mul_f32_e64 v5, -v57, v25\n\t"
  "v_mfma_f32_16x16x32_bf16 v[12:15], v[188:191], v[16:19], v[12:15]\n\t"
  GST(20)
  "ds_read_b64_tr_b16 v[36:37], v0 offset:27136\n\tds_read2_b32 v[30:31], v2 offset0:0 offset1:16\n\t"
  "v_mfma_f32_16x16x32_bf16 v[64:67], v[16:19], v[192:195], v[64:67]\n\tds_read_b64_tr_b16 v[38:39], v0 offset:28160\n\tv_mul_f32_e64 v6, -v58, v25\n\t"
  "v_mfma_f32_16x16x32_bf16 v[72:75], v[184:187], v[16:19], v[72:75]\n\tv_mul_f32_e64 v7, -v59, v25\n\ts_waitcnt lgkmcnt(5)\n\t"
  "v_mfma_f32_16x16x32_bf16 v[12:15], v[200:203], v[48:51], v[12:15]\n\tds_read_b64_tr_b16 v[16:17], v0 offset:29184\n\tv_mul_f32_e64 v56, -v26, v20\n\t"
  "v_mfma_f32_16x16x32_bf16 v[64:67], v[48:51], v[204:207], v[64:67]\n\tds_read_b64_tr_b16 v[18:19], v0 offset:30208\n\tv_mul_f32_e64 v57, -v26, v21\n\t"
  "v_mfma_f32_16x16x32_bf16 v[72:75], v[196:199], v[48:51], v[72:75]\n\tv_mul_f32_e64 v58, -v26, v22\n\tv_mul_f32_e64 v59, -v26, v23\n\t"
  "v_mfma_f32_16x16x32_bf16 v[12:15], v[212:215], v[44:47], v[12:15]\n\tds_read_b64_tr_b16 v[48:49], v0 offset:0\n\t"
  "v_mfma_f32_16x16x32_bf16 v[64:67], v[44:47], v[216:219], v[64:67]\n\tds_read_b64_tr_b16 v[50:51], v0 offset:1024\n\t"
  "v_mfma_f32_16x16x32_bf16 v[72:75], v[208:211], v[44:47], v[72:75]\n\ts_waitcnt lgkmcnt(4)\n\t"
  "v_mfma_f32_16x16x32_bf16 v[12:15], v[224:227], v[60:63], v[12:15]\n\tds_read_b64_tr_b16 v[44:45], v0 offset:2048\n\t"
  "v_mfma_f32_16x16x32_bf16 v[64:67], v[60:63], v[228:231], v[64:67]\n\tds_read_b64_tr_b16 v[46:47], v0 offset:3072\n\tv_lshl_add_u32 v26, v1, 2, s60\n\t"
  "v_mfma_f32_16x16x32_bf16 v[72:75], v[220:223], v[60:63], v[72:75]\n\t"
  "v_mfma_f32_16x16x32_bf16 v[12:15], v[236:239], v[36:39], v[12:15]\n\tds_read_b64_tr_b16 v[52:53], v0 offset:4096\n\tds_read_b128 v[32:35], v3 offset:320\n\t"
  "v_mfma_f32_16x16x32_bf16 v[64:67], v[36:39], v[240:243], v[64:67]\n\tds_read_b64_tr_b16 v[54:55], v0 offset:5120\n\t"
  "v_mfma_f32_16x16x32_bf16 v[72:75], v[232:235], v[36:39], v[72:75]\n\tds_read_b128 v[60:63], v3 offset:64\n\ts_waitcnt lgkmcnt(6)\n\t"
  "v_mfma_f32_16x16x32_bf16 v[12:15], v[248:251], v[16:19], v[12:15]\n\tds_read_b64_tr_b16 v[68:69], v0 offset:6144\n\t"
  "v_mfma_f32_16x16x32_bf16 v[64:67], v[16:19], v[252:255], v[64:67]\n\tds_read_b64_tr_b16 v[70:71], v0 offset:7168\n\t"
  "v_mfma_f32_16x16x32_bf16 v[72:75], v[244:247], v[16:19], v[72:75]\n\tv_fma_f32 v12, -v27, v8, v12\n\tv_fma_f32 v13, -v27, v9, v13\n\t"
  "v_mfma_f32_16x16x32_bf16 v[40:43], v[80:83], v[48:51], v[40:43]\n\tds_read_b64_tr_b16 v[36:37], v0 offset:8192\n\tv_fma_f32 v14, -v27, v10, v14\n\t"
  "v_mfma_f32_16x16x32_bf16 v[4:7], v[48:51], v[84:87], v[4:7]\n\tds_read_b64_tr_b16 v[38:39], v0 offset:9216\n\tv_fma_f32 v15, -v27, v11, v15\n\t"
  "v_mfma_f32_16x16x32_bf16 v[56:59], v[76:79], v[48:51], v[56:59]\n\tv_mul_f32 v12, v12, v31\n\ts_waitcnt lgkmcnt(5)\n\t"
  "v_mfma_f32_16x16x32_bf16 v[40:43], v[92:95], v[44:47], v[40:43]\n\tds_read_b64_tr_b16 v[48:49], v0 offset:10240\n\tv_mul_f32 v13, v13, v31\n\t"
  "v_mfma_f32_16x16x32_bf16 v[4:7], v[44:47], v[96:99], v[4:7]\n\tds_read_b64_tr_b16 v[50:51], v0 offset:11264\n\tv_mul_f32 v14, v14, v31\n\t"
  "v_mfma_f32_16x16x32_bf16 v[56:59], v[88:91], v[44:47], v[56:59]\n\t"
  GST(17)
  "v_mul_f32 v15, v15, v31\n\tv_cvt_pk_bf16_f32 v12, v12, v13\n\t"
  "v_mfma_f32_16x16x32_bf16 v[40:43], v[104:107], v[52:55], v[40:43]\n\tds_read_b64_tr_b16 v[44:45], v0 offset:12288\n\tv_cvt_pk_bf16_f32 v14, v14, v15\n\t"
  "v_mfma_f32_16x16x32_bf16 v[4:7], v[52:55], v[108:111], v[4:7]\n\tds_read_b64_tr_b16 v[46:47], v0 offset:13312\n\tv_mov_b32 v13, s62\n\t"
  "v_mfma_f32_16x16x32_bf16 v[56:59], v[100:103], v[52:55], v[56:59]\n\tv_mov_b32 v15, s62\n\ts_waitcnt lgkmcnt(4)\n\t"
  "v_mfma_f32_16x16x32_bf16 v[40:43], v[116:119], v[68:71], v[40:43]\n\tds_read_b64_tr_b16 v[16:17], v0 offset:14336\n\tv_fma_f32 v64, -v32, v25, v64\n\t"
  "v_mfma_f32_16x16x32_bf16 v[4:7], v[68:71], v[120:123], v[4:7]\n\tds_read_b64_tr_b16 v[18:19], v0 offset:15360\n\tv_fma_f32 v65, -v33, v25, v65\n\t"
  "v_mfma_f32_16x16x32_bf16 v[56:59], v[112:115], v[68:71], v[56:59]\n\tv_fma_f32 v66, -v34, v25, v66\n\tv_fma_f32 v67, -v35, v25, v67\n\t"
  "v_mfma_f32_16x16x32_bf16 v[40:43], v[128:131], v[36:39], v[40:43]\n\tds_read_b64_tr_b16 v[68:69], v0 offset:16384\n\tv_mul_f32 v64, v64, v60\n\t"
  "v_mfma_f32_16x16x32_bf16 v[4:7], v[36:39], v[132:135], v[4:7]\n\tds_read_b64_tr_b16 v[70:71], v0 offset:17408\n\tv_mul_f32 v65, v65, v61\n\t"
  "v_mfma_f32_16x16x32_bf16 v[56:59], v[124:127], v[36:39], v[56:59]\n\tv_mul_f32 v66, v66, v62\n\ts_waitcnt lgkmcnt(4)\n\t"
  "v_mfma_f32_16x16x32_bf16 v[40:43], v[140:143], v[48:51], v[40:43]\n\tds_read_b64_tr_b16 v[36:37], v0 offset:18432\n\tv_mul_f32 v67, v67, v63\n\t"
  "v_mfma_f32_16x16x32_bf16 v[4:7], v[48:51], v[144:147], v[4:7]\n\tds_read_b64_tr_b16 v[38:39], v0 offset:19456\n\tv_cvt_pk_bf16_f32 v64, v64, v65\n\t"
  "v_mfma_f32_16x16x32_bf16 v[56:59], v[136:139], v[48:51], v[56:59]\n\tv_cvt_pk_bf16_f32 v66, v66, v67\n\tv_mov_b32 v65, s62\n\t"
  "v_mfma_f32_16x16x32_bf16 v[40:43], v[152:155], v[44:47], v[40:43]\n\tds_read_b64_tr_b16 v[32:33], v0 offset:20480\n\tv_mov_b32 v67, s62\n\t"
  "v_mfma_f32_16x16x32_bf16 v[4:7], v[44:47], v[156:159], v[4:7]\n\tds_read_b64_tr_b16 v[34:35], v0 offset:21504\n\tv_fma_f32 v72, -v27, v20, v72\n\t"
  "v_mfma_f32_16x16x32_bf16 v[56:59], v[148:151], v[44:47], v[56:59]\n\tv_add_u32 v24, s64, v1\n\ts_waitcnt lgkmcnt(4)\n\t"
  "v_mfma_f32_16x16x32_bf16 v[40:43], v[164:167], v[16:19], v[40:43]\n\tds_read_b64_tr_b16 v[60:61], v0 offset:22528\n\tv_fma_f32 v73, -v27, v21, v73\n\t"
  "v_mfma_f32_16x16x32_bf16 v[4:7], v[16:19], v[168:171], v[4:7]\n\tds_read_b64_tr_b16 v[62:63], v0 offset:23552\n\tv_fma_f32 v74, -v27, v22, v74\n\t"
  "v_mfma_f32_16x16x32_bf16 v[56:59], v[160:163], v[16:19], v[56:59]\n\tv_fma_f32 v75, -v27, v23, v75\n\tv_mul_f32 v72, v72, v31\n\t"
  "v_mfma_f32_16x16x32_bf16 v[40:43], v[176:179], v[68:71], v[40:43]\n\tds_read_b64_tr_b16 v[44:45], v0 offset:24576\n\tv_mul_f32 v73, v73, v31\n\t"
  "v_mfma_f32_16x16x32_bf16 v[4:7], v[68:71], v[180:183], v[4:7]\n\tds_read_b64_tr_b16 v[46:47], v0 offset:25600\n\tv_mul_f32 v74, v74, v31\n\t"
  "v_mfma_f32_16x16x32_bf16 v[56:59], v[172:175], v[68:71], v[56:59]\n\tv_mul_f32 v75, v75, v31\n\ts_waitcnt lgkmcnt(4)\n\t"
  "v_mfma_f32_16x16x32_bf16 v[40:43], v[188:191], v[36:39], v[40:43]\n\tds_read_b64_tr_b16 v[68:69], v0 offset:26624\n\tv_cvt_pk_bf16_f32 v72, v72, v73\n\t"
  "v_mfma_f32_16x16x32_bf16 v[4:7], v[36:39], v[192:195], v[4:7]\n\tds_read_b64_tr_b16 v[70:71], v0 offset:27648\n\tv_cvt_pk_bf16_f32 v74, v74, v75\n\t"
  "v_mfma_f32_16x16x32_bf16 v[56:59], v[184:187], v[36:39], v[56:59]\n\t"
  "v_mfma_f32_16x16x32_bf16 v[40:43], v[200:203], v[32:35], v[40:43]\n\t"
  GST(13)
  "ds_read_b64_tr_b16 v[52:53], v0 offset:28672\n\t"
  "v_mfma_f32_16x16x32_bf16 v[4:7], v[32:35], v[204:207], v[4:7]\n\ts_barrier\n\tds_read_b64_tr_b16 v[54:55], v0 offset:29696\n\t"
  "v_mfma_f32_16x16x32_bf16 v[56:59], v[196:199], v[32:35], v[56:59]\n\tbuffer_store_dwordx4 v[12:15], v26, s[56:59], 0 offen offset:0 sc1\n\ts_waitcnt lgkmcnt(4)\n\t"
  "v_mfma_f32_16x16x32_bf16 v[40:43], v[212:215], v[60:63], v[40:43]\n\tds_read_b64_tr_b16 v[36:37], v0 offset:30720\n\tbuffer_store_dwordx4 v[64:67], v26, s[56:59], 0 offen offset:1024 sc1\n\t"
  "v_mfma_f32_16x16x32_bf16 v[4:7], v[60:63], v[216:219], v[4:7]\n\tds_read_b64_tr_b16 v[38:39], v0 offset:31744\n\tv_lshl_add_u32 v13, v1, 2, s66\n\tv_add_u32 v0, 0x8000, v0\n\t"
  "v_mfma_f32_16x16x32_bf16 v[56:59], v[208:211], v[60:63], v[56:59]\n\tds_read_b128 v[26:29], v13 offset:1024\n\tds_read_b128 v[60:63], v13 offset:2048\n\t"
  "v_mfma_f32_16x16x32_bf16 v[40:43], v[224:227], v[44:47], v[40:43]\n\tds_read_b64_tr_b16 v[32:33], v0 offset:0\n\tds_read_b128 v[16:19], v13 offset:0\n\t"
  "v_mfma_f32_16x16x32_bf16 v[4:7], v[44:47], v[228:231], v[4:7]\n\tds_read_b64_tr_b16 v[34:35], v0 offset:1024\n\tv_min_u32 v73, 188, v1\n\t"
  "v_mfma_f32_16x16x32_bf16 v[56:59], v[220:223], v[44:47], v[56:59]\n\tds_read_b128 v[48:51], v3\n\ts_waitcnt lgkmcnt(8)\n\t"
  "v_mfma_f32_16x16x32_bf16 v[40:43], v[236:239], v[68:71], v[40:43]\n\tds_read_b64_tr_b16 v[44:45], v0 offset:2048\n\tv_lshlrev_b32 v73, 2, v73\n\t"
  "v_mfma_f32_16x16x32_bf16 v[4:7], v[68:71], v[240:243], v[4:7]\n\tds_read_b64_tr_b16 v[46:47], v0 offset:3072\n\tv_add_u32 v73, 0x22c00, v73\n\t"
  "v_mfma_f32_16x16x32_bf16 v[56:59], v[232:235], v[68:71], v[56:59]\n\t"
  "v_mfma_f32_16x16x32_bf16 v[40:43], v[248:251], v[52:55], v[40:43]\n\tds_read_b64_tr_b16 v[68:69], v0 offset:8192\n\t"
  "v_mfma_f32_16x16x32_bf16 v[4:7], v[52:55], v[252:255], v[4:7]\n\tds_read_b64_tr_b16 v[70:71], v0 offset:9216\n\t"
  "v_mfma_f32_16x16x32_bf16 v[56:59], v[244:247], v[52:55], v[56:59]\n\ts_waitcnt lgkmcnt(5)\n\t"
  "v_mfma_f32_16x16x32_bf16 v[40:43], v[26:29], v[36:39], v[40:43]\n\tds_read_b64_tr_b16 v[26:27], v0 offset:10240\n\t"
  "v_mfma_f32_16x16x32_bf16 v[4:7], v[36:39], v[60:63], v[4:7]\n\tds_read_b64_tr_b16 v[28:29], v0 offset:11264\n\t"
  "v_mfma_f32_16x16x32_bf16 v[56:59], v[16:19], v[36:39], v[56:59]\n\t"
  GST(12)
  "v_mul_f32 v40, v40, v30\n\tv_mul_f32 v41, v41, v30\n\t"
  "v_mfma_f32_16x16x32_bf16 v[60:63], v[80:83], v[32:35], 0\n\tds_read_b64_tr_b16 v[36:37], v0 offset:16384\n\tv_mul_f32 v42, v42, v30\n\t"
  "v_mfma_f32_16x16x32_bf16 v[16:19], v[32:35], v[84:87], 0\n\tds_read_b64_tr_b16 v[38:39], v0 offset:17408\n\tv_mul_f32 v43, v43, v30\n\t"
  "v_mfma_f32_16x16x32_bf16 v[52:55], v[76:79], v[32:35], 0\n\tv_cvt_pk_bf16_f32 v40, v40, v41\n\ts_waitcnt lgkmcnt(4)\n\t"
  "v_mfma_f32_16x16x32_bf16 v[60:63], v[92:95], v[44:47], v[60:63]\n\tds_read_b64_tr_b16 v[32:33], v0 offset:18432\n\tv_cvt_pk_bf16_f32 v42, v42, v43\n\t"
  "v_mfma_f32_16x16x32_bf16 v[16:19], v[44:47], v[96:99], v[16:19]\n\tds_read_b64_tr_b16 v[34:35], v0 offset:19456\n\tv_mul_f32 v56, v56, v30\n\t"
  "v_mfma_f32_16x16x32_bf16 v[52:55], v[88:91], v[44:47], v[52:55]\n\tv_mul_f32 v57, v57, v30\n\tv_mul_f32 v58, v58, v30\n\t"
  "v_mfma_f32_16x16x32_bf16 v[60:63], v[128:131], v[68:71], v[60:63]\n\tds_read_b64_tr_b16 v[44:45], v0 offset:24576\n\tds_write2st64_b32 v24, v12, v14 offset0:0 offset1:1\n\tds_write2st64_b32 v24, v40, v42 offset0:6 offset1:7\n\t"
  "v_mfma_f32_16x16x32_bf16 v[16:19], v[68:71], v[132:135], v[16:19]\n\tds_read_b64_tr_b16 v[46:47], v0 offset:25600\n\tds_write2st64_b32 v24, v64, v66 offset0:2 offset1:3\n\t"
  "v_mfma_f32_16x16x32_bf16 v[52:55], v[124:127], v[68:71], v[52:55]\n\tds_write2st64_b32 v24, v72, v74 offset0:4 offset1:5\n\tv_mul_f32 v59, v59, v30\n\tv_cvt_pk_bf16_f32 v56, v56, v57\n\t"
  "v_cvt_pk_bf16_f32 v58, v58, v59\n\ts_waitcnt lgkmcnt(8)\n\t"
  "v_mfma_f32_16x16x32_bf16 v[60:63], v[140:143], v[26:29], v[60:63]\n\tds_read_b64_tr_b16 v[40:41], v0 offset:26624\n\tds_write2st64_b32 v24, v56, v58 offset0:10 offset1:11\n\t"
  "v_mfma_f32_16x16x32_bf16 v[16:19], v[26:29], v[144:147], v[16:19]\n\tds_read_b64_tr_b16 v[42:43], v0 offset:27648\n\tv_mul_f32 v4, v4, v48\n\t"
  "v_mfma_f32_16x16x32_bf16 v[52:55], v[136:139], v[26:29], v[52:55]\n\tv_mul_f32 v5, v5, v49\n\tv_mul_f32 v6, v6, v50\n\tv_mul_f32 v7, v7, v51\n\t"
  "v_cvt_pk_bf16_f32 v4, v4, v5\n\tv_cvt_pk_bf16_f32 v6, v6, v7\n\t"
  "v_mfma_f32_16x16x32_bf16 v[60:63], v[176:179], v[36:39], v[60:63]\n\tds_write2st64_b32 v24, v4, v6 offset0:8 offset1:9\n\t"
  "v_mfma_f32_16x16x32_bf16 v[16:19], v[36:39], v[180:183], v[16:19]\n\t"
  "v_mfma_f32_16x16x32_bf16 v[52:55], v[172:175], v[36:39], v[52:55]\n\tv_and_b32 v27, 0x40, v1\n\ts_waitcnt lgkmcnt(4)\n\t"
  "v_mfma_f32_16x16x32_bf16 v[60:63], v[188:191], v[32:35], v[60:63]\n\tv_lshl_add_u32 v27, v27, 8, s61\n\tv_and_b32 v26, 0x3c, v1\n\t"
  "v_mfma_f32_16x16x32_bf16 v[16:19], v[32:35], v[192:195], v[16:19]\n\ts_waitcnt lgkmcnt(0)\n\ts_barrier\n\tds_read_b32 v24, v73 offset:5700\n\t"
  "ds_read_b128 v[64:67], v73\n\tds_read_b128 v[48:51], v73 offset:768\n\t"
  "v_mfma_f32_16x16x32_bf16 v[52:55], v[184:187], v[32:35], v[52:55]\n\tds_read_b64_tr_b16 v[4:5], v0 offset:4096\n\tds_read_b64_tr_b16 v[6:7], v0 offset:5120\n\t"
  "v_mfma_f32_16x16x32_bf16 v[60:63], v[224:227], v[44:47], v[60:63]\n\tds_read_b64_tr_b16 v[72:73], v0 offset:6144\n\tds_read_b64_tr_b16 v[74:75], v0 offset:7168\n\t"
  "v_mfma_f32_16x16x32_bf16 v[16:19], v[44:47], v[228:231], v[16:19]\n\tds_read_b64_tr_b16 v[28:29], v0 offset:12288\n\tv_lshl_add_u32 v27, v26, 2, v27\n\t"
  "v_mfma_f32_16x16x32_bf16 v[52:55], v[220:223], v[44:47], v[52:55]\n\tds_read_b64_tr_b16 v[30:31], v0 offset:13312\n\tv_and_b32 v26, 0x80, v1\n\t"
  "v_mfma_f32_16x16x32_bf16 v[60:63], v[236:239], v[40:43], v[60:63]\n\tds_read_b64_tr_b16 v[68:69], v0 offset:14336\n\tv_lshl_add_u32 v27, v26, 1, v27\n\t"
  "v_mfma_f32_16x16x32_bf16 v[16:19], v[40:43], v[240:243], v[16:19]\n\tds_read_b64_tr_b16 v[70:71], v0 offset:15360\n\t"
  "v_mfma_f32_16x16x32_bf16 v[52:55], v[232:235], v[40:43], v[52:55]\n\ts_waitcnt lgkmcnt(9)\n\ts_waitcnt lgkmcnt(6)\n\t"
  "v_mfma_f32_16x16x32_bf16 v[60:63], v[104:107], v[4:7], v[60:63]\n\tds_read_b64_tr_b16 v[12:13], v0 offset:20480\n\tv_readfirstlane_b32 s54, v24\n\t"
  "v_mfma_f32_16x16x32_bf16 v[16:19], v[4:7], v[108:111], v[16:19]\n\tds_read_b64_tr_b16 v[14:15], v0 offset:21504\n\tv_add_f32 v64, v64, v65\n\t"
  "v_mfma_f32_16x16x32_bf16 v[52:55], v[100:103], v[4:7], v[52:55]\n\ts_cmp_eq_u32 s54, s63\n\ts_cbranch_scc1 .Lg_redo\n.Lg_back:\n\ts_waitcnt lgkmcnt(5)\n\t"
  "v_mfma_f32_16x16x32_bf16 v[60:63], v[116:119], v[72:75], v[60:63]\n\tds_read_b64_tr_b16 v[44:45], v0 offset:22528\n\tv_add_f32 v66, v66, v67\n\t"
  "v_mfma_f32_16x16x32_bf16 v[16:19], v[72:75], v[120:123], v[16:19]\n\tds_read_b64_tr_b16 v[46:47], v0 offset:23552\n\tv_add_f32 v48, v48, v49\n\t"
  "v_mfma_f32_16x16x32_bf16 v[52:55], v[112:115], v[72:75], v[52:55]\n\tv_lshl_add_u32 v24, v1, 2, s66\n\ts_waitcnt lgkmcnt(4)\n\t"
  "v_mfma_f32_16x16x32_bf16 v[60:63], v[152:155], v[28:31], v[60:63]\n\tds_read_b64_tr_b16 v[4:5], v0 offset:30720\n\tv_add_f32 v64, v64, v66\n\t"
  "v_mfma_f32_16x16x32_bf16 v[16:19], v[28:31], v[156:159], v[16:19]\n\tds_read_b64_tr_b16 v[6:7], v0 offset:31744\n\tds_read_b128 v[36:39], v24 offset:1024\n\t"
  "v_mfma_f32_16x16x32_bf16 v[52:55], v[148:151], v[28:31], v[52:55]\n\tv_add_f32 v50, v50, v51\n\tv_mul_f32 v65, 0x3b000000, v64\n\t"
  "v_mfma_f32_16x16x32_bf16 v[60:63], v[164:167], v[68:71], v[60:63]\n\tds_read_b64_tr_b16 v[40:41], v0 offset:28672\n\tv_add_f32 v48, v48, v50\n\t"
  "v_mfma_f32_16x16x32_bf16 v[16:19], v[68:71], v[168:171], v[16:19]\n\tds_read_b64_tr_b16 v[42:43], v0 offset:29696\n\tv_mul_f32_e64 v66, -v65, v65\n\tv_fmac_f32 v66, 0x3b000000, v48\n\t"
  "v_max_f32 v66, 0, v66\n\tv_add_f32 v66, 0x3727c5ac, v66\n\t"
  "v_mfma_f32_16x16x32_bf16 v[52:55], v[160:163], v[68:71], v[52:55]\n\tds_read_b64_tr_b16 v[68:69], v0 offset:49152\n\tv_rsq_f32 v66, v66\n\tv_add_u32 v67, s65, v1\n\t"
  "ds_write2st64_b32 v67, v66, v65 offset1:1\n\ts_waitcnt lgkmcnt(6)\n\t"
  "v_mfma_f32_16x16x32_bf16 v[60:63], v[200:203], v[12:15], v[60:63]\n\tds_read_b64_tr_b16 v[70:71], v0 offset:49664\n\tds_read_b128 v[32:35], v24 offset:2048\n\tds_read2_b32 v[66:67], v2 offset0:80 offset1:96\n\t"
  "ds_read_b128 v[48:51], v24 offset:0\n\t"
  "v_mfma_f32_16x16x32_bf16 v[16:19], v[12:15], v[204:207], v[16:19]\n\tds_read_b64_tr_b16 v[56:57], v0 offset:50176\n\t"
  "v_mfma_f32_16x16x32_bf16 v[52:55], v[196:199], v[12:15], v[52:55]\n\tds_read_b64_tr_b16 v[58:59], v0 offset:50688\n\t"
  "v_mfma_f32_16x16x32_bf16 v[60:63], v[212:215], v[44:47], v[60:63]\n\tds_read_b64_tr_b16 v[72:73], v0 offset:51200\n\t"
  "v_mfma_f32_16x16x32_bf16 v[16:19], v[44:47], v[216:219], v[16:19]\n\tds_read_b64_tr_b16 v[74:75], v0 offset:51712\n\t"
  "v_mfma_f32_16x16x32_bf16 v[52:55], v[208:211], v[44:47], v[52:55]\n\tds_read_b64_tr_b16 v[44:45], v0 offset:52224\n\ts_waitcnt lgkmcnt(10)\n\t"
  "v_mfma_f32_16x16x32_bf16 v[60:63], v[36:39], v[4:7], v[60:63]\n\tds_read_b64_tr_b16 v[46:47], v0 offset:52736\n\ts_waitcnt lgkmcnt(8)\n\t"
  "v_mfma_f32_16x16x32_bf16 v[16:19], v[4:7], v[32:35], v[16:19]\n\tds_read_b64_tr_b16 v[32:33], v0 offset:53248\n\ts_waitcnt lgkmcnt(7)\n\t"
  "v_mfma_f32_16x16x32_bf16 v[52:55], v[48:51], v[4:7], v[52:55]\n\tds_read_b64_tr_b16 v[34:35], v0 offset:53760\n\tds_read_b128 v[4:7], v3 offset:320\n\t"
  "v_mfma_f32_16x16x32_bf16 v[60:63], v[248:251], v[40:43], v[60:63]\n\tds_read_b64_tr_b16 v[28:29], v0 offset:54272\n\tv_mul_f32_e64 v48, -v67, v8\n\t"
  "v_mfma_f32_16x16x32_bf16 v[16:19], v[40:43], v[252:255], v[16:19]\n\tds_read_b64_tr_b16 v[30:31], v0 offset:54784\n\tv_mul_f32_e64 v49, -v67, v9\n\t"
  "v_mfma_f32_16x16x32_bf16 v[52:55], v[244:247], v[40:43], v[52:55]\n\tds_read_b64_tr_b16 v[36:37], v0 offset:55296\n\tv_fma_f32 v60, -v66, v8, v60\n\t"
  "v_mfma_f32_16x16x32_bf16 v[12:15], v[80:83], v[68:71], 0\n\t"
  GST(18)
  "ds_read_b64_tr_b16 v[38:39], v0 offset:55808\n\tv_fma_f32 v61, -v66, v9, v61\n\t"
  "v_mfma_f32_16x16x32_bf16 v[40:43], v[68:71], v[84:87], 0\n\tds_read_b64_tr_b16 v[68:69], v0 offset:56320\n\ts_waitcnt lgkmcnt(8)\n\t"
  "v_mfma_f32_16x16x32_bf16 v[12:15], v[92:95], v[56:59], v[12:15]\n\tds_read_b64_tr_b16 v[70:71], v0 offset:56832\n\tv_fma_f32 v62, -v66, v10, v62\n\t"
  "v_mfma_f32_16x16x32_bf16 v[40:43], v[56:59], v[96:99], v[40:43]\n\tds_read_b64_tr_b16 v[56:57], v0 offset:57344\n\tv_fma_f32 v63, -v66, v11, v63\n\t"
  "v_mfma_f32_16x16x32_bf16 v[12:15], v[104:107], v[72:75], v[12:15]\n\tds_read_b64_tr_b16 v[58:59], v0 offset:57856\n\tv_cvt_pk_bf16_f32 v64, v60, v61\n\t"
  "v_mfma_f32_16x16x32_bf16 v[40:43], v[72:75], v[108:111], v[40:43]\n\tds_read_b64_tr_b16 v[60:61], v0 offset:58368\n\tv_cvt_pk_bf16_f32 v65, v62, v63\n\t"
  "v_mfma_f32_16x16x32_bf16 v[12:15], v[116:119], v[44:47], v[12:15]\n\tds_read_b64_tr_b16 v[62:63], v0 offset:58880\n\tv_fma_f32 v52, -v66, v20, v52\n\t"
  "v_mfma_f32_16x16x32_bf16 v[40:43], v[44:47], v[120:123], v[40:43]\n\tds_read_b64_tr_b16 v[72:73], v0 offset:59392\n\ts_waitcnt lgkmcnt(7)\n\t"
  "v_mfma_f32_16x16x32_bf16 v[12:15], v[128:131], v[32:35], v[12:15]\n\tds_read_b64_tr_b16 v[74:75], v0 offset:59904\n\tv_fma_f32 v16, -v4, v25, v16\n\t"
  "v_mfma_f32_16x16x32_bf16 v[40:43], v[32:35], v[132:135], v[40:43]\n\tds_read_b64_tr_b16 v[32:33], v0 offset:60416\n\tv_fma_f32 v17, -v5, v25, v17\n\t"
  "v_mfma_f32_16x16x32_bf16 v[12:15], v[140:143], v[28:31], v[12:15]\n\tds_read_b64_tr_b16 v[34:35], v0 offset:60928\n\tv_fma_f32 v18, -v6, v25, v18\n\tv_fma_f32 v19, -v7, v25, v19\n\t"
  "v_cvt_pk_bf16_f32 v46, v16, v17\n\tv_cvt_pk_bf16_f32 v47, v18, v19\n\t"
  "v_mfma_f32_16x16x32_bf16 v[40:43], v[28:31], v[144:147], v[40:43]\n\tds_read_b64_tr_b16 v[28:29], v0 offset:61440\n\tv_fma_f32 v53, -v66, v21, v53\n\t"
  "v_mfma_f32_16x16x32_bf16 v[12:15], v[152:155], v[36:39], v[12:15]\n\tds_read_b64_tr_b16 v[30:31], v0 offset:61952\n\tv_fma_f32 v54, -v66, v22, v54\n\t"
  "v_mfma_f32_16x16x32_bf16 v[40:43], v[36:39], v[156:159], v[40:43]\n\tds_read_b64_tr_b16 v[16:17], v0 offset:62464\n\ts_waitcnt lgkmcnt(6)\n\t"
  "v_mfma_f32_16x16x32_bf16 v[12:15], v[164:167], v[68:71], v[12:15]\n\tds_read_b64_tr_b16 v[18:19], v0 offset:62976\n\tv_fma_f32 v55, -v66, v23, v55\n\t"
  "v_mfma_f32_16x16x32_bf16 v[40:43], v[68:71], v[168:171], v[40:43]\n\tds_read_b64_tr_b16 v[36:37], v0 offset:63488\n\tv_cvt_pk_bf16_f32 v44, v52, v53\n\t"
  "v_mfma_f32_16x16x32_bf16 v[12:15], v[176:179], v[56:59], v[12:15]\n\tds_read_b64_tr_b16 v[38:39], v0 offset:64000\n\tv_cvt_pk_bf16_f32 v45, v54, v55\n\t"
  "v_mfma_f32_16x16x32_bf16 v[40:43], v[56:59], v[180:183], v[40:43]\n\tds_read_b64_tr_b16 v[56:57], v0 offset:64512\n\tv_mul_f32_e64 v50, -v67, v10\n\t"
  "v_mfma_f32_16x16x32_bf16 v[12:15], v[188:191], v[60:63], v[12:15]\n\tds_read_b64_tr_b16 v[58:59], v0 offset:65024\n\tv_lshl_add_u32 v66, v1, 2, s66\n\t"
  "v_mfma_f32_16x16x32_bf16 v[40:43], v[60:63], v[192:195], v[40:43]\n\tds_read_b32 v24, v2 offset:256\n\ts_waitcnt lgkmcnt(7)\n\t"
  "v_mfma_f32_16x16x32_bf16 v[12:15], v[200:203], v[72:75], v[12:15]\n\tds_read_b64_tr_b16 v[4:5], v0 offset:31232\n\tv_and_or_b32 v26, v1, 4, v3\n\tv_add_u32 v63, 64, v26\n\t"
  "v_mfma_f32_16x16x32_bf16 v[40:43], v[72:75], v[204:207], v[40:43]\n\tds_read_b64_tr_b16 v[6:7], v0 offset:32256\n\tv_cndmask_b32_e64 v68, v26, v63, s[74:75]\n\tv_cndmask_b32_e64 v26, v26, v63, s[76:77]\n\t"
  "v_mfma_f32_16x16x32_bf16 v[12:15], v[212:215], v[32:35], v[12:15]\n\tds_read_b128 v[60:63], v66 offset:1024\n\tds_read_b128 v[72:75], v66 offset:2048\n\t"
  "v_mfma_f32_16x16x32_bf16 v[40:43], v[32:35], v[216:219], v[40:43]\n\tds_read_b64_tr_b16 v[52:53], v0 offset:512\n\tv_mul_f32_e64 v51, -v67, v11\n\t"
  "v_mfma_f32_16x16x32_bf16 v[12:15], v[224:227], v[28:31], v[12:15]\n\tds_read_b64_tr_b16 v[54:55], v0 offset:1536\n\tds_read_b128 v[32:35], v66 offset:0\n\t"
  "v_mfma_f32_16x16x32_bf16 v[40:43], v[28:31], v[228:231], v[40:43]\n\tds_read2_b32 v[70:71], v68 offset0:0 offset1:16\n\ts_waitcnt lgkmcnt(9)\n\t"
  "v_mfma_f32_16x16x32_bf16 v[12:15], v[236:239], v[16:19], v[12:15]\n\tds_read_b64_tr_b16 v[28:29], v0 offset:2560\n\tds_read2_b32 v[68:69], v26 offset0:2 offset1:18\n\t"
  "v_mfma_f32_16x16x32_bf16 v[40:43], v[16:19], v[240:243], v[40:43]\n\tds_read_b64_tr_b16 v[30:31], v0 offset:3584\n\tv_mul_f32_e64 v16, -v67, v20\n\t"
  "v_mfma_f32_16x16x32_bf16 v[12:15], v[248:251], v[36:39], v[12:15]\n\tv_mul_f32_e64 v17, -v67, v21\n\tv_mul_f32_e64 v18, -v67, v22\n\tv_mul_f32_e64 v19, -v67, v23\n\t"
  "v_mfma_f32_16x16x32_bf16 v[40:43], v[36:39], v[252:255], v[40:43]\n\tds_read_b64_tr_b16 v[20:21], v0 offset:4608\n\ts_waitcnt lgkmcnt(8)\n\t"
  "v_mfma_f32_16x16x32_bf16 v[12:15], v[60:63], v[56:59], v[12:15]\n\tds_read_b64_tr_b16 v[22:23], v0 offset:5632\n\tds_read_b128 v[36:39], v3 offset:256\n\t"
  "v_mfma_f32_16x16x32_bf16 v[40:43], v[56:59], v[72:75], v[40:43]\n\tds_read2_b32 v[66:67], v2 offset0:16 offset1:32\n\t"
  "v_mfma_f32_16x16x32_bf16 v[48:51], v[60:63], v[4:7], v[48:51]\n\t"
  GST(19)
  "ds_read_b64_tr_b16 v[60:61], v0 offset:6656\n\ts_waitcnt lgkmcnt(8)\n\t"
  "v_mfma_f32_16x16x32_bf16 v[16:19], v[32:35], v[4:7], v[16:19]\n\tds_read_b64_tr_b16 v[62:63], v0 offset:7680\n\tv_fma_f32 v12, -v24, v8, v12\n\t"
  "v_mfma_f32_16x16x32_bf16 v[32:35], v[4:7], v[72:75], 0\n\tv_fma_f32 v13, -v24, v9, v13\n\tv_fma_f32 v14, -v24, v10, v14\n\t"
  "v_mfma_f32_16x16x32_bf16 v[48:51], v[80:83], v[52:55], v[48:51]\n\tds_read_b64_tr_b16 v[8:9], v0 offset:8704\n\tv_mfma_f32_16x16x16_bf16 v[56:59], v[64:65], v[44:45], 0\n\tv_fma_f32 v15, -v24, v11, v15\n\t"
  "v_cvt_pk_bf16_f32 v12, v12, v13\n\t"
  "v_mfma_f32_16x16x32_bf16 v[16:19], v[76:79], v[52:55], v[16:19]\n\tds_read_b64_tr_b16 v[10:11], v0 offset:9728\n\tv_cvt_pk_bf16_f32 v13, v14, v15\n\t"
  "v_mfma_f32_16x16x32_bf16 v[32:35], v[52:55], v[84:87], v[32:35]\n\tv_cndmask_b32_e64 v26, v56, v57, s[68:69]\n\tv_mfma_f32_16x16x16_bf16 v[4:7], v[12:13], v[44:45], 0\n\tv_cndmask_b32_e64 v24, v58, v59, s[68:69]\n\t"
  "s_waitcnt lgkmcnt(2)\n\t"
  "v_mfma_f32_16x16x32_bf16 v[48:51], v[92:95], v[28:31], v[48:51]\n\tds_read_b64_tr_b16 v[12:13], v0 offset:10752\n\tv_mul_f32 v66, 0x3eb8aa3b, v66\n\t"
  "v_mfma_f32_16x16x32_bf16 v[16:19], v[88:91], v[28:31], v[16:19]\n\tds_read_b64_tr_b16 v[14:15], v0 offset:11776\n\tv_cndmask_b32_e64 v52, v4, v5, s[68:69]\n\t"
  "v_mfma_f32_16x16x32_bf16 v[32:35], v[28:31], v[96:99], v[32:35]\n\tv_cndmask_b32_e64 v26, v52, v26, s[74:75]\n\tv_mul_f32 v26, v26, v70\n\t"
  "v_mfma_f32_16x16x32_bf16 v[48:51], v[104:107], v[20:23], v[48:51]\n\tds_read_b64_tr_b16 v[4:5], v0 offset:12800\n\tv_cndmask_b32_e64 v53, v6, v7, s[68:69]\n\t"
  "v_mfma_f32_16x16x32_bf16 v[16:19], v[100:103], v[20:23], v[16:19]\n\tds_read_b64_tr_b16 v[6:7], v0 offset:13824\n\tv_cndmask_b32_e64 v24, v53, v24, s[76:77]\n\t"
  "v_mfma_f32_16x16x32_bf16 v[32:35], v[20:23], v[108:111], v[32:35]\n\tv_mul_f32 v24, v24, v68\n\tv_max_f32 v53, v26, v24\n\t"
  "v_mfma_f32_16x16x32_bf16 v[48:51], v[116:119], v[60:63], v[48:51]\n\tds_read_b64_tr_b16 v[56:57], v0 offset:14848\n\tv_mul_f32 v67, 0x3eb8aa3b, v67\n\t"
  "v_mfma_f32_16x16x32_bf16 v[16:19], v[112:115], v[60:63], v[16:19]\n\tds_read_b64_tr_b16 v[58:59], v0 offset:15872\n\tv_max_f32 v52, v26, v24\n\t"
  "v_mfma_f32_16x16x32_bf16 v[32:35], v[60:63], v[120:123], v[32:35]\n\tv_fma_f32 v40, -v36, v25, v40\n\ts_waitcnt lgkmcnt(4)\n\t"
  "v_mfma_f32_16x16x32_bf16 v[48:51], v[128:131], v[8:11], v[48:51]\n\tds_read_b64_tr_b16 v[60:61], v0 offset:16896\n\tv_fma_f32 v41, -v37, v25, v41\n\t"
  "v_mfma_f32_16x16x32_bf16 v[16:19], v[124:127], v[8:11], v[16:19]\n\tds_read_b64_tr_b16 v[62:63], v0 offset:17920\n\tv_lshlrev_b32 v45, 2, v1\n\t"
  "v_mfma_f32_16x16x32_bf16 v[32:35], v[8:11], v[132:135], v[32:35]\n\tds_read_b64_tr_b16 v[28:29], v0 offset:18944\n\tv_alignbit_b32 v55, s67, s67, v45\n\t"
  "v_mfma_f32_16x16x32_bf16 v[48:51], v[140:143], v[12:15], v[48:51]\n\tds_read_b64_tr_b16 v[30:31], v0 offset:19968\n\tv_alignbit_b32 v45, s55, s55, v45\n\t"
  "v_mfma_f32_16x16x32_bf16 v[16:19], v[136:139], v[12:15], v[16:19]\n\tds_read_b64_tr_b16 v[20:21], v0 offset:20992\n\tv_mov_b32 v36, 0x0c0c0c0c\n\t"
  "v_mfma_f32_16x16x32_bf16 v[32:35], v[12:15], v[144:147], v[32:35]\n\tds_read_b64_tr_b16 v[22:23], v0 offset:22016\n\ts_waitcnt lgkmcnt(6)\n\t"
  "v_mfma_f32_16x16x32_bf16 v[48:51], v[152:155], v[4:7], v[48:51]\n\tds_read_b64_tr_b16 v[12:13], v0 offset:23040\n\tv_mov_b32 v8, 0x3f803f80\n\t"
  "v_mfma_f32_16x16x32_bf16 v[16:19], v[148:151], v[4:7], v[16:19]\n\tds_read_b64_tr_b16 v[14:15], v0 offset:24064\n\tv_mov_b32 v9, 0x3f803f80\n\t"
  "v_mfma_f32_16x16x32_bf16 v[32:35], v[4:7], v[156:159], v[32:35]\n\tds_read_b64_tr_b16 v[4:5], v0 offset:25088\n\tv_mov_b32 v11, 0\n\t"
  "v_mfma_f32_16x16x32_bf16 v[48:51], v[164:167], v[56:59], v[48:51]\n\tds_read_b64_tr_b16 v[6:7], v0 offset:26112\n\tv_cndmask_b32_e64 v2, v55, v36, s[74:75]\n\t"
  "v_mfma_f32_16x16x32_bf16 v[16:19], v[160:163], v[56:59], v[16:19]\n\tds_read_b64_tr_b16 v[72:73], v0 offset:27136\n\tv_cndmask_b32_e64 v1, v36, v55, s[74:75]\n\t"
  "v_mfma_f32_16x16x32_bf16 v[32:35], v[56:59], v[168:171], v[32:35]\n\tds_read_b64_tr_b16 v[74:75], v0 offset:28160\n\ts_waitcnt lgkmcnt(7)\n\t"
  "v_mfma_f32_16x16x32_bf16 v[48:51], v[176:179], v[60:63], v[48:51]\n\tds_read_b64_tr_b16 v[56:57], v0 offset:29184\n\tv_cndmask_b32_e64 v37, v45, v36, s[76:77]\n\t"
  "v_mfma_f32_16x16x32_bf16 v[16:19], v[172:175], v[60:63], v[16:19]\n\tds_read_b64_tr_b16 v[58:59], v0 offset:30208\n\tv_cndmask_b32_e64 v0, v36, v45, s[76:77]\n\t"
  "v_mfma_f32_16x16x32_bf16 v[48:51], v[188:191], v[28:31], v[48:51]\n\tv_permlane16_swap_b32 v52, v53\n\t"
  "v_mfma_f32_16x16x32_bf16 v[16:19], v[184:187], v[28:31], v[16:19]\n\tv_max_f32 v54, v52, v53\n\ts_waitcnt lgkmcnt(5)\n\t"
  "v_mfma_f32_16x16x32_bf16 v[48:51], v[200:203], v[20:23], v[48:51]\n\tv_max_f32 v55, v52, v53\n\tv_fma_f32 v42, -v38, v25, v42\n\t"
  "v_mfma_f32_16x16x32_bf16 v[16:19], v[196:199], v[20:23], v[16:19]\n\tv_permlane32_swap_b32 v54, v55\n\t"
  "v_mfma_f32_16x16x32_bf16 v[48:51], v[212:215], v[12:15], v[48:51]\n\tv_max_f32 v52, v54, v55\n\tv_mul_f32_e64 v52, -v52, v66\n\t"
  "v_mfma_f32_16x16x32_bf16 v[16:19], v[208:211], v[12:15], v[16:19]\n\tv_fma_f32 v26, v26, v66, v52\n\ts_waitcnt lgkmcnt(1)\n\t"
  "v_mfma_f32_16x16x32_bf16 v[48:51], v[224:227], v[4:7], v[48:51]\n\tv_fma_f32 v24, v24, v66, v52\n\tv_fma_f32 v43, -v39, v25, v43\n\t"
  "v_mfma_f32_16x16x32_bf16 v[16:19], v[220:223], v[4:7], v[16:19]\n\tv_exp_f32 v26, v26\n\t"
  "v_mfma_f32_16x16x32_bf16 v[48:51], v[236:239], v[72:75], v[48:51]\n\tv_cvt_pk_bf16_f32 v44, v40, v41\n\tv_cvt_pk_bf16_f32 v45, v42, v43\n\t"
  "v_mfma_f32_16x16x32_bf16 v[16:19], v[232:235], v[72:75], v[16:19]\n\tds_read_b128 v[40:43], v3 offset:384\n\ts_waitcnt lgkmcnt(1)\n\t"
  "v_mfma_f32_16x16x32_bf16 v[48:51], v[248:251], v[56:59], v[48:51]\n\tv_exp_f32 v24, v24\n\t"
  "v_mfma_f32_16x16x32_bf16 v[16:19], v[244:247], v[56:59], v[16:19]\n\tv_cvt_pk_bf16_f32 v10, v26, v24\n\tv_mul_f32 v26, v26, v70\n\t"
  "v_mfma_f32_16x16x32_bf16 v[32:35], v[60:63], v[180:183], v[32:35]\n\tv_cvt_pk_bf16_f32 v48, v48, v49\n\tv_cvt_pk_bf16_f32 v49, v50, v51\n\tv_mfma_f32_16x16x16_bf16 v[52:55], v[8:9], v[10:11], 0\n\t"
  "v_cvt_pk_bf16_f32 v16, v16, v17\n\tv_cvt_pk_bf16_f32 v17, v18, v19\n\t"
  "v_mfma_f32_16x16x32_bf16 v[32:35], v[28:31], v[192:195], v[32:35]\n\tv_mul_f32 v24, v24, v68\n\tv_mfma_f32_16x16x16_bf16 v[28:31], v[48:49], v[16:17], 0\n\tv_mfma_f32_16x16x16_bf16 v[48:51], v[64:65], v[16:17], 0\n\t"
  "v_cvt_pk_bf16_f32 v26, v26, v24\n\tv_perm_b32 v16, v26, v26, v2\n\t"
  "v_mfma_f32_16x16x32_bf16 v[32:35], v[20:23], v[204:207], v[32:35]\n\tv_cndmask_b32_e64 v39, v28, v29, s[68:69]\n\tv_cndmask_b32_e64 v24, v30, v31, s[68:69]\n\t"
  "v_mfma_f32_16x16x32_bf16 v[32:35], v[12:15], v[216:219], v[32:35]\n\tv_cndmask_b32_e64 v28, v48, v49, s[68:69]\n\tv_cndmask_b32_e64 v39, v28, v39, s[74:75]\n\t"
  "v_mfma_f32_16x16x32_bf16 v[32:35], v[4:7], v[228:231], v[32:35]\n\tv_mul_f32 v39, v39, v71\n\tv_cndmask_b32_e64 v29, v50, v51, s[68:69]\n\t"
  "v_mfma_f32_16x16x32_bf16 v[32:35], v[72:75], v[240:243], v[32:35]\n\tv_cndmask_b32_e64 v24, v29, v24, s[76:77]\n\tv_mul_f32 v24, v24, v69\n\t"
  "v_mfma_f32_16x16x32_bf16 v[32:35], v[56:59], v[252:255], v[32:35]\n\t"
  GST(14)
  "v_max_f32 v29, v39, v24\n\tv_max_f32 v28, v39, v24\n\tv_perm_b32 v17, v26, v26, v37\n\tv_perm_b32 v18, v26, v26, v1\n\t"
  "v_permlane16_swap_b32 v28, v29\n\tv_max_f32 v30, v28, v29\n\tv_max_f32 v31, v28, v29\n\tv_perm_b32 v19, v26, v26, v0\n\t"
  "s_waitcnt lgkmcnt(0)\n\tv_fma_f32 v32, -v40, v25, v32\n\tv_permlane32_swap_b32 v30, v31\n\tv_max_f32 v28, v30, v31\n\t"
  "v_mul_f32_e64 v28, -v28, v67\n\tv_fma_f32 v39, v39, v67, v28\n\tv_fma_f32 v24, v24, v67, v28\n\tv_exp_f32 v39, v39\n\t"
  "v_exp_f32 v24, v24\n\tv_fma_f32 v33, -v41, v25, v33\n\tv_cvt_pk_bf16_f32 v10, v39, v24\n\tv_mul_f32 v39, v39, v71\n\t"
  "v_fma_f32 v34, -v42, v25, v34\n\tv_fma_f32 v35, -v43, v25, v35\n\tv_cvt_pk_bf16_f32 v48, v32, v33\n\tv_cvt_pk_bf16_f32 v49, v34, v35\n\t"
  "v_mul_f32 v24, v24, v69\n\tv_mfma_f32_16x16x16_bf16 v[28:31], v[8:9], v[10:11], 0\n\tv_cvt_pk_bf16_f32 v39, v39, v24\n\tv_perm_b32 v40, v39, v39, v2\n\t"
  "v_perm_b32 v41, v39, v39, v37\n\tv_perm_b32 v42, v39, v39, v1\n\t"
  "v_mfma_f32_16x16x32_bf16 v[22:25], v[44:47], v[16:19], 0\n\tv_perm_b32 v43, v39, v39, v0\n\tv_rcp_f32 v52, v52\n\tv_rcp_f32 v28, v28\n\t"
  "v_mfma_f32_16x16x32_bf16 v[4:7], v[46:49], v[40:43], 0\n\ts_nop 0\n\tv_mul_f32 v22, v22, v52\n\tv_mul_f32 v23, v23, v52\n\t"
  "v_mul_f32 v24, v24, v52\n\tv_mul_f32 v25, v25, v52\n\tv_cvt_pk_bf16_f32 v64, v22, v23\n\tv_cvt_pk_bf16_f32 v65, v24, v25\n\t"
  "s_nop 0\n\tv_mul_f32 v4, v4, v28\n\tv_mul_f32 v5, v5, v28\n\tv_mul_f32 v6, v6, v28\n\t"
  "v_mul_f32 v7, v7, v28\n\tv_cvt_pk_bf16_f32 v66, v4, v5\n\tv_cvt_pk_bf16_f32 v67, v6, v7\n\ts_nop 0\n\t"
  "v_permlane16_swap_b32 v64, v66\n\tv_permlane16_swap_b32 v65, v67\n\tbuffer_store_dwordx4 v[64:67], v27, s[56:59], 0 offen sc1\n\t"
  "s_cmp_eq_u32 s63, 2\n\ts_cbranch_scc1 .Lg_ret\n\ts_nop 0"
  G_OPS);
  else asm volatile("s_branch .Lg_body\n.Lg_ret:\n\ts_nop 0" G_OPS);
  static_for<0, 45>(pin);
  ST(!L1, 16);
  if (L1) {
    KSLP(7, SLP_OB1);
    cnt_add(smem, C_OB, 0);
    mid();
    cnt_wait(smem, C_X2, 4);
    return;
  }
  u32 t16 = KADD(8, now_(), DB2);
  cnt_add(smem, C_OB, 0);
  const int bid = BIDX, lane = lane_id();
  int og0 = OFF_O2 + MB_ + RB_OF(bid) * 32768 + g * 4096, og1 = og0 + 16384, vo = lane * 16;
  u32 sa = (u32)(L_TB + g * 4096 + lane * 64 + (lane & 14) * 4 + 4);
  asm volatile("" : "+s"(og0), "+s"(og1), "+v"(vo), "+v"(sa));
  u32 dl;
  for (unsigned n = 0; n < (1u << 24); ++n) {
    asm volatile("" ::: "memory");
    const u32 c0 = *(const volatile LAS u32*)(size_t)(u32)(L_MI + C_OV), c1 = *(const volatile LAS u32*)(size_t)(u32)(L_MI + C_OB);
    dl = *(const volatile LAS u32*)(size_t)(u32)(L_MI + M_DL);
    if ((u32)__builtin_amdgcn_readfirstlane(min(c0 + 11u, c1)) >= 16u) break;
  }
  gate(t16 + dl);
  ST(1, 15);
  const u32 mark = x0_mark(tag);
  o_get(wrs, vo, og0, og1, L_TB + g * 4096, sa, mark, 25);
  asm volatile("s_barrier\n\ts_waitcnt vmcnt(0)\n\ts_barrier" ::: "memory");
  if (__any(*(const LAS u32*)(size_t)(sa + 16384) == mark)) {
    FAILC(12);
    for (unsigned spins = 0; spins <= SPIN_CAP; ++spins) {
      dma4k(wrs, vo, og0, L_TA + g * 4096);
      dma4k(wrs, vo, og1, L_TA + 16384 + g * 4096);
      asm volatile("s_waitcnt vmcnt(0)" ::: "memory");
      if (!__any(*(const LAS u32*)(size_t)(sa - (L_TB - L_TA)) == mark || *(const LAS u32*)(size_t)(sa - (L_TB - L_TA) + 16384) == mark)) break;
    }
    smem[L_MI + M_FX + g] = 1;
    asm volatile("s_waitcnt lgkmcnt(0)" ::: "memory");
  }
}

using u32x8 = __attribute__((ext_vector_type(8))) unsigned;
using u32x16 = __attribute__((ext_vector_type(16))) unsigned;
__global__ __launch_bounds__(512, 2) KATTR void k_mega(const u16*, const u16*, const u16*, const u16*, const u16*, const u16*, const u16*, const u16*,
                                                       const u16*, const u16*, const u16*, char*, u32*) {
  extern __shared__ __attribute__((aligned(16))) char smem[];
  u32x16 ka;
  u32x8 kb;
  u32x2 kc;
  asm volatile("s_load_dwordx16 %0, %3, 0x0\n\ts_load_dwordx8 %1, %3, 0x40\n\ts_load_dwordx2 %2, %3, 0x60"
               : "=&s"(ka), "=&s"(kb), "=&s"(kc) : "s"(__builtin_amdgcn_kernarg_segment_ptr()) : "memory");
  const int tid = TIDX, lane = tid & 63;
  const int w = __builtin_amdgcn_readfirstlane(tid >> 6);
  const int bid = BIDX;
  ST_INIT(); ST(0, 0); ST(1, 0);
  if (w == 0) {
    u64 pc;
    int clen;
    asm volatile("s_getpc_b64 %0\n.Lcw_base:\n\ts_mov_b32 %1, .Lcw_end-.Lcw_base-4" : "=s"(pc), "=s"(clen));
    asm volatile("s_mov_b32 m0, %2\n\ts_nop 0\n\tglobal_load_lds_dword %0, %1" :: "v"(min(min(lane, 15) * 128 + (bid >> 3) * 2048, clen)), "s"(pc), "s"(L_FS) : "memory");
  }
  const int kp = (bid & 7) * 32 + (bid >> 3), hb = HB_OF(bid);
  const int c = (w >> 1) & 1, g = w & 3;
  u32 po = (u32)(kp * 512 + (tid & 255) * 2);
  const int pb = (bid & 7) + 8 * (bid >> 6), ps = (bid >> 3) & 7, gr = pb * 8 + ps;
  u32 xo = (u32)((w < 4 ? (pb * 64 + 16 * w) * 1024 + ps * 128 : gr * 8192 + g * 2048) + (lane & 7) * 16 + (lane >> 3) * (w < 4 ? 2048 : 128));
  asm volatile("" : "+v"(po), "+v"(xo) :: "memory");
  asm volatile("s_waitcnt lgkmcnt(0)" : "+s"(ka), "+s"(kb), "+s"(kc) :: "memory");
  ST_FIX();
#define KP(T_, v_, i_) ((T_)(((u64)v_[2 * (i_) + 1] << 32) | v_[2 * (i_)]))
  const u16 *x0 = KP(const u16*, ka, 0), *A_re = KP(const u16*, ka, 1), *A_im = KP(const u16*, ka, 2), *B_re = KP(const u16*, ka, 3), *B_im = KP(const u16*, ka, 4);
  const u16 *C_re = KP(const u16*, ka, 5), *C_im = KP(const u16*, ka, 6), *log_dt = KP(const u16*, ka, 7), *Dskip = KP(const u16*, kb, 0), *wqkv = KP(const u16*, kb, 1), *wo = KP(const u16*, kb, 2);
  char* ws = KP(char*, kb, 3);
  u32* out = KP(u32*, kc, 0);
#define LD_A() asm volatile("global_load_ushort v248, %0, %1\n\tglobal_load_ushort v249, %0, %2" :: "v"(po), "s"(A_re), "s"(A_im) : "memory", "v248", "v249")
  u32 ldtw = 0, dskw = 0;
  if (w < 4) {
    asm volatile("s_load_dword %0, %2, %4\n\ts_load_dword %1, %3, %4" : "=&s"(ldtw), "=&s"(dskw) : "s"(log_dt), "s"(Dskip), "s"(4 * kp) : "memory");
    LD_A();
  }
  asm volatile("global_load_dwordx4 v[240:243], %0, %1\n\tglobal_load_dwordx4 v[244:247], %0, %1 offset:1024\n\tglobal_load_dword v254, %2, %3 sc1"
               :: "v"(xo), "s"(x0), "v"(OFF_EP), "s"(ws) : "memory", "v240", "v254");
  if (w >= 4) {
    asm volatile("global_load_ushort v250, %0, %1\n\tglobal_load_ushort v251, %0, %2\n\tglobal_load_ushort v252, %0, %3\n\tglobal_load_ushort v253, %0, %4"
                 :: "v"(po), "s"(B_re), "s"(B_im), "s"(C_re), "s"(C_im) : "memory", "v250", "v253");
    LD_A();
  }
  __builtin_amdgcn_sched_barrier(0);
  const rsrc_t wrs = __builtin_amdgcn_make_buffer_rsrc((void*)ws, 0, WSB, 0x00020000);
  if (w >= 4) {
    asm volatile("s_mov_b32 m0, %2\n\ts_nop 0\n\tglobal_load_lds_dword %0, %1" :: "v"((bid * 16 + g * 4 + min(lane, 3)) * 128), "s"(wqkv), "s"(L_FS + w * 256) : "memory");
    u32x4 zz = {0, 0, 0, 0};
    asm volatile("" : "+v"(zz));
    const int z0 = (int)opaque((u32)(L_U + (lane >> 5) * 4608 + (lane & 31) * 16)), z1 = (int)opaque((u32)(L_MI + min((tid & 255) * 16 - 1024, 1088)));
    *(u32x4*)(smem + (g == 0 ? z0 : z1)) = zz;
    asm volatile("s_nop 1\n\ts_waitcnt lgkmcnt(0)" :: "v"(zz) : "memory");
  }
  if (w >= 4) asm volatile("s_load_dword %0, %1, %2" : "=s"(ldtw) : "s"(log_dt), "s"(4 * kp) : "memory");
  __builtin_amdgcn_sched_barrier(0);
#define XT_GET(x_) asm volatile("v_mov_b64 %0, v[240:241]\n\tv_mov_b64 %1, v[242:243]\n\tv_mov_b64 %2, v[244:245]\n\tv_mov_b64 %3, v[246:247]" \
                   : "=v"(x_[0].xy), "=v"(x_[0].zw), "=v"(x_[1].xy), "=v"(x_[1].zw) :: "memory")
  const int ftb = c ? L_TA : 0;
#define PUT(e_, r_, i_) *(LAS u32*)(wa + (e_) * FTS) = pkh(r_, i_)
#define CMR(a_, b_) fmaf(a_[0], b_[0], -(a_[1] * b_[1]))
#define CMI(a_, b_) fmaf(a_[0], b_[1], a_[1] * b_[0])
#define CMUL(a_, b_) (f32x2){CMR(a_, b_), CMI(a_, b_)}
  struct P0A { u32 gad[12], sad[8], ua, ur; };
  auto p0_addr = [&](P0A& A, int lane, int ut) __attribute__((always_inline)) {
    const int i = lane & 15, kk = lane >> 4;
    const u32 sb = (u32)((L_KT - KTO + c * TBS - 0x7E00) & 0xFFFF) * 0x10001u;
    const float f0 = __uint_as_float(opaque(__float_as_uint((float)((ut < 2 ? 1023 : 2047) - 512 * (w & 1) - 8 * lane))));
#pragma unroll
    for (int e = 0; e < 6; ++e) { const u32 ga = tb_addr(pk2(f0 - (float)(2 * e), f0 - (float)(2 * e + 1)), sb); A.gad[2 * e] = ga & 0xFFFFu; A.gad[2 * e + 1] = ga >> 16; }
    if (ut < 2) {
      A.ua = (u32)(ftb + i * FTS + kk * 16); A.ur = A.ua + ut * 16 * FTS;
      const int a0 = 128 * (i >> 3) + (i & 7), a1 = i < 8 ? 256 + 128 * (i >> 2) + 2 * (i & 3) : 128 * (i >> 1) + 4 * (i & 1), a2 = min(1024 + 128 * i, 2064);
#pragma unroll
      for (int t = 0; t < 2; ++t) {
        const float st = (t && i >= 8) ? 16.f : 8.f;
        const float fb = (float)(t ? a2 : ut == 0 ? a0 : a1) + st * (float)(4 * kk);
        const u32 s0 = tb_addr(pk2(fb, fb + st), sb), s1 = tb_addr(pk2(fb + 2.f * st, fb + 3.f * st), sb);
        A.sad[4 * t] = s0 & 0xFFFFu; A.sad[4 * t + 1] = s0 >> 16; A.sad[4 * t + 2] = s1 & 0xFFFFu; A.sad[4 * t + 3] = s1 >> 16;
      }
      asm volatile("" : "+v"(A.ua), "+v"(A.ur), "+v"(A.sad[0]), "+v"(A.sad[1]), "+v"(A.sad[2]), "+v"(A.sad[3]), "+v"(A.sad[4]), "+v"(A.sad[5]), "+v"(A.sad[6]), "+v"(A.sad[7]));
    }
    asm volatile("" : "+v"(A.gad[0]), "+v"(A.gad[1]), "+v"(A.gad[2]), "+v"(A.gad[3]), "+v"(A.gad[4]), "+v"(A.gad[5]), "+v"(A.gad[6]), "+v"(A.gad[7]), "+v"(A.gad[8]), "+v"(A.gad[9]), "+v"(A.gad[10]));
    __builtin_amdgcn_sched_barrier(0);
  };
  auto k_unit = [&](const P0A& A, float dk, bool lag0) __attribute__((always_inline)) {
    f16x8 fr[8], fc[8];
    const f32x2 bd = *(const f32x2*)(smem + L_P0 + 512 + c * 8);
    __builtin_amdgcn_sched_barrier(0);
#pragma unroll
    for (int e = 0; e < 8; ++e) {
      fc[e] = *(const LAS f16x8*)(size_t)(A.ua + e * 64 + 41 * FTS);
      fr[e] = *(const LAS f16x8*)(size_t)(A.ur + e * 64);
      __builtin_amdgcn_sched_barrier(0);
    }
    f16x8 f2[8];
    if (!(w & 1)) {
#pragma unroll
      for (int e = 0; e < 8; ++e) { f2[e] = *(const LAS f16x8*)(size_t)(A.ua + e * 64 + 32 * FTS); __builtin_amdgcn_sched_barrier(0); }
    }
    const int ec = bexp(bd[0] + bd[1] + fabsf(dk));
    const float f0 = __uint_as_float((u32)max(127 + bexp(bd[0]) - ec, 0) << 23), f1 = __uint_as_float((u32)max(127 + bexp(bd[1]) - ec, 0) << 23);
    const float d0s = lag0 ? dk * __uint_as_float((u32)(268 - ec) << 23) : 0.f;
    auto unit = [&](const f16x8 (&fr)[8], u32x4 sa, float add) __attribute__((always_inline)) {
      f32x4 k0 = {0.f, 0.f, 0.f, 0.f}, k1 = k0;
#pragma unroll
      for (int e = 0; e < 4; ++e) k0 = __builtin_amdgcn_mfma_f32_16x16x32_f16(fc[e], fr[e], k0, 0, 0, 0);
#pragma unroll
      for (int e = 4; e < 8; ++e) k1 = __builtin_amdgcn_mfma_f32_16x16x32_f16(fc[e], fr[e], k1, 0, 0, 0);
      k0 *= f0; k0[0] += add;
      const u32 h01 = pkh(fmaf(k1[0], f1, k0[0]), fmaf(k1[1], f1, k0[1])), h23 = pkh(fmaf(k1[2], f1, k0[2]), fmaf(k1[3], f1, k0[3]));
      *(LAS u16*)((LAS char*)(size_t)sa[0] + KTO) = (u16)h01; *(LAS u16*)((LAS char*)(size_t)sa[1] + KTO) = (u16)(h01 >> 16);
      *(LAS u16*)((LAS char*)(size_t)sa[2] + KTO) = (u16)h23; *(LAS u16*)((LAS char*)(size_t)sa[3] + KTO) = (u16)(h23 >> 16);
    };
    if (!(w & 1)) {
      unit(fr, (u32x4){A.sad[0], A.sad[1], A.sad[2], A.sad[3]}, d0s);
      unit(f2, (u32x4){A.sad[4], A.sad[5], A.sad[6], A.sad[7]}, 0.f);
    } else {
      unit(fr, (u32x4){A.sad[0], A.sad[1], A.sad[2], A.sad[3]}, 0.f);
      *(float*)(smem + L_MI + 4 * c) = __uint_as_float((u32)(ec - 14) << 23);
    }
  };
  auto k_get = [&](u32 (&kh)[11], const P0A& A) __attribute__((always_inline)) {
#pragma unroll
    for (int e = 0; e < 11; ++e) kh[e] = *(const LAS u16*)((const LAS char*)(size_t)A.gad[e] + KTO);
    __builtin_amdgcn_sched_barrier(0);
  };
  auto k_copies = [&](const u32 (&kh)[11], int lane, bool low) __attribute__((always_inline)) {
    u32 kw[6];
#pragma unroll
    for (int e = 0; e < 5; ++e) kw[e] = __builtin_amdgcn_perm(kh[2 * e + 1], kh[2 * e], 0x05040100u);
    kw[5] = kh[10];
    if (low && (w & 1) && lane == 63) { kw[4] = 0; kw[5] = 0; }
    build_copies(smem, c, (w & 1) * 64 + lane + (low ? 128 : 0), kw);
    __builtin_amdgcn_sched_barrier(0);
  };
  __builtin_amdgcn_sched_barrier(0);
  int w2 = w;
  asm volatile("" : "+s"(w2));
  if (w < 4) {
  P0A pa;
  p0_addr(pa, lane, w & 1);
  LAS char* wa = (LAS char*)(size_t)opaque((u32)(ftb + (tid & 127) * 4));
  u32 tg;
  const bool lo = pb < 16;
  const int xc8 = lane >> 3, xrp = lane & 7;
  const int xts = lo ? 128 : XTS, xt2 = 2 * xts;
  const int xto = (int)opaque((u32)(lo ? OFF_X1 + (pb * 4 + w) * XBS + (ps * 32 + 4 * xc8) * 128 + xrp * 16
                                       : XTO(ps * 32 + 4 * xc8) + ((pb & 15) * 64 + 16 * w + 2 * xrp) * 8));
  auto xpub = [&]() __attribute__((always_inline)) {
    u32x4 q[4];
    u32x4 xt[2];
    __builtin_amdgcn_sched_barrier(0);
    asm volatile("s_waitcnt vmcnt(0)\n\tv_add_u32 %0, 1, v254" : "=v"(tg) :: "memory");
    XT_GET(xt);
    const int sl = ((lane & 7) * 8 + (lane >> 3)) * 4;
#pragma unroll
    for (int e = 0; e < 8; ++e) xt[e & 1][e >> 1] = (u32)__builtin_amdgcn_ds_bpermute(sl, (int)xt[e & 1][e >> 1]);
#pragma unroll
    for (int e = 0; e < 4; ++e) q[e] = (u32x4){xt[0][e], x0_mark(tg), xt[1][e], x0_mark(tg)};
    asm volatile("" : "+v"(q[0]), "+v"(q[1]), "+v"(q[2]), "+v"(q[3]));
#pragma unroll
    for (int e = 0; e < 4; ++e) stw(wrs, xto + (e & 1) * xts + (e >> 1) * xt2, q[e]);
    asm volatile("s_nop 1" :: "v"(q[0]), "v"(q[1]), "v"(q[2]), "v"(q[3]) : "memory");
    ST(0, 3);
    __builtin_amdgcn_sched_barrier(0);
  };
  u32 ha, hz, hs, ht;
  C1Pre cp;
  int xs0, xs1, xl0, xl1, xv;
  u32 za;
  auto setup1 = [&]() __attribute__((always_inline)) {
    SB();
    const int t = (w & 1) * 64 + lane;
    ha = (u32)(L_KT + c * TBS - 0x7E00 + 2 * (0x44FF - t)); hz = ha - (t == 127 ? 2 : 0);
    hs = (lane & 1) ? 0x01000302u : 0x03020302u; ht = (lane & 1) ? 0x01000100u : 0x01000302u;
    asm volatile("" : "+v"(ha), "+v"(hz), "+v"(hs), "+v"(ht));
    SB();
  };
  auto setup2 = [&]() __attribute__((always_inline)) {
    SB();
    c1_pre(cp, kp, w, lane);
    xs0 = OFF_X1 + RB_OF(bid) * XBS + w * 8192; xs1 = xs0 + 4096; xl0 = L_TA + w * 8192; xl1 = xl0 + 4096; xv = x_lane(lane);
    za = (u32)(((lane >> 1) & 7) * CSTR + 4096 + (lane & 1) * 16);
    asm volatile("" : "+s"(xs0), "+s"(xs1), "+s"(xl0), "+s"(xl1), "+v"(xv), "+v"(za));
    SB();
  };
  {
    float ar, ai;
#define B0() do { SB(); __builtin_amdgcn_s_barrier(); SB(); } while (0)
    B0();
    asm volatile("s_waitcnt vmcnt(3)\n\tv_lshlrev_b32 %0, 16, v248\n\tv_lshlrev_b32 %1, 16, v249" : "=v"(ar), "=v"(ai) :: "memory");
    asm volatile("s_waitcnt lgkmcnt(0)" : "+s"(ldtw), "+s"(dskw) :: "memory");
    ST(0, 1);
    const float dt = rbf(__expf(c ? hi2f(ldtw) : lo2f(ldtw)));
    const float zr = dt * ar * 1.4426950408889634f, zi = dt * ai * 0.15915494309189535f;
    float cs = __builtin_amdgcn_exp2f(fmaxf(zr, 0.f) * -1928.f);
    f32x2 P[8], Q1, R[17];
    { float a_, b_; cexp2(zr, zi, a_, b_); P[1] = (f32x2){a_, b_}; cexp2(zr * 128.f, zi * 128.f, a_, b_); Q1 = (f32x2){a_, b_}; }
    asm volatile("" : "+v"(cs), "+v"(P[1]), "+v"(Q1));
    xpub();
    P[2] = CMUL(P[1], P[1]);
    const f32x2 Q2 = CMUL(Q1, Q1);
    R[1] = Q1 * cs; R[2] = Q2 * cs;
    P[3] = CMUL(P[2], P[1]); P[4] = CMUL(P[2], P[2]);
    const f32x2 Q4 = CMUL(Q2, Q2);
    R[3] = CMUL(R[1], Q2); R[4] = Q4 * cs;
    P[5] = CMUL(P[4], P[1]); P[6] = CMUL(P[3], P[3]); P[7] = CMUL(P[4], P[3]);
#pragma unroll
    for (int j = 5; j <= 16; ++j) R[j] = CMUL(R[j - 4], Q4);
    asm volatile("" : "+v"(R[13]), "+v"(R[14]), "+v"(R[15]), "+v"(R[16]), "+v"(P[7]), "+v"(P[6]), "+v"(P[5]));
    PUT(0, cs, 0.f); PUT(8, R[1][0], R[1][1]);
#pragma unroll
    for (int r = 1; r < 8; ++r) { PUT(r, P[r][0] * cs, P[r][1] * cs); PUT(8 + r, CMR(R[1], P[r]), CMI(R[1], P[r])); }
    asm volatile("" ::: "memory");
#pragma unroll
    for (int j = 2; j < 4; ++j) {
      PUT(16 + 4 * (j - 2), R[j][0], R[j][1]);
#pragma unroll
      for (int r = 1; r < 4; ++r) PUT(16 + 4 * (j - 2) + r, CMR(R[j], P[2 * r]), CMI(R[j], P[2 * r]));
    }
#pragma unroll
    for (int j = 4; j < 8; ++j) { PUT(24 + 2 * (j - 4), R[j][0], R[j][1]); PUT(25 + 2 * (j - 4), CMR(R[j], P[4]), CMI(R[j], P[4])); }
#pragma unroll
    for (int j = 8; j <= 16; ++j) PUT(24 + j, R[j][0], R[j][1]);
    __builtin_amdgcn_sched_barrier(0);
  }
  ST(0, 2);
  cnt_add(smem, C_F, lane);
  u32 stag = __builtin_amdgcn_readfirstlane(tg), g2;
  asm volatile("" : "+s"(stag));
  cnt_spin(smem, C_F, 8);
  k_unit(pa, c ? hi2f(dskw) : lo2f(dskw), (tid & 127) == 0);
  cnt_add(smem, C_A, lane);
  setup1();
  setup2();
  cnt_spin(smem, C_A, 4);
  ST(0, 4);
  {
    u32 kl[11];
    k_get(kl, pa);
    const u32 h0 = *(const LAS u16*)(size_t)ha, h1 = *(const LAS u16*)(size_t)(ha + 2), h2 = *(const LAS u16*)(size_t)hz;
    SB();
    u32x4 zz = {0, 0, 0, 0};
    asm volatile("" : "+v"(zz));
    *(LAS u32x4*)(size_t)za = zz;
    k_copies(kl, lane, true);
    asm volatile("" :: "v"(zz));
    cnt_add(smem, C_A, lane);
    ST(0, 5);
    {
      const u32 p = h0 | (h1 << 16), hh = __builtin_amdgcn_perm(p, p, 0x03020302u), ll = __builtin_amdgcn_perm(p, p, 0x01000100u);
      const u32 hx = __builtin_amdgcn_perm(p, p, hs), xl = __builtin_amdgcn_perm(p, p, ht);
      u32x4 c0 = {hh, hx, ll, ll}, c1 = {hh, xl, ll, ll}, c2 = {hx, ll, ll, ll}, c3 = {xl, ll, ll, h0 | (h2 << 16)};
      asm volatile("" : "+v"(c0), "+v"(c1), "+v"(c2), "+v"(c3));
      char* cop = smem + c * 4 * CSTR + ((w & 1) * 64 + lane) * 16;
      *(u32x4*)(cop + 0 * CSTR) = c0; *(u32x4*)(cop + 1 * CSTR) = c1; *(u32x4*)(cop + 2 * CSTR) = c2; *(u32x4*)(cop + 3 * CSTR) = c3;
      asm volatile("s_nop 1" :: "v"(c0), "v"(c1), "v"(c2), "v"(c3) : "memory");
      cnt_add(smem, C_KH, lane);
    }
  }
  cnt_spin(smem, C_A, 8);
  bf16x8 wof[16];
  u32x4 pfl, wfl;
  XTake xt;
  AttC ac;
  a_conv1(wrs, w, cp, pfl, wfl, g2);
  asm volatile("" : "+v"(pfl), "+v"(wfl));
  SB();
  KSLP(15, SLP_XA1);
  tags_wait(pfl, wrs, OFF_ST0 + tid * 16, stag, 19);
  dma4k(wrs, xv, xs0, xl0);
  dma4k(wrs, xv, xs1, xl1);
  KSLP(14, SLP_WE);
  SB();
  ConvK ck;
  a_blayer<true>(smem, wrs, ws, out, tid, w, wof, stag, wfl, ck, xt, ac);
  ST(1, 2);
  a_conv<false>(smem, wrs, RB_OF(bid), w, ck);
  a_blayer<false>(smem, wrs, ws, out, tid, w, wof, stag, wfl, ck, xt, ac);
  ST_OUT();
  }
  if (w2 >= 4) {
    const int lane = lane_id();
    LAS char* wa = (LAS char*)(size_t)opaque((u32)(ftb + (w & 1) * 256 + lane * 4));
    u32 tag;
    u32 st0, st1;
    int pbo;
    auto cols = [&]() __attribute__((always_inline)) {
    {
      float ar, ai, br, bi, cr, ci;
      asm volatile("v_lshlrev_b32 %0, 16, v248\n\tv_lshlrev_b32 %1, 16, v249\n\tv_lshlrev_b32 %2, 16, v250\n\t"
                   "v_lshlrev_b32 %3, 16, v251\n\tv_lshlrev_b32 %4, 16, v252\n\tv_lshlrev_b32 %5, 16, v253"
                   : "=v"(ar), "=v"(ai), "=v"(br), "=v"(bi), "=v"(cr), "=v"(ci) :: "memory");
      asm volatile("s_waitcnt lgkmcnt(0)" : "+s"(ldtw) :: "memory");
      const float dt = rbf(__expf(c ? hi2f(ldtw) : lo2f(ldtw)));
      const float dbr = dt * br, dbi = dt * bi;
      const float zr = dt * ar * 1.4426950408889634f, zi = dt * ai * 0.15915494309189535f;
      const float zp = fmaxf(zr, 0.f), g1 = __builtin_amdgcn_exp2f(zp * 1928.f);
      const float wr = (cr * dbr - ci * dbi) * g1, wi = (cr * dbi + ci * dbr) * g1;
      float bd = (fabsf(wr) + fabsf(wi)) * __builtin_amdgcn_exp2f(zp * 120.f);
      f32x2 S1, C[16];
      { float a_, b_; cexp2(zr * 8.f, zi * 8.f, a_, b_); S1 = (f32x2){a_, b_}; }
      bd = dpp_add<0x111, 0xf>(bd); bd = dpp_add<0x112, 0xf>(bd);
      const f32x2 S2 = CMUL(S1, S1);
      bd = dpp_add<0x114, 0xf>(bd); bd = dpp_add<0x118, 0xf>(bd);
      const f32x2 S4 = CMUL(S2, S2);
      bd = dpp_add<0x142, 0xa>(bd); bd = dpp_add<0x143, 0xc>(bd);
      bd = __int_as_float(__builtin_amdgcn_readlane(__float_as_int(bd), 63));
      *(float*)(smem + L_P0 + 512 + g * 4) = bd;
      const float sc = __uint_as_float((u32)(268 - bexp(bd)) << 23);
      C[0] = (f32x2){wr * sc, wi * sc};
      C[1] = CMUL(C[0], S1); C[2] = CMUL(C[0], S2); C[3] = CMUL(C[1], S2);
#pragma unroll
      for (int e = 4; e < 16; ++e) C[e] = CMUL(C[e - 4], S4);
#pragma unroll
      for (int e = 0; e < 16; ++e) *(LAS u32*)(wa + (41 + e) * FTS) = pkhn(C[e][0], C[e][1]);
    }
      cnt_add(smem, C_F, lane);
      ST(0, 2);
    };
    auto stats = [&]() __attribute__((always_inline)) {
    {
      const u32 ONES = opaque(0x3F803F80u);
      u32x4 xt[2];
      XT_GET(xt);
      float sm[2] = {0.f, 0.f}, sq[2] = {0.f, 0.f};
#pragma unroll
      for (int e = 0; e < 8; ++e) { sm[e & 1] = dot2acc(xt[e & 1][e >> 1], ONES, sm[e & 1]); sq[e & 1] = dot2acc(xt[e & 1][e >> 1], xt[e & 1][e >> 1], sq[e & 1]); }
      const bool b0 = lane & 1, b1 = lane & 2;
      const float v0 = (b0 ? sm[1] : sm[0]) + dpp_mov<DPP_XOR1>(b0 ? sm[0] : sm[1]), v1 = (b0 ? sq[1] : sq[0]) + dpp_mov<DPP_XOR1>(b0 ? sq[0] : sq[1]);
      float v = (b1 ? v1 : v0) + dpp_mov<DPP_XOR2>(b1 ? v0 : v1);
      v += dpp_mov<0x124>(v); v += dpp_mov<DPP_ROR8>(v);
      auto r = __builtin_amdgcn_permlane16_swap(__float_as_uint(v), __float_as_uint(v), false, false);
      v = __uint_as_float(r[0]) + __uint_as_float(r[1]);
      auto q = __builtin_amdgcn_permlane32_swap(__float_as_uint(v), __float_as_uint(v), false, false);
      v = (__uint_as_float(q[0]) + __uint_as_float(q[1])) * (1.f / 512.f);
      st0 = pack_stats(v, fmaxf(dpp_mov<DPP_XOR2>(v) - v * v, 0.f));
      st1 = dpp_u<DPP_XOR1>(st0);
    }
    };
    auto publish = [&]() __attribute__((always_inline)) {
      asm volatile("v_add_u32 %0, 1, v254" : "=v"(tag) :: "memory");
      stw(wrs, pbo, (u32x4){st0, tag, st1, tag});
      __builtin_amdgcn_sched_barrier(0);
    };
#define VMW(n_) asm volatile("s_waitcnt vmcnt(%0)" :: "n"(n_) : "memory")
    constexpr int PTN = 2;
    const int ut = 16 + (bid >> 3), us = ut / 3, um = ut % 3;
    const rsrc_t qrs = __builtin_amdgcn_make_buffer_rsrc((void*)wqkv, 0, 1572864, 0x00020000), ors = __builtin_amdgcn_make_buffer_rsrc((void*)wo, 0, 524288, 0x00020000);
    const int wv0 = (8 * g + (lane >> 3)) * 3072 + (((lane & 7) ^ ((lane >> 3) & 6)) << 4);
#define W_DMA1(r_, v_, s_, l_) asm volatile("s_mov_b32 m0, %3\n\ts_nop 0\n\tbuffer_load_dwordx4 %0, %1, %2 offen lds" :: "v"(v_), "s"(r_), "s"(s_), "s"(l_) : "memory")
    W_DMA1(qrs, wv0, us * 98304 + um * 1024 + (bid & 7) * 128, L_WR + g * 1024);
    const int hs = (3 - (int)(kb[2] >> 7)) & 7, xt_ = bid - 117, xs = (xt_ / 3) * 98304 + (xt_ % 3) * 1024;
    const bool xp = bid >= 128 && bid < 133;
    W_DMA1(xp ? qrs : ors, xp ? wv0 : ((bid >> 3) * 32 + 8 * g + (lane >> 3)) * 1024 + (((lane & 7) ^ (((lane >> 3) & 2) | ((g & 1) << 2))) << 4), xp ? xs + hs * 128 : (bid & 7) * 128, L_WR + 4096 + g * 1024);
    B0();
    const int ln = lane;
    pbo = (int)opaque((u32)(lane == 0 ? OFF_SG + gr * 64 + g * 16 : WSB));
    const int wvo = (int)opaque((u32)(wv0 + hb * 128)), wvx = (int)opaque((u32)(hb == hs ? WSB : wvo));
#define WSO(s_) ((s_) < 4 ? 40960 + (s_) * 12288 : L_WX - L_WS)
    auto wraw = [&](auto S_) __attribute__((always_inline)) { constexpr int s = decltype(S_)::value; w_dma(qrs, s < 4 ? wvo : wvx, s < 3 ? wvo : wvx, s * 98304, L_WS + WSO(s) + g * 1024); };
    __builtin_amdgcn_s_setprio(1);
    VMW(8 + PTN);
    ST(0, 1);
    stats();
    VMW(7 + PTN);
    publish();
    ST(0, 10);
    VMW(2 + PTN);
    cols();
    SB();
    __builtin_amdgcn_s_setprio(0);
    static_for<0, R1D>(wraw);
    VMW(3 * R1D + 1); cnt_add(smem, C_B, lane);
    SB();
    const u32 wga = opaque((u32)(L_WS + (ln >> 2) * 128 + (((2 * g + ((ln & 3) >> 1)) ^ ((ln >> 2) & 6)) << 4) + (ln & 1) * 8)), wgb = opaque(wga + 53248u);
    const u32 to = opaque(xp ? wga + (L_WR - L_WS + 4096) : (u32)(L_WR + 4096 + (8 * (lane >> 4) + ((lane >> 2) & 3)) * 128 + (((2 * g + ((lane & 3) >> 1)) ^ (((lane >> 2) & 2) | ((lane >> 2) & 4))) << 4) + (lane & 1) * 8));
    const u32 tp = opaque(to + (xp ? 2048 : 512));
    const int us0 = (int)opaque((u32)(OFF_WQ + us * 98304 + ((bid & 7) * 4 + g) * 3072 + um * 1024 + lane * 16));
    const int us1 = (int)opaque((u32)((bid < 128 ? OFF_WO + (((bid & 7) * 16 + (bid >> 3)) * 4 + g) * 1024 : xp ? OFF_WQ + xs + (hs * 4 + g) * 3072 : WSB) + lane * 16));
    cnt_spin(smem, C_B, 4);
    {
#define TR4(a_) __builtin_amdgcn_ds_read_tr16_b64_v4i16((LAS bf16x4*)(size_t)(a_))
      const bf16x4 q0 = TR4(wgb + (L_WR - L_WS - 53248)), q1 = TR4(wgb + (L_WR - L_WS - 53248 + 2048)), q2 = TR4(to), q3 = TR4(tp);
      cnt_add(smem, C_B, lane);
      stw(wrs, us0, __builtin_bit_cast(u32x4, __builtin_shufflevector(q0, q1, 0, 1, 2, 3, 4, 5, 6, 7)));
      stw(wrs, us1, __builtin_bit_cast(u32x4, __builtin_shufflevector(q2, q3, 0, 1, 2, 3, 4, 5, 6, 7)));
      SB();
    }
    static_for<R1D, R1F>(wraw);
    SB();
    const u32 stag = __builtin_amdgcn_readfirstlane(tag);
    const int fs0 = OFF_ST0 + 4096 + ((bid & 7) * 4 + g) * 128 + (bid >> 3) * 4, fs1 = bid < 128 ? fs0 + 4096 : bid < 136 ? OFF_ST0 + 8192 + (hs * 4 + g) * 128 + (bid - 112) * 4 : WSB;
    const int fso = (int)opaque((u32)(lane == 0 ? fs0 : lane == 1 ? fs1 : WSB));
    const int wbo = (int)opaque((u32)((hb * 4 + g) * 3072 + ln * 16));
    const int fo = (int)opaque((u32)(OFF_ST0 + 4096 + (hb * 4 + g) * 128 + (hb == hs && (ln & 8) ? 4096 + 64 + (ln & 1) * 16 : (ln & 7) * 16)));
    const int r0 = 256 * g + 2 * ln;
    const int sto = (int)opaque((u32)(OFF_SG + r0 * 8)), xto = (int)opaque((u32)(XTO(kp) + r0 * 8));
    const int xlo = (int)opaque((u32)(OFF_X1 + (16 * g + (ln >> 3)) * XBS + kp * 128 + (ln & 7) * 16));
    const u32 uw = opaque((u32)(L_U + (256 + r0) * 2)), xw = opaque((u32)(L_X + r0 * 4));
    const int mso = (int)opaque((u32)(ln == 0 ? OFF_ST0 + kp * 16 + g * 4 : WSB));
    const int p2o = (int)opaque((u32)(OFF_X2 + (kp * XS_ + 256 * g + 4 * ln) * 4)), pso = (int)opaque((u32)(g == 0 && ln < 8 ? OFF_ST2 + bid * 128 + ln * 16 : WSB));
    SB();
    VMW(3 * (R1F - R1D));
    __builtin_amdgcn_raw_buffer_store_b32(tag, wrs, fso, 0, 16);
    ST(0, 6);
    SB();
    static_for<R1F, 5>(wraw);
    W_DMA1(qrs, wvx, 5 * 98304, L_SP + g * 1024);
    bf16x8 wf[48];
    GPre gp;
    {
      auto wget = [&](auto S_) __attribute__((always_inline)) {
        constexpr int s = decltype(S_)::value, o = WSO(s) >= 53248 ? WSO(s) - 53248 : WSO(s);
        w_get<3 * s, o>(wf[3 * s], WSO(s) >= 53248 ? wgb : wga);
        w_get<3 * s + 1, o + 4096>(wf[3 * s + 1], WSO(s) >= 53248 ? wgb : wga);
        w_get<3 * s + 2, o + 8192>(wf[3 * s + 2], WSO(s) >= 53248 ? wgb : wga);
      };
      const u32 mk = x0_mark(stag);
      auto wld = [&](auto I_) __attribute__((always_inline)) { w_ld<decltype(I_)::value>(wf[decltype(I_)::value], wbo, wrs); };
      u32x4 gs[4], gx[4];
#define G_LD(e_) do { gs[e_] = ldws(wrs, sto + ((e_) & 1) * 1024, ((e_) >> 1) * 8192); gx[e_] = (e_) < 2 ? ldws(wrs, xlo, (e_) * 8 * XBS) : ldw(wrs, xto + ((e_) - 2) * 1024); } while (0)
#define G_B2(v_, t_) opaque((v_[1] ^ (t_)) | (v_[3] ^ (t_)))
#define G_OK(h_) (__builtin_amdgcn_ballot_w64(((G_B2(gs[2 * (h_)], stag) | G_B2(gs[2 * (h_) + 1], stag)) | (G_B2(gx[2 * (h_)], mk) | G_B2(gx[2 * (h_) + 1], mk))) != 0u) == 0ull)
      auto take = [&](auto H_) __attribute__((always_inline)) {
        constexpr int H = decltype(H_)::value;
        if (__builtin_expect(!G_OK(H), 0)) {
          for (unsigned spins = 0;; ++spins) {
            FAILC(H ? 4 : 0);
            __builtin_amdgcn_s_sleep(SLP_U);
            asm volatile("" ::: "memory");
            G_LD(2 * H); G_LD(2 * H + 1);
            if (G_OK(H) || spins > SPIN_CAP) break;
          }
        }
#pragma unroll
        for (int e = 2 * H; e < 2 * H + 2; ++e) {
          const float rs0 = __builtin_amdgcn_rcpf(hi2f(gs[e][0])), rs1 = __builtin_amdgcn_rcpf(hi2f(gs[e][2])), n0 = -lo2f(gs[e][0]) * rs0, n1 = -lo2f(gs[e][2]) * rs1;
          const int r = (e & 1) * 128 + (e >> 1) * 1024;
          *(LAS u32*)(size_t)(uw + r * 2) = pkh(fmaf(lo2f(gx[e][0]), rs0, n0), fmaf(lo2f(gx[e][2]), rs1, n1));
          *(LAS u32*)(size_t)(uw + r * 2 + 4608) = pkh(fmaf(hi2f(gx[e][0]), rs0, n0), fmaf(hi2f(gx[e][2]), rs1, n1));
          *(LAS u32x2*)(size_t)(xw + r * 4) = (u32x2){gx[e][0], gx[e][2]};
        }
        __builtin_amdgcn_sched_barrier(0);
      };
      KSLP(16, SLP_G);
      asm volatile("" ::: "memory");
#pragma unroll
      for (int e = 0; e < 4; ++e) G_LD(e);
      __builtin_amdgcn_sched_barrier(0);
      ST(0, 5);
      f32x4 cs[3];
      u32x4 SENT = {0xFFFFFFFFu, 0xFFFFFFFFu, 0xFFFFFFFFu, 0xFFFFFFFFu};
      const u32 o1 = ((ln >> 1) & 1) == (ln >> 5) ? 0x3F803F80u : 0u;
      bf16x8 one8 = __builtin_bit_cast(bf16x8, (u32x4){o1, o1, o1, o1});
      u32 sidx = (ln & 1) ? 0xEEEEu : 0x4444u;
      cs[0] = cs[1] = cs[2] = (f32x4){0.f, 0.f, 0.f, 0.f};
      asm volatile("" : "+v"(one8), "+v"(sidx), "+v"(cs[0]), "+v"(cs[1]), "+v"(cs[2]), "+v"(SENT));
      auto wsum = [&](auto I_) __attribute__((always_inline)) { constexpr int i = 2 * decltype(I_)::value + 1; w_sum<i>(cs[i % 3], one8, sidx, wf); };
      cnt_spin(smem, C_B, 8);
      __builtin_amdgcn_s_setprio(1);
      VMW(8);
      cnt_add(smem, C_W, ln);
      u32x4 fl;
      asm volatile("" : "+v"(gs[0]), "+v"(gs[1]), "+v"(gx[0]), "+v"(gx[1]) :: "memory");
      take(IC<0>{});
      cnt_add(smem, C_U, ln);
      __builtin_amdgcn_raw_buffer_store_b32(tag, wrs, mso, 0, 16);
      ST(0, 3);
      stw(wrs, p2o, SENT); __builtin_amdgcn_raw_buffer_store_b128(SENT, wrs, p2o, 4096, 16);
      stw(wrs, pso, __builtin_bit_cast(u32x4, cs[0]));
      asm volatile("s_nop 1" :: "v"(SENT) : "memory");
      SB();
      auto rest = [&]() __attribute__((always_inline)) {
        cnt_spin(smem, C_W, 4);
        static_for<0, 5>(wget);
        w_get<15, L_SP - L_WS - 53248>(wf[15], wgb);
        cnt_add(smem, C_B, ln);
        take(IC<1>{});
        cnt_add(smem, C_P2, ln);
        ST(0, 4);
      };
      rest();
      { KSLP(17, SLP_FL); fl = ldw(wrs, fo); SB(); }
      asm volatile("" : "+v"(gs[2]), "+v"(gs[3]), "+v"(gx[2]), "+v"(gx[3]), "+v"(fl) :: "memory");
      tags_wait(fl, wrs, fo, stag, 15);
      asm volatile("s_waitcnt vmcnt(0) lgkmcnt(0)" ::: "memory");
      cnt_add(smem, C_P3, ln);
      ST(0, 7);
      asm volatile("s_cmp_lg_u32 %5, %6\n\ts_cbranch_scc1 1f\n\tbuffer_load_dwordx4 %0, %7, %8, %9 offen offset:2048 sc1\n\t"
                   "buffer_load_dwordx4 %1, %7, %8, %10 offen sc1\n\tbuffer_load_dwordx4 %2, %7, %8, %10 offen offset:1024 sc1\n\tbuffer_load_dwordx4 %3, %7, %8, %10 offen offset:2048 sc1\n\t"
                   "buffer_load_dwordx4 %4, %7, %8, %11 offen sc1\n1:"
                   : "+{v[120:123]}"(wf[11]), "+{v[124:127]}"(wf[12]), "+{v[128:131]}"(wf[13]), "+{v[132:135]}"(wf[14]), "+{v[136:139]}"(wf[15])
                   : "s"(hb), "s"(hs), "v"(wbo), "s"(wrs), "s"(OFF_WQ + 3 * 98304), "s"(OFF_WQ + 4 * 98304), "s"(OFF_WQ + 5 * 98304) : "memory", "scc");
      static_for<45, 48>(wld);
      static_for<16, 45>(wld);
      __builtin_amdgcn_s_setprio(0);
      ST(0, 9);
      g_pre<true>(gp, g, stag);
      asm volatile("s_waitcnt lgkmcnt(0)" ::: "memory");
      static_for<0, 5>(wsum);
      cnt_spin(smem, C_B, 12);
      VMW(29);
#pragma unroll
      for (int e = 0; e < 3; ++e) *(LAS bf16x8*)(size_t)(gp.wx + e * 1024) = wf[45 + e];
      static_for<22, 24>(wsum);
      static_for<5, 7>(wsum);
      VMW(12); static_for<7, 16>(wsum);
      VMW(6); static_for<16, 19>(wsum);
      VMW(2); static_for<19, 21>(wsum);
      VMW(0); static_for<21, 22>(wsum);
      asm volatile("s_nop 7" : "+v"(cs[0]), "+v"(cs[1]), "+v"(cs[2]));
#pragma unroll
      for (int e = 0; e < 3; ++e) *(LAS float*)(size_t)(gp.fr + 512 + e * 64) = (cs[e][0] + cs[e][1]) + (cs[(e + 2) % 3][2] + cs[(e + 2) % 3][3]);
      asm volatile("" ::: "memory");
      ST(0, 8);
    }
    {
      ConvPre pa2, pb2;
      u32x4 xa, xb;
      int ln2, l2, xo2, xo, so;
      u32 ua, rp0, rp1;
      float isc;
      g_blayer<true>(smem, wrs, g, stag, wf, gp, [&]() __attribute__((always_inline)) {
        ln2 = lane_id();
        l2 = 256 * g + 4 * ln2;
        xo2 = (int)opaque((u32)(OFF_X3 + (8 * g + (ln2 >> 3)) * XBS + kp * 128 + (ln2 & 1) * 64 + (ln2 & 6) * 8));
        xo = (int)opaque((u32)(OFF_X2 + (kp * XS_ + l2) * 4)); so = (int)opaque((u32)(OFF_ST2 + (256 * g + ln2) * 16));
        ua = opaque((u32)(L_U + (256 + l2) * 2));
        rp0 = opaque((u32)(L_REDA + (ln2 & 1) * 8192 + (g * 64 + (ln2 ^ ((ln2 >> 3) & 3))) * 16)); rp1 = opaque(rp0 ^ 16u);
        isc = *(const float*)(smem + L_MI + 4 * (ln2 & 1));
        asm volatile("" : "+v"(isc));
      });
#define VM0() __builtin_amdgcn_s_waitcnt(0x0F70)
      KSLP(9, SLP_C2);
      u32x4 gg[4];
      ST(1, 2);
      xa = ldw(wrs, xo);
      asm volatile("" : "+v"(xa));
      if (__builtin_expect(!x2_ok(xa), 0)) { FAILC(16); __builtin_amdgcn_s_sleep(SLP_XP); xa = x2_ld(wrs, kp, l2); x2_poll(wrs, kp, l2, xa); VM0(); }
      KSLP(10, SLP_S);
      asm volatile("" ::: "memory");
#pragma unroll
      for (int e = 0; e < 4; ++e) gg[e] = ldw(wrs, so + e * 1024);
      SB();
      conv_unp(pa2, xa);
      asm volatile("" : "+v"(gg[0]), "+v"(gg[1]), "+v"(gg[2]), "+v"(gg[3]));
      if (__builtin_expect(!st2_ok(gg), 0)) { FAILC(7); __builtin_amdgcn_s_sleep(SLP_SP); st2_ld(wrs, l2, gg); st2_poll(wrs, l2, gg); VM0(); }
      conv2_in<false>(smem, ua, pa2, xa, gg);
      ST(1, 3);
      lds_barrier();
      ST(1, 7);
      conv_res(pa2);
      const int so2 = (int)opaque((u32)(so + 16384));
      auto rq_b = [&]() __attribute__((always_inline)) {
        KSLP(11, SLP_C3);
        asm volatile("" ::: "memory");
        xb = ldws(wrs, xo, 4096);
        SB();
#pragma unroll
        for (int e = 0; e < 4; ++e) gg[e] = ldw(wrs, so2 + e * 1024);
        SB();
      };
      rq_b();
      __builtin_amdgcn_s_barrier();
      asm volatile("" ::: "memory");
      ST(1, 8);
      stw(wrs, xo2, conv_epi(rp0, rp1, 0, isc, pa2));
      SB();
      asm volatile("" : "+v"(xb), "+v"(gg[0]), "+v"(gg[1]), "+v"(gg[2]), "+v"(gg[3]) :: "memory");
      ST(1, 4);
      if (__builtin_expect(!x2st_ok(xb, gg), 0)) {
        FAILC(18);
        xb = x2_ld(wrs, kp, l2 + 1024);
        st2_ld(wrs, l2 + 1024, gg);
        if (!x2_ok(xb)) {
          x2_poll(wrs, kp, l2 + 1024, xb);
          st2_ld(wrs, l2 + 1024, gg);
        }
        st2_poll(wrs, l2 + 1024, gg);
        VM0();
      }
      ST(1, 1);
      const int rb = RB_OF(bid);
      if (XPB == 2) { x_dma(wrs, OFF_X3, rb, L_TA, g, ln2); SB(); }
      conv2_in<true>(smem, ua + 2048, pb2, xb, gg);
      ST(1, 5);
      lds_barrier();
      ST(1, 6);
      if (XPB == 1) { KSLP(12, SLP_XA2); x_dma(wrs, OFF_X3, rb, L_TA, g, ln2); SB(); }
      conv_res(pb2);
      if (XPB == 0) { KSLP(12, SLP_XA2); x_dma(wrs, OFF_X3, rb, L_TA, g, ln2); }
      SB();
      g_pre<false>(gp, g, stag);
      const int xo3 = (int)opaque((u32)(xo2 + 32 * XBS));
      if (GPE) __builtin_amdgcn_s_setprio(GPE);
      asm volatile("s_waitcnt vmcnt(0)\n\ts_barrier" ::: "memory");
      ST(1, 9);
      u32x4 ov = conv_epi(rp0, rp1, 16384, isc, pb2);
      stw(wrs, xo3, ov);
      if (GPE) __builtin_amdgcn_s_setprio(0);
      asm volatile("s_nop 1" :: "v"(ov));
    }
    g_blayer<false>(smem, wrs, g, stag, wf, gp, []() {});

    ST_OUT();
  }
}

__device__ __attribute__((used, noinline)) void k_text_end() {
  asm volatile(
  ".Lg_redo:\n\ts_waitcnt lgkmcnt(0)\n\t"
  "ds_read_b64_tr_b16 v[72:73], v0 offset:0\n\tds_read_b64_tr_b16 v[74:75], v0 offset:1024\n\ts_waitcnt lgkmcnt(0)\n\t"
  "v_mfma_f32_16x16x32_bf16 v[60:63], v[80:83], v[72:75], 0\n\tv_mfma_f32_16x16x32_bf16 v[16:19], v[72:75], v[84:87], 0\n\tv_mfma_f32_16x16x32_bf16 v[52:55], v[76:79], v[72:75], 0\n\t"
  "ds_read_b64_tr_b16 v[72:73], v0 offset:2048\n\tds_read_b64_tr_b16 v[74:75], v0 offset:3072\n\ts_waitcnt lgkmcnt(0)\n\t"
  "v_mfma_f32_16x16x32_bf16 v[60:63], v[92:95], v[72:75], v[60:63]\n\tv_mfma_f32_16x16x32_bf16 v[16:19], v[72:75], v[96:99], v[16:19]\n\tv_mfma_f32_16x16x32_bf16 v[52:55], v[88:91], v[72:75], v[52:55]\n\t"
  "ds_read_b64_tr_b16 v[72:73], v0 offset:8192\n\tds_read_b64_tr_b16 v[74:75], v0 offset:9216\n\ts_waitcnt lgkmcnt(0)\n\t"
  "v_mfma_f32_16x16x32_bf16 v[60:63], v[128:131], v[72:75], v[60:63]\n\tv_mfma_f32_16x16x32_bf16 v[16:19], v[72:75], v[132:135], v[16:19]\n\tv_mfma_f32_16x16x32_bf16 v[52:55], v[124:127], v[72:75], v[52:55]\n\t"
  "ds_read_b64_tr_b16 v[72:73], v0 offset:10240\n\tds_read_b64_tr_b16 v[74:75], v0 offset:11264\n\ts_waitcnt lgkmcnt(0)\n\t"
  "v_mfma_f32_16x16x32_bf16 v[60:63], v[140:143], v[72:75], v[60:63]\n\tv_mfma_f32_16x16x32_bf16 v[16:19], v[72:75], v[144:147], v[16:19]\n\tv_mfma_f32_16x16x32_bf16 v[52:55], v[136:139], v[72:75], v[52:55]\n\t"
  "ds_read_b64_tr_b16 v[72:73], v0 offset:16384\n\tds_read_b64_tr_b16 v[74:75], v0 offset:17408\n\ts_waitcnt lgkmcnt(0)\n\t"
  "v_mfma_f32_16x16x32_bf16 v[60:63], v[176:179], v[72:75], v[60:63]\n\tv_mfma_f32_16x16x32_bf16 v[16:19], v[72:75], v[180:183], v[16:19]\n\tv_mfma_f32_16x16x32_bf16 v[52:55], v[172:175], v[72:75], v[52:55]\n\t"
  "ds_read_b64_tr_b16 v[72:73], v0 offset:18432\n\tds_read_b64_tr_b16 v[74:75], v0 offset:19456\n\ts_waitcnt lgkmcnt(0)\n\t"
  "v_mfma_f32_16x16x32_bf16 v[60:63], v[188:191], v[72:75], v[60:63]\n\tv_mfma_f32_16x16x32_bf16 v[16:19], v[72:75], v[192:195], v[16:19]\n\tv_mfma_f32_16x16x32_bf16 v[52:55], v[184:187], v[72:75], v[52:55]\n\t"
  "ds_read_b64_tr_b16 v[72:73], v0 offset:24576\n\tds_read_b64_tr_b16 v[74:75], v0 offset:25600\n\ts_waitcnt lgkmcnt(0)\n\t"
  "v_mfma_f32_16x16x32_bf16 v[60:63], v[224:227], v[72:75], v[60:63]\n\tv_mfma_f32_16x16x32_bf16 v[16:19], v[72:75], v[228:231], v[16:19]\n\tv_mfma_f32_16x16x32_bf16 v[52:55], v[220:223], v[72:75], v[52:55]\n\t"
  "ds_read_b64_tr_b16 v[72:73], v0 offset:26624\n\tds_read_b64_tr_b16 v[74:75], v0 offset:27648\n\ts_waitcnt lgkmcnt(0)\n\t"
  "v_mfma_f32_16x16x32_bf16 v[60:63], v[236:239], v[72:75], v[60:63]\n\tv_mfma_f32_16x16x32_bf16 v[16:19], v[72:75], v[240:243], v[16:19]\n\tv_mfma_f32_16x16x32_bf16 v[52:55], v[232:235], v[72:75], v[52:55]\n\t"
  "ds_read_b64_tr_b16 v[72:73], v0 offset:4096\n\tds_read_b64_tr_b16 v[74:75], v0 offset:5120\n\ts_waitcnt lgkmcnt(0)\n\t"
  "v_mfma_f32_16x16x32_bf16 v[60:63], v[104:107], v[72:75], v[60:63]\n\tv_mfma_f32_16x16x32_bf16 v[16:19], v[72:75], v[108:111], v[16:19]\n\tv_mfma_f32_16x16x32_bf16 v[52:55], v[100:103], v[72:75], v[52:55]\n\t"
  "ds_read_b64_tr_b16 v[72:73], v0 offset:6144\n\tds_read_b64_tr_b16 v[74:75], v0 offset:7168\n\tds_read_b64_tr_b16 v[28:29], v0 offset:12288\n\tds_read_b64_tr_b16 v[30:31], v0 offset:13312\n\tds_read_b64_tr_b16 v[68:69], v0 offset:14336\n\tds_read_b64_tr_b16 v[70:71], v0 offset:15360\n\tds_read_b64_tr_b16 v[12:13], v0 offset:20480\n\tds_read_b64_tr_b16 v[14:15], v0 offset:21504\n\t"
  "s_waitcnt lgkmcnt(0)\n\ts_branch .Lg_back\n\t"
  ".Lc0_kh:\n\ts_mov_b32 vcc_hi, 0x1000000\n\t.Lc0_kh_s:\n\tds_read_b32 v101, v27 offset:48\n\ts_sub_u32 vcc_hi, vcc_hi, 1\n\ts_cselect_b32 vcc_lo, 0, 4\n\ts_waitcnt lgkmcnt(0)\n\tv_readfirstlane_b32 s61, v101\n\ts_nop 1\n\ts_cmp_lt_u32 s61, vcc_lo\n\ts_cbranch_scc1 .Lc0_kh_s\n\t"
  "s_waitcnt lgkmcnt(0)\n\ts_branch .Lc0_kh_b\n\t"
  ".Lc0_x0:\n\ts_mov_b32 vcc_hi, 0x1000000\n\t.Lc0_x0_s:\n\tds_read_b32 v101, v21 offset:8\n\ts_sub_u32 vcc_hi, vcc_hi, 1\n\ts_cselect_b32 vcc_lo, 0, 2\n\ts_waitcnt lgkmcnt(0)\n\tv_readfirstlane_b32 s61, v101\n\ts_nop 1\n\ts_cmp_lt_u32 s61, vcc_lo\n\ts_cbranch_scc1 .Lc0_x0_s\n\t"
  "ds_read_b128 v[144:147], v20 offset:2048\n\tds_read_b128 v[204:207], v20 offset:3072\n\ts_waitcnt lgkmcnt(0)\n\ts_branch .Lc0_x0_b\n\t"
  ".Lc0_ub:\n\ts_mov_b32 vcc_hi, 0x1000000\n\t.Lc0_ub_s:\n\tds_read_b32 v101, v27 offset:52\n\ts_sub_u32 vcc_hi, vcc_hi, 1\n\ts_cselect_b32 vcc_lo, 0, 4\n\ts_waitcnt lgkmcnt(0)\n\tv_readfirstlane_b32 s61, v101\n\ts_nop 1\n\ts_cmp_lt_u32 s61, vcc_lo\n\ts_cbranch_scc1 .Lc0_ub_s\n\t"
  "s_waitcnt lgkmcnt(0)\n\ts_branch .Lc0_ub_b\n\t"
  ".Lc0_p3:\n\ts_mov_b32 vcc_hi, 0x1000000\n\t.Lc0_p3_s:\n\tds_read_b32 v101, v27 offset:64\n\ts_sub_u32 vcc_hi, vcc_hi, 1\n\ts_cselect_b32 vcc_lo, 0, 4\n\ts_waitcnt lgkmcnt(0)\n\tv_readfirstlane_b32 s61, v101\n\ts_nop 1\n\ts_cmp_lt_u32 s61, vcc_lo\n\ts_cbranch_scc1 .Lc0_p3_s\n\t"
  "s_waitcnt lgkmcnt(0)\n\ts_branch .Lc0_p3_b\n\t"
  ".Lc0_x4:\n\ts_mov_b32 vcc_hi, 0x1000000\n\t.Lc0_x4_s:\n\tds_read_b32 v36, v27 offset:16\n\ts_sub_u32 vcc_hi, vcc_hi, 1\n\ts_cselect_b32 vcc_lo, 0, 4\n\ts_waitcnt lgkmcnt(0)\n\tv_readfirstlane_b32 s61, v36\n\ts_nop 1\n\ts_cmp_lt_u32 s61, vcc_lo\n\ts_cbranch_scc1 .Lc0_x4_s\n\t"
  "ds_read_b128 v[40:43], v20 offset:59392\n\tds_read_b128 v[44:47], v20 offset:60416\n\ts_waitcnt lgkmcnt(0)\n\ts_branch .Lc0_x4_b\n\t"
  ".Lc1_kh:\n\ts_mov_b32 vcc_hi, 0x1000000\n\t.Lc1_kh_s:\n\tds_read_b32 v101, v27 offset:48\n\ts_sub_u32 vcc_hi, vcc_hi, 1\n\ts_cselect_b32 vcc_lo, 0, 4\n\ts_waitcnt lgkmcnt(0)\n\tv_readfirstlane_b32 s61, v101\n\ts_nop 1\n\ts_cmp_lt_u32 s61, vcc_lo\n\ts_cbranch_scc1 .Lc1_kh_s\n\t"
  "s_waitcnt lgkmcnt(0)\n\ts_branch .Lc1_kh_b\n\t"
  ".Lc1_x0:\n\ts_mov_b32 vcc_hi, 0x1000000\n\t.Lc1_x0_s:\n\tds_read_b32 v101, v21 offset:8\n\ts_sub_u32 vcc_hi, vcc_hi, 1\n\ts_cselect_b32 vcc_lo, 0, 2\n\ts_waitcnt lgkmcnt(0)\n\tv_readfirstlane_b32 s61, v101\n\ts_nop 1\n\ts_cmp_lt_u32 s61, vcc_lo\n\ts_cbranch_scc1 .Lc1_x0_s\n\t"
  "ds_read_b128 v[148:151], v20 offset:0\n\tds_read_b128 v[204:207], v20 offset:1024\n\ts_waitcnt lgkmcnt(0)\n\ts_branch .Lc1_x0_b\n\t"
  ".Lc1_ub:\n\ts_mov_b32 vcc_hi, 0x1000000\n\t.Lc1_ub_s:\n\tds_read_b32 v101, v27 offset:52\n\ts_sub_u32 vcc_hi, vcc_hi, 1\n\ts_cselect_b32 vcc_lo, 0, 4\n\ts_waitcnt lgkmcnt(0)\n\tv_readfirstlane_b32 s61, v101\n\ts_nop 1\n\ts_cmp_lt_u32 s61, vcc_lo\n\ts_cbranch_scc1 .Lc1_ub_s\n\t"
  "s_waitcnt lgkmcnt(0)\n\ts_branch .Lc1_ub_b\n\t"
  ".Lc1_p3:\n\ts_mov_b32 vcc_hi, 0x1000000\n\t.Lc1_p3_s:\n\tds_read_b32 v101, v27 offset:64\n\ts_sub_u32 vcc_hi, vcc_hi, 1\n\ts_cselect_b32 vcc_lo, 0, 4\n\ts_waitcnt lgkmcnt(0)\n\tv_readfirstlane_b32 s61, v101\n\ts_nop 1\n\ts_cmp_lt_u32 s61, vcc_lo\n\ts_cbranch_scc1 .Lc1_p3_s\n\t"
  "s_waitcnt lgkmcnt(0)\n\ts_branch .Lc1_p3_b\n\t"
  ".Lc1_x4:\n\ts_mov_b32 vcc_hi, 0x1000000\n\t.Lc1_x4_s:\n\tds_read_b32 v36, v27 offset:16\n\ts_sub_u32 vcc_hi, vcc_hi, 1\n\ts_cselect_b32 vcc_lo, 0, 4\n\ts_waitcnt lgkmcnt(0)\n\tv_readfirstlane_b32 s61, v36\n\ts_nop 1\n\ts_cmp_lt_u32 s61, vcc_lo\n\ts_cbranch_scc1 .Lc1_x4_s\n\t"
  "ds_read_b128 v[40:43], v20 offset:57344\n\tds_read_b128 v[44:47], v20 offset:58368\n\ts_waitcnt lgkmcnt(0)\n\ts_branch .Lc1_x4_b\n\t"
  ".Lcw_end:");
}

extern "C" void kernel_launch(void* const* d_in, const int* in_sizes, int n_in, void* d_out, int out_size,
                              void* d_ws, size_t ws_size, hipStream_t stream) {
  (void)hipFuncSetAttribute((const void*)k_mega, hipFuncAttributeMaxDynamicSharedMemorySize, MEGA_LDS);
  k_mega<<<256, 512, MEGA_LDS, stream>>>((const u16*)d_in[0], (const u16*)d_in[1], (const u16*)d_in[2], (const u16*)d_in[3],
                                         (const u16*)d_in[4], (const u16*)d_in[5], (const u16*)d_in[6], (const u16*)d_in[7],
                                         (const u16*)d_in[8], (const u16*)d_in[10], (const u16*)d_in[11], (char*)d_ws,
                                         (u32*)d_out);
}
#if defined(__HIP_DEVICE_COMPILE__)
#pragma clang attribute pop
#endif
```

```cpp
#if defined(__HIP_DEVICE_COMPILE__)
#pragma clang attribute push (__attribute__((target("no-packed-fp32-ops"))), apply_to = function)
#endif
#include <hip/hip_runtime.h>
#include <hip/hip_bf16.h>
#include <stdint.h>
#define TIDX ((int)__builtin_amdgcn_workitem_id_x())
#define BIDX ((int)__builtin_amdgcn_workgroup_id_x())
#define ST(set, k)
#define ST_INIT()
#define ST_OUT()
#define KATTR
#define FAILC(k)

#define ST_FIX()
#define CST(k)
#define GST(k)
typedef unsigned short u16;
typedef unsigned int u32;
typedef unsigned long long u64;
using bf16x8 = __attribute__((ext_vector_type(8))) short;
using bf16x4 = __attribute__((ext_vector_type(4))) short;
using f16x8 = __attribute__((ext_vector_type(8))) _Float16;
using f16x4 = __attribute__((ext_vector_type(4))) _Float16;
using f32x2 = __attribute__((ext_vector_type(2))) float;
using f32x4 = __attribute__((ext_vector_type(4))) float;
using u32x2 = __attribute__((ext_vector_type(2))) unsigned;
using u32x4 = __attribute__((ext_vector_type(4))) unsigned;
#define LAS __attribute__((address_space(3)))
#define GAS __attribute__((address_space(1)))

#define D_ 512
#define NS_ 128
#define XS_ 2080

#define MB_ (1 << 20)
#define XTO(kp_) (OFF_O1 + (((kp_) >> 4) & 1) * MB_ + ((kp_) >> 5) * 131072 + ((kp_) & 15) * 8192)
#define XTS 8192
#define RB_OF(b_) (((b_) & 7) * 4 + ((b_) >> 6))
#define HB_OF(b_) (((b_) >> 3) & 7)
#define OFF_ST0 (5 * MB_)
#define OFF_TS (OFF_ST0 + 16384)
#define OFF_SG (OFF_ST0 + 32768)
#define OFF_ST2 (OFF_ST0 + 65536)
#define OFF_T5 (OFF_ST0 + 262144)
#define OFF_EP (OFF_ST0 + 131072)
#define OFF_WQ (9 * MB_ - 262144)
#define OFF_WO (10 * MB_ + 393216)
#define OFF_X1 (11 * MB_)
#define OFF_O1 (14 * MB_)
#define OFF_X2 (16 * MB_)
#define OFF_X3 (19 * MB_)
#define OFF_O2 (22 * MB_)
#define OFF_KV2 (26 * MB_)
#define KVO(L1_, rb_, h_) (((L1_) ? OFF_O2 : OFF_KV2) + (((h_) >> 3) & 1) * MB_ + (rb_) * 32768 + (((h_) >> 2) & 1) * 16384 + ((h_) >> 4) * 8192 + ((h_) & 3) * 2048)
#define WSB (32 * MB_)
#define XBS 32896
#define SPIN_CAP (1u << 14)
#define RS1 4
#define RS2 0
#define RS3 0
#define XPB 1
#define GPE 1
#define B6 0
#define OUTA 2
#define OUTB 0
#define WLD 8
#define SLP_FL 4
#define SLP_WE 0
#define R1F 3
#define D9 1300
#define WS5 5
#define SLP_B4 4
#define DB2 450
#define D5 200
#define DQ2 0
#define DO2 800
#define SLP_U 2
#define SLP_F 2
#define SLP_G 0
#define R1D 1
#define WOE 4
#define SLP_H1 0
#define SLP_H2 2
#define WO0 0
#define SLP_XA2 2
#define CPF 5
#define SLP_XP 2
#define SLP_SP 4
#define SLP_S 6
#define SLP_XA1 4
#define SLP_XB1 6
#define SLP_OB1 6
#define DO1 800
#define DO5 4800
#define WX 4
#define WA 0
#define WM 4
#define SLP_C2 3
#define SLP_C3 14

__device__ __forceinline__ float bf2f(u16 v) { return __uint_as_float(((u32)v) << 16); }
typedef __attribute__((ext_vector_type(2))) __bf16 bf16x2v;
__device__ __forceinline__ u32 pk2(float a, float b) {
  f32x2 f = {a, b};
  bf16x2v v = __builtin_convertvector(f, bf16x2v);
  return __builtin_bit_cast(u32, v);
}
__device__ __forceinline__ float rbf(float f) { return __uint_as_float(pk2(f, 0.f) << 16); }
__device__ __forceinline__ float lo2f(u32 w) { return __uint_as_float(w << 16); }
__device__ __forceinline__ float hi2f(u32 w) { return __uint_as_float(w & 0xFFFF0000u); }
template <int CTRL>
__device__ __forceinline__ float dpp_mov(float v) {
  return __int_as_float(__builtin_amdgcn_update_dpp(0, __float_as_int(v), CTRL, 0xf, 0xf, false));
}
#define DPP_XOR1 0xB1
#define DPP_XOR2 0x4E
#define DPP_ROR8 0x128
template <int CTRL, int RMASK>
__device__ __forceinline__ float dpp_add(float v) {
  return v + __int_as_float(__builtin_amdgcn_update_dpp(0, __float_as_int(v), CTRL, RMASK, 0xf, false));
}
__device__ __forceinline__ void wave_sum3(float& a, float& b, float& c) {
#define WS3(CT_, RM_) a = dpp_add<CT_, RM_>(a); b = dpp_add<CT_, RM_>(b); c = dpp_add<CT_, RM_>(c); __builtin_amdgcn_sched_barrier(0);
  WS3(0x111, 0xf) WS3(0x112, 0xf) WS3(0x114, 0xf) WS3(0x118, 0xf) WS3(0x142, 0xa) WS3(0x143, 0xc)
  a = __int_as_float(__builtin_amdgcn_readlane(__float_as_int(a), 63));
  b = __int_as_float(__builtin_amdgcn_readlane(__float_as_int(b), 63));
  c = __int_as_float(__builtin_amdgcn_readlane(__float_as_int(c), 63));
}
__device__ __forceinline__ float rmax(float a, float b) {
  float r;
  asm("v_max_f32 %0, %1, %2" : "=v"(r) : "v"(a), "v"(b));
  return r;
}
__device__ __forceinline__ void lds_barrier() {
  asm volatile("s_waitcnt lgkmcnt(0)" ::: "memory");
  __builtin_amdgcn_s_barrier();
  asm volatile("" ::: "memory");
}
__device__ __forceinline__ void cexp2(float re2, float rev, float& orr, float& oi) {
  float e = __builtin_amdgcn_exp2f(re2);
  rev = __builtin_amdgcn_fractf(rev);
  float s = __builtin_amdgcn_sinf(rev);
  float c = __builtin_amdgcn_cosf(rev);
  orr = e * c;
  oi = e * s;
}
__device__ __forceinline__ void cexp3(float re2, float rev, float& e, float& c, float& s) {
  e = __builtin_amdgcn_exp2f(re2);
  rev = __builtin_amdgcn_fractf(rev);
  s = __builtin_amdgcn_sinf(rev); c = __builtin_amdgcn_cosf(rev);
}
__device__ __forceinline__ float dot2acc(u32 a, u32 b, float c) {
  return __builtin_amdgcn_fdot2_f32_bf16(__builtin_bit_cast(bf16x2v, a), __builtin_bit_cast(bf16x2v, b), c, false);
}
__device__ __forceinline__ u32 opaque(u32 v) { asm volatile("" : "+v"(v)); return v; }
__device__ __forceinline__ int lane_id() { return (int)__builtin_amdgcn_mbcnt_hi(~0u, __builtin_amdgcn_mbcnt_lo(~0u, opaque(0u))); }
__device__ __forceinline__ u32 pack_stats(float mean, float var) {
  float vb = rbf(var);
  float ve = rbf(vb + 1e-5f);
  float sd = __builtin_amdgcn_sqrtf(ve);
  return pk2(mean, sd);
}

#define SB() __builtin_amdgcn_sched_barrier(0)

#define KADD(id_, t_, v_) kadd_<(v_)>(t_)
#define KSLP(id_, v_) do { if ((v_) > 0) __builtin_amdgcn_s_sleep(v_); } while (0)
template <int V>
__device__ __forceinline__ u32 kadd_(u32 t) { u32 r; asm("s_add_i32 %0, %1, %2" : "=s"(r) : "s"(__builtin_amdgcn_readfirstlane(t)), "n"(V) : "scc"); return r; }
template <int V> struct IC { static constexpr int value = V; };
template <int B, int E, class F>
__device__ __forceinline__ void static_for(F& f) { if constexpr (B < E) { f(IC<B>{}); static_for<B + 1, E>(f); } }
typedef __amdgpu_buffer_rsrc_t rsrc_t;
__device__ __forceinline__ u32x4 ldw(rsrc_t r, int off) { return __builtin_amdgcn_raw_buffer_load_b128(r, off, 0, 16); }
__device__ __forceinline__ u32x4 ldws(rsrc_t r, int off, int soff) { return __builtin_amdgcn_raw_buffer_load_b128(r, off, soff, 16); }
__device__ __forceinline__ void stw(rsrc_t r, int off, u32x4 v) { __builtin_amdgcn_raw_buffer_store_b128(v, r, off, 0, 16); }
__device__ __forceinline__ u32 umax4(u32 m, u32x4 v) { return max(max(max(m, v[0]), v[1]), max(v[2], v[3])); }
template <int CTRL>
__device__ __forceinline__ u32 dpp_u(u32 v) { return (u32)__builtin_amdgcn_update_dpp(0, (int)v, CTRL, 0xf, 0xf, false); }

#define CSTR 4160
#define L_U 33280
#define L_TA 42496
#define L_TB (L_TA + 32768)
#define L_QB (L_TB + 32768)
#define L_TH (L_QB + 16384)
#define L_SP (L_QB + 32768)
#define L_FS (L_SP + 4096)
#define L_MI (L_FS + 3072)
#define L_XS (L_MI + 80)
#define L_WX (L_XS + 1024)
#define MEGA_LDS (L_WX + 12288)
#define L_REDA L_QB
#define L_REDB (L_QB + 16384)
#define L_XC L_REDB
#define L_X (L_REDB + 8192)
#define TBS 3136
#define L_WR (L_QB + 24576)
#define L_WS L_TA
#define L_KT L_TB
#define KTO 65024
#define FTS 528
#define L_P0 (L_FS + 2048)
#define STAT_SCALE 33554432.0f
#define C_OB 20
#define C_OT 24
#define M_FX 72
#define C_X2 28
#define C_G2 32
#define C_A 36
#define C_B 40
#define C_F 44
#define C_KH 48
#define C_P2 52
#define C_XC 8
#define C_P3 64
#define M_RD 68
#define M_DL 76
#define C_OV 28
#define C_U 56
#define C_W 60

typedef __attribute__((ext_vector_type(2))) _Float16 f16x2v;
typedef __attribute__((ext_vector_type(2))) short i16x2;
__device__ __forceinline__ u32 pkh(float a, float b) {
  u32 r;
  asm("v_cvt_pk_f16_f32 %0, %1, %2" : "=v"(r) : "v"(a), "v"(b));
  return r;
}
__device__ __forceinline__ u32 pkhn(float a, float b) {
  u32 r;
  asm("v_cvt_pk_f16_f32 %0, %1, -%2" : "=v"(r) : "v"(a), "v"(b));
  return r;
}
__device__ __forceinline__ u32 tb_addr(u32 bits2, u32 base2) {
  i16x2 b = __builtin_elementwise_max(__builtin_bit_cast(i16x2, bits2), (i16x2){0x3F00, 0x3F00});
  u32 r;
  asm("v_pk_mad_u16 %0, %1, 2, %2 op_sel_hi:[1,0,1]" : "=v"(r) : "v"(b), "s"(base2));
  return r;
}
__device__ __forceinline__ int bexp(float b) { return min(max((int)((__float_as_uint(b) >> 23) & 0xFF), 20), 240); }
__device__ __forceinline__ void cnt_add(char* smem, int off, int lane) {
  asm volatile("s_mov_b64 exec, 1\n\tds_add_u32 %0, %1\n\ts_mov_b64 exec, -1" :: "v"(L_MI + off), "v"(1u) : "memory");
}
__device__ __forceinline__ void cnt_wait(char* smem, int off, u32 target) {
#pragma unroll 1
  for (unsigned n = 0; n < (1u << 23) && (u32)__builtin_amdgcn_readfirstlane(__hip_atomic_load((LAS u32*)(smem + L_MI + off), __ATOMIC_RELAXED, __HIP_MEMORY_SCOPE_WORKGROUP)) < target; ++n)
    __builtin_amdgcn_s_sleep(2);
  asm volatile("" ::: "memory");
}
__device__ __forceinline__ void cnt_spin(char* smem, int off, u32 target) {
  for (unsigned n = 0; n < (1u << 24) && (u32)__builtin_amdgcn_readfirstlane(__hip_atomic_load((LAS u32*)(smem + L_MI + off), __ATOMIC_RELAXED, __HIP_MEMORY_SCOPE_WORKGROUP)) < target; ++n) {}
  asm volatile("" ::: "memory");
}
__device__ __forceinline__ void cnt_spin2(int o0, u32 t0, int o1, u32 t1) {
  for (unsigned n = 0; n < (1u << 24); ++n) {
    asm volatile("" ::: "memory");
    const u32 c0 = *(const volatile LAS u32*)(size_t)(u32)(L_MI + o0), c1 = *(const volatile LAS u32*)(size_t)(u32)(L_MI + o1);
    if (__builtin_amdgcn_readfirstlane(min((int)(c0 - t0), (int)(c1 - t1))) >= 0) break;
  }
  asm volatile("" ::: "memory");
}
__device__ __forceinline__ u32 now_() { return (u32)__builtin_amdgcn_s_memtime(); }
__device__ __forceinline__ u32 tmax(u32 a, u32 b) { return (int)(a - b) > 0 ? a : b; }
__device__ __forceinline__ void gate(u32 t) {
  u32 n = 256;
  asm volatile("1:\n\ts_memtime vcc\n\ts_sub_u32 %0, %0, 1\n\ts_waitcnt lgkmcnt(0)\n\ts_cbranch_scc1 2f\n\ts_sub_i32 vcc_lo, %1, vcc_lo\n\ts_cmp_lt_i32 vcc_lo, 1\n\ts_cbranch_scc1 2f\n\t"
               "s_cmp_lt_i32 vcc_lo, 64\n\ts_cbranch_scc1 1b\n\ts_sleep 1\n\ts_cmp_lt_i32 vcc_lo, 0x200\n\ts_cbranch_scc1 1b\n\ts_sleep 6\n\ts_branch 1b\n2:"
               : "+s"(n) : "s"(__builtin_amdgcn_readfirstlane(t)) : "memory", "vcc", "scc");
}
__device__ __forceinline__ u32 x0_mark(u32 tag) { return 0xFF807F80u + (((tag & 31u) + 1u) << 16) + ((tag >> 5) & 31u) + 1u; }
__device__ __forceinline__ bool tags_ok(const u32x4& f, u32 tag) {
  return __builtin_amdgcn_ballot_w64((opaque((f[0] ^ tag) | (f[1] ^ tag)) | opaque((f[2] ^ tag) | (f[3] ^ tag))) != 0u) == 0ull;
}
__device__ __forceinline__ void tags_wait(u32x4& f, rsrc_t wrs, int fo, u32 tag, int fid) {
  if (__builtin_expect(!tags_ok(f, tag), 0)) {
    for (unsigned spins = 0; spins <= SPIN_CAP; ++spins) {
      FAILC(fid);
      __builtin_amdgcn_s_sleep(SLP_F);
      asm volatile("" ::: "memory");
      if (tags_ok(ldw(wrs, fo), tag)) break;
    }
    __builtin_amdgcn_s_waitcnt(0x0F70);
  }
  asm volatile("" ::: "memory");
}
__device__ __forceinline__ void build_copies(char* smem, int cc, int ch, const u32 (&kw)[6]) {
  char* cop = smem + cc * 4 * CSTR + ch * 16;
  u32 sh[5];
#pragma unroll
  for (int k = 0; k < 5; ++k) sh[k] = __builtin_amdgcn_alignbit(kw[k + 1], kw[k], 16);
  u32x4 c0 = {kw[0], kw[1], kw[2], kw[3]}, c1 = {sh[0], sh[1], sh[2], sh[3]}, c2 = {kw[1], kw[2], kw[3], kw[4]}, c3 = {sh[1], sh[2], sh[3], sh[4]};
  asm volatile("" : "+v"(c0), "+v"(c1), "+v"(c2), "+v"(c3));
  *(u32x4*)(cop + 0 * CSTR) = c0; *(u32x4*)(cop + 1 * CSTR) = c1; *(u32x4*)(cop + 2 * CSTR) = c2; *(u32x4*)(cop + 3 * CSTR) = c3;
  asm volatile("s_nop 1" :: "v"(c0), "v"(c1), "v"(c2), "v"(c3) : "memory");
}
struct ConvPre { f32x4 x0, x1; };
__device__ __forceinline__ void conv_unp(ConvPre& P, u32x4 xw) {
#pragma unroll
  for (int e = 0; e < 4; ++e) { P.x0[e] = lo2f(xw[e]); P.x1[e] = hi2f(xw[e]); }
  asm volatile("" : "+v"(P.x0), "+v"(P.x1));
}
__device__ __forceinline__ u32x4 x2_ld(rsrc_t wrs, int kp, int l0) { return ldw(wrs, OFF_X2 + (kp * XS_ + l0) * 4); }
__device__ __forceinline__ bool x2_ok(const u32x4& xw) { return __all(umax4(0u, xw) != 0xFFFFFFFFu); }
__device__ __forceinline__ void x2_poll(rsrc_t wrs, int kp, int l0, u32x4& xw) {
  for (unsigned spins = 0; !x2_ok(xw) && spins <= SPIN_CAP; ++spins) {
    FAILC(17);
    __builtin_amdgcn_s_sleep(SLP_XP);
    asm volatile("" ::: "memory");
    xw = x2_ld(wrs, kp, l0);
  }
}
__device__ __forceinline__ void st2_ld(rsrc_t wrs, int l0, u32x4 (&g)[4]) {
#pragma unroll
  for (int e = 0; e < 4; ++e) g[e] = ldw(wrs, OFF_ST2 + ((l0 & ~255) + ((l0 >> 2) & 63)) * 16 + e * 1024);
}
__device__ __forceinline__ bool st2_ok(const u32x4 (&g)[4]) {
  u32 ok = 32u;
#pragma unroll
  for (int e = 0; e < 4; ++e) ok &= g[e][0] & g[e][2];
  return __all(ok != 0u);
}
__device__ __forceinline__ bool x2st_ok(const u32x4& xw, const u32x4 (&g)[4]) {
  u32 ok = umax4(0u, xw) != 0xFFFFFFFFu ? 32u : 0u;
#pragma unroll
  for (int e = 0; e < 4; ++e) ok &= g[e][0] & g[e][2];
  return __all(ok != 0u);
}
__device__ __forceinline__ void st2_poll(rsrc_t wrs, int l0, u32x4 (&g)[4]) {
  for (unsigned spins = 0; !st2_ok(g) && spins <= SPIN_CAP; ++spins) {
    FAILC(8);
    __builtin_amdgcn_s_sleep(SLP_SP);
    asm volatile("" ::: "memory");
    st2_ld(wrs, l0, g);
  }
}
template <bool UNP>
__device__ __forceinline__ void conv2_in(char* smem, u32 ua, ConvPre& P, u32x4 xw, u32x4 (&g)[4]) {
  f32x4 rs, nm;
#pragma unroll
  for (int e = 0; e < 4; ++e) {
    const float c1 = 4294967296.0f / (64.0f * 512.0f * STAT_SCALE), c2 = 1.0f / 4294967296.0f;
    const float t1 = fmaf((float)g[e][0], c2, (float)(int)g[e][1]), t2 = fmaf((float)g[e][2], c2, (float)(int)g[e][3]);
    const float m1 = -c1 * t1;
    rs[e] = __builtin_amdgcn_rsqf(fmaf(rmax(fmaf(m1, t1, t2), 0.f), c1, 1e-5f));
    nm[e] = m1 * rs[e];
  }
  if (UNP) conv_unp(P, xw);
  u32x2 u0 = {pkh(fmaf(P.x0[0], rs[0], nm[0]), fmaf(P.x0[1], rs[1], nm[1])), pkh(fmaf(P.x0[2], rs[2], nm[2]), fmaf(P.x0[3], rs[3], nm[3]))};
  u32x2 u1 = {pkh(fmaf(P.x1[0], rs[0], nm[0]), fmaf(P.x1[1], rs[1], nm[1])), pkh(fmaf(P.x1[2], rs[2], nm[2]), fmaf(P.x1[3], rs[3], nm[3]))};
  asm volatile("" : "+v"(u0), "+v"(u1));
  *(LAS u32x2*)(size_t)ua = u0;
  *(LAS u32x2*)(size_t)(ua + 4608) = u1;
  asm volatile("s_nop 1" :: "v"(u0), "v"(u1) : "memory");
}
__device__ __forceinline__ void pair_x(f32x4& lo, f32x4& hi, const f32x4& a, const f32x4& b) {
#define PXD " quad_perm:[1,0,3,2] row_mask:0xf bank_mask:0xf\n\t"
  asm volatile("s_mov_b64 vcc, %16\n\ts_nop 0\n\t"
               "v_cndmask_b32_dpp %0, %12, %8, vcc" PXD "v_cndmask_b32_dpp %1, %13, %9, vcc" PXD "v_cndmask_b32_dpp %2, %14, %10, vcc" PXD "v_cndmask_b32_dpp %3, %15, %11, vcc" PXD
               "s_not_b64 vcc, vcc\n\t"
               "v_cndmask_b32_dpp %4, %8, %12, vcc" PXD "v_cndmask_b32_dpp %5, %9, %13, vcc" PXD "v_cndmask_b32_dpp %6, %10, %14, vcc" PXD "v_cndmask_b32_dpp %7, %11, %15, vcc" PXD
               : "=&v"(lo[0]), "=&v"(lo[1]), "=&v"(lo[2]), "=&v"(lo[3]), "=&v"(hi[0]), "=&v"(hi[1]), "=&v"(hi[2]), "=&v"(hi[3])
               : "v"(a[0]), "v"(a[1]), "v"(a[2]), "v"(a[3]), "v"(b[0]), "v"(b[1]), "v"(b[2]), "v"(b[3]), "s"(0x5555555555555555ull) : "vcc", "scc");
}
__device__ __forceinline__ void conv_res(ConvPre& P) {
  f32x4 lo, hi;
  pair_x(lo, hi, P.x0, P.x1);
  P.x0 = lo; P.x1 = hi;
}
__device__ __forceinline__ u32x4 conv_epi(u32 rp0, u32 rp1, int off, float isc, const ConvPre& P) {
  const f32x4 a0 = *(const LAS f32x4*)(size_t)(rp0 + off), a1 = *(const LAS f32x4*)(size_t)(rp0 + off + 4096), b0 = *(const LAS f32x4*)(size_t)(rp1 + off), b1 = *(const LAS f32x4*)(size_t)(rp1 + off + 4096);
  __builtin_amdgcn_sched_barrier(0);
  u32x4 r;
#pragma unroll
  for (int e = 0; e < 2; ++e) {
    r[e] = pk2(fmaf(a1[2 * e], isc, fmaf(a0[2 * e], isc, P.x0[2 * e])), fmaf(a1[2 * e + 1], isc, fmaf(a0[2 * e + 1], isc, P.x0[2 * e + 1])));
    r[2 + e] = pk2(fmaf(b1[2 * e], isc, fmaf(b0[2 * e], isc, P.x1[2 * e])), fmaf(b1[2 * e + 1], isc, fmaf(b0[2 * e + 1], isc, P.x1[2 * e + 1])));
  }
  return r;
}
__device__ __forceinline__ void dma4k(rsrc_t r, int voff, int soff, int lds) {
  asm volatile("s_mov_b32 m0, %3\n\ts_nop 0\n\t"
               "buffer_load_dwordx4 %0, %1, %2 offen sc1 lds\n\t"
               "buffer_load_dwordx4 %0, %1, %2 offen offset:1024 sc1 lds\n\t"
               "buffer_load_dwordx4 %0, %1, %2 offen offset:2048 sc1 lds\n\t"
               "buffer_load_dwordx4 %0, %1, %2 offen offset:3072 sc1 lds"
               :: "v"(voff), "s"(r), "s"(soff), "s"(lds) : "memory");
}
__device__ __forceinline__ void dma1k(rsrc_t r, int voff, int soff, int lds) {
  asm volatile("s_mov_b32 m0, %3\n\ts_nop 0\n\tbuffer_load_dwordx4 %0, %1, %2 offen sc1 lds" :: "v"(voff), "s"(r), "s"(soff), "s"(lds) : "memory");
}
__device__ __forceinline__ int x_lane(int lane) { return ((lane & 31) >> 1) * 64 + (lane >> 5) * 32 + (lane & 1) * 16; }
__device__ __forceinline__ void x_dma(rsrc_t wrs, int xin, int rb, int tile, int a, int lane) {
  const int vo = x_lane(lane);
  dma4k(wrs, vo, xin + rb * XBS + a * 8192, tile + a * 8192);
  dma4k(wrs, vo, xin + rb * XBS + a * 8192 + 4096, tile + a * 8192 + 4096);
  if (tile == L_TB) {
    int so = xin + (rb - 1) * XBS + a * 8192;
    asm volatile("s_mov_b32 m0, %3\n\ts_nop 0\n\tbuffer_load_dwordx4 %1, %2, %0 offen sc1 lds\n\t"
                 "s_sub_u32 m0, m0, 1024\n\ts_nop 0\n\tbuffer_load_dwordx4 %1, %2, %0 offen offset:2048 sc1 lds\n\t"
                 "s_add_u32 m0, m0, 3072\n\ts_add_u32 %0, %0, 4096\n\tbuffer_load_dwordx4 %1, %2, %0 offen sc1 lds\n\t"
                 "s_sub_u32 m0, m0, 1024\n\ts_nop 0\n\tbuffer_load_dwordx4 %1, %2, %0 offen offset:2048 sc1 lds"
                 : "+s"(so) : "v"((lane >> 1) * 64 + 32 + (lane & 1) * 16), "s"(wrs), "s"(L_TH + a * 4096) : "memory", "scc");
  }
}
template <int S>
__device__ __forceinline__ bf16x8 ld_tr8(const LAS char* p) {
  const bf16x4 lo = __builtin_amdgcn_ds_read_tr16_b64_v4i16((LAS bf16x4*)p), hi = __builtin_amdgcn_ds_read_tr16_b64_v4i16((LAS bf16x4*)(p + S));
  return __builtin_shufflevector(lo, hi, 0, 1, 2, 3, 4, 5, 6, 7);
}

struct ConvK { u32 ah, ah2, bp, ra; f16x8 k[16]; };
__device__ __forceinline__ void a_conv_pre(ConvK& C, int a, int lane) {
  lane = (int)opaque((u32)lane);
  const int c = a >> 1, qq = a & 1, j16 = lane & 15, kg = lane >> 4;
  const int y00 = 2031 - j16 + 8 * kg - 128 * qq;
  const int sg = y00 & 3;
  C.ah = opaque((u32)(c * 4 * CSTR + sg * CSTR + 2 * (y00 - sg) - 3776));
  C.ah2 = opaque(C.ah + 8);
  C.bp = opaque((u32)(L_U + c * 4608 + (256 + 16 * j16 - 16 + 8 * kg - 128 * qq) * 2 - 192));
  C.ra = opaque((u32)(L_REDA + (a * 256 + 4 * j16 + (kg ^ ((j16 >> 1) & 3) ^ c)) * 16));
#pragma unroll
  for (int e = 0; e < 16; ++e) {
    const int ao = 3776 - (512 * (e & 3) + 64 * (e >> 2));
    const f16x4 h0 = *(const LAS f16x4*)(size_t)(C.ah + ao), h1 = *(const LAS f16x4*)(size_t)(C.ah2 + ao);
    C.k[e] = __builtin_shufflevector(h0, h1, 0, 1, 2, 3, 4, 5, 6, 7);
    __builtin_amdgcn_sched_barrier(0);
  }
#pragma unroll
  for (int e = 0; e < 16; ++e) asm volatile("" : "+v"(C.k[e]));
}
template <bool L1>
__device__ __forceinline__ void a_conv(char* smem, rsrc_t wrs, int rb, int a, const ConvK& C) {
  const LAS char* ah = (const LAS char*)(size_t)C.ah;
  const LAS char* ah2 = (const LAS char*)(size_t)C.ah2;
  const LAS char* bp = (const LAS char*)(size_t)C.bp;
  LAS f32x4* ra = (LAS f32x4*)(size_t)C.ra;
  f32x4 acc[8];
#pragma unroll
  for (int e = 0; e < 8; ++e) acc[e] = (f32x4){0.f, 0.f, 0.f, 0.f};
  f16x8 bf[4][8], fh[4][8];
#define S_RR(s_) ((s_) < 16 ? (s_) >> 2 : (s_) < 44 ? ((s_) - 16) / 7 : ((s_) - 44) >> 2)
#define S_MU(s_) ((s_) < 16 ? (s_) & 3 : (s_) < 44 ? ((s_) - 16) % 7 + 1 : ((s_) - 44) & 3)
#define S_NEW(s_) ((s_) < 16 || ((s_) < 44 && S_MU(s_) >= 4))
#define LDA(s_) do { const int ao_ = 3776 - (512 * S_MU(s_) + 64 * S_RR(s_));                                        \
      f16x4 h0_ = *(const LAS f16x4*)(ah + ao_), h1_ = *(const LAS f16x4*)(ah2 + ao_);                                \
      fh[S_RR(s_)][S_MU(s_)] = __builtin_shufflevector(h0_, h1_, 0, 1, 2, 3, 4, 5, 6, 7); } while (0)
#define LDB(r_, t_) bf[r_][t_] = *(const LAS f16x8*)(bp + 512 * (t_) + 192 - 64 * (r_))
#define CSTEPS(s0_, s1_)                                                                                               \
  _Pragma("unroll") for (int st = s0_; st < s1_; ++st) {                                                               \
    const int rr = S_RR(st), mu = S_MU(st);                                                                           \
    if (S_NEW(st + CPF) && (L1 || st + CPF >= 16)) LDA(st + CPF);                                                     \
    if (st < 4) { LDB(2 + (st >> 1), 2 * (st & 1)); LDB(2 + (st >> 1), 2 * (st & 1) + 1); }                           \
    if (st >= 44 && st < 48) { LDB(2 + ((st - 44) >> 1), 4 + 2 * (st & 1)); LDB(2 + ((st - 44) >> 1), 5 + 2 * (st & 1)); } \
    __builtin_amdgcn_sched_barrier(0);                                                                                \
    _Pragma("unroll") for (int cb = 0; cb < 8; ++cb) {                                                                \
      const int t = cb - mu;                                                                                          \
      const bool on = st < 16 ? (cb < 4 && t >= 0) : st < 44 ? (cb >= 4 && t >= 0 && t < 4) : (t >= 4);               \
      if (on) acc[cb] = __builtin_amdgcn_mfma_f32_16x16x32_f16(fh[rr][mu], bf[rr][t < 0 ? 0 : t], acc[cb], 0, 0, 0);  \
    }                                                                                                                 \
    __builtin_amdgcn_sched_barrier(0);                                                                                \
    if (st >= 13 && st < 16) ra[(st - 13) * 64] = acc[st - 13];                                                       \
    if (st >= 57) ra[1024 + (st - 57) * 64] = acc[4 + st - 57];                                                       \
  }
#pragma unroll
  for (int e = 0; e < 16; ++e) fh[e >> 2][e & 3] = C.k[e];
  __builtin_amdgcn_s_barrier();
  asm volatile("" ::: "memory");
  ST(!L1, 7);
#pragma unroll
  for (int e = 0; e < CPF; ++e) { if (L1) LDA(e); __builtin_amdgcn_sched_barrier(0); if (e < 2) { LDB(e, 0); __builtin_amdgcn_sched_barrier(0); LDB(e, 1); __builtin_amdgcn_sched_barrier(0); LDB(e, 2); __builtin_amdgcn_sched_barrier(0); LDB(e, 3); __builtin_amdgcn_sched_barrier(0); } }
  __builtin_amdgcn_sched_barrier(0);
  CSTEPS(0, 16)
  CSTEPS(16, 17)
  ra[3 * 64] = acc[3];
  __builtin_amdgcn_sched_barrier(0);
  asm volatile("" ::: "memory");
  CSTEPS(17, 18)
  if (L1) asm volatile("s_waitcnt vmcnt(0)" ::: "memory");
  asm volatile("s_waitcnt lgkmcnt(2)" ::: "memory");
  __builtin_amdgcn_s_barrier();
  asm volatile("" ::: "memory");
  ST(!L1, 8);
  CSTEPS(18, 44)
  if (!L1) ST(1, 5);
  if (!L1) __builtin_amdgcn_s_barrier();
  if (!L1) ST(1, 6);
  asm volatile("" ::: "memory");
#pragma unroll
  for (int e = 0; e < 8; ++e) { LDB(e >> 2, 4 + (e & 3)); __builtin_amdgcn_sched_barrier(0); }
  CSTEPS(44, 60)
  ra[1024 + 3 * 64] = acc[7];
  lds_barrier();
  ST(!L1, 9);
}

struct C1Pre { u32 ah, ah2, bp, xp, xc, cn; int so, fo0, fo1; };
#define C1_OPS "+{v16}"(C.ah), "+{v17}"(C.ah2), "+{v18}"(C.bp), "+{v19}"(C.xp), "+{v20}"(C.xc), "+{v21}"(C.cn), "+{v22}"(C.so), "+{v23}"(C.fo0), "+{v24}"(C.fo1)
__device__ __forceinline__ void c1_pre(C1Pre& C, int kp, int a, int lane) {
  lane = (int)opaque((u32)lane);
  const int c = a >> 1, j16 = lane & 15, kg = lane >> 4, P = a & 1;
  const int y0 = 2031 - j16 + 8 * kg - 128 * P, sg = y0 & 3;
  C.ah = (u32)(c * 4 * CSTR + sg * CSTR + 2 * (y0 - sg) - 3776);
  C.ah2 = C.ah + 8;
  C.bp = (u32)(L_U + c * 4608 + (240 + 16 * j16 + 8 * kg - 128 * P) * 2 - 192);
  C.xp = (u32)(L_X + j16 * 64 + kg * 16);
  C.xc = (u32)(L_TA + 24576 + c * 4096 + lane * 16);
  C.cn = (u32)(L_MI + 4 * c);
  C.so = OFF_X1 + (8 * ((kg & 1) ? 3 - P : P) + (j16 >> 1)) * XBS + kp * 128 + c * 64 + (j16 & 1) * 32 + (kg >> 1) * 16;
  C.fo0 = OFF_ST0 + (a * 64 + lane) * 16;
  C.fo1 = OFF_ST0 + 8192 + (HB_OF(BIDX) * 4 + a) * 128 + (lane & 3) * 16;
  asm volatile("" : C1_OPS);
}
static_assert(L_XC - L_TA - 24576 == 57344 && C_XC == 8 && C_KH == 48 && C_P2 == 52 && C_U == 56 && C_P3 == 64 && C_G2 == 32 && L_MI % 8 == 0, "generated offsets");
__device__ __forceinline__ void a_conv1(rsrc_t wrs, int a, C1Pre& C, u32x4& fl, u32x4& wfl, u32& g2) {
  asm volatile("s_bitcmp1_b32 %[a], 0\n\ts_cbranch_scc1 .Lc_odd\n\t"
  "ds_read_b64 v[36:37], v16 offset:3776\n\tds_read_b64 v[38:39], v17 offset:3776\n\tds_read_b64 v[40:41], v16 offset:3264\n\tds_read_b64 v[42:43], v17 offset:3264\n\t"
  "ds_read_b64 v[44:45], v16 offset:2752\n\tds_read_b64 v[46:47], v17 offset:2752\n\tds_read_b64 v[48:49], v16 offset:2240\n\tds_read_b64 v[50:51], v17 offset:2240\n\t"
  "ds_read_b64 v[52:53], v16 offset:3712\n\tds_read_b64 v[54:55], v17 offset:3712\n\tds_read_b64 v[56:57], v16 offset:3200\n\tds_read_b64 v[58:59], v17 offset:3200\n\t"
  "ds_read_b64 v[60:61], v16 offset:2688\n\tds_read_b64 v[62:63], v17 offset:2688\n\tds_read_b64 v[64:65], v16 offset:2176\n\tds_read_b64 v[66:67], v17 offset:2176\n\t"
  "ds_read_b64 v[68:69], v16 offset:3648\n\tds_read_b64 v[70:71], v17 offset:3648\n\tds_read_b64 v[72:73], v16 offset:3136\n\tds_read_b64 v[74:75], v17 offset:3136\n\t"
  "ds_read_b64 v[76:77], v16 offset:2624\n\tds_read_b64 v[78:79], v17 offset:2624\n\tds_read_b64 v[80:81], v16 offset:2112\n\tds_read_b64 v[82:83], v17 offset:2112\n\t"
  "ds_read_b64 v[84:85], v16 offset:3584\n\tds_read_b64 v[86:87], v17 offset:3584\n\tds_read_b64 v[88:89], v16 offset:3072\n\tds_read_b64 v[90:91], v17 offset:3072\n\t"
  "ds_read_b64 v[92:93], v16 offset:2560\n\tds_read_b64 v[94:95], v17 offset:2560\n\tds_read_b64 v[96:97], v16 offset:2048\n\tds_read_b64 v[98:99], v17 offset:2048\n\t"
  "ds_read_b32 v26, v21\n\tv_and_b32 v27, -8, v21\n\tv_mov_b32 v100, 1\n\t"
  CST(6)
  "s_mov_b32 vcc_hi, 0x1000000\n\t.Lc0_u:\n\tds_read_b32 v101, v27 offset:56\n\ts_sub_u32 vcc_hi, vcc_hi, 1\n\ts_cselect_b32 vcc_lo, 0, 4\n\ts_waitcnt lgkmcnt(0)\n\tv_readfirstlane_b32 s61, v101\n\ts_nop 1\n\ts_cmp_lt_u32 s61, vcc_lo\n\ts_cbranch_scc1 .Lc0_u\n\t"
  CST(7)
  "ds_read_b128 v[102:105], v18 offset:192\n\tds_read_b128 v[106:109], v18 offset:704\n\tds_read_b128 v[110:113], v18 offset:1216\n\tds_read_b128 v[116:119], v18 offset:128\n\t"
  "ds_read_b128 v[120:123], v18 offset:640\n\tds_read_b128 v[124:127], v18 offset:1152\n\tds_read_b128 v[128:131], v18 offset:64\n\ts_waitcnt lgkmcnt(6)\n\t"
  "v_mfma_f32_16x16x32_f16 v[132:135], v[40:43], v[102:105], 0\n\tds_read_b128 v[136:139], v18 offset:576\n\tds_read_b128 v[140:143], v18 offset:1728\n\t"
  "v_mfma_f32_16x16x32_f16 v[144:147], v[44:47], v[102:105], 0\n\tds_read_b128 v[148:151], v18 offset:1088\n\ts_waitcnt lgkmcnt(8)\n\t"
  "v_mfma_f32_16x16x32_f16 v[132:135], v[36:39], v[106:109], v[132:135]\n\tds_read_b128 v[152:155], v18 offset:1664\n\tds_read_b128 v[156:159], v18 offset:1600\n\t"
  "v_mfma_f32_16x16x32_f16 v[144:147], v[40:43], v[106:109], v[144:147]\n\tds_read_b128 v[160:163], v18 offset:0\n\tds_read_b32 v101, v27 offset:48\n\ts_waitcnt lgkmcnt(7)\n\t"
  "v_mfma_f32_16x16x32_f16 v[144:147], v[36:39], v[110:113], v[144:147]\n\tds_read_b128 v[164:167], v18 offset:512\n\t"
  "v_mfma_f32_16x16x32_f16 v[132:135], v[56:59], v[116:119], v[132:135]\n\tds_read_b128 v[168:171], v18 offset:1024\n\t"
  "v_mfma_f32_16x16x32_f16 v[144:147], v[60:63], v[116:119], v[144:147]\n\tds_read_b128 v[172:175], v18 offset:1536\n\t"
  "v_mfma_f32_16x16x32_f16 v[132:135], v[52:55], v[120:123], v[132:135]\n\t"
  "v_mfma_f32_16x16x32_f16 v[144:147], v[56:59], v[120:123], v[144:147]\n\t"
  "v_mfma_f32_16x16x32_f16 v[144:147], v[52:55], v[124:127], v[144:147]\n\t"
  "v_mfma_f32_16x16x32_f16 v[132:135], v[72:75], v[128:131], v[132:135]\n\tds_read_b128 v[176:179], v19 offset:0\n\tds_read_b128 v[180:183], v19 offset:3072\n\t"
  "v_mfma_f32_16x16x32_f16 v[144:147], v[76:79], v[128:131], v[144:147]\n\ts_waitcnt lgkmcnt(5)\n\tv_readfirstlane_b32 s61, v101\n\t"
  "v_mfma_f32_16x16x32_f16 v[132:135], v[68:71], v[136:139], v[132:135]\n\t"
  "v_mfma_f32_16x16x32_f16 v[144:147], v[72:75], v[136:139], v[144:147]\n\ts_cmp_lt_u32 s61, 4\n\ts_cbranch_scc1 .Lc0_kh\n.Lc0_kh_b:\n\t"
  "v_mfma_f32_16x16x32_f16 v[144:147], v[68:71], v[148:151], v[144:147]\n\tds_read_b64 v[184:185], v16 offset:1728\n\tds_read_b64 v[186:187], v17 offset:1728\n\t"
  "v_mfma_f32_16x16x32_f16 v[132:135], v[88:91], v[160:163], v[132:135]\n\tds_read_b64 v[188:189], v16 offset:1216\n\tds_read_b64 v[190:191], v17 offset:1216\n\t"
  "v_mfma_f32_16x16x32_f16 v[144:147], v[92:95], v[160:163], v[144:147]\n\tds_read_b64 v[192:193], v16 offset:704\n\ts_waitcnt lgkmcnt(7)\n\t"
  "v_mfma_f32_16x16x32_f16 v[132:135], v[84:87], v[164:167], v[132:135]\n\tds_read_b64 v[194:195], v17 offset:704\n\tds_read_b64 v[196:197], v16 offset:192\n\tds_read_b64 v[198:199], v17 offset:192\n\t"
  "v_mfma_f32_16x16x32_f16 v[144:147], v[88:91], v[164:167], v[144:147]\n\ts_waitcnt lgkmcnt(8)\n\tv_perm_b32 v176, v176, v176, s60\n\t"
  "v_mfma_f32_16x16x32_f16 v[144:147], v[84:87], v[168:171], v[144:147]\n\tds_write_b128 v20, v[132:135] offset:0\n\tv_perm_b32 v177, v177, v177, s60\n\t"
  "v_mfma_f32_16x16x32_f16 v[200:203], v[36:39], v[102:105], 0\n\tv_perm_b32 v178, v178, v178, s60\n\tv_perm_b32 v179, v179, v179, s60\n\t"
  "v_mfma_f32_16x16x32_f16 v[132:135], v[48:51], v[102:105], 0\n\tds_write_b128 v20, v[144:147] offset:1024\n\tv_perm_b32 v180, v180, v180, s60\n\t"
  "v_mfma_f32_16x16x32_f16 v[132:135], v[44:47], v[106:109], v[132:135]\n\ts_mov_b64 exec, 1\n\tds_add_u32 v21, v100 offset:8\n\ts_mov_b64 exec, -1\n\t"
  "v_mfma_f32_16x16x32_f16 v[132:135], v[40:43], v[110:113], v[132:135]\n\tv_perm_b32 v181, v181, v181, s60\n\tv_perm_b32 v182, v182, v182, s60\n\t"
  "v_mfma_f32_16x16x32_f16 v[132:135], v[36:39], v[140:143], v[132:135]\n\tv_perm_b32 v183, v183, v183, s60\n\t"
  "v_mfma_f32_16x16x32_f16 v[200:203], v[52:55], v[116:119], v[200:203]\n\t"
  "v_mfma_f32_16x16x32_f16 v[132:135], v[64:67], v[116:119], v[132:135]\n\t"
  "v_mfma_f32_16x16x32_f16 v[132:135], v[60:63], v[120:123], v[132:135]\n\t"
  "v_mfma_f32_16x16x32_f16 v[132:135], v[56:59], v[124:127], v[132:135]\n\t"
  "v_mfma_f32_16x16x32_f16 v[132:135], v[52:55], v[152:155], v[132:135]\n\t"
  "v_mfma_f32_16x16x32_f16 v[200:203], v[68:71], v[128:131], v[200:203]\n\t"
  "v_mfma_f32_16x16x32_f16 v[132:135], v[80:83], v[128:131], v[132:135]\n\tds_read_b32 v101, v21 offset:8\n\tds_read_b128 v[144:147], v20 offset:2048\n\tds_read_b128 v[204:207], v20 offset:3072\n\t"
  "v_mfma_f32_16x16x32_f16 v[132:135], v[76:79], v[136:139], v[132:135]\n\t"
  "v_mfma_f32_16x16x32_f16 v[132:135], v[72:75], v[148:151], v[132:135]\n\t"
  "v_mfma_f32_16x16x32_f16 v[132:135], v[68:71], v[156:159], v[132:135]\n\t"
  "v_mfma_f32_16x16x32_f16 v[200:203], v[84:87], v[160:163], v[200:203]\n\t"
  "v_mfma_f32_16x16x32_f16 v[132:135], v[96:99], v[160:163], v[132:135]\n\t"
  "v_mfma_f32_16x16x32_f16 v[132:135], v[92:95], v[164:167], v[132:135]\n\t"
  "v_mfma_f32_16x16x32_f16 v[132:135], v[88:91], v[168:171], v[132:135]\n\t"
  "v_mfma_f32_16x16x32_f16 v[132:135], v[84:87], v[172:175], v[132:135]\n\ts_waitcnt lgkmcnt(0)\n\tv_readfirstlane_b32 s61, v101\n\t"
  "v_mfma_f32_16x16x32_f16 v[208:211], v[40:43], v[140:143], 0\n\t"
  "v_mfma_f32_16x16x32_f16 v[208:211], v[44:47], v[110:113], v[208:211]\n\ts_cmp_lt_u32 s61, 2\n\ts_cbranch_scc1 .Lc0_x0\n.Lc0_x0_b:\n\tv_fmac_f32 v176, v144, v26\n\tv_fmac_f32 v177, v145, v26\n\t"
  "v_fmac_f32 v178, v146, v26\n\t"
  "v_mfma_f32_16x16x32_f16 v[212:215], v[44:47], v[140:143], 0\n\tv_fmac_f32 v179, v147, v26\n\tv_fmac_f32 v180, v204, v26\n\tv_fmac_f32 v181, v205, v26\n\t"
  "v_fmac_f32 v182, v206, v26\n\tv_fmac_f32 v183, v207, v26\n\t"
  "v_mfma_f32_16x16x32_f16 v[208:211], v[48:51], v[106:109], v[208:211]\n\tv_fmac_f32 v176, v200, v26\n\tv_fmac_f32 v177, v201, v26\n\t"
  "v_mfma_f32_16x16x32_f16 v[212:215], v[48:51], v[110:113], v[212:215]\n\tv_fmac_f32 v178, v202, v26\n\tv_fmac_f32 v179, v203, v26\n\tv_cvt_pk_bf16_f32 v144, v176, v177\n\t"
  "v_cvt_pk_bf16_f32 v145, v178, v179\n\tv_fmac_f32 v180, v132, v26\n\t"
  "v_mfma_f32_16x16x32_f16 v[176:179], v[48:51], v[140:143], 0\n\tv_fmac_f32 v181, v133, v26\n\tv_fmac_f32 v182, v134, v26\n\tv_fmac_f32 v183, v135, v26\n\t"
  "v_cvt_pk_bf16_f32 v146, v180, v181\n\tv_cvt_pk_bf16_f32 v147, v182, v183\n\t"
  "v_mfma_f32_16x16x32_f16 v[208:211], v[184:187], v[102:105], v[208:211]\n\tv_permlane16_swap_b32 v144, v146\n\tv_permlane16_swap_b32 v145, v147\n\ts_setprio 3\n\tbuffer_store_dwordx4 v[144:147], v22, s[56:59], 0 offen sc1\n\ts_setprio 0\n\t"
  CST(8)
  "v_mfma_f32_16x16x32_f16 v[212:215], v[184:187], v[106:109], v[212:215]\n\tds_read_b64 v[132:133], v16 offset:1664\n\tds_read_b64 v[134:135], v17 offset:1664\n\t"
  "v_mfma_f32_16x16x32_f16 v[176:179], v[184:187], v[110:113], v[176:179]\n\tds_read_b64 v[144:145], v16 offset:1152\n\tds_read_b64 v[146:147], v17 offset:1152\n\t"
  "v_mfma_f32_16x16x32_f16 v[180:183], v[184:187], v[140:143], 0\n\tds_read_b64 v[140:141], v16 offset:640\n\tds_read_b64 v[142:143], v17 offset:640\n\t"
  "v_mfma_f32_16x16x32_f16 v[212:215], v[188:191], v[102:105], v[212:215]\n\t"
  "v_mfma_f32_16x16x32_f16 v[176:179], v[188:191], v[106:109], v[176:179]\n\tds_read_b64 v[184:185], v16 offset:128\n\tds_read_b64 v[186:187], v17 offset:128\n\t"
  "v_mfma_f32_16x16x32_f16 v[180:183], v[188:191], v[110:113], v[180:183]\n\t"
  "v_mfma_f32_16x16x32_f16 v[176:179], v[192:195], v[102:105], v[176:179]\n\t"
  "v_mfma_f32_16x16x32_f16 v[180:183], v[192:195], v[106:109], v[180:183]\n\t"
  "v_mfma_f32_16x16x32_f16 v[180:183], v[196:199], v[102:105], v[180:183]\n\t"
  "v_mfma_f32_16x16x32_f16 v[208:211], v[56:59], v[152:155], v[208:211]\n\t"
  "v_mfma_f32_16x16x32_f16 v[208:211], v[60:63], v[124:127], v[208:211]\n\t"
  "v_mfma_f32_16x16x32_f16 v[212:215], v[60:63], v[152:155], v[212:215]\n\tds_read_b64 v[102:103], v16 offset:1600\n\tds_read_b64 v[104:105], v17 offset:1600\n\t"
  "v_mfma_f32_16x16x32_f16 v[208:211], v[64:67], v[120:123], v[208:211]\n\t"
  "v_mfma_f32_16x16x32_f16 v[212:215], v[64:67], v[124:127], v[212:215]\n\t"
  "v_mfma_f32_16x16x32_f16 v[176:179], v[64:67], v[152:155], v[176:179]\n\ts_waitcnt lgkmcnt(2)\n\t"
  "v_mfma_f32_16x16x32_f16 v[208:211], v[132:135], v[116:119], v[208:211]\n\tds_read_b64 v[108:109], v16 offset:1088\n\tds_read_b64 v[110:111], v17 offset:1088\n\t"
  "v_mfma_f32_16x16x32_f16 v[212:215], v[132:135], v[120:123], v[212:215]\n\t"
  "v_mfma_f32_16x16x32_f16 v[176:179], v[132:135], v[124:127], v[176:179]\n\t"
  "v_mfma_f32_16x16x32_f16 v[180:183], v[132:135], v[152:155], v[180:183]\n\tds_read_b64 v[112:113], v16 offset:576\n\tds_read_b64 v[114:115], v17 offset:576\n\t"
  "v_mfma_f32_16x16x32_f16 v[212:215], v[144:147], v[116:119], v[212:215]\n\t"
  "v_mfma_f32_16x16x32_f16 v[176:179], v[144:147], v[120:123], v[176:179]\n\tds_read_b64 v[132:133], v16 offset:64\n\tds_read_b64 v[134:135], v17 offset:64\n\t"
  "v_mfma_f32_16x16x32_f16 v[180:183], v[144:147], v[124:127], v[180:183]\n\t"
  "v_mfma_f32_16x16x32_f16 v[176:179], v[140:143], v[116:119], v[176:179]\n\t"
  "v_mfma_f32_16x16x32_f16 v[180:183], v[140:143], v[120:123], v[180:183]\n\tv_add_u32 v22, 0x101000, v22\n\t"
  "v_mfma_f32_16x16x32_f16 v[180:183], v[184:187], v[116:119], v[180:183]\n\t"
  "v_mfma_f32_16x16x32_f16 v[208:211], v[72:75], v[156:159], v[208:211]\n\t"
  "v_mfma_f32_16x16x32_f16 v[208:211], v[76:79], v[148:151], v[208:211]\n\t"
  "v_mfma_f32_16x16x32_f16 v[212:215], v[76:79], v[156:159], v[212:215]\n\tds_read_b64 v[116:117], v16 offset:1536\n\tds_read_b64 v[118:119], v17 offset:1536\n\t"
  "v_mfma_f32_16x16x32_f16 v[208:211], v[80:83], v[136:139], v[208:211]\n\t"
  "v_mfma_f32_16x16x32_f16 v[212:215], v[80:83], v[148:151], v[212:215]\n\t"
  "v_mfma_f32_16x16x32_f16 v[176:179], v[80:83], v[156:159], v[176:179]\n\ts_waitcnt lgkmcnt(2)\n\t"
  "v_mfma_f32_16x16x32_f16 v[208:211], v[102:105], v[128:131], v[208:211]\n\tds_read_b32 v101, v27 offset:52\n\tds_read_b64 v[120:121], v16 offset:1024\n\t"
  "v_mfma_f32_16x16x32_f16 v[212:215], v[102:105], v[136:139], v[212:215]\n\tds_read_b64 v[122:123], v17 offset:1024\n\t"
  "v_mfma_f32_16x16x32_f16 v[176:179], v[102:105], v[148:151], v[176:179]\n\t"
  "v_mfma_f32_16x16x32_f16 v[180:183], v[102:105], v[156:159], v[180:183]\n\tds_read_b64 v[102:103], v16 offset:512\n\tds_read_b64 v[104:105], v17 offset:512\n\t"
  "v_mfma_f32_16x16x32_f16 v[212:215], v[108:111], v[128:131], v[212:215]\n\t"
  "v_mfma_f32_16x16x32_f16 v[176:179], v[108:111], v[136:139], v[176:179]\n\tds_read_b64 v[124:125], v16 offset:0\n\tds_read_b64 v[126:127], v17 offset:0\n\t"
  "v_mfma_f32_16x16x32_f16 v[180:183], v[108:111], v[148:151], v[180:183]\n\t"
  "v_mfma_f32_16x16x32_f16 v[176:179], v[112:115], v[128:131], v[176:179]\n\t"
  "v_mfma_f32_16x16x32_f16 v[180:183], v[112:115], v[136:139], v[180:183]\n\ts_waitcnt lgkmcnt(5)\n\tv_readfirstlane_b32 s61, v101\n\t"
  "v_mfma_f32_16x16x32_f16 v[180:183], v[132:135], v[128:131], v[180:183]\n\t"
  "v_mfma_f32_16x16x32_f16 v[208:211], v[88:91], v[172:175], v[208:211]\n\ts_cmp_lt_u32 s61, 4\n\ts_cbranch_scc1 .Lc0_ub\n.Lc0_ub_b:\n\t"
  "v_mfma_f32_16x16x32_f16 v[208:211], v[92:95], v[168:171], v[208:211]\n\tds_read_b128 v[108:111], v18 offset:2240\n\tds_read_b128 v[112:115], v18 offset:2752\n\t"
  "v_mfma_f32_16x16x32_f16 v[212:215], v[92:95], v[172:175], v[212:215]\n\tds_read_b128 v[128:131], v18 offset:3264\n\tds_read_b128 v[132:135], v18 offset:3776\n\t"
  "v_mfma_f32_16x16x32_f16 v[208:211], v[96:99], v[164:167], v[208:211]\n\t"
  "v_mfma_f32_16x16x32_f16 v[212:215], v[96:99], v[168:171], v[212:215]\n\t"
  "v_mfma_f32_16x16x32_f16 v[176:179], v[96:99], v[172:175], v[176:179]\n\tds_read_b128 v[136:139], v18 offset:2176\n\tds_read_b128 v[140:143], v18 offset:2688\n\t"
  "v_mfma_f32_16x16x32_f16 v[208:211], v[116:119], v[160:163], v[208:211]\n\tds_read_b128 v[144:147], v18 offset:3200\n\tds_read_b128 v[148:151], v18 offset:3712\n\t"
  "v_mfma_f32_16x16x32_f16 v[212:215], v[116:119], v[164:167], v[212:215]\n\t"
  "v_mfma_f32_16x16x32_f16 v[176:179], v[116:119], v[168:171], v[176:179]\n\t"
  "v_mfma_f32_16x16x32_f16 v[180:183], v[116:119], v[172:175], v[180:183]\n\ts_waitcnt lgkmcnt(6)\n\t"
  "v_mfma_f32_16x16x32_f16 v[212:215], v[120:123], v[160:163], v[212:215]\n\tds_read_b128 v[116:119], v18 offset:2112\n\tds_read_b128 v[152:155], v18 offset:2624\n\t"
  "v_mfma_f32_16x16x32_f16 v[176:179], v[120:123], v[164:167], v[176:179]\n\tds_read_b128 v[156:159], v18 offset:3136\n\tds_read_b128 v[172:175], v18 offset:3648\n\t"
  "v_mfma_f32_16x16x32_f16 v[180:183], v[120:123], v[168:171], v[180:183]\n\t"
  "v_mfma_f32_16x16x32_f16 v[176:179], v[102:105], v[160:163], v[176:179]\n\t"
  "v_mfma_f32_16x16x32_f16 v[180:183], v[102:105], v[164:167], v[180:183]\n\t"
  "v_mfma_f32_16x16x32_f16 v[180:183], v[124:127], v[160:163], v[180:183]\n\tds_read_b128 v[104:107], v18 offset:2048\n\tds_read_b128 v[120:123], v18 offset:2560\n\t"
  "v_mfma_f32_16x16x32_f16 v[212:215], v[40:43], v[108:111], v[212:215]\n\tds_read_b128 v[124:127], v18 offset:3072\n\tds_read_b128 v[160:163], v18 offset:3584\n\t"
  "v_mfma_f32_16x16x32_f16 v[176:179], v[44:47], v[108:111], v[176:179]\n\t"
  "v_mfma_f32_16x16x32_f16 v[212:215], v[36:39], v[112:115], v[212:215]\n\t"
  "v_mfma_f32_16x16x32_f16 v[176:179], v[40:43], v[112:115], v[176:179]\n\ts_waitcnt lgkmcnt(4)\n\t"
  "v_mfma_f32_16x16x32_f16 v[176:179], v[36:39], v[128:131], v[176:179]\n\t"
  "v_mfma_f32_16x16x32_f16 v[212:215], v[56:59], v[136:139], v[212:215]\n\t"
  "v_mfma_f32_16x16x32_f16 v[176:179], v[60:63], v[136:139], v[176:179]\n\t"
  "v_mfma_f32_16x16x32_f16 v[212:215], v[52:55], v[140:143], v[212:215]\n\t"
  "v_mfma_f32_16x16x32_f16 v[176:179], v[56:59], v[140:143], v[176:179]\n\t"
  "v_mfma_f32_16x16x32_f16 v[176:179], v[52:55], v[144:147], v[176:179]\n\t"
  "v_mfma_f32_16x16x32_f16 v[212:215], v[72:75], v[116:119], v[212:215]\n\tds_read_b128 v[164:167], v19 offset:4096\n\tds_read_b128 v[168:171], v19 offset:7168\n\t"
  "v_mfma_f32_16x16x32_f16 v[176:179], v[76:79], v[116:119], v[176:179]\n\t"
  "v_mfma_f32_16x16x32_f16 v[212:215], v[68:71], v[152:155], v[212:215]\n\t"
  "v_mfma_f32_16x16x32_f16 v[176:179], v[72:75], v[152:155], v[176:179]\n\t"
  "v_mfma_f32_16x16x32_f16 v[176:179], v[68:71], v[156:159], v[176:179]\n\ts_waitcnt lgkmcnt(2)\n\t"
  "v_mfma_f32_16x16x32_f16 v[212:215], v[88:91], v[104:107], v[212:215]\n\t"
  "v_mfma_f32_16x16x32_f16 v[176:179], v[92:95], v[104:107], v[176:179]\n\t"
  "v_mfma_f32_16x16x32_f16 v[212:215], v[84:87], v[120:123], v[212:215]\n\t"
  "v_mfma_f32_16x16x32_f16 v[176:179], v[88:91], v[120:123], v[176:179]\n\tds_read_b32 v101, v27 offset:64\n\ts_waitcnt lgkmcnt(1)\n\t"
  "v_mfma_f32_16x16x32_f16 v[176:179], v[84:87], v[124:127], v[176:179]\n\tds_write_b128 v20, v[212:215] offset:57344\n\tv_perm_b32 v164, v164, v164, s60\n\t"
  "v_mfma_f32_16x16x32_f16 v[208:211], v[36:39], v[108:111], v[208:211]\n\ts_setprio 3\n\tbuffer_load_dwordx4 v[28:31], v23, s[56:59], 0 offen sc1\n\tbuffer_load_dwordx4 v[32:35], v24, s[56:59], 0 offen sc1\n\ts_setprio 0\n\t"
  "v_mfma_f32_16x16x32_f16 v[180:183], v[48:51], v[108:111], v[180:183]\n\tds_write_b128 v20, v[176:179] offset:58368\n\tv_perm_b32 v165, v165, v165, s60\n\t"
  "v_mfma_f32_16x16x32_f16 v[180:183], v[44:47], v[112:115], v[180:183]\n\ts_mov_b64 exec, 1\n\tds_add_u32 v27, v100 offset:16\n\ts_mov_b64 exec, -1\n\t"
  "v_mfma_f32_16x16x32_f16 v[180:183], v[40:43], v[128:131], v[180:183]\n\tv_perm_b32 v166, v166, v166, s60\n\tv_perm_b32 v167, v167, v167, s60\n\t"
  "v_mfma_f32_16x16x32_f16 v[180:183], v[36:39], v[132:135], v[180:183]\n\tv_perm_b32 v168, v168, v168, s60\n\tv_perm_b32 v169, v169, v169, s60\n\t"
  "v_mfma_f32_16x16x32_f16 v[208:211], v[52:55], v[136:139], v[208:211]\n\tv_perm_b32 v170, v170, v170, s60\n\tv_perm_b32 v171, v171, v171, s60\n\t"
  "v_mfma_f32_16x16x32_f16 v[180:183], v[64:67], v[136:139], v[180:183]\n\ts_waitcnt lgkmcnt(3)\n\tv_readfirstlane_b32 s61, v101\n\t"
  "v_mfma_f32_16x16x32_f16 v[180:183], v[60:63], v[140:143], v[180:183]\n\t"
  "v_mfma_f32_16x16x32_f16 v[180:183], v[56:59], v[144:147], v[180:183]\n\ts_cmp_lt_u32 s61, 4\n\ts_cbranch_scc1 .Lc0_p3\n.Lc0_p3_b:\n\t"
  "v_mfma_f32_16x16x32_f16 v[180:183], v[52:55], v[148:151], v[180:183]\n\tds_read_b32 v36, v27 offset:16\n\tds_read_b128 v[40:43], v20 offset:59392\n\tds_read_b128 v[44:47], v20 offset:60416\n\t"
  "v_mfma_f32_16x16x32_f16 v[208:211], v[68:71], v[116:119], v[208:211]\n\t"
  "v_mfma_f32_16x16x32_f16 v[180:183], v[80:83], v[116:119], v[180:183]\n\t"
  "v_mfma_f32_16x16x32_f16 v[180:183], v[76:79], v[152:155], v[180:183]\n\t"
  "v_mfma_f32_16x16x32_f16 v[180:183], v[72:75], v[156:159], v[180:183]\n\t"
  "v_mfma_f32_16x16x32_f16 v[180:183], v[68:71], v[172:175], v[180:183]\n\t"
  "v_mfma_f32_16x16x32_f16 v[208:211], v[84:87], v[104:107], v[208:211]\n\t"
  "v_mfma_f32_16x16x32_f16 v[180:183], v[96:99], v[104:107], v[180:183]\n\t"
  "v_mfma_f32_16x16x32_f16 v[180:183], v[92:95], v[120:123], v[180:183]\n\ts_waitcnt lgkmcnt(0)\n\tv_readfirstlane_b32 s61, v36\n\tds_read_b32 v25, v27 offset:32\n\t"
  "v_mfma_f32_16x16x32_f16 v[180:183], v[88:91], v[124:127], v[180:183]\n\ts_cmp_lt_u32 s61, 4\n\ts_cbranch_scc1 .Lc0_x4\n.Lc0_x4_b:\n\t"
  "v_mfma_f32_16x16x32_f16 v[180:183], v[84:87], v[160:163], v[180:183]\n\tv_fmac_f32 v164, v40, v26\n\tv_fmac_f32 v165, v41, v26\n\tv_fmac_f32 v166, v42, v26\n\t"
  "v_fmac_f32 v167, v43, v26\n\tv_fmac_f32 v168, v44, v26\n\tv_fmac_f32 v169, v45, v26\n\tv_fmac_f32 v170, v46, v26\n\t"
  "v_fmac_f32 v171, v47, v26\n\tv_fmac_f32 v164, v208, v26\n\tv_fmac_f32 v165, v209, v26\n\tv_fmac_f32 v166, v210, v26\n\t"
  "v_fmac_f32 v167, v211, v26\n\tv_cvt_pk_bf16_f32 v36, v164, v165\n\tv_cvt_pk_bf16_f32 v37, v166, v167\n\tv_fmac_f32 v168, v180, v26\n\t"
  "v_fmac_f32 v169, v181, v26\n\tv_fmac_f32 v170, v182, v26\n\tv_fmac_f32 v171, v183, v26\n\tv_cvt_pk_bf16_f32 v38, v168, v169\n\t"
  "v_cvt_pk_bf16_f32 v39, v170, v171\n\ts_nop 0\n\tv_permlane16_swap_b32 v36, v38\n\tv_permlane16_swap_b32 v37, v39\n\t"
  "s_setprio 3\n\tbuffer_store_dwordx4 v[36:39], v22, s[56:59], 0 offen sc1\n\ts_setprio 0\n\ts_waitcnt lgkmcnt(0)\n\ts_nop 0\n\t"
  "s_branch .Lc_end\n.Lc_odd:\n\t"
  "ds_read_b64 v[36:37], v16 offset:3776\n\tds_read_b64 v[38:39], v17 offset:3776\n\tds_read_b64 v[40:41], v16 offset:3264\n\tds_read_b64 v[42:43], v17 offset:3264\n\t"
  "ds_read_b64 v[44:45], v16 offset:2752\n\tds_read_b64 v[46:47], v17 offset:2752\n\tds_read_b64 v[48:49], v16 offset:2240\n\tds_read_b64 v[50:51], v17 offset:2240\n\t"
  "ds_read_b64 v[52:53], v16 offset:3712\n\tds_read_b64 v[54:55], v17 offset:3712\n\tds_read_b64 v[56:57], v16 offset:3200\n\tds_read_b64 v[58:59], v17 offset:3200\n\t"
  "ds_read_b64 v[60:61], v16 offset:2688\n\tds_read_b64 v[62:63], v17 offset:2688\n\tds_read_b64 v[64:65], v16 offset:2176\n\tds_read_b64 v[66:67], v17 offset:2176\n\t"
  "ds_read_b64 v[68:69], v16 offset:3648\n\tds_read_b64 v[70:71], v17 offset:3648\n\tds_read_b64 v[72:73], v16 offset:3136\n\tds_read_b64 v[74:75], v17 offset:3136\n\t"
  "ds_read_b64 v[76:77], v16 offset:2624\n\tds_read_b64 v[78:79], v17 offset:2624\n\tds_read_b64 v[80:81], v16 offset:2112\n\tds_read_b64 v[82:83], v17 offset:2112\n\t"
  "ds_read_b64 v[84:85], v16 offset:3584\n\tds_read_b64 v[86:87], v17 offset:3584\n\tds_read_b64 v[88:89], v16 offset:3072\n\tds_read_b64 v[90:91], v17 offset:3072\n\t"
  "ds_read_b64 v[92:93], v16 offset:2560\n\tds_read_b64 v[94:95], v17 offset:2560\n\tds_read_b64 v[96:97], v16 offset:2048\n\tds_read_b64 v[98:99], v17 offset:2048\n\t"
  "ds_read_b32 v26, v21\n\tv_and_b32 v27, -8, v21\n\tv_mov_b32 v100, 1\n\t"
  CST(6)
  "s_mov_b32 vcc_hi, 0x1000000\n\t.Lc1_u:\n\tds_read_b32 v101, v27 offset:56\n\ts_sub_u32 vcc_hi, vcc_hi, 1\n\ts_cselect_b32 vcc_lo, 0, 4\n\ts_waitcnt lgkmcnt(0)\n\tv_readfirstlane_b32 s61, v101\n\ts_nop 1\n\ts_cmp_lt_u32 s61, vcc_lo\n\ts_cbranch_scc1 .Lc1_u\n\t"
  CST(7)
  "ds_read_b128 v[102:105], v18 offset:192\n\tds_read_b128 v[106:109], v18 offset:704\n\tds_read_b128 v[110:113], v18 offset:1216\n\tds_read_b128 v[114:117], v18 offset:1728\n\t"
  "ds_read_b128 v[120:123], v18 offset:128\n\tds_read_b128 v[124:127], v18 offset:640\n\tds_read_b128 v[128:131], v18 offset:1152\n\tds_read_b128 v[132:135], v18 offset:1664\n\t"
  "ds_read_b128 v[136:139], v18 offset:64\n\ts_waitcnt lgkmcnt(8)\n\t"
  "v_mfma_f32_16x16x32_f16 v[140:143], v[36:39], v[102:105], 0\n\tds_read_b128 v[144:147], v18 offset:576\n\t"
  "v_mfma_f32_16x16x32_f16 v[148:151], v[48:51], v[102:105], 0\n\tds_read_b128 v[152:155], v18 offset:1088\n\ts_waitcnt lgkmcnt(9)\n\t"
  "v_mfma_f32_16x16x32_f16 v[148:151], v[44:47], v[106:109], v[148:151]\n\tds_read_b128 v[156:159], v18 offset:1600\n\ts_waitcnt lgkmcnt(3)\n\t"
  "v_mfma_f32_16x16x32_f16 v[148:151], v[40:43], v[110:113], v[148:151]\n\tds_read_b128 v[160:163], v18 offset:0\n\tds_read_b32 v101, v27 offset:48\n\t"
  "v_mfma_f32_16x16x32_f16 v[148:151], v[36:39], v[114:117], v[148:151]\n\tds_read_b128 v[164:167], v18 offset:512\n\t"
  "v_mfma_f32_16x16x32_f16 v[140:143], v[52:55], v[120:123], v[140:143]\n\tds_read_b128 v[168:171], v18 offset:1024\n\t"
  "v_mfma_f32_16x16x32_f16 v[148:151], v[64:67], v[120:123], v[148:151]\n\tds_read_b128 v[172:175], v18 offset:1536\n\t"
  "v_mfma_f32_16x16x32_f16 v[148:151], v[60:63], v[124:127], v[148:151]\n\t"
  "v_mfma_f32_16x16x32_f16 v[148:151], v[56:59], v[128:131], v[148:151]\n\t"
  "v_mfma_f32_16x16x32_f16 v[148:151], v[52:55], v[132:135], v[148:151]\n\t"
  "v_mfma_f32_16x16x32_f16 v[140:143], v[68:71], v[136:139], v[140:143]\n\tds_read_b128 v[176:179], v19 offset:1024\n\tds_read_b128 v[180:183], v19 offset:2048\n\t"
  "v_mfma_f32_16x16x32_f16 v[148:151], v[80:83], v[136:139], v[148:151]\n\ts_waitcnt lgkmcnt(5)\n\tv_readfirstlane_b32 s61, v101\n\t"
  "v_mfma_f32_16x16x32_f16 v[148:151], v[76:79], v[144:147], v[148:151]\n\t"
  "v_mfma_f32_16x16x32_f16 v[148:151], v[72:75], v[152:155], v[148:151]\n\ts_cmp_lt_u32 s61, 4\n\ts_cbranch_scc1 .Lc1_kh\n.Lc1_kh_b:\n\t"
  "v_mfma_f32_16x16x32_f16 v[148:151], v[68:71], v[156:159], v[148:151]\n\tds_read_b64 v[184:185], v16 offset:1728\n\tds_read_b64 v[186:187], v17 offset:1728\n\t"
  "v_mfma_f32_16x16x32_f16 v[140:143], v[84:87], v[160:163], v[140:143]\n\tds_read_b64 v[188:189], v16 offset:1216\n\tds_read_b64 v[190:191], v17 offset:1216\n\t"
  "v_mfma_f32_16x16x32_f16 v[148:151], v[96:99], v[160:163], v[148:151]\n\tds_read_b64 v[192:193], v16 offset:704\n\ts_waitcnt lgkmcnt(7)\n\t"
  "v_mfma_f32_16x16x32_f16 v[148:151], v[92:95], v[164:167], v[148:151]\n\tds_read_b64 v[194:195], v17 offset:704\n\tds_read_b64 v[196:197], v16 offset:192\n\tds_read_b64 v[198:199], v17 offset:192\n\t"
  "v_mfma_f32_16x16x32_f16 v[148:151], v[88:91], v[168:171], v[148:151]\n\ts_waitcnt lgkmcnt(8)\n\tv_perm_b32 v176, v176, v176, s60\n\t"
  "v_mfma_f32_16x16x32_f16 v[148:151], v[84:87], v[172:175], v[148:151]\n\tds_write_b128 v20, v[140:143] offset:2048\n\tv_perm_b32 v177, v177, v177, s60\n\t"
  "v_mfma_f32_16x16x32_f16 v[200:203], v[40:43], v[102:105], 0\n\tv_perm_b32 v178, v178, v178, s60\n\tv_perm_b32 v179, v179, v179, s60\n\t"
  "v_mfma_f32_16x16x32_f16 v[140:143], v[44:47], v[102:105], 0\n\tds_write_b128 v20, v[148:151] offset:3072\n\tv_perm_b32 v180, v180, v180, s60\n\t"
  "v_mfma_f32_16x16x32_f16 v[200:203], v[36:39], v[106:109], v[200:203]\n\ts_mov_b64 exec, 1\n\tds_add_u32 v21, v100 offset:8\n\ts_mov_b64 exec, -1\n\t"
  "v_mfma_f32_16x16x32_f16 v[140:143], v[40:43], v[106:109], v[140:143]\n\tv_perm_b32 v181, v181, v181, s60\n\tv_perm_b32 v182, v182, v182, s60\n\t"
  "v_mfma_f32_16x16x32_f16 v[140:143], v[36:39], v[110:113], v[140:143]\n\tv_perm_b32 v183, v183, v183, s60\n\t"
  "v_mfma_f32_16x16x32_f16 v[200:203], v[56:59], v[120:123], v[200:203]\n\t"
  "v_mfma_f32_16x16x32_f16 v[140:143], v[60:63], v[120:123], v[140:143]\n\t"
  "v_mfma_f32_16x16x32_f16 v[200:203], v[52:55], v[124:127], v[200:203]\n\t"
  "v_mfma_f32_16x16x32_f16 v[140:143], v[56:59], v[124:127], v[140:143]\n\t"
  "v_mfma_f32_16x16x32_f16 v[140:143], v[52:55], v[128:131], v[140:143]\n\t"
  "v_mfma_f32_16x16x32_f16 v[200:203], v[72:75], v[136:139], v[200:203]\n\t"
  "v_mfma_f32_16x16x32_f16 v[140:143], v[76:79], v[136:139], v[140:143]\n\tds_read_b32 v101, v21 offset:8\n\tds_read_b128 v[148:151], v20 offset:0\n\tds_read_b128 v[204:207], v20 offset:1024\n\t"
  "v_mfma_f32_16x16x32_f16 v[200:203], v[68:71], v[144:147], v[200:203]\n\t"
  "v_mfma_f32_16x16x32_f16 v[140:143], v[72:75], v[144:147], v[140:143]\n\t"
  "v_mfma_f32_16x16x32_f16 v[140:143], v[68:71], v[152:155], v[140:143]\n\t"
  "v_mfma_f32_16x16x32_f16 v[200:203], v[88:91], v[160:163], v[200:203]\n\t"
  "v_mfma_f32_16x16x32_f16 v[140:143], v[92:95], v[160:163], v[140:143]\n\t"
  "v_mfma_f32_16x16x32_f16 v[200:203], v[84:87], v[164:167], v[200:203]\n\t"
  "v_mfma_f32_16x16x32_f16 v[140:143], v[88:91], v[164:167], v[140:143]\n\t"
  "v_mfma_f32_16x16x32_f16 v[140:143], v[84:87], v[168:171], v[140:143]\n\ts_waitcnt lgkmcnt(0)\n\tv_readfirstlane_b32 s61, v101\n\t"
  "v_mfma_f32_16x16x32_f16 v[208:211], v[40:43], v[114:117], 0\n\t"
  "v_mfma_f32_16x16x32_f16 v[208:211], v[44:47], v[110:113], v[208:211]\n\ts_cmp_lt_u32 s61, 2\n\ts_cbranch_scc1 .Lc1_x0\n.Lc1_x0_b:\n\tv_fmac_f32 v176, v148, v26\n\tv_fmac_f32 v177, v149, v26\n\t"
  "v_fmac_f32 v178, v150, v26\n\t"
  "v_mfma_f32_16x16x32_f16 v[212:215], v[44:47], v[114:117], 0\n\tv_fmac_f32 v179, v151, v26\n\tv_fmac_f32 v180, v204, v26\n\tv_fmac_f32 v181, v205, v26\n\t"
  "v_fmac_f32 v182, v206, v26\n\tv_fmac_f32 v183, v207, v26\n\t"
  "v_mfma_f32_16x16x32_f16 v[208:211], v[48:51], v[106:109], v[208:211]\n\tv_fmac_f32 v176, v200, v26\n\tv_fmac_f32 v177, v201, v26\n\t"
  "v_mfma_f32_16x16x32_f16 v[212:215], v[48:51], v[110:113], v[212:215]\n\tv_fmac_f32 v178, v202, v26\n\tv_fmac_f32 v179, v203, v26\n\tv_cvt_pk_bf16_f32 v148, v176, v177\n\t"
  "v_cvt_pk_bf16_f32 v149, v178, v179\n\tv_fmac_f32 v180, v140, v26\n\t"
  "v_mfma_f32_16x16x32_f16 v[176:179], v[48:51], v[114:117], 0\n\tv_fmac_f32 v181, v141, v26\n\tv_fmac_f32 v182, v142, v26\n\tv_fmac_f32 v183, v143, v26\n\t"
  "v_cvt_pk_bf16_f32 v150, v180, v181\n\tv_cvt_pk_bf16_f32 v151, v182, v183\n\t"
  "v_mfma_f32_16x16x32_f16 v[208:211], v[184:187], v[102:105], v[208:211]\n\tv_permlane16_swap_b32 v148, v150\n\tv_permlane16_swap_b32 v149, v151\n\ts_setprio 3\n\tbuffer_store_dwordx4 v[148:151], v22, s[56:59], 0 offen sc1\n\ts_setprio 0\n\t"
  CST(8)
  "v_mfma_f32_16x16x32_f16 v[212:215], v[184:187], v[106:109], v[212:215]\n\tds_read_b64 v[140:141], v16 offset:1664\n\tds_read_b64 v[142:143], v17 offset:1664\n\t"
  "v_mfma_f32_16x16x32_f16 v[176:179], v[184:187], v[110:113], v[176:179]\n\tds_read_b64 v[148:149], v16 offset:1152\n\tds_read_b64 v[150:151], v17 offset:1152\n\t"
  "v_mfma_f32_16x16x32_f16 v[180:183], v[184:187], v[114:117], 0\n\tds_read_b64 v[116:117], v16 offset:640\n\tds_read_b64 v[118:119], v17 offset:640\n\t"
  "v_mfma_f32_16x16x32_f16 v[212:215], v[188:191], v[102:105], v[212:215]\n\t"
  "v_mfma_f32_16x16x32_f16 v[176:179], v[188:191], v[106:109], v[176:179]\n\tds_read_b64 v[184:185], v16 offset:128\n\tds_read_b64 v[186:187], v17 offset:128\n\t"
  "v_mfma_f32_16x16x32_f16 v[180:183], v[188:191], v[110:113], v[180:183]\n\t"
  "v_mfma_f32_16x16x32_f16 v[176:179], v[192:195], v[102:105], v[176:179]\n\t"
  "v_mfma_f32_16x16x32_f16 v[180:183], v[192:195], v[106:109], v[180:183]\n\t"
  "v_mfma_f32_16x16x32_f16 v[180:183], v[196:199], v[102:105], v[180:183]\n\t"
  "v_mfma_f32_16x16x32_f16 v[208:211], v[56:59], v[132:135], v[208:211]\n\t"
  "v_mfma_f32_16x16x32_f16 v[208:211], v[60:63], v[128:131], v[208:211]\n\t"
  "v_mfma_f32_16x16x32_f16 v[212:215], v[60:63], v[132:135], v[212:215]\n\tds_read_b64 v[102:103], v16 offset:1600\n\tds_read_b64 v[104:105], v17 offset:1600\n\t"
  "v_mfma_f32_16x16x32_f16 v[208:211], v[64:67], v[124:127], v[208:211]\n\t"
  "v_mfma_f32_16x16x32_f16 v[212:215], v[64:67], v[128:131], v[212:215]\n\t"
  "v_mfma_f32_16x16x32_f16 v[176:179], v[64:67], v[132:135], v[176:179]\n\ts_waitcnt lgkmcnt(2)\n\t"
  "v_mfma_f32_16x16x32_f16 v[208:211], v[140:143], v[120:123], v[208:211]\n\tds_read_b64 v[108:109], v16 offset:1088\n\tds_read_b64 v[110:111], v17 offset:1088\n\t"
  "v_mfma_f32_16x16x32_f16 v[212:215], v[140:143], v[124:127], v[212:215]\n\t"
  "v_mfma_f32_16x16x32_f16 v[176:179], v[140:143], v[128:131], v[176:179]\n\t"
  "v_mfma_f32_16x16x32_f16 v[180:183], v[140:143], v[132:135], v[180:183]\n\tds_read_b64 v[112:113], v16 offset:576\n\tds_read_b64 v[114:115], v17 offset:576\n\t"
  "v_mfma_f32_16x16x32_f16 v[212:215], v[148:151], v[120:123], v[212:215]\n\t"
  "v_mfma_f32_16x16x32_f16 v[176:179], v[148:151], v[124:127], v[176:179]\n\tds_read_b64 v[132:133], v16 offset:64\n\tds_read_b64 v[134:135], v17 offset:64\n\t"
  "v_mfma_f32_16x16x32_f16 v[180:183], v[148:151], v[128:131], v[180:183]\n\t"
  "v_mfma_f32_16x16x32_f16 v[176:179], v[116:119], v[120:123], v[176:179]\n\t"
  "v_mfma_f32_16x16x32_f16 v[180:183], v[116:119], v[124:127], v[180:183]\n\tv_add_u32 v22, 0x101000, v22\n\t"
  "v_mfma_f32_16x16x32_f16 v[180:183], v[184:187], v[120:123], v[180:183]\n\t"
  "v_mfma_f32_16x16x32_f16 v[208:211], v[72:75], v[156:159], v[208:211]\n\t"
  "v_mfma_f32_16x16x32_f16 v[208:211], v[76:79], v[152:155], v[208:211]\n\t"
  "v_mfma_f32_16x16x32_f16 v[212:215], v[76:79], v[156:159], v[212:215]\n\tds_read_b64 v[116:117], v16 offset:1536\n\tds_read_b64 v[118:119], v17 offset:1536\n\t"
  "v_mfma_f32_16x16x32_f16 v[208:211], v[80:83], v[144:147], v[208:211]\n\t"
  "v_mfma_f32_16x16x32_f16 v[212:215], v[80:83], v[152:155], v[212:215]\n\t"
  "v_mfma_f32_16x16x32_f16 v[176:179], v[80:83], v[156:159], v[176:179]\n\ts_waitcnt lgkmcnt(2)\n\t"
  "v_mfma_f32_16x16x32_f16 v[208:211], v[102:105], v[136:139], v[208:211]\n\tds_read_b32 v101, v27 offset:52\n\tds_read_b64 v[120:121], v16 offset:1024\n\t"
  "v_mfma_f32_16x16x32_f16 v[212:215], v[102:105], v[144:147], v[212:215]\n\tds_read_b64 v[122:123], v17 offset:1024\n\t"
  "v_mfma_f32_16x16x32_f16 v[176:179], v[102:105], v[152:155], v[176:179]\n\t"
  "v_mfma_f32_16x16x32_f16 v[180:183], v[102:105], v[156:159], v[180:183]\n\tds_read_b64 v[102:103], v16 offset:512\n\tds_read_b64 v[104:105], v17 offset:512\n\t"
  "v_mfma_f32_16x16x32_f16 v[212:215], v[108:111], v[136:139], v[212:215]\n\t"
  "v_mfma_f32_16x16x32_f16 v[176:179], v[108:111], v[144:147], v[176:179]\n\tds_read_b64 v[124:125], v16 offset:0\n\tds_read_b64 v[126:127], v17 offset:0\n\t"
  "v_mfma_f32_16x16x32_f16 v[180:183], v[108:111], v[152:155], v[180:183]\n\t"
  "v_mfma_f32_16x16x32_f16 v[176:179], v[112:115], v[136:139], v[176:179]\n\t"
  "v_mfma_f32_16x16x32_f16 v[180:183], v[112:115], v[144:147], v[180:183]\n\ts_waitcnt lgkmcnt(5)\n\tv_readfirstlane_b32 s61, v101\n\t"
  "v_mfma_f32_16x16x32_f16 v[180:183], v[132:135], v[136:139], v[180:183]\n\t"
  "v_mfma_f32_16x16x32_f16 v[208:211], v[88:91], v[172:175], v[208:211]\n\ts_cmp_lt_u32 s61, 4\n\ts_cbranch_scc1 .Lc1_ub\n.Lc1_ub_b:\n\t"
  "v_mfma_f32_16x16x32_f16 v[208:211], v[92:95], v[168:171], v[208:211]\n\tds_read_b128 v[108:111], v18 offset:2240\n\tds_read_b128 v[112:115], v18 offset:2752\n\t"
  "v_mfma_f32_16x16x32_f16 v[212:215], v[92:95], v[172:175], v[212:215]\n\tds_read_b128 v[128:131], v18 offset:3264\n\tds_read_b128 v[132:135], v18 offset:3776\n\t"
  "v_mfma_f32_16x16x32_f16 v[208:211], v[96:99], v[164:167], v[208:211]\n\t"
  "v_mfma_f32_16x16x32_f16 v[212:215], v[96:99], v[168:171], v[212:215]\n\t"
  "v_mfma_f32_16x16x32_f16 v[176:179], v[96:99], v[172:175], v[176:179]\n\tds_read_b128 v[136:139], v18 offset:2176\n\tds_read_b128 v[140:143], v18 offset:2688\n\t"
  "v_mfma_f32_16x16x32_f16 v[208:211], v[116:119], v[160:163], v[208:211]\n\tds_read_b128 v[144:147], v18 offset:3200\n\tds_read_b128 v[148:151], v18 offset:3712\n\t"
  "v_mfma_f32_16x16x32_f16 v[212:215], v[116:119], v[164:167], v[212:215]\n\t"
  "v_mfma_f32_16x16x32_f16 v[176:179], v[116:119], v[168:171], v[176:179]\n\t"
  "v_mfma_f32_16x16x32_f16 v[180:183], v[116:119], v[172:175], v[180:183]\n\ts_waitcnt lgkmcnt(6)\n\t"
  "v_mfma_f32_16x16x32_f16 v[212:215], v[120:123], v[160:163], v[212:215]\n\tds_read_b128 v[116:119], v18 offset:2112\n\tds_read_b128 v[152:155], v18 offset:2624\n\t"
  "v_mfma_f32_16x16x32_f16 v[176:179], v[120:123], v[164:167], v[176:179]\n\tds_read_b128 v[156:159], v18 offset:3136\n\tds_read_b128 v[172:175], v18 offset:3648\n\t"
  "v_mfma_f32_16x16x32_f16 v[180:183], v[120:123], v[168:171], v[180:183]\n\t"
  "v_mfma_f32_16x16x32_f16 v[176:179], v[102:105], v[160:163], v[176:179]\n\t"
  "v_mfma_f32_16x16x32_f16 v[180:183], v[102:105], v[164:167], v[180:183]\n\t"
  "v_mfma_f32_16x16x32_f16 v[180:183], v[124:127], v[160:163], v[180:183]\n\tds_read_b128 v[104:107], v18 offset:2048\n\tds_read_b128 v[120:123], v18 offset:2560\n\t"
  "v_mfma_f32_16x16x32_f16 v[208:211], v[36:39], v[108:111], v[208:211]\n\tds_read_b128 v[124:127], v18 offset:3072\n\tds_read_b128 v[160:163], v18 offset:3584\n\t"
  "v_mfma_f32_16x16x32_f16 v[180:183], v[48:51], v[108:111], v[180:183]\n\t"
  "v_mfma_f32_16x16x32_f16 v[180:183], v[44:47], v[112:115], v[180:183]\n\ts_waitcnt lgkmcnt(6)\n\t"
  "v_mfma_f32_16x16x32_f16 v[180:183], v[40:43], v[128:131], v[180:183]\n\t"
  "v_mfma_f32_16x16x32_f16 v[180:183], v[36:39], v[132:135], v[180:183]\n\t"
  "v_mfma_f32_16x16x32_f16 v[208:211], v[52:55], v[136:139], v[208:211]\n\t"
  "v_mfma_f32_16x16x32_f16 v[180:183], v[64:67], v[136:139], v[180:183]\n\t"
  "v_mfma_f32_16x16x32_f16 v[180:183], v[60:63], v[140:143], v[180:183]\n\t"
  "v_mfma_f32_16x16x32_f16 v[180:183], v[56:59], v[144:147], v[180:183]\n\t"
  "v_mfma_f32_16x16x32_f16 v[180:183], v[52:55], v[148:151], v[180:183]\n\t"
  "v_mfma_f32_16x16x32_f16 v[208:211], v[68:71], v[116:119], v[208:211]\n\tds_read_b128 v[48:51], v19 offset:5120\n\tds_read_b128 v[64:67], v19 offset:6144\n\t"
  "v_mfma_f32_16x16x32_f16 v[180:183], v[80:83], v[116:119], v[180:183]\n\t"
  "v_mfma_f32_16x16x32_f16 v[180:183], v[76:79], v[152:155], v[180:183]\n\ts_waitcnt lgkmcnt(2)\n\t"
  "v_mfma_f32_16x16x32_f16 v[180:183], v[72:75], v[156:159], v[180:183]\n\t"
  "v_mfma_f32_16x16x32_f16 v[180:183], v[68:71], v[172:175], v[180:183]\n\t"
  "v_mfma_f32_16x16x32_f16 v[208:211], v[84:87], v[104:107], v[208:211]\n\t"
  "v_mfma_f32_16x16x32_f16 v[180:183], v[96:99], v[104:107], v[180:183]\n\t"
  "v_mfma_f32_16x16x32_f16 v[180:183], v[92:95], v[120:123], v[180:183]\n\t"
  "v_mfma_f32_16x16x32_f16 v[180:183], v[88:91], v[124:127], v[180:183]\n\tds_read_b32 v101, v27 offset:64\n\ts_waitcnt lgkmcnt(1)\n\t"
  "v_mfma_f32_16x16x32_f16 v[180:183], v[84:87], v[160:163], v[180:183]\n\tds_write_b128 v20, v[208:211] offset:59392\n\tv_perm_b32 v48, v48, v48, s60\n\t"
  "v_mfma_f32_16x16x32_f16 v[212:215], v[40:43], v[108:111], v[212:215]\n\ts_setprio 3\n\tbuffer_load_dwordx4 v[28:31], v23, s[56:59], 0 offen sc1\n\tbuffer_load_dwordx4 v[32:35], v24, s[56:59], 0 offen sc1\n\ts_setprio 0\n\t"
  "v_mfma_f32_16x16x32_f16 v[176:179], v[44:47], v[108:111], v[176:179]\n\tds_write_b128 v20, v[180:183] offset:60416\n\tv_perm_b32 v49, v49, v49, s60\n\t"
  "v_mfma_f32_16x16x32_f16 v[212:215], v[36:39], v[112:115], v[212:215]\n\ts_mov_b64 exec, 1\n\tds_add_u32 v27, v100 offset:16\n\ts_mov_b64 exec, -1\n\t"
  "v_mfma_f32_16x16x32_f16 v[176:179], v[40:43], v[112:115], v[176:179]\n\tv_perm_b32 v50, v50, v50, s60\n\tv_perm_b32 v51, v51, v51, s60\n\t"
  "v_mfma_f32_16x16x32_f16 v[176:179], v[36:39], v[128:131], v[176:179]\n\tv_perm_b32 v64, v64, v64, s60\n\tv_perm_b32 v65, v65, v65, s60\n\t"
  "v_mfma_f32_16x16x32_f16 v[212:215], v[56:59], v[136:139], v[212:215]\n\tv_perm_b32 v66, v66, v66, s60\n\tv_perm_b32 v67, v67, v67, s60\n\t"
  "v_mfma_f32_16x16x32_f16 v[176:179], v[60:63], v[136:139], v[176:179]\n\ts_waitcnt lgkmcnt(3)\n\tv_readfirstlane_b32 s61, v101\n\t"
  "v_mfma_f32_16x16x32_f16 v[212:215], v[52:55], v[140:143], v[212:215]\n\t"
  "v_mfma_f32_16x16x32_f16 v[176:179], v[56:59], v[140:143], v[176:179]\n\ts_cmp_lt_u32 s61, 4\n\ts_cbranch_scc1 .Lc1_p3\n.Lc1_p3_b:\n\t"
  "v_mfma_f32_16x16x32_f16 v[176:179], v[52:55], v[144:147], v[176:179]\n\tds_read_b32 v36, v27 offset:16\n\tds_read_b128 v[40:43], v20 offset:57344\n\tds_read_b128 v[44:47], v20 offset:58368\n\t"
  "v_mfma_f32_16x16x32_f16 v[212:215], v[72:75], v[116:119], v[212:215]\n\t"
  "v_mfma_f32_16x16x32_f16 v[176:179], v[76:79], v[116:119], v[176:179]\n\t"
  "v_mfma_f32_16x16x32_f16 v[212:215], v[68:71], v[152:155], v[212:215]\n\t"
  "v_mfma_f32_16x16x32_f16 v[176:179], v[72:75], v[152:155], v[176:179]\n\t"
  "v_mfma_f32_16x16x32_f16 v[176:179], v[68:71], v[156:159], v[176:179]\n\t"
  "v_mfma_f32_16x16x32_f16 v[212:215], v[88:91], v[104:107], v[212:215]\n\t"
  "v_mfma_f32_16x16x32_f16 v[176:179], v[92:95], v[104:107], v[176:179]\n\t"
  "v_mfma_f32_16x16x32_f16 v[212:215], v[84:87], v[120:123], v[212:215]\n\ts_waitcnt lgkmcnt(0)\n\tv_readfirstlane_b32 s61, v36\n\tds_read_b32 v25, v27 offset:32\n\t"
  "v_mfma_f32_16x16x32_f16 v[176:179], v[88:91], v[120:123], v[176:179]\n\ts_cmp_lt_u32 s61, 4\n\ts_cbranch_scc1 .Lc1_x4\n.Lc1_x4_b:\n\t"
  "v_mfma_f32_16x16x32_f16 v[176:179], v[84:87], v[124:127], v[176:179]\n\tv_fmac_f32 v48, v40, v26\n\tv_fmac_f32 v49, v41, v26\n\tv_fmac_f32 v50, v42, v26\n\t"
  "v_fmac_f32 v51, v43, v26\n\tv_fmac_f32 v64, v44, v26\n\tv_fmac_f32 v65, v45, v26\n\tv_fmac_f32 v66, v46, v26\n\t"
  "v_fmac_f32 v67, v47, v26\n\tv_fmac_f32 v48, v212, v26\n\tv_fmac_f32 v49, v213, v26\n\tv_fmac_f32 v50, v214, v26\n\t"
  "v_fmac_f32 v51, v215, v26\n\tv_cvt_pk_bf16_f32 v36, v48, v49\n\tv_cvt_pk_bf16_f32 v37, v50, v51\n\tv_fmac_f32 v64, v176, v26\n\t"
  "v_fmac_f32 v65, v177, v26\n\tv_fmac_f32 v66, v178, v26\n\tv_fmac_f32 v67, v179, v26\n\tv_cvt_pk_bf16_f32 v38, v64, v65\n\t"
  "v_cvt_pk_bf16_f32 v39, v66, v67\n\ts_nop 0\n\tv_permlane16_swap_b32 v36, v38\n\tv_permlane16_swap_b32 v37, v39\n\t"
  "s_setprio 3\n\tbuffer_store_dwordx4 v[36:39], v22, s[56:59], 0 offen sc1\n\ts_setprio 0\n\ts_waitcnt lgkmcnt(0)\n\ts_nop 0\n\t"
  ".Lc_end:\n\ts_waitcnt vmcnt(1) lgkmcnt(0)"
  : C1_OPS, "=&{v[28:31]}"(fl), "=&{v[32:35]}"(wfl), "=&{v25}"(g2)
  : "{s[56:59]}"(wrs), "{s60}"((a >> 1) ? 0x03020c0cu : 0x01000c0cu), [a] "s"(a)
  : "memory", "scc", "vcc", "s61", "v26", "v27", "v36", "v37", "v38", "v39", "v40", "v41", "v42", "v43", "v44", "v45", "v46", "v47", "v48", "v49", "v50", "v51", "v52", "v53", "v54", "v55", "v56", "v57", "v58", "v59", "v60", "v61", "v62", "v63", "v64", "v65", "v66", "v67", "v68", "v69", "v70", "v71", "v72", "v73", "v74", "v75", "v76", "v77", "v78", "v79", "v80", "v81", "v82", "v83", "v84", "v85", "v86", "v87", "v88", "v89", "v90", "v91", "v92", "v93", "v94", "v95", "v96", "v97", "v98", "v99", "v100", "v101", "v102", "v103", "v104", "v105", "v106", "v107", "v108", "v109", "v110", "v111", "v112", "v113", "v114", "v115", "v116", "v117", "v118", "v119", "v120", "v121", "v122", "v123", "v124", "v125", "v126", "v127", "v128", "v129", "v130", "v131", "v132", "v133", "v134", "v135", "v136", "v137", "v138", "v139", "v140", "v141", "v142", "v143", "v144", "v145", "v146", "v147", "v148", "v149", "v150", "v151", "v152", "v153", "v154", "v155", "v156", "v157", "v158", "v159", "v160", "v161", "v162", "v163", "v164", "v165", "v166", "v167", "v168", "v169", "v170", "v171", "v172", "v173", "v174", "v175", "v176", "v177", "v178", "v179", "v180", "v181", "v182", "v183", "v184", "v185", "v186", "v187", "v188", "v189", "v190", "v191", "v192", "v193", "v194", "v195", "v196", "v197", "v198", "v199", "v200", "v201", "v202", "v203", "v204", "v205", "v206", "v207", "v208", "v209", "v210", "v211", "v212", "v213", "v214", "v215", "v216", "v217", "v218", "v219", "v220", "v221", "v222", "v223");
  ST(0, 9);
}

struct XTake { u32 p, sw, idx; bf16x8 one; u64 m0, m1, m2; };
typedef __attribute__((ext_vector_type(16))) short bf16x16;
__device__ __forceinline__ void x_pre(XTake& X, int a, int lane) {
  X.p = (u32)(L_TA + a * 8192 + lane * 8);
  X.sw = (u32)(L_SP + ((lane & 15) * 4 + a) * 4 + ((lane >> 4) == ((lane >> 2) & 3) ? 768 : 0));
  X.idx = (lane & 1) ? 0xEEEEu : 0x4444u;
  X.one = __builtin_bit_cast(bf16x8, (u32x4){0x3F803F80u, 0x3F803F80u, 0x3F803F80u, 0x3F803F80u});
  X.m0 = 0xAAAAAAAAAAAAAAAAull; X.m1 = 0xCCCCCCCCCCCCCCCCull; X.m2 = 0xF0000F0000F0000Full;
  asm volatile("" : "+v"(X.p), "+v"(X.sw), "+v"(X.idx), "+v"(X.one));
  asm volatile("" : "+s"(X.m0), "+s"(X.m1), "+s"(X.m2));
}
struct NoMid { __device__ void operator()() const {} };
template <int H, int NY = 0, bool ALL = false, class F = NoMid>
__device__ __forceinline__ void x_take(const XTake& X, char* smem, rsrc_t wrs, int xin, int rb, int a, int lane, u32 sent = 0xFFFFFFFFu, F mid = F()) {
  constexpr int tile = H ? L_TB : L_TA, NT = H ? 3 : 2;
  const LAS char* p = (const LAS char*)(size_t)(H ? opaque(X.p + (L_TB - L_TA)) : X.p);
  const LAS char* ph = (const LAS char*)(size_t)(H ? opaque(X.p + (L_TH - L_TA) - a * 4096) : 0u);
  LAS float* spw = (LAS float*)(size_t)X.sw + H * 384;
  float s = 0.f, dv[NT];
  bf16x16 xf[NT][2];
  f32x4 ss[NT], sg[NT];
  auto ld_ = [&](int o, int j) __attribute__((always_inline)) {
    if (o < 2) xf[o][j] = __builtin_shufflevector(ld_tr8<1024>(p + o * 512 + j * 4096), ld_tr8<1024>(p + o * 512 + j * 4096 + 2048), 0, 1, 2, 3, 4, 5, 6, 7, 8, 9, 10, 11, 12, 13, 14, 15);
    else xf[o][j] = __builtin_shufflevector(ld_tr8<512>(ph + j * 2048), ld_tr8<512>(ph + j * 2048 + 1024), 0, 1, 2, 3, 4, 5, 6, 7, 8, 9, 10, 11, 12, 13, 14, 15);
    SB();
  };
  auto gr_ = [&](int o, int j) __attribute__((always_inline)) {
#define XLO(v_) __builtin_shufflevector(v_, v_, 0, 1, 2, 3, 4, 5, 6, 7)
#define XHI(v_) __builtin_shufflevector(v_, v_, 8, 9, 10, 11, 12, 13, 14, 15)
    if (j == 0) asm volatile("v_mfma_f32_16x16x32_bf16 %0, %1, %1, 0" : "=&v"(sg[o]) : "v"(XLO(xf[o][0])));
    else asm volatile("v_mfma_f32_16x16x32_bf16 %0, %1, %1, %0" : "+v"(sg[o]) : "v"(XLO(xf[o][1])));
    asm volatile("v_mfma_f32_16x16x32_bf16 %0, %1, %1, %0" : "+v"(sg[o]) : "v"(XHI(xf[o][j])));
    SB();
  };
  auto sm_ = [&](int o, int j) __attribute__((always_inline)) {
    asm volatile("v_smfmac_f32_16x16x64_bf16 %0, %1, %2, %3" : "+v"(ss[o]) : "v"(X.one), "v"(xf[o][j]), "v"(X.idx));
    SB();
  };
  auto fin_ = [&](int o, auto V_) __attribute__((always_inline)) {
    float t;
    float& d = dv[o];
    asm volatile("v_cndmask_b32_e64 %0, %3, %4, %9\n\tv_cndmask_b32_e64 %1, %5, %6, %9\n\tv_cndmask_b32_e64 %0, %0, %1, %10\n\t.if %12 >= 0\n\ts_nop %12\n\t.endif\n\t"
                 "v_add_f32 %1, %7, %8\n\tv_cndmask_b32_e64 %0, %1, %0, %11\n\tv_add_f32 %2, %2, %1"
                 : "=&v"(d), "=&v"(t), "+v"(s) : "v"(sg[o][0]), "v"(sg[o][1]), "v"(sg[o][2]), "v"(sg[o][3]), "v"(ss[o][0]), "v"(ss[o][1]),
                   "s"(X.m0), "s"(X.m1), "s"(X.m2), "n"(decltype(V_)::value - 1), "v"(ss[o]));
    SB();
  };
  auto zero_ = [&]() __attribute__((always_inline)) {
#pragma unroll
    for (int o = 0; o < NT; ++o) { ss[o] = (f32x4){0.f, 0.f, 0.f, 0.f}; asm volatile("" : "+v"(ss[o])); }
    asm volatile("" : "+v"(s));
  };
  auto first_ = [&]() __attribute__((always_inline)) { gr_(0, 0); sm_(0, 0); gr_(1, 0); sm_(1, 0); };
  auto second_ = [&]() __attribute__((always_inline)) {
    gr_(0, 1); sm_(0, 1); gr_(1, 1); fin_(0, IC<0>{}); sm_(1, 1);
    if (NT == 2) { fin_(1, IC<5>{}); spw[0] = dv[0]; spw[64] = dv[1]; }
  };
  auto third_ = [&]() __attribute__((always_inline)) {
    gr_(2, 0); fin_(1, IC<0>{}); gr_(2, 1);
    spw[64] = dv[0]; spw[128] = dv[1];
    SB();
    sm_(2, 0); sm_(2, 1); fin_(NT - 1, IC<5>{});
    spw[0] = dv[NT - 1];
  };
  zero_();
  SB();
  if (H) {
    const u32 sa = opaque((u32)(L_TB + a * 8192 + (lane >> 4) * 1024 + (lane & 15) * 32 + ((lane >> 3) & 1) * 512 + ((lane >> 4) & 3) * 8 + 4));
#pragma unroll 1
    for (unsigned spins = 0; spins <= SPIN_CAP; ++spins) {
      x_dma(wrs, xin, rb, tile, a, lane);
      asm volatile("s_waitcnt vmcnt(8)" ::: "memory");
      if (__builtin_amdgcn_ballot_w64(*(const LAS u32*)(size_t)sa == sent) == 0ull) break;
      FAILC(20);
      if (RS2) __builtin_amdgcn_s_sleep(RS2);
    }
  } else asm volatile("s_waitcnt vmcnt(%0)" :: "n"(4 + NY) : "memory");
  if (H && B6 == 0) { __builtin_amdgcn_s_barrier(); ST(xin != OFF_X1, 14); asm volatile("" ::: "memory"); }
  ld_(0, 0); ld_(1, 0);
  if (!ALL && !(H && B6 == 1)) first_();
  asm volatile("s_waitcnt vmcnt(%0)" :: "n"(4 * (NT - 2) + NY) : "memory");
  if (H && B6) { __builtin_amdgcn_s_barrier(); ST(xin != OFF_X1, 14); asm volatile("" ::: "memory"); }
  ld_(0, 1); ld_(1, 1);
  if (ALL || (H && B6 == 1)) first_();
  second_();
  if (NT == 3) {
    asm volatile("s_waitcnt vmcnt(%0)" :: "n"(NY) : "memory");
    ld_(2, 0); ld_(2, 1);
    mid();
    third_();
  }
  if (__builtin_expect(__builtin_amdgcn_ballot_w64(s != s) != 0ull, 0)) {
    for (unsigned spins = 0;; ++spins) {
      FAILC((xin == OFF_X1 ? 1 : 9) + H);
      if (H) *(u32*)(smem + L_MI + M_RD) = xin == OFF_X1 ? 1 : 2;
      x_dma(wrs, xin, rb, tile, a, lane);
      asm volatile("s_waitcnt vmcnt(0)" ::: "memory");
      s = 0.f;
#pragma unroll
      for (int o = 0; o < 2 * NT; ++o) ld_(o >> 1, o & 1);
      zero_();
      first_(); second_();
      if (NT == 3) third_();
      if (!__any(s != s)) break;
      bool st = false;
#pragma unroll 1
      for (int e = 0; e < 4 * NT; ++e) { const u32x4 v = *(const u32x4*)(smem + (e < 8 ? tile + a * 8192 : L_TH + a * 4096 - 8192) + lane * 16 + e * 1024); st |= v[1] == sent || v[3] == sent; }
      if (!__any(st) || spins > SPIN_CAP) break;
    }
  }
}
#define PIN(x_) asm volatile("" : "+v"(x_))
struct HaloMsg { u32x4 k, v; };
__device__ __forceinline__ void halo_ld(HaloMsg& h, rsrc_t wrs, int ho) { h.k = ldw(wrs, ho); h.v = ldw(wrs, ho + 1024); }
__device__ __forceinline__ bool halo_ok(const HaloMsg& h, u32 tag) {
  return __builtin_amdgcn_ballot_w64((opaque((h.k[1] ^ tag) | (h.k[3] ^ tag)) | opaque((h.v[1] ^ tag) | (h.v[3] ^ tag))) != 0u) == 0ull;
}
struct AttC { f32x4 mp, mo, mf; u32x2 one; u32 so0, so1, sp0, sp1; };
__device__ __forceinline__ void att_pre(AttC& C, int lane, bool first) {
  const float NINF = -__builtin_inff();
  const u64 pm[4] = {0x0fff00ff000f0000ull, 0x3fff03ff003f0003ull, 0xf000ff00fff0ffffull, 0xc000fc00ffc0fffcull};
#pragma unroll
  for (int e = 0; e < 4; ++e) {
    const u64 par = (e & 1) ? 0xAAAAAAAAAAAAAAAAull : 0x5555555555555555ull;
    C.mp[e] = __builtin_amdgcn_inverse_ballot_w64(pm[e >> 1] & par) ? 0.f : NINF;
    C.mo[e] = __builtin_amdgcn_inverse_ballot_w64(pm[2 + (e >> 1)] & par) ? 0.f : NINF;
    C.mf[e] = first ? NINF : C.mp[e];
  }
  C.one = (u32x2){0x3F803F80u, 0x3F803F80u};
  const u32 sl = (lane & 1) ? 0x01000c0cu : 0x0c0c0100u, su = (lane & 1) ? 0x03020c0cu : 0x0c0c0302u;
  const bool o0 = __builtin_amdgcn_inverse_ballot_w64(pm[2]), o1 = __builtin_amdgcn_inverse_ballot_w64(pm[3]);
  C.so0 = o0 ? sl : 0x0c0c0c0cu; C.sp0 = o0 ? 0x0c0c0c0cu : sl; C.so1 = o1 ? su : 0x0c0c0c0cu; C.sp1 = o1 ? 0x0c0c0c0cu : su;
  asm volatile("" : "+v"(C.mp), "+v"(C.mo), "+v"(C.mf), "+v"(C.one), "+v"(C.so0), "+v"(C.so1), "+v"(C.sp0), "+v"(C.sp1));
}
template <class F, class F2>
__device__ __forceinline__ void attention(const AttC& C, u32 qp, HaloMsg& hm, int ho, u32 tag, rsrc_t wrs, int oo, int fid, F mid, F2 mid2) {
  u32x4 vv, ow;
  u32x2 q0;
  f32x4 l0, o1;
  float inv;
#define SOFT(F1_, F2_, W_, V_, O_)                                                                                                                      \
  F1_ "v_permlane16_swap_b32 v232, v233\n\tv_max_f32 v234, v232, v233\n\tv_max_f32 v235, v232, v233\n\t"                                                \
  F2_ "v_permlane32_swap_b32 v234, v235\n\tv_max_f32 v232, v234, v235\n\tv_mul_f32 v232, 0xbeb8aa3b, v232\n\t"                                          \
  "v_fmamk_f32 v220, v220, 0x3eb8aa3b, v232\n\tv_fmamk_f32 v221, v221, 0x3eb8aa3b, v232\n\t"                                                            \
  "v_exp_f32 v220, v220\n\tv_exp_f32 v221, v221\n\tv_mov_b32 v233, 0\n\tv_cvt_pk_bf16_f32 v232, v220, v221\n\t"                                         \
  "v_perm_b32 v226, v232, v232, %[sp0]\n\tv_perm_b32 v227, v232, v232, %[sp1]\n\t"                                                                      \
  "v_mfma_f32_16x16x16_bf16 v[236:239], %[one], v[232:233], 0\n\t"                                                                                      \
  "v_perm_b32 v224, v232, v232, %[so0]\n\tv_perm_b32 v225, v232, v232, %[so1]\n\t" W_ "\n\ts_nop 0\n\t"                                                 \
  "v_mfma_f32_16x16x32_bf16 " O_ ", " V_ ", v[224:227], 0\n\t"
  asm volatile(
      "ds_read2st64_b32 v[214:215], %[qp] offset0:4 offset1:5\n\tds_read2st64_b32 v[216:217], %[qp] offset0:6 offset1:7\n\t"
      "ds_read2st64_b32 v[212:213], %[qp] offset0:0 offset1:1\n\tds_read2st64_b32 v[218:219], %[qp] offset0:10 offset1:11\n\t"
      "ds_read2st64_b32 v[200:201], %[qp] offset0:2 offset1:3\n\tds_read2st64_b32 v[202:203], %[qp] offset0:8 offset1:9\n\t"
      "s_waitcnt lgkmcnt(4)\n\tv_mfma_f32_16x16x16_bf16 v[220:223], v[216:217], v[214:215], %[mp]\n\t"
      "s_waitcnt lgkmcnt(3)\n\tv_mfma_f32_16x16x16_bf16 v[224:227], v[212:213], v[214:215], %[mo]\n\t"
      "s_waitcnt lgkmcnt(2)\n\tv_mfma_f32_16x16x16_bf16 v[228:231], v[216:217], v[218:219], %[mo]\n\t"
      "v_max_f32 v220, v220, v221\n\tv_max_f32 v221, v222, v223\n\ts_nop 1\n\t"
      "v_max3_f32 v220, v220, v224, v225\n\tv_max3_f32 v221, v221, v226, v227\n\t"
      "v_max_f32 v233, v220, v221\n\tv_max_f32 v232, v220, v221\n\t"
      SOFT("v_max_f32 v228, v228, v229\n\tv_max_f32 v229, v230, v231\n\t", "s_nop 1\n\t", "s_waitcnt lgkmcnt(0)", "v[200:203]", "v[244:247]")
      "v_rcp_f32 v248, v236"
      : "=&{v[200:203]}"(vv), "=&{v[218:219]}"(q0), "=&{v[228:231]}"(l0), "=&{v[244:247]}"(o1), "=&{v248}"(inv)
      : [qp] "v"(qp), [mp] "v"(C.mp), [mo] "v"(C.mo), [one] "v"(C.one), [so0] "v"(C.so0), [so1] "v"(C.so1), [sp0] "v"(C.sp0), [sp1] "v"(C.sp1)
      : "memory", "v212", "v213", "v214", "v215", "v216", "v217", "v220", "v221", "v222", "v223", "v224", "v225", "v226", "v227", "v232", "v233", "v234", "v235", "v236", "v237", "v238", "v239");
  mid();
  {
    u32 n = SPIN_CAP;
    asm volatile("1:\n\tv_bitop3_b32 v232, v209, v211, %[tg] bitop3:0x7e\n\tv_bitop3_b32 v233, v205, v207, %[tg] bitop3:0x7e\n\tv_or_b32 v232, v232, v233\n\tv_cmp_ne_u32 vcc, 0, v232\n\t"
                 "s_cbranch_vccz 2f\n\ts_sleep %[sl]\n\tbuffer_load_dwordx4 v[208:211], %[ho], %[rs], 0 offen sc1\n\tbuffer_load_dwordx4 v[204:207], %[ho], %[rs], 0 offen offset:1024 sc1\n\t"
                 "s_sub_u32 %[n], %[n], 1\n\ts_waitcnt vmcnt(0)\n\ts_cbranch_scc0 1b\n2:"
                 : "+{v[208:211]}"(hm.k), "+{v[204:207]}"(hm.v), [n] "+s"(n) : [tg] "s"(tag), [ho] "v"(ho), [rs] "s"(wrs), [sl] "n"(RS3) : "memory", "vcc", "scc", "v232", "v233");
  }
  mid2();
  asm volatile(
      "v_mov_b32 v209, v210\n\tv_mov_b32 v205, v206\n\ts_nop 0\n\tv_mfma_f32_16x16x16_bf16 v[220:223], v[208:209], v[218:219], %[mf]\n\t"
      "v_mul_f32 v244, v244, v248\n\tv_mul_f32 v245, v245, v248\n\tv_mul_f32 v246, v246, v248\n\tv_mul_f32 v247, v247, v248\n\t"
      "v_cvt_pk_bf16_f32 v242, v244, v245\n\tv_cvt_pk_bf16_f32 v243, v246, v247\n\ts_nop 1\n\t"
      "v_max3_f32 v220, v220, v221, v228\n\tv_max3_f32 v221, v222, v223, v229\n\t"
      "v_max_f32 v233, v220, v221\n\tv_max_f32 v232, v220, v221\n\t"
      SOFT("s_nop 1\n\t", "s_nop 1\n\t", "s_nop 0", "v[202:205]", "v[244:247]")
      "v_rcp_f32 v248, v236\n\ts_nop 6\n\t"
      "v_mul_f32 v244, v244, v248\n\tv_mul_f32 v245, v245, v248\n\tv_mul_f32 v246, v246, v248\n\tv_mul_f32 v247, v247, v248\n\t"
      "v_cvt_pk_bf16_f32 v240, v244, v245\n\tv_cvt_pk_bf16_f32 v241, v246, v247\n\ts_nop 0\n\t"
      "v_permlane16_swap_b32 v240, v242\n\tv_permlane16_swap_b32 v241, v243"
      : "=&{v[240:243]}"(ow), "+{v[208:211]}"(hm.k), "+{v[204:207]}"(hm.v), "+{v[228:231]}"(l0), "+{v[244:247]}"(o1), "+{v248}"(inv)
      : "{v[200:203]}"(vv), "{v[218:219]}"(q0), [mf] "v"(C.mf), [one] "v"(C.one), [so0] "v"(C.so0), [so1] "v"(C.so1), [sp0] "v"(C.sp0), [sp1] "v"(C.sp1)
      : "v220", "v221", "v222", "v223", "v224", "v225", "v226", "v227", "v232", "v233", "v234", "v235", "v236", "v237", "v238", "v239");
  stw(wrs, oo, ow);
  asm volatile("s_nop 1" :: "v"(ow) : "memory");
}
__device__ __forceinline__ void o_dma(rsrc_t wrs, int og, int ot, int a, int lane) {
  dma4k(wrs, lane * 16, og + a * 4096, ot + a * 4096);
  dma4k(wrs, lane * 16, og + 16384 + a * 4096, ot + 16384 + a * 4096);
}
__device__ __forceinline__ void o_get(rsrc_t wrs, int vo, int g0, int g1, int ot, u32 sa, u32 mark, int fid) {
#pragma unroll 1
  for (unsigned spins = 0; spins <= SPIN_CAP; ++spins) {
    dma4k(wrs, vo, g0, ot);
    dma4k(wrs, vo, g1, ot + 16384);
    asm volatile("s_waitcnt vmcnt(4)" ::: "memory");
    if (__builtin_amdgcn_ballot_w64(*(const LAS u32*)(size_t)sa == mark) == 0ull) break;
    FAILC(fid);
    if (RS1) __builtin_amdgcn_s_sleep(RS1);
  }
}
template <bool L1, class F>
__device__ __forceinline__ void wo_mma2(f32x4 (&acc)[2], char* smem, int ot, rsrc_t wrs, int og, int a, int lane, const bf16x8 (&wof)[16], u32 cnt, const f32x4 (&r0)[2], const u32 (&qa)[4], u32 sent, F late) {
  u32x4 of[32];
#define LDO2(s_) of[s_] = *(const LAS u32x4*)((const LAS char*)(size_t)qa[(s_) >> 3] + (ot - L_TA) + ((s_) & 1) * 16384 + (((s_) >> 1) & 3) * 1024)
#define WMM(s_) do { SB(); if ((s_) < 2) asm volatile("v_mfma_f32_16x16x32_bf16 %0, %1, %2, %3" : "=&v"(acc[(s_) & 1]) \
        : "v"(L1 ? __builtin_bit_cast(bf16x8, of[s_]) : wof[0]), "v"(L1 ? wof[0] : __builtin_bit_cast(bf16x8, of[s_])), "v"(r0[(s_) & 1])); \
      else if (L1) asm volatile("v_mfma_f32_16x16x32_bf16 %0, %1, %2, %0" : "+v"(acc[(s_) & 1]) : "v"(of[s_]), "v"(wof[(s_) >> 1])); \
      else asm volatile("v_mfma_f32_16x16x32_bf16 %0, %1, %2, %0" : "+v"(acc[(s_) & 1]) : "v"(wof[(s_) >> 1]), "v"(of[s_])); SB(); } while (0)
  u32 mx = 0, cv;
#pragma unroll
  for (int s = 0; s < 8; ++s) { LDO2(s); SB(); }
#pragma unroll
  for (int s = 0; s < 3; ++s) {
    WMM(s);
#pragma unroll
    for (int e = 3 * s; e < 3 * s + 3 && e < 8; ++e) mx = max(max(mx, of[e][1]), of[e][3]);
    asm volatile("" : "+v"(mx));
  }
  if (__builtin_expect(!__all(mx < 0xFF810000u), 0)) {
    for (unsigned spins = 0; spins <= SPIN_CAP; ++spins) {
      FAILC((L1 ? 5 : 13) + (ot == L_TB));
      __builtin_amdgcn_s_sleep(2);
      o_dma(wrs, og, ot, a, lane);
      asm volatile("s_waitcnt vmcnt(0)" ::: "memory");
      bool st = false;
#pragma unroll
      for (int s = 0; s < 8; ++s) { LDO2(s); st |= of[s][1] == sent || of[s][3] == sent; }
      if (!__any(st)) break;
    }
    WMM(0); WMM(1); WMM(2);
    asm volatile("s_nop 7" : "+v"(acc[0]), "+v"(acc[1]));
  }
  cnt_add(smem, C_OT, lane);
  cv = *(const volatile LAS u32*)(size_t)(u32)(L_MI + C_OT);
  SB();
#pragma unroll
  for (int s = 3; s < WS5; ++s) WMM(s);
  {
    u32 n = 1u << 24;
    asm volatile("1:\n\tv_readfirstlane_b32 vcc_lo, %0\n\ts_sub_u32 %1, %1, 1\n\ts_cbranch_scc1 2f\n\ts_cmp_ge_u32 vcc_lo, %3\n\ts_cbranch_scc1 2f\n\tds_read_b32 %0, %2\n\ts_waitcnt lgkmcnt(0)\n\ts_branch 1b\n2:"
                 : "+v"(cv), "+s"(n) : "v"(L_MI + C_OT), "s"(cnt) : "memory", "vcc", "scc");
  }
#pragma unroll
  for (int s = 8; s < WS5 + 8; ++s) { LDO2(s); SB(); }
#pragma unroll
  for (int s = WS5; s < 32; ++s) {
    WMM(s);
    if (s + 8 < 32) LDO2(s + 8);
    if (s == 24) late();
  }
  asm volatile("s_nop 7" : "+v"(acc[0]), "+v"(acc[1]));
}
template <class F>
__device__ __forceinline__ void wo_last(f32x4 (&acc)[2], char* smem, rsrc_t wrs, int og, int a, int lane, const bf16x8 (&wof)[16], const f32x4 (&r0)[2], const u32 (&qa)[4], u32 sent, F put) {
  u32x4 of[32];
#define LDO3(q_, o_, s_) of[s_] = *(const LAS u32x4*)((const LAS char*)(size_t)q_[((s_) >> 2) & 3] + (o_) + ((s_) >> 4) * 16384 + ((s_) & 3) * 1024)
#define WM3(s_) do { SB(); if (((s_) & 15) == 0) asm volatile("v_mfma_f32_16x16x32_bf16 %0, %1, %2, %3" : "=&v"(acc[(s_) >> 4]) : "v"(wof[0]), "v"(of[s_]), "v"(r0[(s_) >> 4])); \
      else asm volatile("v_mfma_f32_16x16x32_bf16 %0, %1, %2, %0" : "+v"(acc[(s_) >> 4]) : "v"(wof[(s_) & 15]), "v"(of[s_])); SB(); } while (0)
  u32 qb[4];
#pragma unroll
  for (int e = 0; e < 4; ++e) qb[e] = qa[e];
  SB();
  __builtin_amdgcn_s_barrier();
  ST(1, 19);
#pragma unroll 1
  for (u32 pass = 0;; ++pass) {
    asm volatile("" : "+v"(qb[0]), "+v"(qb[1]), "+v"(qb[2]), "+v"(qb[3]) :: "memory");
#pragma unroll
    for (int s = 0; s < WLD; ++s) { LDO3(qb, L_TB - L_TA, s); SB(); }
#pragma unroll
    for (int s = 0; s < 32; ++s) {
      WM3(s);
      if (s + WLD == 16) asm volatile("s_cmp_lg_u32 %0, 0\n\ts_cbranch_scc1 1f\n\ts_barrier\n1:" :: "s"(__builtin_amdgcn_readfirstlane(pass)) : "memory", "scc");
      if (s + WLD < 32) LDO3(qb, L_TB - L_TA, s + WLD);
    }
    asm volatile("s_nop 7" : "+v"(acc[0]), "+v"(acc[1]));
    put();
    SB();
    if (__builtin_expect(__builtin_amdgcn_ballot_w64(__builtin_isunordered(acc[0][0], acc[1][0])) == 0ull, 1)) break;
    asm volatile("s_waitcnt vmcnt(0)" ::: "memory");
    FAILC(14);
    if (pass == 0) {
      lds_barrier();
      const u32 fx = *(const u32*)(smem + L_MI + M_FX);
#pragma unroll
      for (int e = 0; e < 4; ++e) qb[e] -= ((fx >> (8 * ((a + e) & 3))) & 1u) * (L_TB - L_TA);
    } else {
      u32 m = 1;
#pragma unroll 1
      for (int s = 0; s < 32; ++s) {
        const u32x4 v = *(const LAS u32x4*)(size_t)((s & 8 ? (s & 4 ? qb[3] : qb[2]) : (s & 4 ? qb[1] : qb[0])) + (L_TB - L_TA) + (s >> 4) * 16384 + (s & 3) * 1024);
        m = min(min(m, v[1] ^ sent), v[3] ^ sent);
      }
      if (__builtin_amdgcn_ballot_w64(m == 0u) == 0ull || pass > SPIN_CAP) break;
      lds_barrier();
      o_dma(wrs, og, __builtin_amdgcn_readfirstlane(qb[0]) + (L_TB - L_TA) - a * 4096, a, lane);
      asm volatile("s_waitcnt vmcnt(0)" ::: "memory");
      lds_barrier();
    }
  }
}
struct WoPre { f32x4 r[2]; int so; u32 xs, ao; };
template <bool L1>
__device__ __forceinline__ void wo_pre(WoPre& P, const u32x2 (&res)[2], char* ws, int H, int rb, int hb, int a, int lane) {
  const int i16 = lane & 15, kg = lane >> 4, R0 = 1024 * H + 32 * rb;
#pragma unroll
  for (int t = 0; t < 2; ++t) P.r[t] = (f32x4){lo2f(res[t][0]), hi2f(res[t][0]), lo2f(res[t][1]), hi2f(res[t][1])};
  if (L1) {
    P.so = OFF_X2 + ((32 * hb + 8 * a + (i16 >> 1)) * XS_ + R0 + 16 * (lane & 1) + 4 * kg) * 4;
    P.xs = (u32)(L_XS + H * 512 + lane * 8);
    P.ao = (u32)(OFF_ST2 + ((R0 & ~255) + ((lane >> 1) & 3) * 64 + ((R0 >> 2) & 63) + 4 * (lane & 1) + kg) * 16 + (lane & 8));
    asm volatile("" : "+v"(P.r[0]), "+v"(P.r[1]), "+v"(P.so), "+v"(P.xs), "+v"(P.ao));
  } else {
    P.so = ((R0 + 16 * (kg & 1) + i16) * 256 + 32 * hb + 8 * a + 4 * (kg >> 1)) * 4;
    asm volatile("" : "+v"(P.r[0]), "+v"(P.r[1]), "+v"(P.so));
  }
}
template <bool L1>
__device__ __forceinline__ void wo_epi(const f32x4 (&acc)[2], const WoPre& P, char* smem, rsrc_t wrs, char* ws, u32* out, int H, int lane, u32 ONES) {
  if (L1) {
    f32x4 lo_, hi_;
    pair_x(lo_, hi_, acc[0], acc[1]);
    u32x4 v;
#pragma unroll
    for (int e = 0; e < 4; ++e) v[e] = pk2(lo_[e], hi_[e]);
    asm volatile("" : "+v"(v));
    stw(wrs, P.so, v);
    SB();
    float sv, q0, q1, q2, q3;
#define RD_(d_, a_, c_, m_) "v_add_f32_dpp " d_ ", " a_ ", " a_ " " c_ " row_mask:0xf bank_mask:" m_ "\n\t"
    asm volatile("v_dot2_f32_bf16 %5, %13, %17, 0\n\tv_dot2_f32_bf16 %6, %14, %17, 0\n\tv_dot2_f32_bf16 %7, %15, %17, 0\n\tv_dot2_f32_bf16 %8, %16, %17, 0\n\t"
                 "v_dot2_f32_bf16 %9, %13, %13, 0\n\tv_dot2_f32_bf16 %10, %14, %14, 0\n\tv_dot2_f32_bf16 %11, %15, %15, 0\n\tv_dot2_f32_bf16 %12, %16, %16, 0\n\t"
                 RD_("%1", "%5", "row_ror:8", "0x3") RD_("%2", "%6", "row_ror:8", "0x3") RD_("%3", "%7", "row_ror:8", "0x3") RD_("%4", "%8", "row_ror:8", "0x3")
                 RD_("%1", "%9", "row_ror:8", "0xc") RD_("%2", "%10", "row_ror:8", "0xc") RD_("%3", "%11", "row_ror:8", "0xc") RD_("%4", "%12", "row_ror:8", "0xc")
                 RD_("%5", "%1", "row_shl:4", "0x5") RD_("%6", "%2", "row_shl:4", "0x5") RD_("%5", "%3", "row_shr:4", "0xa") RD_("%6", "%4", "row_shr:4", "0xa")
                 "s_nop 0\n\t"
                 RD_("%1", "%5", "quad_perm:[2,3,0,1]", "0xf") RD_("%2", "%6", "quad_perm:[2,3,0,1]", "0xf")
                 "v_cndmask_b32_e64 %0, %1, %2, %18"
                 : "=&v"(sv), "=&v"(q0), "=&v"(q1), "=&v"(q2), "=&v"(q3), "=&v"(lo_[0]), "=&v"(lo_[1]), "=&v"(lo_[2]), "=&v"(lo_[3]), "=&v"(hi_[0]), "=&v"(hi_[1]), "=&v"(hi_[2]), "=&v"(hi_[3])
                 : "v"(v[0]), "v"(v[1]), "v"(v[2]), "v"(v[3]), "v"(ONES), "s"(0xCCCCCCCCCCCCCCCCull));
    const float hf = __builtin_floorf(sv * 0.5f);
    const u32 lo = (u32)(fmaf(sv, 0.5f, -hf) * 4294967296.0f);
    const u64 mine = ((u64)(u32)(int)hf << 32) | (u64)((lo & ~63u) | 1u);
    const u64 old = __hip_atomic_fetch_add((LAS u64*)(size_t)P.xs, mine, __ATOMIC_RELAXED, __HIP_MEMORY_SCOPE_WORKGROUP);
    if (((u32)old & 63u) == 3u) __hip_atomic_fetch_add((GAS u64*)((GAS char*)ws + P.ao), old + mine, __ATOMIC_RELAXED, __HIP_MEMORY_SCOPE_AGENT);
  } else {
    auto r0s = __builtin_amdgcn_permlane16_swap(pk2(acc[0][0], acc[0][1]), pk2(acc[1][0], acc[1][1]), false, false);
    auto r1s = __builtin_amdgcn_permlane16_swap(pk2(acc[0][2], acc[0][3]), pk2(acc[1][2], acc[1][3]), false, false);
    const rsrc_t ors = __builtin_amdgcn_make_buffer_rsrc((void*)out, 0, 2 * MB_, 0x00020000);
    if (H == 0) __builtin_amdgcn_raw_buffer_store_b128((u32x4){r0s[0], r1s[0], r0s[1], r1s[1]}, ors, P.so, 0, OUTA);
    else __builtin_amdgcn_raw_buffer_store_b128((u32x4){r0s[0], r1s[0], r0s[1], r1s[1]}, ors, P.so, 0, OUTB);
  }
}
template <bool L1>
__device__ __forceinline__ void res_ld(u32x2 (&res)[2], const char* smem, int tile, int hb, int a, int lane) {
#pragma unroll
  for (int t = 0; t < 2; ++t) {
    const int o = tile + t * 512 + (4 * hb + a) * 1024;
    if (L1) res[t] = *(const u32x2*)(smem + o + (lane & 15) * 32 + (lane >> 4) * 8);
    else res[t] = __builtin_bit_cast(u32x2, __builtin_amdgcn_ds_read_tr16_b64_v4i16((LAS bf16x4*)(size_t)(u32)(o + lane * 8)));
  }
}
template <bool L1>
__device__ __forceinline__ void a_blayer(char* smem, rsrc_t wrs, char* ws, u32* out, int t, int a, bf16x8 (&wof)[16], u32 tag, u32x4 wfl, ConvK& ck, XTake& xt, AttC& ac) {
  const u32 t9 = L1 ? 0u : KADD(2, now_(), D9);
  t = (int)opaque((u32)t);
  const int lane = t & 63, i16 = lane & 15, kg = lane >> 4;
  const int XIN = L1 ? OFF_X1 : OFF_X3, OBUF = L1 ? OFF_O1 : OFF_O2;
  const int bid = BIDX;
  const int rb = RB_OF(bid), hb = HB_OF(bid);
  if (!L1) { x_pre(xt, a, lane); SB(); x_take<0, 0, true>(xt, smem, wrs, XIN, rb, a, lane); SB(); }
  const int og = OBUF + rb * 32768;
  u32x2 resa[2], resb[2];
  const int wfo = OFF_ST0 + 8192 + (hb * 4 + a) * 128 + (lane & 3) * 16;
#define LDWO(e_) wof[e_] = __builtin_bit_cast(bf16x8, ldws(wrs, ((hb * 64 + a) * 64 + lane) * 16, OFF_WO + ((4 * a + (e_)) & 15) * 4096))
  auto ld_wo = [&](int e0, int e1) __attribute__((always_inline)) {
#pragma unroll
    for (int e = e0; e < e1; ++e) LDWO(e);
    __builtin_amdgcn_sched_barrier(0);
  };
  const u32 mark = x0_mark(tag), sent = L1 ? mark : 0xFFFFFFFFu;
  if (L1) {
    tags_wait(wfl, wrs, wfo, tag, 6); ld_wo(0, WOE);
    SB();
    x_pre(xt, a, lane); SB();
    x_take<0, WOE, true>(xt, smem, wrs, XIN, rb, a, lane, sent);
  }
  ST(!L1, 10);
  SB();
  lds_barrier();
  ST(!L1, 11);
  const u32 t5 = L1 ? (u32)__builtin_amdgcn_s_memrealtime() : KADD(20, now_(), D5), c5 = L1 ? opaque(KADD(18, now_(), DO5)) : 0u;
  res_ld<L1>(resa, smem, L_TA, hb, a, lane);
  auto pf_x3 = [&]() __attribute__((always_inline)) {
    const u32x4 SENT = {0xFFFFFFFFu, 0xFFFFFFFFu, 0xFFFFFFFFu, 0xFFFFFFFFu};
    const int xo = OFF_X3 + (8 * a + (lane >> 3)) * XBS + ((bid & 7) * 32 + (bid >> 3)) * 128 + (lane & 7) * 16;
    stw(wrs, xo, SENT); stw(wrs, xo + 32 * XBS, SENT);
  };
  const int oo = (int)opaque((u32)(OBUF + (((rb * 2 + (kg & 1)) * 16 + hb * 2 + (a >> 1)) * 64 + (2 * (a & 1) + (kg >> 1)) * 16 + i16) * 16));
  const u32 ONES = opaque(0x3F803F80u);
  att_pre(ac, lane, rb == 0);
  if (L1) pf_x3();
  if (L1) SB();
  const int ho = (int)opaque((u32)((rb == 0 ? WSB : KVO(L1, rb, hb * 4 + a)) + lane * 16));
  const u32 qp = opaque((u32)(L_QB + a * 3072 + lane * 4));
  const int tso = (int)opaque((u32)(L1 ? OFF_T5 + rb * 1024 + (lane & 7) * 128 : OFF_TS + rb * 128 + (lane & 7) * 8)), tsw = (int)opaque((u32)(t == hb ? tso : WSB));
  if (L1) __builtin_amdgcn_raw_buffer_store_b64((u32x2){t5, tag}, wrs, tsw, 0, 16);
  HaloMsg hm;
  SB();
  if (L1) KSLP(1, SLP_XB1); else gate(tmax(t9, t5));
  ST(!L1, 12);
  x_take<1, 0, false>(xt, smem, wrs, XIN, 32 + rb, a, lane, sent, [&]() __attribute__((always_inline)) { if (L1) ld_wo(WOE, WOE + WX); });
  ST(!L1, 13);
  if (L1) SB();
  lds_barrier();
  ST(!L1, 15);
  if (L1) KSLP(3, SLP_H1); else KSLP(4, SLP_H2);
  halo_ld(hm, wrs, ho);
  if (L1) { SB(); ld_wo(WOE + WX, WOE + WX + WA); }
  u32x2 ts;
  u32 t7 = L1 ? t5 : 0u, tq = 0;
  if (!L1) {
    t7 = (u32)__builtin_amdgcn_s_memrealtime();
    tq = KADD(6, now_(), DQ2);
    __builtin_amdgcn_raw_buffer_store_b64((u32x2){t7, tag}, wrs, tsw, 0, 16);
  }
  SB();
  attention(ac, qp, hm, ho, rb == 0 ? 0u : L1 ? mark : tag, wrs, oo, L1 ? 3 : 11, [&]() __attribute__((always_inline)) { if (L1) SB(); }, [&]() __attribute__((always_inline)) {
    if (L1) {
      asm volatile("buffer_load_dwordx2 %0, %1, %2, 0 offen sc1" : "=&{v[250:251]}"(ts) : "v"(tso), "s"(wrs) : "memory");
      ld_wo(WOE + WX + WA, WOE + WX + WA + WM);
    }
  });
  const u32 to = L1 ? KADD(5, now_(), DO1) : KADD(21, now_(), DO2);
  ST(!L1, 16);
  if (L1) __builtin_amdgcn_raw_buffer_store_b32(tag, wrs, (bid | t) == 0 ? OFF_EP : WSB, 0, 16);
  if (L1) ld_wo(WOE + WX + WA + WM, 16);
  res_ld<L1>(resb, smem, L_TB, hb, a, lane);
  cnt_add(smem, C_OB, lane);
  WoPre wp, wq;
  u32 qa[4];
#pragma unroll
  for (int e = 0; e < 4; ++e) qa[e] = opaque((u32)(L_TA + ((a + e) & 3) * 4096 + lane * 16));
  wo_pre<L1>(wp, resa, ws, 0, rb, hb, a, lane);
  wo_pre<L1>(wq, resb, ws, 1, rb, hb, a, lane);
  const u32 sa = opaque((u32)(L_TA + a * 4096 + lane * 64 + (lane & 14) * 4 + 4));
  SB();
  int d;
  auto lead = [&]() __attribute__((always_inline)) {
    asm volatile("v_cmp_eq_u32 vcc, %3, v251\n\tv_subrev_u32 %0, %2, v250\n\tv_min_i32 %0, 0x96, %0\n\tv_cndmask_b32 %0, 64, %0, vcc\n\t"
                 "s_nop 1\n\tv_max_i32_dpp %0, %0, %0 quad_perm:[1,0,3,2] row_mask:0xf bank_mask:0xf\n\ts_nop 1\n\tv_max_i32_dpp %0, %0, %0 quad_perm:[2,3,0,1] row_mask:0xf bank_mask:0xf\n\t"
                 "s_nop 1\n\tv_max_i32_dpp %0, %0, %0 row_half_mirror row_mask:0xf bank_mask:0xf\n\tv_max_i32 %0, 0, %0\n\tv_mul_u32_u24 %0, 20, %0"
                 : "=&v"(d), "+{v[250:251]}"(ts) : "s"(t7), "s"(tag) : "vcc", "memory");
  };
  if (L1) {
    asm volatile("s_waitcnt vmcnt(%0)" :: "n"(18 - WOE - WX - WA) : "memory");
    lead();
    gate(tmax(to, __builtin_amdgcn_readfirstlane(c5 + d)));
  } else gate(tmax(tq, to));
  ST(!L1, 17);
  if (!L1) asm volatile("buffer_load_dwordx2 %0, %1, %2, 0 offen sc1" : "=&{v[250:251]}"(ts) : "v"(tso), "s"(wrs) : "memory");
  o_get(wrs, lane * 16, og + a * 4096, og + 16384 + a * 4096, L_TA + a * 4096, sa, mark, L1 ? 21 : 23);
  if (!L1) {
    lead();
    *(int*)(smem + L_MI + M_DL) = d;
    cnt_add(smem, C_OV, lane);
  }
  u32 ob = 0;
  int ob0 = og + MB_ + a * 4096, ob1 = ob0 + 16384;
  if (L1) asm volatile("" : "+s"(ob0), "+s"(ob1));
  asm volatile("s_waitcnt vmcnt(0)" ::: "memory");
  f32x4 acc[2];
  wo_mma2<L1>(acc, smem, L_TA, wrs, og, a, lane, wof, L1 ? 4 : 12, wp.r, qa, mark, [&]() __attribute__((always_inline)) { if (L1) ob = *(const volatile LAS u32*)(size_t)(u32)(L_MI + C_OB); });
  ST(!L1, 18);
  wo_epi<L1>(acc, wp, smem, wrs, ws, out, 0, lane, ONES);
  if (L1) {
    cnt_add(smem, C_X2, lane);
    if (__builtin_expect((u32)__builtin_amdgcn_readfirstlane(ob) < 8u, 0)) cnt_spin(smem, C_OB, 8);
    dma4k(wrs, lane * 16, ob0, L_TB + a * 4096);
    dma4k(wrs, lane * 16, ob1, L_TB + 16384 + a * 4096);
    a_conv_pre(ck, a, lane);
    ST(0, 19);
    asm volatile("s_waitcnt vmcnt(0)" ::: "memory");
    wo_mma2<L1>(acc, smem, L_TB, wrs, og + MB_, a, lane, wof, 8, wq.r, qa, mark, []() {});
    wo_epi<L1>(acc, wq, smem, wrs, ws, out, 1, lane, ONES);
  } else wo_last(acc, smem, wrs, og + MB_, a, lane, wof, wq.r, qa, mark, [&]() __attribute__((always_inline)) { wo_epi<L1>(acc, wq, smem, wrs, ws, out, 1, lane, ONES); });
  ST(!L1, 20); if (L1) ST(1, 1);
}

#define WREGS(X) \
  X(0, 76, 77, 78, 79) X(1, 80, 81, 82, 83) X(2, 84, 85, 86, 87) X(3, 88, 89, 90, 91) X(4, 92, 93, 94, 95) X(5, 96, 97, 98, 99) \
  X(6, 100, 101, 102, 103) X(7, 104, 105, 106, 107) X(8, 108, 109, 110, 111) X(9, 112, 113, 114, 115) X(10, 116, 117, 118, 119) X(11, 120, 121, 122, 123) \
  X(12, 124, 125, 126, 127) X(13, 128, 129, 130, 131) X(14, 132, 133, 134, 135) X(15, 136, 137, 138, 139) X(16, 140, 141, 142, 143) X(17, 144, 145, 146, 147) \
  X(18, 148, 149, 150, 151) X(19, 152, 153, 154, 155) X(20, 156, 157, 158, 159) X(21, 160, 161, 162, 163) X(22, 164, 165, 166, 167) X(23, 168, 169, 170, 171) \
  X(24, 172, 173, 174, 175) X(25, 176, 177, 178, 179) X(26, 180, 181, 182, 183) X(27, 184, 185, 186, 187) X(28, 188, 189, 190, 191) X(29, 192, 193, 194, 195) \
  X(30, 196, 197, 198, 199) X(31, 200, 201, 202, 203) X(32, 204, 205, 206, 207) X(33, 208, 209, 210, 211) X(34, 212, 213, 214, 215) X(35, 216, 217, 218, 219) \
  X(36, 220, 221, 222, 223) X(37, 224, 225, 226, 227) X(38, 228, 229, 230, 231) X(39, 232, 233, 234, 235) X(40, 236, 237, 238, 239) X(41, 240, 241, 242, 243) \
  X(42, 244, 245, 246, 247) X(43, 248, 249, 250, 251) X(44, 252, 253, 254, 255) X(45, 64, 65, 66, 67) X(46, 68, 69, 70, 71) X(47, 72, 73, 74, 75)
__device__ __forceinline__ void w_dma(rsrc_t r, int voff, int vof2, int soff, int lds) {
  asm volatile("s_mov_b32 m0, %3\n\ts_nop 0\n\tbuffer_load_dwordx4 %0, %1, %2 offen lds\n\t"
               "s_add_u32 m0, m0, 3072\n\ts_nop 0\n\tbuffer_load_dwordx4 %0, %1, %2 offen offset:1024 lds\n\t"
               "s_add_u32 m0, m0, 3072\n\ts_nop 0\n\tbuffer_load_dwordx4 %4, %1, %2 offen offset:2048 lds"
               :: "v"(voff), "s"(r), "s"(soff), "s"(lds), "v"(vof2) : "memory", "scc");
}
template <int I>
__device__ __forceinline__ void w_ld(bf16x8& r, int vo, rsrc_t rs) {
#define X(n_, a_, b_, c_, d_) if constexpr (I == n_) asm volatile("buffer_load_dwordx4 %0, %1, %2, %3 offen offset:%4 sc1" : "={v[" #a_ ":" #d_ "]}"(r) : "v"(vo), "s"(rs), "s"(OFF_WQ + (I / 3) * 98304), "n"((I % 3) * 1024) : "memory");
  WREGS(X)
#undef X
}
template <int I, int O>
__device__ __forceinline__ void w_get(bf16x8& r, u32 a) {
#define X(n_, a_, b_, c_, d_) if constexpr (I == n_) asm volatile("ds_read_b64_tr_b16 v[" #a_ ":" #b_ "], %1 offset:%2\n\tds_read_b64_tr_b16 v[" #c_ ":" #d_ "], %1 offset:%3" : "=&{v[" #a_ ":" #d_ "]}"(r) : "v"(a), "n"(O), "n"(O + 2048) : "memory");
  WREGS(X)
#undef X
}
template <int I>
__device__ __forceinline__ void w_pin(const bf16x8& r) {
#define X(n_, a_, b_, c_, d_) if constexpr (I == n_) asm volatile("" :: "{v[" #a_ ":" #d_ "]}"(r));
  WREGS(X)
#undef X
}
template <int I>
__device__ __forceinline__ void w_sum(f32x4& d, const bf16x8& one, u32 idx, const bf16x8 (&wf)[48]) {
  w_pin<I>(wf[I]); w_pin<(I + 1) % 48>(wf[(I + 1) % 48]);
  asm volatile("v_smfmac_f32_16x16x64_bf16 %0, %1, v[%3:%3+7], %2" : "+v"(d) : "v"(one), "v"(idx), "n"(I < 45 ? 76 + 4 * I : 64 + 4 * (I - 45)));
  w_pin<I>(wf[I]); w_pin<(I + 1) % 48>(wf[(I + 1) % 48]);
}
struct GPre { u32 ap, l4, fr, fq, sp, wx, mo, oo, tg, ly; };
template <bool L1>
__device__ __forceinline__ void g_pre(GPre& P, int g, u32 tag) {
  const int lane = lane_id(), bid = BIDX;
  const int rb = RB_OF(bid), hb = HB_OF(bid);
  P.mo = (u32)KVO(L1, (rb + 1) & 31, hb * 4 + g);
  P.oo = (u32)((L1 ? OFF_O1 : OFF_O2) + MB_ + ((rb * 32 + hb * 2 + (g >> 1)) * 64 + 32 * (g & 1)) * 16);
  P.tg = L1 ? x0_mark(tag) : tag;
  P.ly = L1 ? 1 : 2;
  asm volatile("" : "+{s60}"(P.mo), "+{s61}"(P.oo), "+{s62}"(P.tg), "+{s63}"(P.ly));
  P.ap = (u32)(L_TA + lane * 8);
  P.l4 = (u32)(lane * 4);
  P.fr = (u32)(L_FS + g * 768 + (lane & 15) * 4);
  P.fq = (u32)(L_FS + g * 768 + (lane >> 4) * 16);
  P.sp = (u32)(L_SP + (lane & 31) * 16);
  P.wx = (u32)(L_WX + g * 3072 + lane * 16);
  asm volatile("" : "+{v0}"(P.ap), "+{v1}"(P.l4), "+{v2}"(P.fr), "+{v3}"(P.fq), "+{v4}"(P.sp), "+{v5}"(P.wx));
}
template <bool L1, class F>
__device__ __forceinline__ void g_blayer(char* smem, rsrc_t wrs, int g, u32 tag, bf16x8 (&wf)[48], GPre& P, F mid) {
  if (!L1) ST(1, 10);
  auto pin = [&](auto I_) __attribute__((always_inline)) { w_pin<decltype(I_)::value>(wf[decltype(I_)::value]); };
  static_for<0, 45>(pin);
#define G_OPS : "+{v0}"(P.ap), "+{v1}"(P.l4), "+{v2}"(P.fr), "+{v3}"(P.fq), "+{v4}"(P.sp), "+{v5}"(P.wx) \
  : "{s[56:59]}"(wrs), "{s60}"(P.mo), "{s61}"(P.oo), "{s62}"(P.tg), "{s63}"(P.ly), "{s64}"(L_QB + g * 3072), "{s65}"(L_FS + g * 768), "{s66}"(L_WX + g * 3072) \
  : "memory", "scc", "vcc", "v6", "v7", "v8", "v9", "v10", "v11", "v12", "v13", "v14", "v15", "v16", "v17", "v18", "v19", "v20", "v21", "v22", "v23", "v24", "v25", "v26", "v27", "v28", "v29", "v30", "v31", "v32", "v33", "v34", "v35", "v36", "v37", "v38", "v39", "v40", "v41", "v42", "v43", "v44", "v45", "v46", "v47", "v48", "v49", "v50", "v51", "v52", "v53", "v54", "v55", "v56", "v57", "v58", "v59", "v60", "v61", "v62", "v63", "v64", "v65", "v66", "v67", "v68", "v69", "v70", "v71", "v72", "v73", "v74", "v75", "s54", "s55", "s67", "s68", "s69", "s70", "s71", "s72", "s73", "s74", "s75", "s76", "s77"
  if constexpr (L1) asm volatile(".Lg_body:\n\t"
  "ds_read_b128 v[24:27], v5 offset:1024\n\tds_read_b128 v[72:75], v5 offset:2048\n\tds_read_b128 v[20:23], v5 offset:0\n\t"
  "s_waitcnt lgkmcnt(3)\n\ts_barrier\n\t" GST(11)
  "ds_read_b64_tr_b16 v[60:61], v0 offset:31232\n\tds_read_b64_tr_b16 v[62:63], v0 offset:32256\n\tds_read_b64_tr_b16 v[16:17], v0 offset:512\n\tds_read_b64_tr_b16 v[18:19], v0 offset:1536\n\t"
  "ds_read_b128 v[28:31], v4\n\tds_read_b128 v[44:47], v4 offset:768\n\tds_read_b64_tr_b16 v[36:37], v0 offset:2560\n\tds_read_b64_tr_b16 v[38:39], v0 offset:3584\n\t"
  "ds_read_b64_tr_b16 v[40:41], v0 offset:4608\n\tds_read_b64_tr_b16 v[42:43], v0 offset:5632\n\ts_mov_b32 s68, 0xaaaaaaaa\n\ts_mov_b32 s69, 0xaaaaaaaa\n\t"
  "s_mov_b32 s74, 0xfff0ffff\n\ts_mov_b32 s75, 0xf000ff00\n\ts_mov_b32 s76, 0xffc0fffc\n\ts_mov_b32 s77, 0xc000fc00\n\t"
  "s_mov_b32 s67, 0x0c0c0100\n\ts_mov_b32 s55, 0x0c0c0302\n\ts_waitcnt lgkmcnt(8)\n\t"
  "v_mfma_f32_16x16x32_bf16 v[12:15], v[24:27], v[60:63], 0\n\tds_read_b64_tr_b16 v[68:69], v0 offset:6656\n\t"
  "v_mfma_f32_16x16x32_bf16 v[64:67], v[60:63], v[72:75], 0\n\tds_read_b64_tr_b16 v[70:71], v0 offset:7680\n\t"
  "v_mfma_f32_16x16x32_bf16 v[72:75], v[20:23], v[60:63], 0\n\ts_waitcnt lgkmcnt(2)\n\t"
  "v_mfma_f32_16x16x32_bf16 v[12:15], v[80:83], v[16:19], v[12:15]\n\tds_read_b64_tr_b16 v[4:5], v0 offset:8704\n\tv_add_f32 v28, v28, v29\n\t"
  "v_mfma_f32_16x16x32_bf16 v[64:67], v[16:19], v[84:87], v[64:67]\n\tds_read_b64_tr_b16 v[6:7], v0 offset:9728\n\tv_add_f32 v30, v30, v31\n\t"
  "v_mfma_f32_16x16x32_bf16 v[72:75], v[76:79], v[16:19], v[72:75]\n\tv_add_f32 v44, v44, v45\n\tv_add_f32 v28, v28, v30\n\t"
  "v_mfma_f32_16x16x32_bf16 v[12:15], v[92:95], v[36:39], v[12:15]\n\tds_read_b64_tr_b16 v[32:33], v0 offset:10752\n\tv_add_f32 v46, v46, v47\n\t"
  "v_mfma_f32_16x16x32_bf16 v[64:67], v[36:39], v[96:99], v[64:67]\n\tds_read_b64_tr_b16 v[34:35], v0 offset:11776\n\tv_mul_f32 v29, 0x3b000000, v28\n\t"
  "v_mfma_f32_16x16x32_bf16 v[72:75], v[88:91], v[36:39], v[72:75]\n\tv_add_f32 v44, v44, v46\n\tv_mul_f32_e64 v30, -v29, v29\n\t"
  "v_mfma_f32_16x16x32_bf16 v[12:15], v[104:107], v[40:43], v[12:15]\n\tds_read_b64_tr_b16 v[36:37], v0 offset:12800\n\tv_fmac_f32 v30, 0x3b000000, v44\n\t"
  "v_mfma_f32_16x16x32_bf16 v[64:67], v[40:43], v[108:111], v[64:67]\n\tds_read_b64_tr_b16 v[38:39], v0 offset:13824\n\tv_max_f32 v30, 0, v30\n\t"
  "v_mfma_f32_16x16x32_bf16 v[72:75], v[100:103], v[40:43], v[72:75]\n\tv_add_f32 v30, 0x3727c5ac, v30\n\ts_waitcnt lgkmcnt(4)\n\t"
  "v_mfma_f32_16x16x32_bf16 v[12:15], v[116:119], v[68:71], v[12:15]\n\tds_read_b64_tr_b16 v[60:61], v0 offset:14848\n\tv_add_u32 v31, s65, v1\n\t"
  "v_mfma_f32_16x16x32_bf16 v[64:67], v[68:71], v[120:123], v[64:67]\n\tds_read_b64_tr_b16 v[62:63], v0 offset:15872\n\t"
  "v_mfma_f32_16x16x32_bf16 v[72:75], v[112:115], v[68:71], v[72:75]\n\tv_rsq_f32 v30, v30\n\t"
  "v_mfma_f32_16x16x32_bf16 v[12:15], v[128:131], v[4:7], v[12:15]\n\tds_read_b64_tr_b16 v[52:53], v0 offset:16896\n\tds_write2st64_b32 v31, v30, v29 offset1:1\n\t"
  "v_mfma_f32_16x16x32_bf16 v[64:67], v[4:7], v[132:135], v[64:67]\n\tds_read_b64_tr_b16 v[54:55], v0 offset:17920\n\tds_read_b128 v[8:11], v3 offset:576\n\t"
  "v_mfma_f32_16x16x32_bf16 v[72:75], v[124:127], v[4:7], v[72:75]\n\tds_read2_b32 v[26:27], v2 offset0:64 offset1:80\n\ts_waitcnt lgkmcnt(7)\n\t"
  "v_mfma_f32_16x16x32_bf16 v[12:15], v[140:143], v[32:35], v[12:15]\n\tds_read_b64_tr_b16 v[16:17], v0 offset:18944\n\tds_read_b128 v[56:59], v3 offset:256\n\t"
  "v_mfma_f32_16x16x32_bf16 v[64:67], v[32:35], v[144:147], v[64:67]\n\tds_read_b64_tr_b16 v[18:19], v0 offset:19968\n\tds_read_b128 v[20:23], v3 offset:512\n\t"
  "v_mfma_f32_16x16x32_bf16 v[72:75], v[136:139], v[32:35], v[72:75]\n\tds_read_b32 v25, v2 offset:640\n\t"
  "v_mfma_f32_16x16x32_bf16 v[12:15], v[152:155], v[36:39], v[12:15]\n\tds_read_b64_tr_b16 v[48:49], v0 offset:20992\n\t"
  "v_mfma_f32_16x16x32_bf16 v[64:67], v[36:39], v[156:159], v[64:67]\n\tds_read_b64_tr_b16 v[50:51], v0 offset:22016\n\t"
  "v_mfma_f32_16x16x32_bf16 v[72:75], v[148:151], v[36:39], v[72:75]\n\ts_waitcnt lgkmcnt(9)\n\t"
  "v_mfma_f32_16x16x32_bf16 v[12:15], v[164:167], v[60:63], v[12:15]\n\tds_read_b64_tr_b16 v[44:45], v0 offset:23040\n\ts_waitcnt lgkmcnt(8)\n\t"
  "v_mfma_f32_16x16x32_bf16 v[64:67], v[60:63], v[168:171], v[64:67]\n\tds_read_b64_tr_b16 v[46:47], v0 offset:24064\n\tv_mul_f32_e64 v40, -v26, v8\n\t"
  "v_mfma_f32_16x16x32_bf16 v[72:75], v[160:163], v[60:63], v[72:75]\n\tv_mul_f32_e64 v41, -v26, v9\n\tv_mul_f32_e64 v42, -v26, v10\n\t"
  "v_mfma_f32_16x16x32_bf16 v[12:15], v[176:179], v[52:55], v[12:15]\n\tds_read_b64_tr_b16 v[60:61], v0 offset:25088\n\tv_mul_f32_e64 v43, -v26, v11\n\t"
  "v_mfma_f32_16x16x32_bf16 v[64:67], v[52:55], v[180:183], v[64:67]\n\tds_read_b64_tr_b16 v[62:63], v0 offset:26112\n\ts_waitcnt lgkmcnt(5)\n\t"
  "v_mfma_f32_16x16x32_bf16 v[72:75], v[172:175], v[52:55], v[72:75]\n\tv_mul_f32_e64 v4, -v56, v25\n\tv_mul_f32_e64 v5, -v57, v25\n\t"
  "v_mfma_f32_16x16x32_bf16 v[12:15], v[188:191], v[16:19], v[12:15]\n\t"
  GST(20)
  "ds_read_b64_tr_b16 v[36:37], v0 offset:27136\n\tds_read2_b32 v[30:31], v2 offset0:0 offset1:16\n\t"
  "v_mfma_f32_16x16x32_bf16 v[64:67], v[16:19], v[192:195], v[64:67]\n\tds_read_b64_tr_b16 v[38:39], v0 offset:28160\n\tv_mul_f32_e64 v6, -v58, v25\n\t"
  "v_mfma_f32_16x16x32_bf16 v[72:75], v[184:187], v[16:19], v[72:75]\n\tv_mul_f32_e64 v7, -v59, v25\n\ts_waitcnt lgkmcnt(5)\n\t"
  "v_mfma_f32_16x16x32_bf16 v[12:15], v[200:203], v[48:51], v[12:15]\n\tds_read_b64_tr_b16 v[16:17], v0 offset:29184\n\tv_mul_f32_e64 v56, -v26, v20\n\t"
  "v_mfma_f32_16x16x32_bf16 v[64:67], v[48:51], v[204:207], v[64:67]\n\tds_read_b64_tr_b16 v[18:19], v0 offset:30208\n\tv_mul_f32_e64 v57, -v26, v21\n\t"
  "v_mfma_f32_16x16x32_bf16 v[72:75], v[196:199], v[48:51], v[72:75]\n\tv_mul_f32_e64 v58, -v26, v22\n\tv_mul_f32_e64 v59, -v26, v23\n\t"
  "v_mfma_f32_16x16x32_bf16 v[12:15], v[212:215], v[44:47], v[12:15]\n\tds_read_b64_tr_b16 v[48:49], v0 offset:0\n\t"
  "v_mfma_f32_16x16x32_bf16 v[64:67], v[44:47], v[216:219], v[64:67]\n\tds_read_b64_tr_b16 v[50:51], v0 offset:1024\n\t"
  "v_mfma_f32_16x16x32_bf16 v[72:75], v[208:211], v[44:47], v[72:75]\n\ts_waitcnt lgkmcnt(4)\n\t"
  "v_mfma_f32_16x16x32_bf16 v[12:15], v[224:227], v[60:63], v[12:15]\n\tds_read_b64_tr_b16 v[44:45], v0 offset:2048\n\t"
  "v_mfma_f32_16x16x32_bf16 v[64:67], v[60:63], v[228:231], v[64:67]\n\tds_read_b64_tr_b16 v[46:47], v0 offset:3072\n\tv_lshl_add_u32 v26, v1, 2, s60\n\t"
  "v_mfma_f32_16x16x32_bf16 v[72:75], v[220:223], v[60:63], v[72:75]\n\t"
  "v_mfma_f32_16x16x32_bf16 v[12:15], v[236:239], v[36:39], v[12:15]\n\tds_read_b64_tr_b16 v[52:53], v0 offset:4096\n\tds_read_b128 v[32:35], v3 offset:320\n\t"
  "v_mfma_f32_16x16x32_bf16 v[64:67], v[36:39], v[240:243], v[64:67]\n\tds_read_b64_tr_b16 v[54:55], v0 offset:5120\n\t"
  "v_mfma_f32_16x16x32_bf16 v[72:75], v[232:235], v[36:39], v[72:75]\n\tds_read_b128 v[60:63], v3 offset:64\n\ts_waitcnt lgkmcnt(6)\n\t"
  "v_mfma_f32_16x16x32_bf16 v[12:15], v[248:251], v[16:19], v[12:15]\n\tds_read_b64_tr_b16 v[68:69], v0 offset:6144\n\t"
  "v_mfma_f32_16x16x32_bf16 v[64:67], v[16:19], v[252:255], v[64:67]\n\tds_read_b64_tr_b16 v[70:71], v0 offset:7168\n\t"
  "v_mfma_f32_16x16x32_bf16 v[72:75], v[244:247], v[16:19], v[72:75]\n\tv_fma_f32 v12, -v27, v8, v12\n\tv_fma_f32 v13, -v27, v9, v13\n\t"
  "v_mfma_f32_16x16x32_bf16 v[40:43], v[80:83], v[48:51], v[40:43]\n\tds_read_b64_tr_b16 v[36:37], v0 offset:8192\n\tv_fma_f32 v14, -v27, v10, v14\n\t"
  "v_mfma_f32_16x16x32_bf16 v[4:7], v[48:51], v[84:87], v[4:7]\n\tds_read_b64_tr_b16 v[38:39], v0 offset:9216\n\tv_fma_f32 v15, -v27, v11, v15\n\t"
  "v_mfma_f32_16x16x32_bf16 v[56:59], v[76:79], v[48:51], v[56:59]\n\tv_mul_f32 v12, v12, v31\n\ts_waitcnt lgkmcnt(5)\n\t"
  "v_mfma_f32_16x16x32_bf16 v[40:43], v[92:95], v[44:47], v[40:43]\n\tds_read_b64_tr_b16 v[48:49], v0 offset:10240\n\tv_mul_f32 v13, v13, v31\n\t"
  "v_mfma_f32_16x16x32_bf16 v[4:7], v[44:47], v[96:99], v[4:7]\n\tds_read_b64_tr_b16 v[50:51], v0 offset:11264\n\tv_mul_f32 v14, v14, v31\n\t"
  "v_mfma_f32_16x16x32_bf16 v[56:59], v[88:91], v[44:47], v[56:59]\n\t"
  GST(17)
  "v_mul_f32 v15, v15, v31\n\tv_cvt_pk_bf16_f32 v12, v12, v13\n\t"
  "v_mfma_f32_16x16x32_bf16 v[40:43], v[104:107], v[52:55], v[40:43]\n\tds_read_b64_tr_b16 v[44:45], v0 offset:12288\n\tv_cvt_pk_bf16_f32 v14, v14, v15\n\t"
  "v_mfma_f32_16x16x32_bf16 v[4:7], v[52:55], v[108:111], v[4:7]\n\tds_read_b64_tr_b16 v[46:47], v0 offset:13312\n\tv_mov_b32 v13, s62\n\t"
  "v_mfma_f32_16x16x32_bf16 v[56:59], v[100:103], v[52:55], v[56:59]\n\tv_mov_b32 v15, s62\n\ts_waitcnt lgkmcnt(4)\n\t"
  "v_mfma_f32_16x16x32_bf16 v[40:43], v[116:119], v[68:71], v[40:43]\n\tds_read_b64_tr_b16 v[16:17], v0 offset:14336\n\tv_fma_f32 v64, -v32, v25, v64\n\t"
  "v_mfma_f32_16x16x32_bf16 v[4:7], v[68:71], v[120:123], v[4:7]\n\tds_read_b64_tr_b16 v[18:19], v0 offset:15360\n\tv_fma_f32 v65, -v33, v25, v65\n\t"
  "v_mfma_f32_16x16x32_bf16 v[56:59], v[112:115], v[68:71], v[56:59]\n\tv_fma_f32 v66, -v34, v25, v66\n\tv_fma_f32 v67, -v35, v25, v67\n\t"
  "v_mfma_f32_16x16x32_bf16 v[40:43], v[128:131], v[36:39], v[40:43]\n\tds_read_b64_tr_b16 v[68:69], v0 offset:16384\n\tv_mul_f32 v64, v64, v60\n\t"
  "v_mfma_f32_16x16x32_bf16 v[4:7], v[36:39], v[132:135], v[4:7]\n\tds_read_b64_tr_b16 v[70:71], v0 offset:17408\n\tv_mul_f32 v65, v65, v61\n\t"
  "v_mfma_f32_16x16x32_bf16 v[56:59], v[124:127], v[36:39], v[56:59]\n\tv_mul_f32 v66, v66, v62\n\ts_waitcnt lgkmcnt(4)\n\t"
  "v_mfma_f32_16x16x32_bf16 v[40:43], v[140:143], v[48:51], v[40:43]\n\tds_read_b64_tr_b16 v[36:37], v0 offset:18432\n\tv_mul_f32 v67, v67, v63\n\t"
  "v_mfma_f32_16x16x32_bf16 v[4:7], v[48:51], v[144:147], v[4:7]\n\tds_read_b64_tr_b16 v[38:39], v0 offset:19456\n\tv_cvt_pk_bf16_f32 v64, v64, v65\n\t"
  "v_mfma_f32_16x16x32_bf16 v[56:59], v[136:139], v[48:51], v[56:59]\n\tv_cvt_pk_bf16_f32 v66, v66, v67\n\tv_mov_b32 v65, s62\n\t"
  "v_mfma_f32_16x16x32_bf16 v[40:43], v[152:155], v[44:47], v[40:43]\n\tds_read_b64_tr_b16 v[32:33], v0 offset:20480\n\tv_mov_b32 v67, s62\n\t"
  "v_mfma_f32_16x16x32_bf16 v[4:7], v[44:47], v[156:159], v[4:7]\n\tds_read_b64_tr_b16 v[34:35], v0 offset:21504\n\tv_fma_f32 v72, -v27, v20, v72\n\t"
  "v_mfma_f32_16x16x32_bf16 v[56:59], v[148:151], v[44:47], v[56:59]\n\tv_add_u32 v24, s64, v1\n\ts_waitcnt lgkmcnt(4)\n\t"
  "v_mfma_f32_16x16x32_bf16 v[40:43], v[164:167], v[16:19], v[40:43]\n\tds_read_b64_tr_b16 v[60:61], v0 offset:22528\n\tv_fma_f32 v73, -v27, v21, v73\n\t"
  "v_mfma_f32_16x16x32_bf16 v[4:7], v[16:19], v[168:171], v[4:7]\n\tds_read_b64_tr_b16 v[62:63], v0 offset:23552\n\tv_fma_f32 v74, -v27, v22, v74\n\t"
  "v_mfma_f32_16x16x32_bf16 v[56:59], v[160:163], v[16:19], v[56:59]\n\tv_fma_f32 v75, -v27, v23, v75\n\tv_mul_f32 v72, v72, v31\n\t"
  "v_mfma_f32_16x16x32_bf16 v[40:43], v[176:179], v[68:71], v[40:43]\n\tds_read_b64_tr_b16 v[44:45], v0 offset:24576\n\tv_mul_f32 v73, v73, v31\n\t"
  "v_mfma_f32_16x16x32_bf16 v[4:7], v[68:71], v[180:183], v[4:7]\n\tds_read_b64_tr_b16 v[46:47], v0 offset:25600\n\tv_mul_f32 v74, v74, v31\n\t"
  "v_mfma_f32_16x16x32_bf16 v[56:59], v[172:175], v[68:71], v[56:59]\n\tv_mul_f32 v75, v75, v31\n\ts_waitcnt lgkmcnt(4)\n\t"
  "v_mfma_f32_16x16x32_bf16 v[40:43], v[188:191], v[36:39], v[40:43]\n\tds_read_b64_tr_b16 v[68:69], v0 offset:26624\n\tv_cvt_pk_bf16_f32 v72, v72, v73\n\t"
  "v_mfma_f32_16x16x32_bf16 v[4:7], v[36:39], v[192:195], v[4:7]\n\tds_read_b64_tr_b16 v[70:71], v0 offset:27648\n\tv_cvt_pk_bf16_f32 v74, v74, v75\n\t"
  "v_mfma_f32_16x16x32_bf16 v[56:59], v[184:187], v[36:39], v[56:59]\n\t"
  "v_mfma_f32_16x16x32_bf16 v[40:43], v[200:203], v[32:35], v[40:43]\n\t"
  GST(13)
  "ds_read_b64_tr_b16 v[52:53], v0 offset:28672\n\t"
  "v_mfma_f32_16x16x32_bf16 v[4:7], v[32:35], v[204:207], v[4:7]\n\ts_barrier\n\tds_read_b64_tr_b16 v[54:55], v0 offset:29696\n\t"
  "v_mfma_f32_16x16x32_bf16 v[56:59], v[196:199], v[32:35], v[56:59]\n\tbuffer_store_dwordx4 v[12:15], v26, s[56:59], 0 offen offset:0 sc1\n\ts_waitcnt lgkmcnt(4)\n\t"
  "v_mfma_f32_16x16x32_bf16 v[40:43], v[212:215], v[60:63], v[40:43]\n\tds_read_b64_tr_b16 v[36:37], v0 offset:30720\n\tbuffer_store_dwordx4 v[64:67], v26, s[56:59], 0 offen offset:1024 sc1\n\t"
  "v_mfma_f32_16x16x32_bf16 v[4:7], v[60:63], v[216:219], v[4:7]\n\tds_read_b64_tr_b16 v[38:39], v0 offset:31744\n\tv_lshl_add_u32 v13, v1, 2, s66\n\tv_add_u32 v0, 0x8000, v0\n\t"
  "v_mfma_f32_16x16x32_bf16 v[56:59], v[208:211], v[60:63], v[56:59]\n\tds_read_b128 v[26:29], v13 offset:1024\n\tds_read_b128 v[60:63], v13 offset:2048\n\t"
  "v_mfma_f32_16x16x32_bf16 v[40:43], v[224:227], v[44:47], v[40:43]\n\tds_read_b64_tr_b16 v[32:33], v0 offset:0\n\tds_read_b128 v[16:19], v13 offset:0\n\t"
  "v_mfma_f32_16x16x32_bf16 v[4:7], v[44:47], v[228:231], v[4:7]\n\tds_read_b64_tr_b16 v[34:35], v0 offset:1024\n\tv_min_u32 v73, 188, v1\n\t"
  "v_mfma_f32_16x16x32_bf16 v[56:59], v[220:223], v[44:47], v[56:59]\n\tds_read_b128 v[48:51], v3\n\ts_waitcnt lgkmcnt(8)\n\t"
  "v_mfma_f32_16x16x32_bf16 v[40:43], v[236:239], v[68:71], v[40:43]\n\tds_read_b64_tr_b16 v[44:45], v0 offset:2048\n\tv_lshlrev_b32 v73, 2, v73\n\t"
  "v_mfma_f32_16x16x32_bf16 v[4:7], v[68:71], v[240:243], v[4:7]\n\tds_read_b64_tr_b16 v[46:47], v0 offset:3072\n\tv_add_u32 v73, 0x22c00, v73\n\t"
  "v_mfma_f32_16x16x32_bf16 v[56:59], v[232:235], v[68:71], v[56:59]\n\t"
  "v_mfma_f32_16x16x32_bf16 v[40:43], v[248:251], v[52:55], v[40:43]\n\tds_read_b64_tr_b16 v[68:69], v0 offset:8192\n\t"
  "v_mfma_f32_16x16x32_bf16 v[4:7], v[52:55], v[252:255], v[4:7]\n\tds_read_b64_tr_b16 v[70:71], v0 offset:9216\n\t"
  "v_mfma_f32_16x16x32_bf16 v[56:59], v[244:247], v[52:55], v[56:59]\n\ts_waitcnt lgkmcnt(5)\n\t"
  "v_mfma_f32_16x16x32_bf16 v[40:43], v[26:29], v[36:39], v[40:43]\n\tds_read_b64_tr_b16 v[26:27], v0 offset:10240\n\t"
  "v_mfma_f32_16x16x32_bf16 v[4:7], v[36:39], v[60:63], v[4:7]\n\tds_read_b64_tr_b16 v[28:29], v0 offset:11264\n\t"
  "v_mfma_f32_16x16x32_bf16 v[56:59], v[16:19], v[36:39], v[56:59]\n\t"
  GST(12)
  "v_mul_f32 v40, v40, v30\n\tv_mul_f32 v41, v41, v30\n\t"
  "v_mfma_f32_16x16x32_bf16 v[60:63], v[80:83], v[32:35], 0\n\tds_read_b64_tr_b16 v[36:37], v0 offset:16384\n\tv_mul_f32 v42, v42, v30\n\t"
  "v_mfma_f32_16x16x32_bf16 v[16:19], v[32:35], v[84:87], 0\n\tds_read_b64_tr_b16 v[38:39], v0 offset:17408\n\tv_mul_f32 v43, v43, v30\n\t"
  "v_mfma_f32_16x16x32_bf16 v[52:55], v[76:79], v[32:35], 0\n\tv_cvt_pk_bf16_f32 v40, v40, v41\n\ts_waitcnt lgkmcnt(4)\n\t"
  "v_mfma_f32_16x16x32_bf16 v[60:63], v[92:95], v[44:47], v[60:63]\n\tds_read_b64_tr_b16 v[32:33], v0 offset:18432\n\tv_cvt_pk_bf16_f32 v42, v42, v43\n\t"
  "v_mfma_f32_16x16x32_bf16 v[16:19], v[44:47], v[96:99], v[16:19]\n\tds_read_b64_tr_b16 v[34:35], v0 offset:19456\n\tv_mul_f32 v56, v56, v30\n\t"
  "v_mfma_f32_16x16x32_bf16 v[52:55], v[88:91], v[44:47], v[52:55]\n\tv_mul_f32 v57, v57, v30\n\tv_mul_f32 v58, v58, v30\n\t"
  "v_mfma_f32_16x16x32_bf16 v[60:63], v[128:131], v[68:71], v[60:63]\n\tds_read_b64_tr_b16 v[44:45], v0 offset:24576\n\tds_write2st64_b32 v24, v12, v14 offset0:0 offset1:1\n\tds_write2st64_b32 v24, v40, v42 offset0:6 offset1:7\n\t"
  "v_mfma_f32_16x16x32_bf16 v[16:19], v[68:71], v[132:135], v[16:19]\n\tds_read_b64_tr_b16 v[46:47], v0 offset:25600\n\tds_write2st64_b32 v24, v64, v66 offset0:2 offset1:3\n\t"
  "v_mfma_f32_16x16x32_bf16 v[52:55], v[124:127], v[68:71], v[52:55]\n\tds_write2st64_b32 v24, v72, v74 offset0:4 offset1:5\n\tv_mul_f32 v59, v59, v30\n\tv_cvt_pk_bf16_f32 v56, v56, v57\n\t"
  "v_cvt_pk_bf16_f32 v58, v58, v59\n\ts_waitcnt lgkmcnt(8)\n\t"
  "v_mfma_f32_16x16x32_bf16 v[60:63], v[140:143], v[26:29], v[60:63]\n\tds_read_b64_tr_b16 v[40:41], v0 offset:26624\n\tds_write2st64_b32 v24, v56, v58 offset0:10 offset1:11\n\t"
  "v_mfma_f32_16x16x32_bf16 v[16:19], v[26:29], v[144:147], v[16:19]\n\tds_read_b64_tr_b16 v[42:43], v0 offset:27648\n\tv_mul_f32 v4, v4, v48\n\t"
  "v_mfma_f32_16x16x32_bf16 v[52:55], v[136:139], v[26:29], v[52:55]\n\tv_mul_f32 v5, v5, v49\n\tv_mul_f32 v6, v6, v50\n\tv_mul_f32 v7, v7, v51\n\t"
  "v_cvt_pk_bf16_f32 v4, v4, v5\n\tv_cvt_pk_bf16_f32 v6, v6, v7\n\t"
  "v_mfma_f32_16x16x32_bf16 v[60:63], v[176:179], v[36:39], v[60:63]\n\tds_write2st64_b32 v24, v4, v6 offset0:8 offset1:9\n\t"
  "v_mfma_f32_16x16x32_bf16 v[16:19], v[36:39], v[180:183], v[16:19]\n\t"
  "v_mfma_f32_16x16x32_bf16 v[52:55], v[172:175], v[36:39], v[52:55]\n\tv_and_b32 v27, 0x40, v1\n\ts_waitcnt lgkmcnt(4)\n\t"
  "v_mfma_f32_16x16x32_bf16 v[60:63], v[188:191], v[32:35], v[60:63]\n\tv_lshl_add_u32 v27, v27, 8, s61\n\tv_and_b32 v26, 0x3c, v1\n\t"
  "v_mfma_f32_16x16x32_bf16 v[16:19], v[32:35], v[192:195], v[16:19]\n\ts_waitcnt lgkmcnt(0)\n\ts_barrier\n\tds_read_b32 v24, v73 offset:5700\n\t"
  "ds_read_b128 v[64:67], v73\n\tds_read_b128 v[48:51], v73 offset:768\n\t"
  "v_mfma_f32_16x16x32_bf16 v[52:55], v[184:187], v[32:35], v[52:55]\n\tds_read_b64_tr_b16 v[4:5], v0 offset:4096\n\tds_read_b64_tr_b16 v[6:7], v0 offset:5120\n\t"
  "v_mfma_f32_16x16x32_bf16 v[60:63], v[224:227], v[44:47], v[60:63]\n\tds_read_b64_tr_b16 v[72:73], v0 offset:6144\n\tds_read_b64_tr_b16 v[74:75], v0 offset:7168\n\t"
  "v_mfma_f32_16x16x32_bf16 v[16:19], v[44:47], v[228:231], v[16:19]\n\tds_read_b64_tr_b16 v[28:29], v0 offset:12288\n\tv_lshl_add_u32 v27, v26, 2, v27\n\t"
  "v_mfma_f32_16x16x32_bf16 v[52:55], v[220:223], v[44:47], v[52:55]\n\tds_read_b64_tr_b16 v[30:31], v0 offset:13312\n\tv_and_b32 v26, 0x80, v1\n\t"
  "v_mfma_f32_16x16x32_bf16 v[60:63], v[236:239], v[40:43], v[60:63]\n\tds_read_b64_tr_b16 v[68:69], v0 offset:14336\n\tv_lshl_add_u32 v27, v26, 1, v27\n\t"
  "v_mfma_f32_16x16x32_bf16 v[16:19], v[40:43], v[240:243], v[16:19]\n\tds_read_b64_tr_b16 v[70:71], v0 offset:15360\n\t"
  "v_mfma_f32_16x16x32_bf16 v[52:55], v[232:235], v[40:43], v[52:55]\n\ts_waitcnt lgkmcnt(9)\n\ts_waitcnt lgkmcnt(6)\n\t"
  "v_mfma_f32_16x16x32_bf16 v[60:63], v[104:107], v[4:7], v[60:63]\n\tds_read_b64_tr_b16 v[12:13], v0 offset:20480\n\tv_readfirstlane_b32 s54, v24\n\t"
  "v_mfma_f32_16x16x32_bf16 v[16:19], v[4:7], v[108:111], v[16:19]\n\tds_read_b64_tr_b16 v[14:15], v0 offset:21504\n\tv_add_f32 v64, v64, v65\n\t"
  "v_mfma_f32_16x16x32_bf16 v[52:55], v[100:103], v[4:7], v[52:55]\n\ts_cmp_eq_u32 s54, s63\n\ts_cbranch_scc1 .Lg_redo\n.Lg_back:\n\ts_waitcnt lgkmcnt(5)\n\t"
  "v_mfma_f32_16x16x32_bf16 v[60:63], v[116:119], v[72:75], v[60:63]\n\tds_read_b64_tr_b16 v[44:45], v0 offset:22528\n\tv_add_f32 v66, v66, v67\n\t"
  "v_mfma_f32_16x16x32_bf16 v[16:19], v[72:75], v[120:123], v[16:19]\n\tds_read_b64_tr_b16 v[46:47], v0 offset:23552\n\tv_add_f32 v48, v48, v49\n\t"
  "v_mfma_f32_16x16x32_bf16 v[52:55], v[112:115], v[72:75], v[52:55]\n\tv_lshl_add_u32 v24, v1, 2, s66\n\ts_waitcnt lgkmcnt(4)\n\t"
  "v_mfma_f32_16x16x32_bf16 v[60:63], v[152:155], v[28:31], v[60:63]\n\tds_read_b64_tr_b16 v[4:5], v0 offset:30720\n\tv_add_f32 v64, v64, v66\n\t"
  "v_mfma_f32_16x16x32_bf16 v[16:19], v[28:31], v[156:159], v[16:19]\n\tds_read_b64_tr_b16 v[6:7], v0 offset:31744\n\tds_read_b128 v[36:39], v24 offset:1024\n\t"
  "v_mfma_f32_16x16x32_bf16 v[52:55], v[148:151], v[28:31], v[52:55]\n\tv_add_f32 v50, v50, v51\n\tv_mul_f32 v65, 0x3b000000, v64\n\t"
  "v_mfma_f32_16x16x32_bf16 v[60:63], v[164:167], v[68:71], v[60:63]\n\tds_read_b64_tr_b16 v[40:41], v0 offset:28672\n\tv_add_f32 v48, v48, v50\n\t"
  "v_mfma_f32_16x16x32_bf16 v[16:19], v[68:71], v[168:171], v[16:19]\n\tds_read_b64_tr_b16 v[42:43], v0 offset:29696\n\tv_mul_f32_e64 v66, -v65, v65\n\tv_fmac_f32 v66, 0x3b000000, v48\n\t"
  "v_max_f32 v66, 0, v66\n\tv_add_f32 v66, 0x3727c5ac, v66\n\t"
  "v_mfma_f32_16x16x32_bf16 v[52:55], v[160:163], v[68:71], v[52:55]\n\tds_read_b64_tr_b16 v[68:69], v0 offset:49152\n\tv_rsq_f32 v66, v66\n\tv_add_u32 v67, s65, v1\n\t"
  "ds_write2st64_b32 v67, v66, v65 offset1:1\n\ts_waitcnt lgkmcnt(6)\n\t"
  "v_mfma_f32_16x16x32_bf16 v[60:63], v[200:203], v[12:15], v[60:63]\n\tds_read_b64_tr_b16 v[70:71], v0 offset:49664\n\tds_read_b128 v[32:35], v24 offset:2048\n\tds_read2_b32 v[66:67], v2 offset0:80 offset1:96\n\t"
  "ds_read_b128 v[48:51], v24 offset:0\n\t"
  "v_mfma_f32_16x16x32_bf16 v[16:19], v[12:15], v[204:207], v[16:19]\n\tds_read_b64_tr_b16 v[56:57], v0 offset:50176\n\t"
  "v_mfma_f32_16x16x32_bf16 v[52:55], v[196:199], v[12:15], v[52:55]\n\tds_read_b64_tr_b16 v[58:59], v0 offset:50688\n\t"
  "v_mfma_f32_16x16x32_bf16 v[60:63], v[212:215], v[44:47], v[60:63]\n\tds_read_b64_tr_b16 v[72:73], v0 offset:51200\n\t"
  "v_mfma_f32_16x16x32_bf16 v[16:19], v[44:47], v[216:219], v[16:19]\n\tds_read_b64_tr_b16 v[74:75], v0 offset:51712\n\t"
  "v_mfma_f32_16x16x32_bf16 v[52:55], v[208:211], v[44:47], v[52:55]\n\tds_read_b64_tr_b16 v[44:45], v0 offset:52224\n\ts_waitcnt lgkmcnt(10)\n\t"
  "v_mfma_f32_16x16x32_bf16 v[60:63], v[36:39], v[4:7], v[60:63]\n\tds_read_b64_tr_b16 v[46:47], v0 offset:52736\n\ts_waitcnt lgkmcnt(8)\n\t"
  "v_mfma_f32_16x16x32_bf16 v[16:19], v[4:7], v[32:35], v[16:19]\n\tds_read_b64_tr_b16 v[32:33], v0 offset:53248\n\ts_waitcnt lgkmcnt(7)\n\t"
  "v_mfma_f32_16x16x32_bf16 v[52:55], v[48:51], v[4:7], v[52:55]\n\tds_read_b64_tr_b16 v[34:35], v0 offset:53760\n\tds_read_b128 v[4:7], v3 offset:320\n\t"
  "v_mfma_f32_16x16x32_bf16 v[60:63], v[248:251], v[40:43], v[60:63]\n\tds_read_b64_tr_b16 v[28:29], v0 offset:54272\n\tv_mul_f32_e64 v48, -v67, v8\n\t"
  "v_mfma_f32_16x16x32_bf16 v[16:19], v[40:43], v[252:255], v[16:19]\n\tds_read_b64_tr_b16 v[30:31], v0 offset:54784\n\tv_mul_f32_e64 v49, -v67, v9\n\t"
  "v_mfma_f32_16x16x32_bf16 v[52:55], v[244:247], v[40:43], v[52:55]\n\tds_read_b64_tr_b16 v[36:37], v0 offset:55296\n\tv_fma_f32 v60, -v66, v8, v60\n\t"
  "v_mfma_f32_16x16x32_bf16 v[12:15], v[80:83], v[68:71], 0\n\t"
  GST(18)
  "ds_read_b64_tr_b16 v[38:39], v0 offset:55808\n\tv_fma_f32 v61, -v66, v9, v61\n\t"
  "v_mfma_f32_16x16x32_bf16 v[40:43], v[68:71], v[84:87], 0\n\tds_read_b64_tr_b16 v[68:69], v0 offset:56320\n\ts_waitcnt lgkmcnt(8)\n\t"
  "v_mfma_f32_16x16x32_bf16 v[12:15], v[92:95], v[56:59], v[12:15]\n\tds_read_b64_tr_b16 v[70:71], v0 offset:56832\n\tv_fma_f32 v62, -v66, v10, v62\n\t"
  "v_mfma_f32_16x16x32_bf16 v[40:43], v[56:59], v[96:99], v[40:43]\n\tds_read_b64_tr_b16 v[56:57], v0 offset:57344\n\tv_fma_f32 v63, -v66, v11, v63\n\t"
  "v_mfma_f32_16x16x32_bf16 v[12:15], v[104:107], v[72:75], v[12:15]\n\tds_read_b64_tr_b16 v[58:59], v0 offset:57856\n\tv_cvt_pk_bf16_f32 v64, v60, v61\n\t"
  "v_mfma_f32_16x16x32_bf16 v[40:43], v[72:75], v[108:111], v[40:43]\n\tds_read_b64_tr_b16 v[60:61], v0 offset:58368\n\tv_cvt_pk_bf16_f32 v65, v62, v63\n\t"
  "v_mfma_f32_16x16x32_bf16 v[12:15], v[116:119], v[44:47], v[12:15]\n\tds_read_b64_tr_b16 v[62:63], v0 offset:58880\n\tv_fma_f32 v52, -v66, v20, v52\n\t"
  "v_mfma_f32_16x16x32_bf16 v[40:43], v[44:47], v[120:123], v[40:43]\n\tds_read_b64_tr_b16 v[72:73], v0 offset:59392\n\ts_waitcnt lgkmcnt(7)\n\t"
  "v_mfma_f32_16x16x32_bf16 v[12:15], v[128:131], v[32:35], v[12:15]\n\tds_read_b64_tr_b16 v[74:75], v0 offset:59904\n\tv_fma_f32 v16, -v4, v25, v16\n\t"
  "v_mfma_f32_16x16x32_bf16 v[40:43], v[32:35], v[132:135], v[40:43]\n\tds_read_b64_tr_b16 v[32:33], v0 offset:60416\n\tv_fma_f32 v17, -v5, v25, v17\n\t"
  "v_mfma_f32_16x16x32_bf16 v[12:15], v[140:143], v[28:31], v[12:15]\n\tds_read_b64_tr_b16 v[34:35], v0 offset:60928\n\tv_fma_f32 v18, -v6, v25, v18\n\tv_fma_f32 v19, -v7, v25, v19\n\t"
  "v_cvt_pk_bf16_f32 v46, v16, v17\n\tv_cvt_pk_bf16_f32 v47, v18, v19\n\t"
  "v_mfma_f32_16x16x32_bf16 v[40:43], v[28:31], v[144:147], v[40:43]\n\tds_read_b64_tr_b16 v[28:29], v0 offset:61440\n\tv_fma_f32 v53, -v66, v21, v53\n\t"
  "v_mfma_f32_16x16x32_bf16 v[12:15], v[152:155], v[36:39], v[12:15]\n\tds_read_b64_tr_b16 v[30:31], v0 offset:61952\n\tv_fma_f32 v54, -v66, v22, v54\n\t"
  "v_mfma_f32_16x16x32_bf16 v[40:43], v[36:39], v[156:159], v[40:43]\n\tds_read_b64_tr_b16 v[16:17], v0 offset:62464\n\ts_waitcnt lgkmcnt(6)\n\t"
  "v_mfma_f32_16x16x32_bf16 v[12:15], v[164:167], v[68:71], v[12:15]\n\tds_read_b64_tr_b16 v[18:19], v0 offset:62976\n\tv_fma_f32 v55, -v66, v23, v55\n\t"
  "v_mfma_f32_16x16x32_bf16 v[40:43], v[68:71], v[168:171], v[40:43]\n\tds_read_b64_tr_b16 v[36:37], v0 offset:63488\n\tv_cvt_pk_bf16_f32 v44, v52, v53\n\t"
  "v_mfma_f32_16x16x32_bf16 v[12:15], v[176:179], v[56:59], v[12:15]\n\tds_read_b64_tr_b16 v[38:39], v0 offset:64000\n\tv_cvt_pk_bf16_f32 v45, v54, v55\n\t"
  "v_mfma_f32_16x16x32_bf16 v[40:43], v[56:59], v[180:183], v[40:43]\n\tds_read_b64_tr_b16 v[56:57], v0 offset:64512\n\tv_mul_f32_e64 v50, -v67, v10\n\t"
  "v_mfma_f32_16x16x32_bf16 v[12:15], v[188:191], v[60:63], v[12:15]\n\tds_read_b64_tr_b16 v[58:59], v0 offset:65024\n\tv_lshl_add_u32 v66, v1, 2, s66\n\t"
  "v_mfma_f32_16x16x32_bf16 v[40:43], v[60:63], v[192:195], v[40:43]\n\tds_read_b32 v24, v2 offset:256\n\ts_waitcnt lgkmcnt(7)\n\t"
  "v_mfma_f32_16x16x32_bf16 v[12:15], v[200:203], v[72:75], v[12:15]\n\tds_read_b64_tr_b16 v[4:5], v0 offset:31232\n\tv_and_or_b32 v26, v1, 4, v3\n\tv_add_u32 v63, 64, v26\n\t"
  "v_mfma_f32_16x16x32_bf16 v[40:43], v[72:75], v[204:207], v[40:43]\n\tds_read_b64_tr_b16 v[6:7], v0 offset:32256\n\tv_cndmask_b32_e64 v68, v26, v63, s[74:75]\n\tv_cndmask_b32_e64 v26, v26, v63, s[76:77]\n\t"
  "v_mfma_f32_16x16x32_bf16 v[12:15], v[212:215], v[32:35], v[12:15]\n\tds_read_b128 v[60:63], v66 offset:1024\n\tds_read_b128 v[72:75], v66 offset:2048\n\t"
  "v_mfma_f32_16x16x32_bf16 v[40:43], v[32:35], v[216:219], v[40:43]\n\tds_read_b64_tr_b16 v[52:53], v0 offset:512\n\tv_mul_f32_e64 v51, -v67, v11\n\t"
  "v_mfma_f32_16x16x32_bf16 v[12:15], v[224:227], v[28:31], v[12:15]\n\tds_read_b64_tr_b16 v[54:55], v0 offset:1536\n\tds_read_b128 v[32:35], v66 offset:0\n\t"
  "v_mfma_f32_16x16x32_bf16 v[40:43], v[28:31], v[228:231], v[40:43]\n\tds_read2_b32 v[70:71], v68 offset0:0 offset1:16\n\ts_waitcnt lgkmcnt(9)\n\t"
  "v_mfma_f32_16x16x32_bf16 v[12:15], v[236:239], v[16:19], v[12:15]\n\tds_read_b64_tr_b16 v[28:29], v0 offset:2560\n\tds_read2_b32 v[68:69], v26 offset0:2 offset1:18\n\t"
  "v_mfma_f32_16x16x32_bf16 v[40:43], v[16:19], v[240:243], v[40:43]\n\tds_read_b64_tr_b16 v[30:31], v0 offset:3584\n\tv_mul_f32_e64 v16, -v67, v20\n\t"
  "v_mfma_f32_16x16x32_bf16 v[12:15], v[248:251], v[36:39], v[12:15]\n\tv_mul_f32_e64 v17, -v67, v21\n\tv_mul_f32_e64 v18, -v67, v22\n\tv_mul_f32_e64 v19, -v67, v23\n\t"
  "v_mfma_f32_16x16x32_bf16 v[40:43], v[36:39], v[252:255], v[40:43]\n\tds_read_b64_tr_b16 v[20:21], v0 offset:4608\n\ts_waitcnt lgkmcnt(8)\n\t"
  "v_mfma_f32_16x16x32_bf16 v[12:15], v[60:63], v[56:59], v[12:15]\n\tds_read_b64_tr_b16 v[22:23], v0 offset:5632\n\tds_read_b128 v[36:39], v3 offset:256\n\t"
  "v_mfma_f32_16x16x32_bf16 v[40:43], v[56:59], v[72:75], v[40:43]\n\tds_read2_b32 v[66:67], v2 offset0:16 offset1:32\n\t"
  "v_mfma_f32_16x16x32_bf16 v[48:51], v[60:63], v[4:7], v[48:51]\n\t"
  GST(19)
  "ds_read_b64_tr_b16 v[60:61], v0 offset:6656\n\ts_waitcnt lgkmcnt(8)\n\t"
  "v_mfma_f32_16x16x32_bf16 v[16:19], v[32:35], v[4:7], v[16:19]\n\tds_read_b64_tr_b16 v[62:63], v0 offset:7680\n\tv_fma_f32 v12, -v24, v8, v12\n\t"
  "v_mfma_f32_16x16x32_bf16 v[32:35], v[4:7], v[72:75], 0\n\tv_fma_f32 v13, -v24, v9, v13\n\tv_fma_f32 v14, -v24, v10, v14\n\t"
  "v_mfma_f32_16x16x32_bf16 v[48:51], v[80:83], v[52:55], v[48:51]\n\tds_read_b64_tr_b16 v[8:9], v0 offset:8704\n\tv_mfma_f32_16x16x16_bf16 v[56:59], v[64:65], v[44:45], 0\n\tv_fma_f32 v15, -v24, v11, v15\n\t"
  "v_cvt_pk_bf16_f32 v12, v12, v13\n\t"
  "v_mfma_f32_16x16x32_bf16 v[16:19], v[76:79], v[52:55], v[16:19]\n\tds_read_b64_tr_b16 v[10:11], v0 offset:9728\n\tv_cvt_pk_bf16_f32 v13, v14, v15\n\t"
  "v_mfma_f32_16x16x32_bf16 v[32:35], v[52:55], v[84:87], v[32:35]\n\tv_cndmask_b32_e64 v26, v56, v57, s[68:69]\n\tv_mfma_f32_16x16x16_bf16 v[4:7], v[12:13], v[44:45], 0\n\tv_cndmask_b32_e64 v24, v58, v59, s[68:69]\n\t"
  "s_waitcnt lgkmcnt(2)\n\t"
  "v_mfma_f32_16x16x32_bf16 v[48:51], v[92:95], v[28:31], v[48:51]\n\tds_read_b64_tr_b16 v[12:13], v0 offset:10752\n\tv_mul_f32 v66, 0x3eb8aa3b, v66\n\t"
  "v_mfma_f32_16x16x32_bf16 v[16:19], v[88:91], v[28:31], v[16:19]\n\tds_read_b64_tr_b16 v[14:15], v0 offset:11776\n\tv_cndmask_b32_e64 v52, v4, v5, s[68:69]\n\t"
  "v_mfma_f32_16x16x32_bf16 v[32:35], v[28:31], v[96:99], v[32:35]\n\tv_cndmask_b32_e64 v26, v52, v26, s[74:75]\n\tv_mul_f32 v26, v26, v70\n\t"
  "v_mfma_f32_16x16x32_bf16 v[48:51], v[104:107], v[20:23], v[48:51]\n\tds_read_b64_tr_b16 v[4:5], v0 offset:12800\n\tv_cndmask_b32_e64 v53, v6, v7, s[68:69]\n\t"
  "v_mfma_f32_16x16x32_bf16 v[16:19], v[100:103], v[20:23], v[16:19]\n\tds_read_b64_tr_b16 v[6:7], v0 offset:13824\n\tv_cndmask_b32_e64 v24, v53, v24, s[76:77]\n\t"
  "v_mfma_f32_16x16x32_bf16 v[32:35], v[20:23], v[108:111], v[32:35]\n\tv_mul_f32 v24, v24, v68\n\tv_max_f32 v53, v26, v24\n\t"
  "v_mfma_f32_16x16x32_bf16 v[48:51], v[116:119], v[60:63], v[48:51]\n\tds_read_b64_tr_b16 v[56:57], v0 offset:14848\n\tv_mul_f32 v67, 0x3eb8aa3b, v67\n\t"
  "v_mfma_f32_16x16x32_bf16 v[16:19], v[112:115], v[60:63], v[16:19]\n\tds_read_b64_tr_b16 v[58:59], v0 offset:15872\n\tv_max_f32 v52, v26, v24\n\t"
  "v_mfma_f32_16x16x32_bf16 v[32:35], v[60:63], v[120:123], v[32:35]\n\tv_fma_f32 v40, -v36, v25, v40\n\ts_waitcnt lgkmcnt(4)\n\t"
  "v_mfma_f32_16x16x32_bf16 v[48:51], v[128:131], v[8:11], v[48:51]\n\tds_read_b64_tr_b16 v[60:61], v0 offset:16896\n\tv_fma_f32 v41, -v37, v25, v41\n\t"
  "v_mfma_f32_16x16x32_bf16 v[16:19], v[124:127], v[8:11], v[16:19]\n\tds_read_b64_tr_b16 v[62:63], v0 offset:17920\n\tv_lshlrev_b32 v45, 2, v1\n\t"
  "v_mfma_f32_16x16x32_bf16 v[32:35], v[8:11], v[132:135], v[32:35]\n\tds_read_b64_tr_b16 v[28:29], v0 offset:18944\n\tv_alignbit_b32 v55, s67, s67, v45\n\t"
  "v_mfma_f32_16x16x32_bf16 v[48:51], v[140:143], v[12:15], v[48:51]\n\tds_read_b64_tr_b16 v[30:31], v0 offset:19968\n\tv_alignbit_b32 v45, s55, s55, v45\n\t"
  "v_mfma_f32_16x16x32_bf16 v[16:19], v[136:139], v[12:15], v[16:19]\n\tds_read_b64_tr_b16 v[20:21], v0 offset:20992\n\tv_mov_b32 v36, 0x0c0c0c0c\n\t"
  "v_mfma_f32_16x16x32_bf16 v[32:35], v[12:15], v[144:147], v[32:35]\n\tds_read_b64_tr_b16 v[22:23], v0 offset:22016\n\ts_waitcnt lgkmcnt(6)\n\t"
  "v_mfma_f32_16x16x32_bf16 v[48:51], v[152:155], v[4:7], v[48:51]\n\tds_read_b64_tr_b16 v[12:13], v0 offset:23040\n\tv_mov_b32 v8, 0x3f803f80\n\t"
  "v_mfma_f32_16x16x32_bf16 v[16:19], v[148:151], v[4:7], v[16:19]\n\tds_read_b64_tr_b16 v[14:15], v0 offset:24064\n\tv_mov_b32 v9, 0x3f803f80\n\t"
  "v_mfma_f32_16x16x32_bf16 v[32:35], v[4:7], v[156:159], v[32:35]\n\tds_read_b64_tr_b16 v[4:5], v0 offset:25088\n\tv_mov_b32 v11, 0\n\t"
  "v_mfma_f32_16x16x32_bf16 v[48:51], v[164:167], v[56:59], v[48:51]\n\tds_read_b64_tr_b16 v[6:7], v0 offset:26112\n\tv_cndmask_b32_e64 v2, v55, v36, s[74:75]\n\t"
  "v_mfma_f32_16x16x32_bf16 v[16:19], v[160:163], v[56:59], v[16:19]\n\tds_read_b64_tr_b16 v[72:73], v0 offset:27136\n\tv_cndmask_b32_e64 v1, v36, v55, s[74:75]\n\t"
  "v_mfma_f32_16x16x32_bf16 v[32:35], v[56:59], v[168:171], v[32:35]\n\tds_read_b64_tr_b16 v[74:75], v0 offset:28160\n\ts_waitcnt lgkmcnt(7)\n\t"
  "v_mfma_f32_16x16x32_bf16 v[48:51], v[176:179], v[60:63], v[48:51]\n\tds_read_b64_tr_b16 v[56:57], v0 offset:29184\n\tv_cndmask_b32_e64 v37, v45, v36, s[76:77]\n\t"
  "v_mfma_f32_16x16x32_bf16 v[16:19], v[172:175], v[60:63], v[16:19]\n\tds_read_b64_tr_b16 v[58:59], v0 offset:30208\n\tv_cndmask_b32_e64 v0, v36, v45, s[76:77]\n\t"
  "v_mfma_f32_16x16x32_bf16 v[48:51], v[188:191], v[28:31], v[48:51]\n\tv_permlane16_swap_b32 v52, v53\n\t"
  "v_mfma_f32_16x16x32_bf16 v[16:19], v[184:187], v[28:31], v[16:19]\n\tv_max_f32 v54, v52, v53\n\ts_waitcnt lgkmcnt(5)\n\t"
  "v_mfma_f32_16x16x32_bf16 v[48:51], v[200:203], v[20:23], v[48:51]\n\tv_max_f32 v55, v52, v53\n\tv_fma_f32 v42, -v38, v25, v42\n\t"
  "v_mfma_f32_16x16x32_bf16 v[16:19], v[196:199], v[20:23], v[16:19]\n\tv_permlane32_swap_b32 v54, v55\n\t"
  "v_mfma_f32_16x16x32_bf16 v[48:51], v[212:215], v[12:15], v[48:51]\n\tv_max_f32 v52, v54, v55\n\tv_mul_f32_e64 v52, -v52, v66\n\t"
  "v_mfma_f32_16x16x32_bf16 v[16:19], v[208:211], v[12:15], v[16:19]\n\tv_fma_f32 v26, v26, v66, v52\n\ts_waitcnt lgkmcnt(1)\n\t"
  "v_mfma_f32_16x16x32_bf16 v[48:51], v[224:227], v[4:7], v[48:51]\n\tv_fma_f32 v24, v24, v66, v52\n\tv_fma_f32 v43, -v39, v25, v43\n\t"
  "v_mfma_f32_16x16x32_bf16 v[16:19], v[220:223], v[4:7], v[16:19]\n\tv_exp_f32 v26, v26\n\t"
  "v_mfma_f32_16x16x32_bf16 v[48:51], v[236:239], v[72:75], v[48:51]\n\tv_cvt_pk_bf16_f32 v44, v40, v41\n\tv_cvt_pk_bf16_f32 v45, v42, v43\n\t"
  "v_mfma_f32_16x16x32_bf16 v[16:19], v[232:235], v[72:75], v[16:19]\n\tds_read_b128 v[40:43], v3 offset:384\n\ts_waitcnt lgkmcnt(1)\n\t"
  "v_mfma_f32_16x16x32_bf16 v[48:51], v[248:251], v[56:59], v[48:51]\n\tv_exp_f32 v24, v24\n\t"
  "v_mfma_f32_16x16x32_bf16 v[16:19], v[244:247], v[56:59], v[16:19]\n\tv_cvt_pk_bf16_f32 v10, v26, v24\n\tv_mul_f32 v26, v26, v70\n\t"
  "v_mfma_f32_16x16x32_bf16 v[32:35], v[60:63], v[180:183], v[32:35]\n\tv_cvt_pk_bf16_f32 v48, v48, v49\n\tv_cvt_pk_bf16_f32 v49, v50, v51\n\tv_mfma_f32_16x16x16_bf16 v[52:55], v[8:9], v[10:11], 0\n\t"
  "v_cvt_pk_bf16_f32 v16, v16, v17\n\tv_cvt_pk_bf16_f32 v17, v18, v19\n\t"
  "v_mfma_f32_16x16x32_bf16 v[32:35], v[28:31], v[192:195], v[32:35]\n\tv_mul_f32 v24, v24, v68\n\tv_mfma_f32_16x16x16_bf16 v[28:31], v[48:49], v[16:17], 0\n\tv_mfma_f32_16x16x16_bf16 v[48:51], v[64:65], v[16:17], 0\n\t"
  "v_cvt_pk_bf16_f32 v26, v26, v24\n\tv_perm_b32 v16, v26, v26, v2\n\t"
  "v_mfma_f32_16x16x32_bf16 v[32:35], v[20:23], v[204:207], v[32:35]\n\tv_cndmask_b32_e64 v39, v28, v29, s[68:69]\n\tv_cndmask_b32_e64 v24, v30, v31, s[68:69]\n\t"
  "v_mfma_f32_16x16x32_bf16 v[32:35], v[12:15], v[216:219], v[32:35]\n\tv_cndmask_b32_e64 v28, v48, v49, s[68:69]\n\tv_cndmask_b32_e64 v39, v28, v39, s[74:75]\n\t"
  "v_mfma_f32_16x16x32_bf16 v[32:35], v[4:7], v[228:231], v[32:35]\n\tv_mul_f32 v39, v39, v71\n\tv_cndmask_b32_e64 v29, v50, v51, s[68:69]\n\t"
  "v_mfma_f32_16x16x32_bf16 v[32:35], v[72:75], v[240:243], v[32:35]\n\tv_cndmask_b32_e64 v24, v29, v24, s[76:77]\n\tv_mul_f32 v24, v24, v69\n\t"
  "v_mfma_f32_16x16x32_bf16 v[32:35], v[56:59], v[252:255], v[32:35]\n\t"
  GST(14)
  "v_max_f32 v29, v39, v24\n\tv_max_f32 v28, v39, v24\n\tv_perm_b32 v17, v26, v26, v37\n\tv_perm_b32 v18, v26, v26, v1\n\t"
  "v_permlane16_swap_b32 v28, v29\n\tv_max_f32 v30, v28, v29\n\tv_max_f32 v31, v28, v29\n\tv_perm_b32 v19, v26, v26, v0\n\t"
  "s_waitcnt lgkmcnt(0)\n\tv_fma_f32 v32, -v40, v25, v32\n\tv_permlane32_swap_b32 v30, v31\n\tv_max_f32 v28, v30, v31\n\t"
  "v_mul_f32_e64 v28, -v28, v67\n\tv_fma_f32 v39, v39, v67, v28\n\tv_fma_f32 v24, v24, v67, v28\n\tv_exp_f32 v39, v39\n\t"
  "v_exp_f32 v24, v24\n\tv_fma_f32 v33, -v41, v25, v33\n\tv_cvt_pk_bf16_f32 v10, v39, v24\n\tv_mul_f32 v39, v39, v71\n\t"
  "v_fma_f32 v34, -v42, v25, v34\n\tv_fma_f32 v35, -v43, v25, v35\n\tv_cvt_pk_bf16_f32 v48, v32, v33\n\tv_cvt_pk_bf16_f32 v49, v34, v35\n\t"
  "v_mul_f32 v24, v24, v69\n\tv_mfma_f32_16x16x16_bf16 v[28:31], v[8:9], v[10:11], 0\n\tv_cvt_pk_bf16_f32 v39, v39, v24\n\tv_perm_b32 v40, v39, v39, v2\n\t"
  "v_perm_b32 v41, v39, v39, v37\n\tv_perm_b32 v42, v39, v39, v1\n\t"
  "v_mfma_f32_16x16x32_bf16 v[22:25], v[44:47], v[16:19], 0\n\tv_perm_b32 v43, v39, v39, v0\n\tv_rcp_f32 v52, v52\n\tv_rcp_f32 v28, v28\n\t"
  "v_mfma_f32_16x16x32_bf16 v[4:7], v[46:49], v[40:43], 0\n\ts_nop 0\n\tv_mul_f32 v22, v22, v52\n\tv_mul_f32 v23, v23, v52\n\t"
  "v_mul_f32 v24, v24, v52\n\tv_mul_f32 v25, v25, v52\n\tv_cvt_pk_bf16_f32 v64, v22, v23\n\tv_cvt_pk_bf16_f32 v65, v24, v25\n\t"
  "s_nop 0\n\tv_mul_f32 v4, v4, v28\n\tv_mul_f32 v5, v5, v28\n\tv_mul_f32 v6, v6, v28\n\t"
  "v_mul_f32 v7, v7, v28\n\tv_cvt_pk_bf16_f32 v66, v4, v5\n\tv_cvt_pk_bf16_f32 v67, v6, v7\n\ts_nop 0\n\t"
  "v_permlane16_swap_b32 v64, v66\n\tv_permlane16_swap_b32 v65, v67\n\tbuffer_store_dwordx4 v[64:67], v27, s[56:59], 0 offen sc1\n\t"
  "s_cmp_eq_u32 s63, 2\n\ts_cbranch_scc1 .Lg_ret\n\ts_nop 0"
  G_OPS);
  else asm volatile("s_branch .Lg_body\n.Lg_ret:\n\ts_nop 0" G_OPS);
  static_for<0, 45>(pin);
  ST(!L1, 16);
  if (L1) {
    KSLP(7, SLP_OB1);
    cnt_add(smem, C_OB, 0);
    mid();
    cnt_wait(smem, C_X2, 4);
    return;
  }
  u32 t16 = KADD(8, now_(), DB2);
  cnt_add(smem, C_OB, 0);
  const int bid = BIDX, lane = lane_id();
  int og0 = OFF_O2 + MB_ + RB_OF(bid) * 32768 + g * 4096, og1 = og0 + 16384, vo = lane * 16;
  u32 sa = (u32)(L_TB + g * 4096 + lane * 64 + (lane & 14) * 4 + 4);
  asm volatile("" : "+s"(og0), "+s"(og1), "+v"(vo), "+v"(sa));
  u32 dl;
  for (unsigned n = 0; n < (1u << 24); ++n) {
    asm volatile("" ::: "memory");
    const u32 c0 = *(const volatile LAS u32*)(size_t)(u32)(L_MI + C_OV), c1 = *(const volatile LAS u32*)(size_t)(u32)(L_MI + C_OB);
    dl = *(const volatile LAS u32*)(size_t)(u32)(L_MI + M_DL);
    if ((u32)__builtin_amdgcn_readfirstlane(min(c0 + 11u, c1)) >= 16u) break;
  }
  gate(t16 + dl);
  ST(1, 15);
  const u32 mark = x0_mark(tag);
  o_get(wrs, vo, og0, og1, L_TB + g * 4096, sa, mark, 25);
  asm volatile("s_barrier\n\ts_waitcnt vmcnt(0)\n\ts_barrier" ::: "memory");
  if (__any(*(const LAS u32*)(size_t)(sa + 16384) == mark)) {
    FAILC(12);
    for (unsigned spins = 0; spins <= SPIN_CAP; ++spins) {
      dma4k(wrs, vo, og0, L_TA + g * 4096);
      dma4k(wrs, vo, og1, L_TA + 16384 + g * 4096);
      asm volatile("s_waitcnt vmcnt(0)" ::: "memory");
      if (!__any(*(const LAS u32*)(size_t)(sa - (L_TB - L_TA)) == mark || *(const LAS u32*)(size_t)(sa - (L_TB - L_TA) + 16384) == mark)) break;
    }
    smem[L_MI + M_FX + g] = 1;
    asm volatile("s_waitcnt lgkmcnt(0)" ::: "memory");
  }
}

using u32x8 = __attribute__((ext_vector_type(8))) unsigned;
using u32x16 = __attribute__((ext_vector_type(16))) unsigned;
__global__ __launch_bounds__(512, 2) KATTR void k_mega(const u16*, const u16*, const u16*, const u16*, const u16*, const u16*, const u16*, const u16*,
                                                       const u16*, const u16*, const u16*, char*, u32*) {
  extern __shared__ __attribute__((aligned(16))) char smem[];
  u32x16 ka;
  u32x8 kb;
  u32x2 kc;
  asm volatile("s_load_dwordx16 %0, %3, 0x0\n\ts_load_dwordx8 %1, %3, 0x40\n\ts_load_dwordx2 %2, %3, 0x60"
               : "=&s"(ka), "=&s"(kb), "=&s"(kc) : "s"(__builtin_amdgcn_kernarg_segment_ptr()) : "memory");
  const int tid = TIDX, lane = tid & 63;
  const int w = __builtin_amdgcn_readfirstlane(tid >> 6);
  const int bid = BIDX;
  ST_INIT(); ST(0, 0); ST(1, 0);
  if (w == 0) {
    u64 pc;
    int clen;
    asm volatile("s_getpc_b64 %0\n.Lcw_base:\n\ts_mov_b32 %1, .Lcw_end-.Lcw_base-4" : "=s"(pc), "=s"(clen));
    asm volatile("s_mov_b32 m0, %2\n\ts_nop 0\n\tglobal_load_lds_dword %0, %1" :: "v"(min(min(lane, 15) * 128 + (bid >> 3) * 2048, clen)), "s"(pc), "s"(L_FS) : "memory");
  }
  const int kp = (bid & 7) * 32 + (bid >> 3), hb = HB_OF(bid);
  const int c = (w >> 1) & 1, g = w & 3;
  u32 po = (u32)(kp * 512 + (tid & 255) * 2);
  const int pb = (bid & 7) + 8 * (bid >> 6), ps = (bid >> 3) & 7, gr = pb * 8 + ps;
  u32 xo = (u32)((w < 4 ? (pb * 64 + 16 * w) * 1024 + ps * 128 : gr * 8192 + g * 2048) + (lane & 7) * 16 + (lane >> 3) * (w < 4 ? 2048 : 128));
  asm volatile("" : "+v"(po), "+v"(xo) :: "memory");
  asm volatile("s_waitcnt lgkmcnt(0)" : "+s"(ka), "+s"(kb), "+s"(kc) :: "memory");
  ST_FIX();
#define KP(T_, v_, i_) ((T_)(((u64)v_[2 * (i_) + 1] << 32) | v_[2 * (i_)]))
  const u16 *x0 = KP(const u16*, ka, 0), *A_re = KP(const u16*, ka, 1), *A_im = KP(const u16*, ka, 2), *B_re = KP(const u16*, ka, 3), *B_im = KP(const u16*, ka, 4);
  const u16 *C_re = KP(const u16*, ka, 5), *C_im = KP(const u16*, ka, 6), *log_dt = KP(const u16*, ka, 7), *Dskip = KP(const u16*, kb, 0), *wqkv = KP(const u16*, kb, 1), *wo = KP(const u16*, kb, 2);
  char* ws = KP(char*, kb, 3);
  u32* out = KP(u32*, kc, 0);
#define LD_A() asm volatile("global_load_ushort v248, %0, %1\n\tglobal_load_ushort v249, %0, %2" :: "v"(po), "s"(A_re), "s"(A_im) : "memory", "v248", "v249")
  u32 ldtw = 0, dskw = 0;
  if (w < 4) {
    asm volatile("s_load_dword %0, %2, %4\n\ts_load_dword %1, %3, %4" : "=&s"(ldtw), "=&s"(dskw) : "s"(log_dt), "s"(Dskip), "s"(4 * kp) : "memory");
    LD_A();
  }
  asm volatile("global_load_dwordx4 v[240:243], %0, %1\n\tglobal_load_dwordx4 v[244:247], %0, %1 offset:1024\n\tglobal_load_dword v254, %2, %3 sc1"
               :: "v"(xo), "s"(x0), "v"(OFF_EP), "s"(ws) : "memory", "v240", "v254");
  if (w >= 4) {
    asm volatile("global_load_ushort v250, %0, %1\n\tglobal_load_ushort v251, %0, %2\n\tglobal_load_ushort v252, %0, %3\n\tglobal_load_ushort v253, %0, %4"
                 :: "v"(po), "s"(B_re), "s"(B_im), "s"(C_re), "s"(C_im) : "memory", "v250", "v253");
    LD_A();
  }
  __builtin_amdgcn_sched_barrier(0);
  const rsrc_t wrs = __builtin_amdgcn_make_buffer_rsrc((void*)ws, 0, WSB, 0x00020000);
  if (w >= 4) {
    asm volatile("s_mov_b32 m0, %2\n\ts_nop 0\n\tglobal_load_lds_dword %0, %1" :: "v"((bid * 16 + g * 4 + min(lane, 3)) * 128), "s"(wqkv), "s"(L_FS + w * 256) : "memory");
    u32x4 zz = {0, 0, 0, 0};
    asm volatile("" : "+v"(zz));
    const int z0 = (int)opaque((u32)(L_U + (lane >> 5) * 4608 + (lane & 31) * 16)), z1 = (int)opaque((u32)(L_MI + min((tid & 255) * 16 - 1024, 1088)));
    *(u32x4*)(smem + (g == 0 ? z0 : z1)) = zz;
    asm volatile("s_nop 1\n\ts_waitcnt lgkmcnt(0)" :: "v"(zz) : "memory");
  }
  if (w >= 4) asm volatile("s_load_dword %0, %1, %2" : "=s"(ldtw) : "s"(log_dt), "s"(4 * kp) : "memory");
  __builtin_amdgcn_sched_barrier(0);
#define XT_GET(x_) asm volatile("v_mov_b64 %0, v[240:241]\n\tv_mov_b64 %1, v[242:243]\n\tv_mov_b64 %2, v[244:245]\n\tv_mov_b64 %3, v[246:247]" \
                   : "=v"(x_[0].xy), "=v"(x_[0].zw), "=v"(x_[1].xy), "=v"(x_[1].zw) :: "memory")
  const int ftb = c ? L_TA : 0;
#define PUT(e_, r_, i_) *(LAS u32*)(wa + (e_) * FTS) = pkh(r_, i_)
#define CMR(a_, b_) fmaf(a_[0], b_[0], -(a_[1] * b_[1]))
#define CMI(a_, b_) fmaf(a_[0], b_[1], a_[1] * b_[0])
#define CMUL(a_, b_) (f32x2){CMR(a_, b_), CMI(a_, b_)}
  struct P0A { u32 gad[12], sad[8], ua, ur; };
  auto p0_addr = [&](P0A& A, int lane, int ut) __attribute__((always_inline)) {
    const int i = lane & 15, kk = lane >> 4;
    const u32 sb = (u32)((L_KT - KTO + c * TBS - 0x7E00) & 0xFFFF) * 0x10001u;
    const float f0 = __uint_as_float(opaque(__float_as_uint((float)((ut < 2 ? 1023 : 2047) - 512 * (w & 1) - 8 * lane))));
#pragma unroll
    for (int e = 0; e < 6; ++e) { const u32 ga = tb_addr(pk2(f0 - (float)(2 * e), f0 - (float)(2 * e + 1)), sb); A.gad[2 * e] = ga & 0xFFFFu; A.gad[2 * e + 1] = ga >> 16; }
    if (ut < 2) {
      A.ua = (u32)(ftb + i * FTS + kk * 16); A.ur = A.ua + ut * 16 * FTS;
      const int a0 = 128 * (i >> 3) + (i & 7), a1 = i < 8 ? 256 + 128 * (i >> 2) + 2 * (i & 3) : 128 * (i >> 1) + 4 * (i & 1), a2 = min(1024 + 128 * i, 2064);
#pragma unroll
      for (int t = 0; t < 2; ++t) {
        const float st = (t && i >= 8) ? 16.f : 8.f;
        const float fb = (float)(t ? a2 : ut == 0 ? a0 : a1) + st * (float)(4 * kk);
        const u32 s0 = tb_addr(pk2(fb, fb + st), sb), s1 = tb_addr(pk2(fb + 2.f * st, fb + 3.f * st), sb);
        A.sad[4 * t] = s0 & 0xFFFFu; A.sad[4 * t + 1] = s0 >> 16; A.sad[4 * t + 2] = s1 & 0xFFFFu; A.sad[4 * t + 3] = s1 >> 16;
      }
      asm volatile("" : "+v"(A.ua), "+v"(A.ur), "+v"(A.sad[0]), "+v"(A.sad[1]), "+v"(A.sad[2]), "+v"(A.sad[3]), "+v"(A.sad[4]), "+v"(A.sad[5]), "+v"(A.sad[6]), "+v"(A.sad[7]));
    }
    asm volatile("" : "+v"(A.gad[0]), "+v"(A.gad[1]), "+v"(A.gad[2]), "+v"(A.gad[3]), "+v"(A.gad[4]), "+v"(A.gad[5]), "+v"(A.gad[6]), "+v"(A.gad[7]), "+v"(A.gad[8]), "+v"(A.gad[9]), "+v"(A.gad[10]));
    __builtin_amdgcn_sched_barrier(0);
  };
  auto k_unit = [&](const P0A& A, float dk, bool lag0) __attribute__((always_inline)) {
    f16x8 fr[8], fc[8];
    const f32x2 bd = *(const f32x2*)(smem + L_P0 + 512 + c * 8);
    __builtin_amdgcn_sched_barrier(0);
#pragma unroll
    for (int e = 0; e < 8; ++e) {
      fc[e] = *(const LAS f16x8*)(size_t)(A.ua + e * 64 + 41 * FTS);
      fr[e] = *(const LAS f16x8*)(size_t)(A.ur + e * 64);
      __builtin_amdgcn_sched_barrier(0);
    }
    f16x8 f2[8];
    if (!(w & 1)) {
#pragma unroll
      for (int e = 0; e < 8; ++e) { f2[e] = *(const LAS f16x8*)(size_t)(A.ua + e * 64 + 32 * FTS); __builtin_amdgcn_sched_barrier(0); }
    }
    const int ec = bexp(bd[0] + bd[1] + fabsf(dk));
    const float f0 = __uint_as_float((u32)max(127 + bexp(bd[0]) - ec, 0) << 23), f1 = __uint_as_float((u32)max(127 + bexp(bd[1]) - ec, 0) << 23);
    const float d0s = lag0 ? dk * __uint_as_float((u32)(268 - ec) << 23) : 0.f;
    auto unit = [&](const f16x8 (&fr)[8], u32x4 sa, float add) __attribute__((always_inline)) {
      f32x4 k0 = {0.f, 0.f, 0.f, 0.f}, k1 = k0;
#pragma unroll
      for (int e = 0; e < 4; ++e) k0 = __builtin_amdgcn_mfma_f32_16x16x32_f16(fc[e], fr[e], k0, 0, 0, 0);
#pragma unroll
      for (int e = 4; e < 8; ++e) k1 = __builtin_amdgcn_mfma_f32_16x16x32_f16(fc[e], fr[e], k1, 0, 0, 0);
      k0 *= f0; k0[0] += add;
      const u32 h01 = pkh(fmaf(k1[0], f1, k0[0]), fmaf(k1[1], f1, k0[1])), h23 = pkh(fmaf(k1[2], f1, k0[2]), fmaf(k1[3], f1, k0[3]));
      *(LAS u16*)((LAS char*)(size_t)sa[0] + KTO) = (u16)h01; *(LAS u16*)((LAS char*)(size_t)sa[1] + KTO) = (u16)(h01 >> 16);
      *(LAS u16*)((LAS char*)(size_t)sa[2] + KTO) = (u16)h23; *(LAS u16*)((LAS char*)(size_t)sa[3] + KTO) = (u16)(h23 >> 16);
    };
    if (!(w & 1)) {
      unit(fr, (u32x4){A.sad[0], A.sad[1], A.sad[2], A.sad[3]}, d0s);
      unit(f2, (u32x4){A.sad[4], A.sad[5], A.sad[6], A.sad[7]}, 0.f);
    } else {
      unit(fr, (u32x4){A.sad[0], A.sad[1], A.sad[2], A.sad[3]}, 0.f);
      *(float*)(smem + L_MI + 4 * c) = __uint_as_float((u32)(ec - 14) << 23);
    }
  };
  auto k_get = [&](u32 (&kh)[11], const P0A& A) __attribute__((always_inline)) {
#pragma unroll
    for (int e = 0; e < 11; ++e) kh[e] = *(const LAS u16*)((const LAS char*)(size_t)A.gad[e] + KTO);
    __builtin_amdgcn_sched_barrier(0);
  };
  auto k_copies = [&](const u32 (&kh)[11], int lane, bool low) __attribute__((always_inline)) {
    u32 kw[6];
#pragma unroll
    for (int e = 0; e < 5; ++e) kw[e] = __builtin_amdgcn_perm(kh[2 * e + 1], kh[2 * e], 0x05040100u);
    kw[5] = kh[10];
    if (low && (w & 1) && lane == 63) { kw[4] = 0; kw[5] = 0; }
    build_copies(smem, c, (w & 1) * 64 + lane + (low ? 128 : 0), kw);
    __builtin_amdgcn_sched_barrier(0);
  };
  __builtin_amdgcn_sched_barrier(0);
  int w2 = w;
  asm volatile("" : "+s"(w2));
  if (w < 4) {
  P0A pa;
  p0_addr(pa, lane, w & 1);
  LAS char* wa = (LAS char*)(size_t)opaque((u32)(ftb + (tid & 127) * 4));
  u32 tg;
  const bool lo = pb < 16;
  const int xc8 = lane >> 3, xrp = lane & 7;
  const int xts = lo ? 128 : XTS, xt2 = 2 * xts;
  const int xto = (int)opaque((u32)(lo ? OFF_X1 + (pb * 4 + w) * XBS + (ps * 32 + 4 * xc8) * 128 + xrp * 16
                                       : XTO(ps * 32 + 4 * xc8) + ((pb & 15) * 64 + 16 * w + 2 * xrp) * 8));
  auto xpub = [&]() __attribute__((always_inline)) {
    u32x4 q[4];
    u32x4 xt[2];
    __builtin_amdgcn_sched_barrier(0);
    asm volatile("s_waitcnt vmcnt(0)\n\tv_add_u32 %0, 1, v254" : "=v"(tg) :: "memory");
    XT_GET(xt);
    const int sl = ((lane & 7) * 8 + (lane >> 3)) * 4;
#pragma unroll
    for (int e = 0; e < 8; ++e) xt[e & 1][e >> 1] = (u32)__builtin_amdgcn_ds_bpermute(sl, (int)xt[e & 1][e >> 1]);
#pragma unroll
    for (int e = 0; e < 4; ++e) q[e] = (u32x4){xt[0][e], x0_mark(tg), xt[1][e], x0_mark(tg)};
    asm volatile("" : "+v"(q[0]), "+v"(q[1]), "+v"(q[2]), "+v"(q[3]));
#pragma unroll
    for (int e = 0; e < 4; ++e) stw(wrs, xto + (e & 1) * xts + (e >> 1) * xt2, q[e]);
    asm volatile("s_nop 1" :: "v"(q[0]), "v"(q[1]), "v"(q[2]), "v"(q[3]) : "memory");
    ST(0, 3);
    __builtin_amdgcn_sched_barrier(0);
  };
  u32 ha, hz, hs, ht;
  C1Pre cp;
  int xs0, xs1, xl0, xl1, xv;
  u32 za;
  auto setup1 = [&]() __attribute__((always_inline)) {
    SB();
    const int t = (w & 1) * 64 + lane;
    ha = (u32)(L_KT + c * TBS - 0x7E00 + 2 * (0x44FF - t)); hz = ha - (t == 127 ? 2 : 0);
    hs = (lane & 1) ? 0x01000302u : 0x03020302u; ht = (lane & 1) ? 0x01000100u : 0x01000302u;
    asm volatile("" : "+v"(ha), "+v"(hz), "+v"(hs), "+v"(ht));
    SB();
  };
  auto setup2 = [&]() __attribute__((always_inline)) {
    SB();
    c1_pre(cp, kp, w, lane);
    xs0 = OFF_X1 + RB_OF(bid) * XBS + w * 8192; xs1 = xs0 + 4096; xl0 = L_TA + w * 8192; xl1 = xl0 + 4096; xv = x_lane(lane);
    za = (u32)(((lane >> 1) & 7) * CSTR + 4096 + (lane & 1) * 16);
    asm volatile("" : "+s"(xs0), "+s"(xs1), "+s"(xl0), "+s"(xl1), "+v"(xv), "+v"(za));
    SB();
  };
  {
    float ar, ai;
#define B0() do { SB(); __builtin_amdgcn_s_barrier(); SB(); } while (0)
    B0();
    asm volatile("s_waitcnt vmcnt(3)\n\tv_lshlrev_b32 %0, 16, v248\n\tv_lshlrev_b32 %1, 16, v249" : "=v"(ar), "=v"(ai) :: "memory");
    asm volatile("s_waitcnt lgkmcnt(0)" : "+s"(ldtw), "+s"(dskw) :: "memory");
    ST(0, 1);
    const float dt = rbf(__expf(c ? hi2f(ldtw) : lo2f(ldtw)));
    const float zr = dt * ar * 1.4426950408889634f, zi = dt * ai * 0.15915494309189535f;
    float cs = __builtin_amdgcn_exp2f(fmaxf(zr, 0.f) * -1928.f);
    f32x2 P[8], Q1, R[17];
    { float a_, b_; cexp2(zr, zi, a_, b_); P[1] = (f32x2){a_, b_}; cexp2(zr * 128.f, zi * 128.f, a_, b_); Q1 = (f32x2){a_, b_}; }
    asm volatile("" : "+v"(cs), "+v"(P[1]), "+v"(Q1));
    xpub();
    P[2] = CMUL(P[1], P[1]);
    const f32x2 Q2 = CMUL(Q1, Q1);
    R[1] = Q1 * cs; R[2] = Q2 * cs;
    P[3] = CMUL(P[2], P[1]); P[4] = CMUL(P[2], P[2]);
    const f32x2 Q4 = CMUL(Q2, Q2);
    R[3] = CMUL(R[1], Q2); R[4] = Q4 * cs;
    P[5] = CMUL(P[4], P[1]); P[6] = CMUL(P[3], P[3]); P[7] = CMUL(P[4], P[3]);
#pragma unroll
    for (int j = 5; j <= 16; ++j) R[j] = CMUL(R[j - 4], Q4);
    asm volatile("" : "+v"(R[13]), "+v"(R[14]), "+v"(R[15]), "+v"(R[16]), "+v"(P[7]), "+v"(P[6]), "+v"(P[5]));
    PUT(0, cs, 0.f); PUT(8, R[1][0], R[1][1]);
#pragma unroll
    for (int r = 1; r < 8; ++r) { PUT(r, P[r][0] * cs, P[r][1] * cs); PUT(8 + r, CMR(R[1], P[r]), CMI(R[1], P[r])); }
    asm volatile("" ::: "memory");
#pragma unroll
    for (int j = 2; j < 4; ++j) {
      PUT(16 + 4 * (j - 2), R[j][0], R[j][1]);
#pragma unroll
      for (int r = 1; r < 4; ++r) PUT(16 + 4 * (j - 2) + r, CMR(R[j], P[2 * r]), CMI(R[j], P[2 * r]));
    }
#pragma unroll
    for (int j = 4; j < 8; ++j) { PUT(24 + 2 * (j - 4), R[j][0], R[j][1]); PUT(25 + 2 * (j - 4), CMR(R[j], P[4]), CMI(R[j], P[4])); }
#pragma unroll
    for (int j = 8; j <= 16; ++j) PUT(24 + j, R[j][0], R[j][1]);
    __builtin_amdgcn_sched_barrier(0);
  }
  ST(0, 2);
  cnt_add(smem, C_F, lane);
  u32 stag = __builtin_amdgcn_readfirstlane(tg), g2;
  asm volatile("" : "+s"(stag));
  cnt_spin(smem, C_F, 8);
  k_unit(pa, c ? hi2f(dskw) : lo2f(dskw), (tid & 127) == 0);
  cnt_add(smem, C_A, lane);
  setup1();
  setup2();
  cnt_spin(smem, C_A, 4);
  ST(0, 4);
  {
    u32 kl[11];
    k_get(kl, pa);
    const u32 h0 = *(const LAS u16*)(size_t)ha, h1 = *(const LAS u16*)(size_t)(ha + 2), h2 = *(const LAS u16*)(size_t)hz;
    SB();
    u32x4 zz = {0, 0, 0, 0};
    asm volatile("" : "+v"(zz));
    *(LAS u32x4*)(size_t)za = zz;
    k_copies(kl, lane, true);
    asm volatile("" :: "v"(zz));
    cnt_add(smem, C_A, lane);
    ST(0, 5);
    {
      const u32 p = h0 | (h1 << 16), hh = __builtin_amdgcn_perm(p, p, 0x03020302u), ll = __builtin_amdgcn_perm(p, p, 0x01000100u);
      const u32 hx = __builtin_amdgcn_perm(p, p, hs), xl = __builtin_amdgcn_perm(p, p, ht);
      u32x4 c0 = {hh, hx, ll, ll}, c1 = {hh, xl, ll, ll}, c2 = {hx, ll, ll, ll}, c3 = {xl, ll, ll, h0 | (h2 << 16)};
      asm volatile("" : "+v"(c0), "+v"(c1), "+v"(c2), "+v"(c3));
      char* cop = smem + c * 4 * CSTR + ((w & 1) * 64 + lane) * 16;
      *(u32x4*)(cop + 0 * CSTR) = c0; *(u32x4*)(cop + 1 * CSTR) = c1; *(u32x4*)(cop + 2 * CSTR) = c2; *(u32x4*)(cop + 3 * CSTR) = c3;
      asm volatile("s_nop 1" :: "v"(c0), "v"(c1), "v"(c2), "v"(c3) : "memory");
      cnt_add(smem, C_KH, lane);
    }
  }
  cnt_spin(smem, C_A, 8);
  bf16x8 wof[16];
  u32x4 pfl, wfl;
  XTake xt;
  AttC ac;
  a_conv1(wrs, w, cp, pfl, wfl, g2);
  asm volatile("" : "+v"(pfl), "+v"(wfl));
  SB();
  KSLP(15, SLP_XA1);
  tags_wait(pfl, wrs, OFF_ST0 + tid * 16, stag, 19);
  dma4k(wrs, xv, xs0, xl0);
  dma4k(wrs, xv, xs1, xl1);
  KSLP(14, SLP_WE);
  SB();
  ConvK ck;
  a_blayer<true>(smem, wrs, ws, out, tid, w, wof, stag, wfl, ck, xt, ac);
  ST(1, 2);
  a_conv<false>(smem, wrs, RB_OF(bid), w, ck);
  a_blayer<false>(smem, wrs, ws, out, tid, w, wof, stag, wfl, ck, xt, ac);
  ST_OUT();
  }
  if (w2 >= 4) {
    const int lane = lane_id();
    LAS char* wa = (LAS char*)(size_t)opaque((u32)(ftb + (w & 1) * 256 + lane * 4));
    u32 tag;
    u32 st0, st1;
    int pbo;
    auto cols = [&]() __attribute__((always_inline)) {
    {
      float ar, ai, br, bi, cr, ci;
      asm volatile("v_lshlrev_b32 %0, 16, v248\n\tv_lshlrev_b32 %1, 16, v249\n\tv_lshlrev_b32 %2, 16, v250\n\t"
                   "v_lshlrev_b32 %3, 16, v251\n\tv_lshlrev_b32 %4, 16, v252\n\tv_lshlrev_b32 %5, 16, v253"
                   : "=v"(ar), "=v"(ai), "=v"(br), "=v"(bi), "=v"(cr), "=v"(ci) :: "memory");
      asm volatile("s_waitcnt lgkmcnt(0)" : "+s"(ldtw) :: "memory");
      const float dt = rbf(__expf(c ? hi2f(ldtw) : lo2f(ldtw)));
      const float dbr = dt * br, dbi = dt * bi;
      const float zr = dt * ar * 1.4426950408889634f, zi = dt * ai * 0.15915494309189535f;
      const float zp = fmaxf(zr, 0.f), g1 = __builtin_amdgcn_exp2f(zp * 1928.f);
      const float wr = (cr * dbr - ci * dbi) * g1, wi = (cr * dbi + ci * dbr) * g1;
      float bd = (fabsf(wr) + fabsf(wi)) * __builtin_amdgcn_exp2f(zp * 120.f);
      f32x2 S1, C[16];
      { float a_, b_; cexp2(zr * 8.f, zi * 8.f, a_, b_); S1 = (f32x2){a_, b_}; }
      bd = dpp_add<0x111, 0xf>(bd); bd = dpp_add<0x112, 0xf>(bd);
      const f32x2 S2 = CMUL(S1, S1);
      bd = dpp_add<0x114, 0xf>(bd); bd = dpp_add<0x118, 0xf>(bd);
      const f32x2 S4 = CMUL(S2, S2);
      bd = dpp_add<0x142, 0xa>(bd); bd = dpp_add<0x143, 0xc>(bd);
      bd = __int_as_float(__builtin_amdgcn_readlane(__float_as_int(bd), 63));
      *(float*)(smem + L_P0 + 512 + g * 4) = bd;
      const float sc = __uint_as_float((u32)(268 - bexp(bd)) << 23);
      C[0] = (f32x2){wr * sc, wi * sc};
      C[1] = CMUL(C[0], S1); C[2] = CMUL(C[0], S2); C[3] = CMUL(C[1], S2);
#pragma unroll
      for (int e = 4; e < 16; ++e) C[e] = CMUL(C[e - 4], S4);
#pragma unroll
      for (int e = 0; e < 16; ++e) *(LAS u32*)(wa + (41 + e) * FTS) = pkhn(C[e][0], C[e][1]);
    }
      cnt_add(smem, C_F, lane);
      ST(0, 2);
    };
    auto stats = [&]() __attribute__((always_inline)) {
    {
      const u32 ONES = opaque(0x3F803F80u);
      u32x4 xt[2];
      XT_GET(xt);
      float sm[2] = {0.f, 0.f}, sq[2] = {0.f, 0.f};
#pragma unroll
      for (int e = 0; e < 8; ++e) { sm[e & 1] = dot2acc(xt[e & 1][e >> 1], ONES, sm[e & 1]); sq[e & 1] = dot2acc(xt[e & 1][e >> 1], xt[e & 1][e >> 1], sq[e & 1]); }
      const bool b0 = lane & 1, b1 = lane & 2;
      const float v0 = (b0 ? sm[1] : sm[0]) + dpp_mov<DPP_XOR1>(b0 ? sm[0] : sm[1]), v1 = (b0 ? sq[1] : sq[0]) + dpp_mov<DPP_XOR1>(b0 ? sq[0] : sq[1]);
      float v = (b1 ? v1 : v0) + dpp_mov<DPP_XOR2>(b1 ? v0 : v1);
      v += dpp_mov<0x124>(v); v += dpp_mov<DPP_ROR8>(v);
      auto r = __builtin_amdgcn_permlane16_swap(__float_as_uint(v), __float_as_uint(v), false, false);
      v = __uint_as_float(r[0]) + __uint_as_float(r[1]);
      auto q = __builtin_amdgcn_permlane32_swap(__float_as_uint(v), __float_as_uint(v), false, false);
      v = (__uint_as_float(q[0]) + __uint_as_float(q[1])) * (1.f / 512.f);
      st0 = pack_stats(v, fmaxf(dpp_mov<DPP_XOR2>(v) - v * v, 0.f));
      st1 = dpp_u<DPP_XOR1>(st0);
    }
    };
    auto publish = [&]() __attribute__((always_inline)) {
      asm volatile("v_add_u32 %0, 1, v254" : "=v"(tag) :: "memory");
      stw(wrs, pbo, (u32x4){st0, tag, st1, tag});
      __builtin_amdgcn_sched_barrier(0);
    };
#define VMW(n_) asm volatile("s_waitcnt vmcnt(%0)" :: "n"(n_) : "memory")
    constexpr int PTN = 2;
    const int ut = 16 + (bid >> 3), us = ut / 3, um = ut % 3;
    const rsrc_t qrs = __builtin_amdgcn_make_buffer_rsrc((void*)wqkv, 0, 1572864, 0x00020000), ors = __builtin_amdgcn_make_buffer_rsrc((void*)wo, 0, 524288, 0x00020000);
    const int wv0 = (8 * g + (lane >> 3)) * 3072 + (((lane & 7) ^ ((lane >> 3) & 6)) << 4);
#define W_DMA1(r_, v_, s_, l_) asm volatile("s_mov_b32 m0, %3\n\ts_nop 0\n\tbuffer_load_dwordx4 %0, %1, %2 offen lds" :: "v"(v_), "s"(r_), "s"(s_), "s"(l_) : "memory")
    W_DMA1(qrs, wv0, us * 98304 + um * 1024 + (bid & 7) * 128, L_WR + g * 1024);
    const int hs = (3 - (int)(kb[2] >> 7)) & 7, xt_ = bid - 117, xs = (xt_ / 3) * 98304 + (xt_ % 3) * 1024;
    const bool xp = bid >= 128 && bid < 133;
    W_DMA1(xp ? qrs : ors, xp ? wv0 : ((bid >> 3) * 32 + 8 * g + (lane >> 3)) * 1024 + (((lane & 7) ^ (((lane >> 3) & 2) | ((g & 1) << 2))) << 4), xp ? xs + hs * 128 : (bid & 7) * 128, L_WR + 4096 + g * 1024);
    B0();
    const int ln = lane;
    pbo = (int)opaque((u32)(lane == 0 ? OFF_SG + gr * 64 + g * 16 : WSB));
    const int wvo = (int)opaque((u32)(wv0 + hb * 128)), wvx = (int)opaque((u32)(hb == hs ? WSB : wvo));
#define WSO(s_) ((s_) < 4 ? 40960 + (s_) * 12288 : L_WX - L_WS)
    auto wraw = [&](auto S_) __attribute__((always_inline)) { constexpr int s = decltype(S_)::value; w_dma(qrs, s < 4 ? wvo : wvx, s < 3 ? wvo : wvx, s * 98304, L_WS + WSO(s) + g * 1024); };
    __builtin_amdgcn_s_setprio(1);
    VMW(8 + PTN);
    ST(0, 1);
    stats();
    VMW(7 + PTN);
    publish();
    ST(0, 10);
    VMW(2 + PTN);
    cols();
    SB();
    __builtin_amdgcn_s_setprio(0);
    static_for<0, R1D>(wraw);
    VMW(3 * R1D + 1); cnt_add(smem, C_B, lane);
    SB();
    const u32 wga = opaque((u32)(L_WS + (ln >> 2) * 128 + (((2 * g + ((ln & 3) >> 1)) ^ ((ln >> 2) & 6)) << 4) + (ln & 1) * 8)), wgb = opaque(wga + 53248u);
    const u32 to = opaque(xp ? wga + (L_WR - L_WS + 4096) : (u32)(L_WR + 4096 + (8 * (lane >> 4) + ((lane >> 2) & 3)) * 128 + (((2 * g + ((lane & 3) >> 1)) ^ (((lane >> 2) & 2) | ((lane >> 2) & 4))) << 4) + (lane & 1) * 8));
    const u32 tp = opaque(to + (xp ? 2048 : 512));
    const int us0 = (int)opaque((u32)(OFF_WQ + us * 98304 + ((bid & 7) * 4 + g) * 3072 + um * 1024 + lane * 16));
    const int us1 = (int)opaque((u32)((bid < 128 ? OFF_WO + (((bid & 7) * 16 + (bid >> 3)) * 4 + g) * 1024 : xp ? OFF_WQ + xs + (hs * 4 + g) * 3072 : WSB) + lane * 16));
    cnt_spin(smem, C_B, 4);
    {
#define TR4(a_) __builtin_amdgcn_ds_read_tr16_b64_v4i16((LAS bf16x4*)(size_t)(a_))
      const bf16x4 q0 = TR4(wgb + (L_WR - L_WS - 53248)), q1 = TR4(wgb + (L_WR - L_WS - 53248 + 2048)), q2 = TR4(to), q3 = TR4(tp);
      cnt_add(smem, C_B, lane);
      stw(wrs, us0, __builtin_bit_cast(u32x4, __builtin_shufflevector(q0, q1, 0, 1, 2, 3, 4, 5, 6, 7)));
      stw(wrs, us1, __builtin_bit_cast(u32x4, __builtin_shufflevector(q2, q3, 0, 1, 2, 3, 4, 5, 6, 7)));
      SB();
    }
    static_for<R1D, R1F>(wraw);
    SB();
    const u32 stag = __builtin_amdgcn_readfirstlane(tag);
    const int fs0 = OFF_ST0 + 4096 + ((bid & 7) * 4 + g) * 128 + (bid >> 3) * 4, fs1 = bid < 128 ? fs0 + 4096 : bid < 136 ? OFF_ST0 + 8192 + (hs * 4 + g) * 128 + (bid - 112) * 4 : WSB;
    const int fso = (int)opaque((u32)(lane == 0 ? fs0 : lane == 1 ? fs1 : WSB));
    const int wbo = (int)opaque((u32)((hb * 4 + g) * 3072 + ln * 16));
    const int fo = (int)opaque((u32)(OFF_ST0 + 4096 + (hb * 4 + g) * 128 + (hb == hs && (ln & 8) ? 4096 + 64 + (ln & 1) * 16 : (ln & 7) * 16)));
    const int r0 = 256 * g + 2 * ln;
    const int sto = (int)opaque((u32)(OFF_SG + r0 * 8)), xto = (int)opaque((u32)(XTO(kp) + r0 * 8));
    const int xlo = (int)opaque((u32)(OFF_X1 + (16 * g + (ln >> 3)) * XBS + kp * 128 + (ln & 7) * 16));
    const u32 uw = opaque((u32)(L_U + (256 + r0) * 2)), xw = opaque((u32)(L_X + r0 * 4));
    const int mso = (int)opaque((u32)(ln == 0 ? OFF_ST0 + kp * 16 + g * 4 : WSB));
    const int p2o = (int)opaque((u32)(OFF_X2 + (kp * XS_ + 256 * g + 4 * ln) * 4)), pso = (int)opaque((u32)(g == 0 && ln < 8 ? OFF_ST2 + bid * 128 + ln * 16 : WSB));
    SB();
    VMW(3 * (R1F - R1D));
    __builtin_amdgcn_raw_buffer_store_b32(tag, wrs, fso, 0, 16);
    ST(0, 6);
    SB();
    static_for<R1F, 5>(wraw);
    W_DMA1(qrs, wvx, 5 * 98304, L_SP + g * 1024);
    bf16x8 wf[48];
    GPre gp;
    {
      auto wget = [&](auto S_) __attribute__((always_inline)) {
        constexpr int s = decltype(S_)::value, o = WSO(s) >= 53248 ? WSO(s) - 53248 : WSO(s);
        w_get<3 * s, o>(wf[3 * s], WSO(s) >= 53248 ? wgb : wga);
        w_get<3 * s + 1, o + 4096>(wf[3 * s + 1], WSO(s) >= 53248 ? wgb : wga);
        w_get<3 * s + 2, o + 8192>(wf[3 * s + 2], WSO(s) >= 53248 ? wgb : wga);
      };
      const u32 mk = x0_mark(stag);
      auto wld = [&](auto I_) __attribute__((always_inline)) { w_ld<decltype(I_)::value>(wf[decltype(I_)::value], wbo, wrs); };
      u32x4 gs[4], gx[4];
#define G_LD(e_) do { gs[e_] = ldws(wrs, sto + ((e_) & 1) * 1024, ((e_) >> 1) * 8192); gx[e_] = (e_) < 2 ? ldws(wrs, xlo, (e_) * 8 * XBS) : ldw(wrs, xto + ((e_) - 2) * 1024); } while (0)
#define G_B2(v_, t_) opaque((v_[1] ^ (t_)) | (v_[3] ^ (t_)))
#define G_OK(h_) (__builtin_amdgcn_ballot_w64(((G_B2(gs[2 * (h_)], stag) | G_B2(gs[2 * (h_) + 1], stag)) | (G_B2(gx[2 * (h_)], mk) | G_B2(gx[2 * (h_) + 1], mk))) != 0u) == 0ull)
      auto take = [&](auto H_) __attribute__((always_inline)) {
        constexpr int H = decltype(H_)::value;
        if (__builtin_expect(!G_OK(H), 0)) {
          for (unsigned spins = 0;; ++spins) {
            FAILC(H ? 4 : 0);
            __builtin_amdgcn_s_sleep(SLP_U);
            asm volatile("" ::: "memory");
            G_LD(2 * H); G_LD(2 * H + 1);
            if (G_OK(H) || spins > SPIN_CAP) break;
          }
        }
#pragma unroll
        for (int e = 2 * H; e < 2 * H + 2; ++e) {
          const float rs0 = __builtin_amdgcn_rcpf(hi2f(gs[e][0])), rs1 = __builtin_amdgcn_rcpf(hi2f(gs[e][2])), n0 = -lo2f(gs[e][0]) * rs0, n1 = -lo2f(gs[e][2]) * rs1;
          const int r = (e & 1) * 128 + (e >> 1) * 1024;
          *(LAS u32*)(size_t)(uw + r * 2) = pkh(fmaf(lo2f(gx[e][0]), rs0, n0), fmaf(lo2f(gx[e][2]), rs1, n1));
          *(LAS u32*)(size_t)(uw + r * 2 + 4608) = pkh(fmaf(hi2f(gx[e][0]), rs0, n0), fmaf(hi2f(gx[e][2]), rs1, n1));
          *(LAS u32x2*)(size_t)(xw + r * 4) = (u32x2){gx[e][0], gx[e][2]};
        }
        __builtin_amdgcn_sched_barrier(0);
      };
      KSLP(16, SLP_G);
      asm volatile("" ::: "memory");
#pragma unroll
      for (int e = 0; e < 4; ++e) G_LD(e);
      __builtin_amdgcn_sched_barrier(0);
      ST(0, 5);
      f32x4 cs[3];
      u32x4 SENT = {0xFFFFFFFFu, 0xFFFFFFFFu, 0xFFFFFFFFu, 0xFFFFFFFFu};
      const u32 o1 = ((ln >> 1) & 1) == (ln >> 5) ? 0x3F803F80u : 0u;
      bf16x8 one8 = __builtin_bit_cast(bf16x8, (u32x4){o1, o1, o1, o1});
      u32 sidx = (ln & 1) ? 0xEEEEu : 0x4444u;
      cs[0] = cs[1] = cs[2] = (f32x4){0.f, 0.f, 0.f, 0.f};
      asm volatile("" : "+v"(one8), "+v"(sidx), "+v"(cs[0]), "+v"(cs[1]), "+v"(cs[2]), "+v"(SENT));
      auto wsum = [&](auto I_) __attribute__((always_inline)) { constexpr int i = 2 * decltype(I_)::value + 1; w_sum<i>(cs[i % 3], one8, sidx, wf); };
      cnt_spin(smem, C_B, 8);
      __builtin_amdgcn_s_setprio(1);
      VMW(8);
      cnt_add(smem, C_W, ln);
      u32x4 fl;
      asm volatile("" : "+v"(gs[0]), "+v"(gs[1]), "+v"(gx[0]), "+v"(gx[1]) :: "memory");
      take(IC<0>{});
      cnt_add(smem, C_U, ln);
      __builtin_amdgcn_raw_buffer_store_b32(tag, wrs, mso, 0, 16);
      ST(0, 3);
      stw(wrs, p2o, SENT); __builtin_amdgcn_raw_buffer_store_b128(SENT, wrs, p2o, 4096, 16);
      stw(wrs, pso, __builtin_bit_cast(u32x4, cs[0]));
      asm volatile("s_nop 1" :: "v"(SENT) : "memory");
      SB();
      auto rest = [&]() __attribute__((always_inline)) {
        cnt_spin(smem, C_W, 4);
        static_for<0, 5>(wget);
        w_get<15, L_SP - L_WS - 53248>(wf[15], wgb);
        cnt_add(smem, C_B, ln);
        take(IC<1>{});
        cnt_add(smem, C_P2, ln);
        ST(0, 4);
      };
      rest();
      { KSLP(17, SLP_FL); fl = ldw(wrs, fo); SB(); }
      asm volatile("" : "+v"(gs[2]), "+v"(gs[3]), "+v"(gx[2]), "+v"(gx[3]), "+v"(fl) :: "memory");
      tags_wait(fl, wrs, fo, stag, 15);
      asm volatile("s_waitcnt vmcnt(0) lgkmcnt(0)" ::: "memory");
      cnt_add(smem, C_P3, ln);
      ST(0, 7);
      asm volatile("s_cmp_lg_u32 %5, %6\n\ts_cbranch_scc1 1f\n\tbuffer_load_dwordx4 %0, %7, %8, %9 offen offset:2048 sc1\n\t"
                   "buffer_load_dwordx4 %1, %7, %8, %10 offen sc1\n\tbuffer_load_dwordx4 %2, %7, %8, %10 offen offset:1024 sc1\n\tbuffer_load_dwordx4 %3, %7, %8, %10 offen offset:2048 sc1\n\t"
                   "buffer_load_dwordx4 %4, %7, %8, %11 offen sc1\n1:"
                   : "+{v[120:123]}"(wf[11]), "+{v[124:127]}"(wf[12]), "+{v[128:131]}"(wf[13]), "+{v[132:135]}"(wf[14]), "+{v[136:139]}"(wf[15])
                   : "s"(hb), "s"(hs), "v"(wbo), "s"(wrs), "s"(OFF_WQ + 3 * 98304), "s"(OFF_WQ + 4 * 98304), "s"(OFF_WQ + 5 * 98304) : "memory", "scc");
      static_for<45, 48>(wld);
      static_for<16, 45>(wld);
      __builtin_amdgcn_s_setprio(0);
      ST(0, 9);
      g_pre<true>(gp, g, stag);
      asm volatile("s_waitcnt lgkmcnt(0)" ::: "memory");
      static_for<0, 5>(wsum);
      cnt_spin(smem, C_B, 12);
      VMW(29);
#pragma unroll
      for (int e = 0; e < 3; ++e) *(LAS bf16x8*)(size_t)(gp.wx + e * 1024) = wf[45 + e];
      static_for<22, 24>(wsum);
      static_for<5, 7>(wsum);
      VMW(12); static_for<7, 16>(wsum);
      VMW(6); static_for<16, 19>(wsum);
      VMW(2); static_for<19, 21>(wsum);
      VMW(0); static_for<21, 22>(wsum);
      asm volatile("s_nop 7" : "+v"(cs[0]), "+v"(cs[1]), "+v"(cs[2]));
#pragma unroll
      for (int e = 0; e < 3; ++e) *(LAS float*)(size_t)(gp.fr + 512 + e * 64) = (cs[e][0] + cs[e][1]) + (cs[(e + 2) % 3][2] + cs[(e + 2) % 3][3]);
      asm volatile("" ::: "memory");
      ST(0, 8);
    }
    {
      ConvPre pa2, pb2;
      u32x4 xa, xb;
      int ln2, l2, xo2, xo, so;
      u32 ua, rp0, rp1;
      float isc;
      g_blayer<true>(smem, wrs, g, stag, wf, gp, [&]() __attribute__((always_inline)) {
        ln2 = lane_id();
        l2 = 256 * g + 4 * ln2;
        xo2 = (int)opaque((u32)(OFF_X3 + (8 * g + (ln2 >> 3)) * XBS + kp * 128 + (ln2 & 1) * 64 + (ln2 & 6) * 8));
        xo = (int)opaque((u32)(OFF_X2 + (kp * XS_ + l2) * 4)); so = (int)opaque((u32)(OFF_ST2 + (256 * g + ln2) * 16));
        ua = opaque((u32)(L_U + (256 + l2) * 2));
        rp0 = opaque((u32)(L_REDA + (ln2 & 1) * 8192 + (g * 64 + (ln2 ^ ((ln2 >> 3) & 3))) * 16)); rp1 = opaque(rp0 ^ 16u);
        isc = *(const float*)(smem + L_MI + 4 * (ln2 & 1));
        asm volatile("" : "+v"(isc));
      });
#define VM0() __builtin_amdgcn_s_waitcnt(0x0F70)
      KSLP(9, SLP_C2);
      u32x4 gg[4];
      ST(1, 2);
      xa = ldw(wrs, xo);
      asm volatile("" : "+v"(xa));
      if (__builtin_expect(!x2_ok(xa), 0)) { FAILC(16); __builtin_amdgcn_s_sleep(SLP_XP); xa = x2_ld(wrs, kp, l2); x2_poll(wrs, kp, l2, xa); VM0(); }
      KSLP(10, SLP_S);
      asm volatile("" ::: "memory");
#pragma unroll
      for (int e = 0; e < 4; ++e) gg[e] = ldw(wrs, so + e * 1024);
      SB();
      conv_unp(pa2, xa);
      asm volatile("" : "+v"(gg[0]), "+v"(gg[1]), "+v"(gg[2]), "+v"(gg[3]));
      if (__builtin_expect(!st2_ok(gg), 0)) { FAILC(7); __builtin_amdgcn_s_sleep(SLP_SP); st2_ld(wrs, l2, gg); st2_poll(wrs, l2, gg); VM0(); }
      conv2_in<false>(smem, ua, pa2, xa, gg);
      ST(1, 3);
      lds_barrier();
      ST(1, 7);
      conv_res(pa2);
      const int so2 = (int)opaque((u32)(so + 16384));
      auto rq_b = [&]() __attribute__((always_inline)) {
        KSLP(11, SLP_C3);
        asm volatile("" ::: "memory");
        xb = ldws(wrs, xo, 4096);
        SB();
#pragma unroll
        for (int e = 0; e < 4; ++e) gg[e] = ldw(wrs, so2 + e * 1024);
        SB();
      };
      rq_b();
      __builtin_amdgcn_s_barrier();
      asm volatile("" ::: "memory");
      ST(1, 8);
      stw(wrs, xo2, conv_epi(rp0, rp1, 0, isc, pa2));
      SB();
      asm volatile("" : "+v"(xb), "+v"(gg[0]), "+v"(gg[1]), "+v"(gg[2]), "+v"(gg[3]) :: "memory");
      ST(1, 4);
      if (__builtin_expect(!x2st_ok(xb, gg), 0)) {
        FAILC(18);
        xb = x2_ld(wrs, kp, l2 + 1024);
        st2_ld(wrs, l2 + 1024, gg);
        if (!x2_ok(xb)) {
          x2_poll(wrs, kp, l2 + 1024, xb);
          st2_ld(wrs, l2 + 1024, gg);
        }
        st2_poll(wrs, l2 + 1024, gg);
        VM0();
      }
      ST(1, 1);
      const int rb = RB_OF(bid);
      if (XPB == 2) { x_dma(wrs, OFF_X3, rb, L_TA, g, ln2); SB(); }
      conv2_in<true>(smem, ua + 2048, pb2, xb, gg);
      ST(1, 5);
      lds_barrier();
      ST(1, 6);
      if (XPB == 1) { KSLP(12, SLP_XA2); x_dma(wrs, OFF_X3, rb, L_TA, g, ln2); SB(); }
      conv_res(pb2);
      if (XPB == 0) { KSLP(12, SLP_XA2); x_dma(wrs, OFF_X3, rb, L_TA, g, ln2); }
      SB();
      g_pre<false>(gp, g, stag);
      const int xo3 = (int)opaque((u32)(xo2 + 32 * XBS));
      if (GPE) __builtin_amdgcn_s_setprio(GPE);
      asm volatile("s_waitcnt vmcnt(0)\n\ts_barrier" ::: "memory");
      ST(1, 9);
      u32x4 ov = conv_epi(rp0, rp1, 16384, isc, pb2);
      stw(wrs, xo3, ov);
      if (GPE) __builtin_amdgcn_s_setprio(0);
      asm volatile("s_nop 1" :: "v"(ov));
    }
    g_blayer<false>(smem, wrs, g, stag, wf, gp, []() {});

    ST_OUT();
  }
}

__device__ __attribute__((used, noinline)) void k_text_end() {
  asm volatile(
  ".Lg_redo:\n\ts_waitcnt lgkmcnt(0)\n\t"
  "ds_read_b64_tr_b16 v[72:73], v0 offset:0\n\tds_read_b64_tr_b16 v[74:75], v0 offset:1024\n\ts_waitcnt lgkmcnt(0)\n\t"
  "v_mfma_f32_16x16x32_bf16 v[60:63], v[80:83], v[72:75], 0\n\tv_mfma_f32_16x16x32_bf16 v[16:19], v[72:75], v[84:87], 0\n\tv_mfma_f32_16x16x32_bf16 v[52:55], v[76:79], v[72:75], 0\n\t"
  "ds_read_b64_tr_b16 v[72:73], v0 offset:2048\n\tds_read_b64_tr_b16 v[74:75], v0 offset:3072\n\ts_waitcnt lgkmcnt(0)\n\t"
  "v_mfma_f32_16x16x32_bf16 v[60:63], v[92:95], v[72:75], v[60:63]\n\tv_mfma_f32_16x16x32_bf16 v[16:19], v[72:75], v[96:99], v[16:19]\n\tv_mfma_f32_16x16x32_bf16 v[52:55], v[88:91], v[72:75], v[52:55]\n\t"
  "ds_read_b64_tr_b16 v[72:73], v0 offset:8192\n\tds_read_b64_tr_b16 v[74:75], v0 offset:9216\n\ts_waitcnt lgkmcnt(0)\n\t"
  "v_mfma_f32_16x16x32_bf16 v[60:63], v[128:131], v[72:75], v[60:63]\n\tv_mfma_f32_16x16x32_bf16 v[16:19], v[72:75], v[132:135], v[16:19]\n\tv_mfma_f32_16x16x32_bf16 v[52:55], v[124:127], v[72:75], v[52:55]\n\t"
  "ds_read_b64_tr_b16 v[72:73], v0 offset:10240\n\tds_read_b64_tr_b16 v[74:75], v0 offset:11264\n\ts_waitcnt lgkmcnt(0)\n\t"
  "v_mfma_f32_16x16x32_bf16 v[60:63], v[140:143], v[72:75], v[60:63]\n\tv_mfma_f32_16x16x32_bf16 v[16:19], v[72:75], v[144:147], v[16:19]\n\tv_mfma_f32_16x16x32_bf16 v[52:55], v[136:139], v[72:75], v[52:55]\n\t"
  "ds_read_b64_tr_b16 v[72:73], v0 offset:16384\n\tds_read_b64_tr_b16 v[74:75], v0 offset:17408\n\ts_waitcnt lgkmcnt(0)\n\t"
  "v_mfma_f32_16x16x32_bf16 v[60:63], v[176:179], v[72:75], v[60:63]\n\tv_mfma_f32_16x16x32_bf16 v[16:19], v[72:75], v[180:183], v[16:19]\n\tv_mfma_f32_16x16x32_bf16 v[52:55], v[172:175], v[72:75], v[52:55]\n\t"
  "ds_read_b64_tr_b16 v[72:73], v0 offset:18432\n\tds_read_b64_tr_b16 v[74:75], v0 offset:19456\n\ts_waitcnt lgkmcnt(0)\n\t"
  "v_mfma_f32_16x16x32_bf16 v[60:63], v[188:191], v[72:75], v[60:63]\n\tv_mfma_f32_16x16x32_bf16 v[16:19], v[72:75], v[192:195], v[16:19]\n\tv_mfma_f32_16x16x32_bf16 v[52:55], v[184:187], v[72:75], v[52:55]\n\t"
  "ds_read_b64_tr_b16 v[72:73], v0 offset:24576\n\tds_read_b64_tr_b16 v[74:75], v0 offset:25600\n\ts_waitcnt lgkmcnt(0)\n\t"
  "v_mfma_f32_16x16x32_bf16 v[60:63], v[224:227], v[72:75], v[60:63]\n\tv_mfma_f32_16x16x32_bf16 v[16:19], v[72:75], v[228:231], v[16:19]\n\tv_mfma_f32_16x16x32_bf16 v[52:55], v[220:223], v[72:75], v[52:55]\n\t"
  "ds_read_b64_tr_b16 v[72:73], v0 offset:26624\n\tds_read_b64_tr_b16 v[74:75], v0 offset:27648\n\ts_waitcnt lgkmcnt(0)\n\t"
  "v_mfma_f32_16x16x32_bf16 v[60:63], v[236:239], v[72:75], v[60:63]\n\tv_mfma_f32_16x16x32_bf16 v[16:19], v[72:75], v[240:243], v[16:19]\n\tv_mfma_f32_16x16x32_bf16 v[52:55], v[232:235], v[72:75], v[52:55]\n\t"
  "ds_read_b64_tr_b16 v[72:73], v0 offset:4096\n\tds_read_b64_tr_b16 v[74:75], v0 offset:5120\n\ts_waitcnt lgkmcnt(0)\n\t"
  "v_mfma_f32_16x16x32_bf16 v[60:63], v[104:107], v[72:75], v[60:63]\n\tv_mfma_f32_16x16x32_bf16 v[16:19], v[72:75], v[108:111], v[16:19]\n\tv_mfma_f32_16x16x32_bf16 v[52:55], v[100:103], v[72:75], v[52:55]\n\t"
  "ds_read_b64_tr_b16 v[72:73], v0 offset:6144\n\tds_read_b64_tr_b16 v[74:75], v0 offset:7168\n\tds_read_b64_tr_b16 v[28:29], v0 offset:12288\n\tds_read_b64_tr_b16 v[30:31], v0 offset:13312\n\tds_read_b64_tr_b16 v[68:69], v0 offset:14336\n\tds_read_b64_tr_b16 v[70:71], v0 offset:15360\n\tds_read_b64_tr_b16 v[12:13], v0 offset:20480\n\tds_read_b64_tr_b16 v[14:15], v0 offset:21504\n\t"
  "s_waitcnt lgkmcnt(0)\n\ts_branch .Lg_back\n\t"
  ".Lc0_kh:\n\ts_mov_b32 vcc_hi, 0x1000000\n\t.Lc0_kh_s:\n\tds_read_b32 v101, v27 offset:48\n\ts_sub_u32 vcc_hi, vcc_hi, 1\n\ts_cselect_b32 vcc_lo, 0, 4\n\ts_waitcnt lgkmcnt(0)\n\tv_readfirstlane_b32 s61, v101\n\ts_nop 1\n\ts_cmp_lt_u32 s61, vcc_lo\n\ts_cbranch_scc1 .Lc0_kh_s\n\t"
  "s_waitcnt lgkmcnt(0)\n\ts_branch .Lc0_kh_b\n\t"
  ".Lc0_x0:\n\ts_mov_b32 vcc_hi, 0x1000000\n\t.Lc0_x0_s:\n\tds_read_b32 v101, v21 offset:8\n\ts_sub_u32 vcc_hi, vcc_hi, 1\n\ts_cselect_b32 vcc_lo, 0, 2\n\ts_waitcnt lgkmcnt(0)\n\tv_readfirstlane_b32 s61, v101\n\ts_nop 1\n\ts_cmp_lt_u32 s61, vcc_lo\n\ts_cbranch_scc1 .Lc0_x0_s\n\t"
  "ds_read_b128 v[144:147], v20 offset:2048\n\tds_read_b128 v[204:207], v20 offset:3072\n\ts_waitcnt lgkmcnt(0)\n\ts_branch .Lc0_x0_b\n\t"
  ".Lc0_ub:\n\ts_mov_b32 vcc_hi, 0x1000000\n\t.Lc0_ub_s:\n\tds_read_b32 v101, v27 offset:52\n\ts_sub_u32 vcc_hi, vcc_hi, 1\n\ts_cselect_b32 vcc_lo, 0, 4\n\ts_waitcnt lgkmcnt(0)\n\tv_readfirstlane_b32 s61, v101\n\ts_nop 1\n\ts_cmp_lt_u32 s61, vcc_lo\n\ts_cbranch_scc1 .Lc0_ub_s\n\t"
  "s_waitcnt lgkmcnt(0)\n\ts_branch .Lc0_ub_b\n\t"
  ".Lc0_p3:\n\ts_mov_b32 vcc_hi, 0x1000000\n\t.Lc0_p3_s:\n\tds_read_b32 v101, v27 offset:64\n\ts_sub_u32 vcc_hi, vcc_hi, 1\n\ts_cselect_b32 vcc_lo, 0, 4\n\ts_waitcnt lgkmcnt(0)\n\tv_readfirstlane_b32 s61, v101\n\ts_nop 1\n\ts_cmp_lt_u32 s61, vcc_lo\n\ts_cbranch_scc1 .Lc0_p3_s\n\t"
  "s_waitcnt lgkmcnt(0)\n\ts_branch .Lc0_p3_b\n\t"
  ".Lc0_x4:\n\ts_mov_b32 vcc_hi, 0x1000000\n\t.Lc0_x4_s:\n\tds_read_b32 v36, v27 offset:16\n\ts_sub_u32 vcc_hi, vcc_hi, 1\n\ts_cselect_b32 vcc_lo, 0, 4\n\ts_waitcnt lgkmcnt(0)\n\tv_readfirstlane_b32 s61, v36\n\ts_nop 1\n\ts_cmp_lt_u32 s61, vcc_lo\n\ts_cbranch_scc1 .Lc0_x4_s\n\t"
  "ds_read_b128 v[40:43], v20 offset:59392\n\tds_read_b128 v[44:47], v20 offset:60416\n\ts_waitcnt lgkmcnt(0)\n\ts_branch .Lc0_x4_b\n\t"
  ".Lc1_kh:\n\ts_mov_b32 vcc_hi, 0x1000000\n\t.Lc1_kh_s:\n\tds_read_b32 v101, v27 offset:48\n\ts_sub_u32 vcc_hi, vcc_hi, 1\n\ts_cselect_b32 vcc_lo, 0, 4\n\ts_waitcnt lgkmcnt(0)\n\tv_readfirstlane_b32 s61, v101\n\ts_nop 1\n\ts_cmp_lt_u32 s61, vcc_lo\n\ts_cbranch_scc1 .Lc1_kh_s\n\t"
  "s_waitcnt lgkmcnt(0)\n\ts_branch .Lc1_kh_b\n\t"
  ".Lc1_x0:\n\ts_mov_b32 vcc_hi, 0x1000000\n\t.Lc1_x0_s:\n\tds_read_b32 v101, v21 offset:8\n\ts_sub_u32 vcc_hi, vcc_hi, 1\n\ts_cselect_b32 vcc_lo, 0, 2\n\ts_waitcnt lgkmcnt(0)\n\tv_readfirstlane_b32 s61, v101\n\ts_nop 1\n\ts_cmp_lt_u32 s61, vcc_lo\n\ts_cbranch_scc1 .Lc1_x0_s\n\t"
  "ds_read_b128 v[148:151], v20 offset:0\n\tds_read_b128 v[204:207], v20 offset:1024\n\ts_waitcnt lgkmcnt(0)\n\ts_branch .Lc1_x0_b\n\t"
  ".Lc1_ub:\n\ts_mov_b32 vcc_hi, 0x1000000\n\t.Lc1_ub_s:\n\tds_read_b32 v101, v27 offset:52\n\ts_sub_u32 vcc_hi, vcc_hi, 1\n\ts_cselect_b32 vcc_lo, 0, 4\n\ts_waitcnt lgkmcnt(0)\n\tv_readfirstlane_b32 s61, v101\n\ts_nop 1\n\ts_cmp_lt_u32 s61, vcc_lo\n\ts_cbranch_scc1 .Lc1_ub_s\n\t"
  "s_waitcnt lgkmcnt(0)\n\ts_branch .Lc1_ub_b\n\t"
  ".Lc1_p3:\n\ts_mov_b32 vcc_hi, 0x1000000\n\t.Lc1_p3_s:\n\tds_read_b32 v101, v27 offset:64\n\ts_sub_u32 vcc_hi, vcc_hi, 1\n\ts_cselect_b32 vcc_lo, 0, 4\n\ts_waitcnt lgkmcnt(0)\n\tv_readfirstlane_b32 s61, v101\n\ts_nop 1\n\ts_cmp_lt_u32 s61, vcc_lo\n\ts_cbranch_scc1 .Lc1_p3_s\n\t"
  "s_waitcnt lgkmcnt(0)\n\ts_branch .Lc1_p3_b\n\t"
  ".Lc1_x4:\n\ts_mov_b32 vcc_hi, 0x1000000\n\t.Lc1_x4_s:\n\tds_read_b32 v36, v27 offset:16\n\ts_sub_u32 vcc_hi, vcc_hi, 1\n\ts_cselect_b32 vcc_lo, 0, 4\n\ts_waitcnt lgkmcnt(0)\n\tv_readfirstlane_b32 s61, v36\n\ts_nop 1\n\ts_cmp_lt_u32 s61, vcc_lo\n\ts_cbranch_scc1 .Lc1_x4_s\n\t"
  "ds_read_b128 v[40:43], v20 offset:57344\n\tds_read_b128 v[44:47], v20 offset:58368\n\ts_waitcnt lgkmcnt(0)\n\ts_branch .Lc1_x4_b\n\t"
  ".Lcw_end:");
}

extern "C" void kernel_launch(void* const* d_in, const int* in_sizes, int n_in, void* d_out, int out_size,
                              void* d_ws, size_t ws_size, hipStream_t stream) {
  (void)hipFuncSetAttribute((const void*)k_mega, hipFuncAttributeMaxDynamicSharedMemorySize, MEGA_LDS);
  k_mega<<<256, 512, MEGA_LDS, stream>>>((const u16*)d_in[0], (const u16*)d_in[1], (const u16*)d_in[2], (const u16*)d_in[3],
                                         (const u16*)d_in[4], (const u16*)d_in[5], (const u16*)d_in[6], (const u16*)d_in[7],
                                         (const u16*)d_in[8], (const u16*)d_in[10], (const u16*)d_in[11], (char*)d_ws,
                                         (u32*)d_out);
}
#if defined(__HIP_DEVICE_COMPILE__)
#pragma clang attribute pop
#endif
```
